```python
import torch

def _softmax(x, axis=-1):
    return torch.softmax(x, dim=axis)
def _gelu(x):
    return torch.nn.functional.gelu(x, approximate="tanh")
def _silu(x):
    return torch.nn.functional.silu(x)
def _sigmoid(x):
    return torch.sigmoid(x)
def _layernorm(x, axis=-1, eps=1e-5):
    m = x.mean(dim=axis, keepdim=True)
    v = x.var(dim=axis, keepdim=True, unbiased=False)
    return (x - m) / torch.sqrt(v + eps)
def _scatter_add(tgt, idx, src, axis=0):
    return tgt.clone().index_add_(axis, idx, src)
def _segment_sum(src, seg, n):
    out = torch.zeros((n,) + src.shape[1:], dtype=src.dtype)
    return out.index_add_(0, seg, src)
def _pad(x, pw):
    flat = tuple(v for pair in reversed(pw) for v in pair)
    return torch.nn.functional.pad(x, flat)

kernel_name = 'interleave_layers__linear_kernel_hedgehog__mixer_s_mlp__prompt'

def setup_inputs():

    _w = (lambda a: (lambda t: t.to(torch.bfloat16) if t.is_floating_point() else t)(torch.as_tensor(a)))
    def _A_setup():
        B, S, D, H = 1, 2048, 1024, 32
        Dh = D // H
        return {
            "x": _w((lambda *s: torch.randn(*s, dtype=torch.bfloat16) * 0.05)(B, S, D)),
            "wqkv": _w((lambda *s: torch.randn(*s, dtype=torch.bfloat16) * 0.2)(D, 3 * D)),
            "w_phi": _w((lambda *s: torch.randn(*s, dtype=torch.bfloat16) * 0.2)(H, Dh, Dh) * (Dh ** -0.5)),
            "wo": _w((lambda *s: torch.randn(*s, dtype=torch.bfloat16) * 0.05)(D, D)),
        }
    _A_in = _A_setup()

    def _B_setup():
        B, S, D, F = 1, 2048, 1024, 3584
        return {
            "x":  _w((lambda *s: torch.randn(*s, dtype=torch.bfloat16) * 0.05)(B, S, D)),
            "w1": _w((lambda *s: torch.randn(*s, dtype=torch.bfloat16) * 3.2)(D, D) * (D ** -0.5)),
            "w2": _w((lambda *s: torch.randn(*s, dtype=torch.bfloat16) * 3.2)(D, D) * (D ** -0.5)),
            "w3": _w((lambda *s: torch.randn(*s, dtype=torch.bfloat16) * 3.2)(D, F) * (D ** -0.5)),
            "w4": _w((lambda *s: torch.randn(*s, dtype=torch.bfloat16) * 1.6)(F, D) * (F ** -0.5)),
        }
    _B_in = _B_setup()

    return {
        "A_x": _A_in["x"],
        "A_wqkv": _A_in["wqkv"],
        "A_w_phi": _A_in["w_phi"],
        "A_wo": _A_in["wo"],
        "B_x": _B_in["x"],
        "B_w1": _B_in["w1"],
        "B_w2": _B_in["w2"],
        "B_w3": _B_in["w3"],
        "B_w4": _B_in["w4"],
    }

def reference(A_x, A_wqkv, A_w_phi, A_wo, B_x, B_w1, B_w2, B_w3, B_w4):
    def _A_ref(x, wqkv, w_phi, wo):
        B, S, D = x.shape
        H = 32; Dh = D // H
        qkv = (lambda x, *d: x.permute(*d))((x @ wqkv).reshape(B, S, 3, H, Dh), 2, 0, 3, 1, 4)
        q, k, v = qkv[0], qkv[1], qkv[2]
        pq = torch.einsum("bhsd,hdf->bhsf", q, w_phi)
        pk = torch.einsum("bhsd,hdf->bhsf", k, w_phi)
        phi_q = torch.concatenate([_softmax(pq, axis=-1), _softmax(-pq, axis=-1)], axis=-1)
        phi_k = torch.concatenate([_softmax(pk, axis=-1), _softmax(-pk, axis=-1)], axis=-1)
        kv = torch.einsum("bhsf,bhsd->bhfd", phi_k, v)
        z  = torch.einsum("bhsf->bhf", phi_k)
        num = torch.einsum("bhqf,bhfd->bhqd", phi_q, kv)
        den = torch.einsum("bhqf,bhf->bhq", phi_q, z)[..., None] + 1e-6
        o = num / den
        return (lambda x, *d: x.permute(*d))(o, 0, 2, 1, 3).reshape(B, S, D) @ wo

    def _B_ref(x, w1, w2, w3, w4):
        B, S, D = x.shape
        h = _gelu(_layernorm(x, axis=-1) @ w1)
        c = D // 4
        g0 = _pad(h[:, :, 0*c:1*c], ((0, 0), (1, 0), (0, 0)))[:, :S, :]
        g1 = _pad(h[:, :, 1*c:2*c], ((0, 0), (0, 1), (0, 0)))[:, 1:, :]
        g2 = _pad(h[:, :, 2*c:3*c], ((0, 0), (2, 0), (0, 0)))[:, :S, :]
        g3 = _pad(h[:, :, 3*c:   ], ((0, 0), (0, 2), (0, 0)))[:, 2:, :]
        hs = torch.concatenate([g0, g1, g2, g3], axis=-1) @ w2
        x1 = x + hs
        h2 = _gelu(_layernorm(x1, axis=-1) @ w3) @ w4
        return x1 + h2

    x = A_x
    x = x + _A_ref(_layernorm(x, axis=-1), A_wqkv, A_w_phi, A_wo)
    x = x + _B_ref(_layernorm(x, axis=-1), B_w1, B_w2, B_w3, B_w4)
    x = x + _A_ref(_layernorm(x, axis=-1), A_wqkv, A_w_phi, A_wo)
    x = x + _B_ref(_layernorm(x, axis=-1), B_w1, B_w2, B_w3, B_w4)
    return x
```

```cpp
#include <hip/hip_runtime.h>
#include <hip/hip_bf16.h>
#include <stdint.h>

typedef short bf16x8 __attribute__((ext_vector_type(8)));
typedef short s16x4 __attribute__((ext_vector_type(4)));
typedef float f32x4 __attribute__((ext_vector_type(4)));
typedef float f32x2 __attribute__((ext_vector_type(2)));
typedef unsigned u32x2 __attribute__((ext_vector_type(2)));
typedef unsigned u32x4 __attribute__((ext_vector_type(4)));
typedef __attribute__((address_space(3))) s16x4* lds_s16x4_p;
typedef unsigned short u16;

constexpr int S_ = 2048, D_ = 1024, H_ = 32, F_ = 3584;

__device__ __forceinline__ float bf2f(u16 v) { return __uint_as_float(((unsigned)v) << 16); }
__device__ __forceinline__ float bflo(unsigned v) { return __uint_as_float(v << 16); }
__device__ __forceinline__ float bfhi(unsigned v) { return __uint_as_float(v & 0xffff0000u); }
__device__ __forceinline__ unsigned pk2(float a, float b) {
  __hip_bfloat162 t = __float22bfloat162_rn(make_float2(a, b));
  return *reinterpret_cast<unsigned*>(&t);
}
__device__ __forceinline__ float rbf(float a) { return bflo(pk2(a, 0.f)); }
__device__ __forceinline__ float gelu_t(float x) {
  const float c1 = -2.0f * 0.7978845608028654f * 1.4426950408889634f;
  const float c2 = c1 * 0.044715f;
  float t = x * x;
  float w = x * __builtin_fmaf(t, c2, c1);
  float e = __builtin_amdgcn_exp2f(w);
  return x * __builtin_amdgcn_rcpf(1.0f + e);
}

#define WAIT_V(n) asm volatile("s_waitcnt vmcnt(" #n ")" ::: "memory")
#define WAIT_L0() asm volatile("s_waitcnt lgkmcnt(0)" ::: "memory")
template <int N> __device__ __forceinline__ void wait_vm() {
  asm volatile("s_waitcnt vmcnt(%0)" ::"i"(N) : "memory");
}

__device__ __forceinline__ void glds16(unsigned voff, const void* sbase, unsigned lds) {
  unsigned keep;
  asm volatile("s_mov_b32 %0, m0\n\ts_mov_b32 m0, %3\n\ts_nop 0\n\tglobal_load_lds_dwordx4 %1, %2\n\ts_mov_b32 m0, %0"
               : "=&s"(keep) : "v"(voff), "s"(sbase), "s"(lds) : "memory");
}

enum { EPI_QKV = 0, EPI_WO = 1, EPI_W1 = 2, EPI_W2 = 3, EPI_W3 = 4, EPI_W4 = 5 };

template <int BN, int NST, int KD, int LDA, int LDB, int EPI>
__global__ __launch_bounds__(256, 1) void gemm_kernel(
    const u16* __restrict__ A, const u16* __restrict__ W,
    const float* __restrict__ cs,
    const f32x2* __restrict__ st_in,
    const u16* __restrict__ r0,
    const u16* __restrict__ r1,
    u16* __restrict__ out,
    f32x2* __restrict__ st_out,
    const u16* __restrict__ wphi,
    float* __restrict__ kvp,
    float* __restrict__ zp)
{
  constexpr int NFULL = BN / 64, HALF = (BN % 64) / 32;
  constexpr int A_BYTES = 128 * 64 * 2, B_BYTES = BN * 64 * 2, STAGE = A_BYTES + B_BYTES;
  constexpr int NT = KD / 64, NTW = 2 * NFULL + HALF, LPS = 4 + 2 * NFULL + HALF;
  constexpr int PB = BN * 2 + 16;
  constexpr int EPI_BYTES = (EPI == EPI_QKV) ? (4 * 64 * 144 + 2 * (64 * 32 + 64) * 4) : 128 * PB;
  constexpr int MAINB = NST * STAGE;
  constexpr int RS_OFF = (MAINB > EPI_BYTES ? MAINB : EPI_BYTES);
  constexpr int SMEM = RS_OFF + 128 * 16;
  static_assert(SMEM <= 163840, "LDS");
  __shared__ __attribute__((aligned(16))) char smem[SMEM];

  const int tid = threadIdx.x, lane = tid & 63;
  const int wid = __builtin_amdgcn_readfirstlane(tid >> 6), wm = wid >> 1, wn = wid & 1;
  const int g = lane >> 4, l15 = lane & 15;
  const int xcd = blockIdx.x & 7, idx = blockIdx.x >> 3;
  const int tm = (xcd >> 1) * 4 + (idx & 3), tn = (xcd & 1) * 8 + (idx >> 2);
  const int row0 = tm * 128;

  f32x4 stp[4];
  if constexpr (EPI == EPI_QKV || EPI == EPI_W1 || EPI == EPI_W3 || EPI == EPI_W2) {
    const f32x4* sp = reinterpret_cast<const f32x4*>(st_in + (size_t)(row0 + (tid >> 1)) * 16 + (tid & 1) * 8);
#pragma unroll
    for (int i = 0; i < 4; ++i) stp[i] = sp[i];
  }
  bf16x8 wf[2];
  if constexpr (EPI == EPI_QKV) {
    const int h = tn * 2 + wn;
#pragma unroll
    for (int ft = 0; ft < 2; ++ft)
#pragma unroll
      for (int j = 0; j < 8; ++j) {
        int d = (j < 4) ? 4 * g + j : 16 + 4 * g + (j - 4);
        wf[ft][j] = (short)wphi[(h * 32 + d) * 32 + ft * 16 + l15];
      }
  }

  unsigned voA[4], voB[NFULL > 0 ? NFULL : 1][2], voH = 0;
#pragma unroll
  for (int i = 0; i < 4; ++i) {
    int r = (wid * 4 + i) * 8 + (lane >> 3);
    int ch = (lane & 7) ^ ((r >> 1) & 7);
    voA[i] = (unsigned)(row0 + r) * (LDA * 2) + ch * 16;
  }
#pragma unroll
  for (int b = 0; b < NFULL; ++b) {
    int colb = (EPI == EPI_QKV) ? (b * 1024 + tn * 64) : (tn * BN + b * 64);
#pragma unroll
    for (int j = 0; j < 2; ++j) {
      int r = (wid * 2 + j) * 8 + (lane >> 3);
      int f = ((r >> 1) & 1) | (((r >> 3) & 1) << 1);
      int chp = lane & 7;
      int gch = (((chp >> 1) ^ f) << 1) | (chp & 1);
      voB[b][j] = (unsigned)r * (LDB * 2) + colb * 2 + gch * 16;
    }
  }
  if constexpr (HALF) {
    int r = wid * 16 + (lane >> 2);
    int chp = lane & 3;
    int gch = (((chp >> 1) ^ ((r >> 3) & 1)) << 1) | (chp & 1);
    voH = (unsigned)r * (LDB * 2) + (tn * BN + NFULL * 64) * 2 + gch * 16;
  }
  const char* Ab = reinterpret_cast<const char*>(A);
  const char* Wb = reinterpret_cast<const char*>(W);

  const unsigned lbase = (unsigned)(uintptr_t)(__attribute__((address_space(3))) char*)smem;
  auto stage = [&](int kt, int buf) {
    const char* a = Ab + (size_t)kt * 128;
    const char* w = Wb + (size_t)kt * (64 * LDB * 2);
    const unsigned s = lbase + buf * STAGE;
#pragma unroll
    for (int i = 0; i < 4; ++i) glds16(voA[i], a, s + (wid * 4 + i) * 1024);
#pragma unroll
    for (int b = 0; b < NFULL; ++b)
#pragma unroll
      for (int j = 0; j < 2; ++j) glds16(voB[b][j], w, s + A_BYTES + b * 8192 + (wid * 2 + j) * 1024);
    if constexpr (HALF) glds16(voH, w, s + A_BYTES + NFULL * 8192 + wid * 1024);
  };

  const int q = l15 >> 2, p = lane & 3;
  const int fB = (q >> 1) | ((g & 1) << 1);
  unsigned aoff[2], boff[2], hoff = 0;
#pragma unroll
  for (int ks = 0; ks < 2; ++ks) aoff[ks] = (wm * 64 + l15) * 128 + (((4 * ks + g) ^ (l15 >> 1)) * 16);
#pragma unroll
  for (int u = 0; u < 2; ++u) boff[u] = A_BYTES + (8 * g + q) * 128 + (((2 * wn + u) ^ fB) * 32) + p * 8;
  if constexpr (HALF) hoff = A_BYTES + NFULL * 8192 + (8 * g + q) * 64 + ((wn ^ (g & 1)) * 32) + p * 8;

  auto ldfrag = [&](int buf, int ks, bf16x8(&af)[4], bf16x8(&bfr)[NTW]) {
    const char* s = smem + buf * STAGE;
#pragma unroll
    for (int b = 0; b < NFULL; ++b)
#pragma unroll
      for (int u = 0; u < 2; ++u) {
        s16x4 lo = __builtin_amdgcn_ds_read_tr16_b64_v4i16((lds_s16x4_p)(s + boff[u] + b * 8192 + ks * 4096));
        s16x4 hi = __builtin_amdgcn_ds_read_tr16_b64_v4i16((lds_s16x4_p)(s + boff[u] + b * 8192 + ks * 4096 + 512));
        bfr[b * 2 + u] = __builtin_shufflevector(lo, hi, 0, 1, 2, 3, 4, 5, 6, 7);
      }
    if constexpr (HALF) {
      s16x4 lo = __builtin_amdgcn_ds_read_tr16_b64_v4i16((lds_s16x4_p)(s + hoff + ks * 2048));
      s16x4 hi = __builtin_amdgcn_ds_read_tr16_b64_v4i16((lds_s16x4_p)(s + hoff + ks * 2048 + 256));
      bfr[NTW - 1] = __builtin_shufflevector(lo, hi, 0, 1, 2, 3, 4, 5, 6, 7);
    }
#pragma unroll
    for (int mt = 0; mt < 4; ++mt) af[mt] = *reinterpret_cast<const bf16x8*>(s + aoff[ks] + mt * 2048);
  };

  f32x4 acc[4][NTW];
#pragma unroll
  for (int mt = 0; mt < 4; ++mt)
#pragma unroll
    for (int j = 0; j < NTW; ++j) acc[mt][j] = (f32x4){0.f, 0.f, 0.f, 0.f};

  auto mma = [&](bf16x8(&af)[4], bf16x8(&bfr)[NTW]) {
#pragma unroll
    for (int mt = 0; mt < 4; ++mt)
#pragma unroll
      for (int j = 0; j < NTW; ++j) {
        if (EPI == EPI_QKV && j >= 4)
          acc[mt][j] = __builtin_amdgcn_mfma_f32_16x16x32_bf16(af[mt], bfr[j], acc[mt][j], 0, 0, 0);
        else
          acc[mt][j] = __builtin_amdgcn_mfma_f32_16x16x32_bf16(bfr[j], af[mt], acc[mt][j], 0, 0, 0);
      }
  };

  bf16x8 af0[4], af1[4], bf0[NTW], bf1[NTW];
#pragma unroll
  for (int s = 0; s < NST; ++s) stage(s, s);
  wait_vm<(NST - 1) * LPS>();
  __builtin_amdgcn_s_barrier();
  asm volatile("" ::: "memory");
  ldfrag(0, 0, af0, bf0);
  int buf = 0;
  for (int kt = 0; kt < NT; ++kt) {
    ldfrag(buf, 1, af1, bf1);
    mma(af0, bf0);
    WAIT_L0();
    if (kt + 1 < NT) {
      int rem = NT - 2 - kt;
      if (rem >= NST - 2) wait_vm<(NST - 2) * LPS>();
      else if (NST > 3 && rem == 1) wait_vm<LPS>();
      else if (NST > 4 && rem == 2) wait_vm<2 * LPS>();
      else if (NST > 5 && rem == 3) wait_vm<3 * LPS>();
      else wait_vm<0>();
    }
    __builtin_amdgcn_s_barrier();
    asm volatile("" ::: "memory");
    if (kt + NST < NT) stage(kt + NST, buf);
    int nb = buf + 1 == NST ? 0 : buf + 1;
    if (kt + 1 < NT) ldfrag(nb, 0, af0, bf0);
    mma(af1, bf1);
    buf = nb;
  }
  WAIT_L0();
  __builtin_amdgcn_s_barrier();
  asm volatile("" ::: "memory");

  f32x4* rs = reinterpret_cast<f32x4*>(smem + RS_OFF);
  if constexpr (EPI == EPI_QKV || EPI == EPI_W1 || EPI == EPI_W3 || EPI == EPI_W2) {
    float sm = 0.f;
#pragma unroll
    for (int i = 0; i < 4; ++i) sm += stp[i][0] + stp[i][2];
    sm += __shfl_xor(sm, 1);
    float mean = sm * (1.0f / 1024.f);
    float m2 = 0.f;
#pragma unroll
    for (int i = 0; i < 4; ++i) {
      float d0 = stp[i][0] * (1.0f / 64.f) - mean, d1 = stp[i][2] * (1.0f / 64.f) - mean;
      m2 += stp[i][1] + stp[i][3] + 64.f * (d0 * d0 + d1 * d1);
    }
    m2 += __shfl_xor(m2, 1);
    float var = m2 * (1.0f / 1024.f);
    float r1 = 1.0f / sqrtf(var + 1e-5f);
    float sc = r1;
    if constexpr (EPI == EPI_W1) sc = r1 / sqrtf(var * r1 * r1 + 1e-5f);
    if ((tid & 1) == 0) rs[tid >> 1] = (f32x4){mean, sc, r1, 0.f};
    __syncthreads();
  }

  if constexpr (EPI == EPI_QKV) {
    const int h = tn * 2 + wn;
    bf16x8 qf[4], kf[4], vf[2][2];
    f32x4 csq[2], csk[2];
    float csv[2];
#pragma unroll
    for (int t = 0; t < 2; ++t) {
      csq[t] = *reinterpret_cast<const f32x4*>(cs + h * 32 + t * 16 + 4 * g);
      csk[t] = *reinterpret_cast<const f32x4*>(cs + 1024 + h * 32 + t * 16 + 4 * g);
      csv[t] = cs[2048 + h * 32 + t * 16 + l15];
    }
#pragma unroll
    for (int mt = 0; mt < 4; ++mt) {
      f32x4 st = rs[wm * 64 + mt * 16 + l15];
      unsigned pq_[4], pk_[4];
#pragma unroll
      for (int t = 0; t < 2; ++t) {
        float a[4], b[4];
#pragma unroll
        for (int i = 0; i < 4; ++i) {
          a[i] = st[1] * (acc[mt][t][i] - st[0] * csq[t][i]);
          b[i] = st[1] * (acc[mt][2 + t][i] - st[0] * csk[t][i]);
        }
        pq_[t * 2] = pk2(a[0], a[1]); pq_[t * 2 + 1] = pk2(a[2], a[3]);
        pk_[t * 2] = pk2(b[0], b[1]); pk_[t * 2 + 1] = pk2(b[2], b[3]);
      }
      qf[mt] = __builtin_bit_cast(bf16x8, (u32x4){pq_[0], pq_[1], pq_[2], pq_[3]});
      kf[mt] = __builtin_bit_cast(bf16x8, (u32x4){pk_[0], pk_[1], pk_[2], pk_[3]});
    }
#pragma unroll
    for (int s = 0; s < 2; ++s) {
      unsigned pv[2][4];
#pragma unroll
      for (int m2 = 0; m2 < 2; ++m2) {
        int mt = s * 2 + m2;
        f32x4 s0 = rs[wm * 64 + mt * 16 + 4 * g + 0], s1 = rs[wm * 64 + mt * 16 + 4 * g + 1];
        f32x4 s2 = rs[wm * 64 + mt * 16 + 4 * g + 2], s3 = rs[wm * 64 + mt * 16 + 4 * g + 3];
#pragma unroll
        for (int t = 0; t < 2; ++t) {
          float a0 = s0[1] * (acc[mt][4 + t][0] - s0[0] * csv[t]);
          float a1 = s1[1] * (acc[mt][4 + t][1] - s1[0] * csv[t]);
          float a2 = s2[1] * (acc[mt][4 + t][2] - s2[0] * csv[t]);
          float a3 = s3[1] * (acc[mt][4 + t][3] - s3[0] * csv[t]);
          pv[t][m2 * 2] = pk2(a0, a1); pv[t][m2 * 2 + 1] = pk2(a2, a3);
        }
      }
#pragma unroll
      for (int t = 0; t < 2; ++t) vf[s][t] = __builtin_bit_cast(bf16x8, (u32x4){pv[t][0], pv[t][1], pv[t][2], pv[t][3]});
    }
    char* pkl = smem + wid * (64 * 144);
    const float L2E = 1.4426950408889634f;
#pragma unroll
    for (int which = 0; which < 2; ++which)
#pragma unroll
      for (int mt = 0; mt < 4; ++mt) {
        f32x4 z4 = (f32x4){0.f, 0.f, 0.f, 0.f};
        f32x4 p0 = __builtin_amdgcn_mfma_f32_16x16x32_bf16(wf[0], which ? kf[mt] : qf[mt], z4, 0, 0, 0);
        f32x4 p1 = __builtin_amdgcn_mfma_f32_16x16x32_bf16(wf[1], which ? kf[mt] : qf[mt], z4, 0, 0, 0);
        float v[8];
#pragma unroll
        for (int i = 0; i < 4; ++i) { v[i] = rbf(p0[i]); v[4 + i] = rbf(p1[i]); }
        float mx = v[0], mn = v[0];
#pragma unroll
        for (int i = 1; i < 8; ++i) { mx = fmaxf(mx, v[i]); mn = fminf(mn, v[i]); }
        mx = fmaxf(mx, __shfl_xor(mx, 16)); mn = fminf(mn, __shfl_xor(mn, 16));
        mx = fmaxf(mx, __shfl_xor(mx, 32)); mn = fminf(mn, __shfl_xor(mn, 32));
        float ep[8], en[8], sp = 0.f, sn = 0.f;
#pragma unroll
        for (int i = 0; i < 8; ++i) {
          ep[i] = __builtin_amdgcn_exp2f((v[i] - mx) * L2E);
          en[i] = __builtin_amdgcn_exp2f((mn - v[i]) * L2E);
          sp += ep[i]; sn += en[i];
        }
        sp += __shfl_xor(sp, 16); sn += __shfl_xor(sn, 16);
        sp += __shfl_xor(sp, 32); sn += __shfl_xor(sn, 32);
        float ip = 1.0f / sp, in_ = 1.0f / sn;
        u32x2 o[4];
        o[0] = (u32x2){pk2(ep[0] * ip, ep[1] * ip), pk2(ep[2] * ip, ep[3] * ip)};
        o[1] = (u32x2){pk2(ep[4] * ip, ep[5] * ip), pk2(ep[6] * ip, ep[7] * ip)};
        o[2] = (u32x2){pk2(en[0] * in_, en[1] * in_), pk2(en[2] * in_, en[3] * in_)};
        o[3] = (u32x2){pk2(en[4] * in_, en[5] * in_), pk2(en[6] * in_, en[7] * in_)};
        if (which == 0) {
          u16* dst = out + ((size_t)(row0 + wm * 64 + mt * 16 + l15) * 32 + h) * 64 + 4 * g;
#pragma unroll
          for (int c = 0; c < 4; ++c) *reinterpret_cast<u32x2*>(dst + c * 16) = o[c];
        } else {
          char* dst = pkl + (mt * 16 + l15) * 144 + g * 8;
#pragma unroll
          for (int c = 0; c < 4; ++c) *reinterpret_cast<u32x2*>(dst + c * 32) = o[c];
        }
      }
    WAIT_L0();
    __builtin_amdgcn_sched_barrier(0);
    f32x4 kv[4][2], zz[4];
#pragma unroll
    for (int ft = 0; ft < 4; ++ft) { kv[ft][0] = kv[ft][1] = zz[ft] = (f32x4){0.f, 0.f, 0.f, 0.f}; }
    const bf16x8 ones = (bf16x8){0x3F80, 0x3F80, 0x3F80, 0x3F80, 0x3F80, 0x3F80, 0x3F80, 0x3F80};
#pragma unroll
    for (int s = 0; s < 2; ++s)
#pragma unroll
      for (int ft = 0; ft < 4; ++ft) {
        const char* src = pkl + (32 * s + 4 * g + q) * 144 + ft * 32 + p * 8;
        s16x4 lo = __builtin_amdgcn_ds_read_tr16_b64_v4i16((lds_s16x4_p)(src));
        s16x4 hi = __builtin_amdgcn_ds_read_tr16_b64_v4i16((lds_s16x4_p)(src + 16 * 144));
        bf16x8 pf = __builtin_shufflevector(lo, hi, 0, 1, 2, 3, 4, 5, 6, 7);
        kv[ft][0] = __builtin_amdgcn_mfma_f32_16x16x32_bf16(vf[s][0], pf, kv[ft][0], 0, 0, 0);
        kv[ft][1] = __builtin_amdgcn_mfma_f32_16x16x32_bf16(vf[s][1], pf, kv[ft][1], 0, 0, 0);
        zz[ft] = __builtin_amdgcn_mfma_f32_16x16x32_bf16(ones, pf, zz[ft], 0, 0, 0);
      }
    float* red = reinterpret_cast<float*>(smem + 4 * 64 * 144) + wn * (64 * 32 + 64);
    if (wm == 1) {
#pragma unroll
      for (int ft = 0; ft < 4; ++ft) {
#pragma unroll
        for (int dt = 0; dt < 2; ++dt) *reinterpret_cast<f32x4*>(red + (ft * 16 + l15) * 32 + dt * 16 + 4 * g) = kv[ft][dt];
        if (g == 0) red[64 * 32 + ft * 16 + l15] = zz[ft][0];
      }
    }
    __syncthreads();
    if (wm == 0) {
      float* kd = kvp + (size_t)(h * 16 + tm) * 2048;
      float* zd = zp + (size_t)(h * 16 + tm) * 64;
#pragma unroll
      for (int ft = 0; ft < 4; ++ft) {
#pragma unroll
        for (int dt = 0; dt < 2; ++dt) {
          f32x4 o = *reinterpret_cast<const f32x4*>(red + (ft * 16 + l15) * 32 + dt * 16 + 4 * g);
          *reinterpret_cast<f32x4*>(kd + (ft * 16 + l15) * 32 + dt * 16 + 4 * g) = o + kv[ft][dt];
        }
        if (g == 0) zd[ft * 16 + l15] = red[64 * 32 + ft * 16 + l15] + zz[ft][0];
      }
    }
  } else {
#pragma unroll
    for (int j = 0; j < NTW; ++j) {
      const int colj = (j < 2 * NFULL) ? ((j >> 1) * 64 + (2 * wn + (j & 1)) * 16) : (NFULL * 64 + wn * 16);
      f32x4 c4;
      if constexpr (EPI == EPI_W1 || EPI == EPI_W3) c4 = *reinterpret_cast<const f32x4*>(cs + tn * BN + colj + 4 * g);
#pragma unroll
      for (int mt = 0; mt < 4; ++mt) {
        float v[4];
        if constexpr (EPI == EPI_W1 || EPI == EPI_W3) {
          f32x4 st = rs[wm * 64 + mt * 16 + l15];
#pragma unroll
          for (int i = 0; i < 4; ++i) v[i] = gelu_t(rbf(st[1] * (acc[mt][j][i] - st[0] * c4[i])));
        } else {
#pragma unroll
          for (int i = 0; i < 4; ++i) v[i] = acc[mt][j][i];
        }
        *reinterpret_cast<u32x2*>(smem + (wm * 64 + mt * 16 + l15) * PB + (colj + 4 * g) * 2) = (u32x2){pk2(v[0], v[1]), pk2(v[2], v[3])};
      }
    }
    __syncthreads();
    constexpr int CH = BN / 8;
#pragma unroll
    for (int it = 0; it < CH / 2; ++it) {
      const int c = tid + 256 * it, r = c / CH, ch = c % CH;
      u32x4 v = *reinterpret_cast<const u32x4*>(smem + r * PB + ch * 16);
      const int grow = row0 + r, gcol = tn * BN + ch * 8;
      if constexpr (EPI == EPI_W3) {
        *reinterpret_cast<u32x4*>(out + (size_t)grow * F_ + gcol) = v;
      } else if constexpr (EPI == EPI_W1) {
        const int grp = tn >> 2;
        const int sh = grp == 0 ? 1 : grp == 1 ? -1 : grp == 2 ? 2 : -2;
        const int dr = grow + sh;
        if (dr >= 0 && dr < S_) *reinterpret_cast<u32x4*>(out + (size_t)dr * D_ + gcol) = v;
        const int zr = grow - sh;
        if (zr < 0 || zr >= S_) *reinterpret_cast<u32x4*>(out + (size_t)grow * D_ + gcol) = (u32x4){0u, 0u, 0u, 0u};
      } else {
        float y[8];
        u32x4 a = *reinterpret_cast<const u32x4*>(r0 + (size_t)grow * D_ + gcol);
        if constexpr (EPI == EPI_WO) {
#pragma unroll
          for (int i = 0; i < 4; ++i) { y[2 * i] = rbf(bflo(a[i]) + bflo(v[i])); y[2 * i + 1] = rbf(bfhi(a[i]) + bfhi(v[i])); }
        } else if constexpr (EPI == EPI_W2) {
          f32x4 st = rs[r];
#pragma unroll
          for (int i = 0; i < 4; ++i) {
            y[2 * i] = rbf(rbf((bflo(a[i]) - st[0]) * st[2]) + bflo(v[i]));
            y[2 * i + 1] = rbf(rbf((bfhi(a[i]) - st[0]) * st[2]) + bfhi(v[i]));
          }
        } else {
          u32x4 b = *reinterpret_cast<const u32x4*>(r1 + (size_t)grow * D_ + gcol);
#pragma unroll
          for (int i = 0; i < 4; ++i) {
            y[2 * i] = rbf(bflo(a[i]) + rbf(bflo(b[i]) + bflo(v[i])));
            y[2 * i + 1] = rbf(bfhi(a[i]) + rbf(bfhi(b[i]) + bfhi(v[i])));
          }
        }
        *reinterpret_cast<u32x4*>(out + (size_t)grow * D_ + gcol) = (u32x4){pk2(y[0], y[1]), pk2(y[2], y[3]), pk2(y[4], y[5]), pk2(y[6], y[7])};
        float sm = ((y[0] + y[1]) + (y[2] + y[3])) + ((y[4] + y[5]) + (y[6] + y[7]));
        sm += __shfl_xor(sm, 1); sm += __shfl_xor(sm, 2); sm += __shfl_xor(sm, 4);
        float mu = sm * (1.0f / 64.f), m2 = 0.f;
#pragma unroll
        for (int i = 0; i < 8; ++i) { float d = y[i] - mu; m2 += d * d; }
        m2 += __shfl_xor(m2, 1); m2 += __shfl_xor(m2, 2); m2 += __shfl_xor(m2, 4);
        if (ch == 0) st_out[(size_t)grow * 16 + tn] = (f32x2){sm, m2};
      }
    }
  }
}

__global__ __launch_bounds__(256) void attn_out_kernel(const u16* __restrict__ phiq, const float* __restrict__ kvp,
                                                       const float* __restrict__ zp, u16* __restrict__ o) {
  __shared__ __attribute__((aligned(16))) u16 kvT[48 * 72];
  const int tid = threadIdx.x, lane = tid & 63, wid = tid >> 6, g = lane >> 4, l15 = lane & 15;
  const int h = blockIdx.x & 31, tg = blockIdx.x >> 5;
  {
    f32x4 a0 = (f32x4){0.f, 0.f, 0.f, 0.f}, a1 = a0;
    const float* src = kvp + (size_t)h * 16 * 2048 + tid * 8;
#pragma unroll
    for (int t = 0; t < 16; ++t) {
      a0 += *reinterpret_cast<const f32x4*>(src + t * 2048);
      a1 += *reinterpret_cast<const f32x4*>(src + t * 2048 + 4);
    }
    const int f = tid >> 2, d0 = (tid & 3) * 8;
#pragma unroll
    for (int i = 0; i < 4; ++i) {
      kvT[(d0 + i) * 72 + f] = (u16)(pk2(a0[i], 0.f) & 0xffff);
      kvT[(d0 + 4 + i) * 72 + f] = (u16)(pk2(a1[i], 0.f) & 0xffff);
    }
    if (tid < 64) {
      float z = 0.f;
#pragma unroll
      for (int t = 0; t < 16; ++t) z += zp[(size_t)(h * 16 + t) * 64 + tid];
      kvT[32 * 72 + tid] = (u16)(pk2(z, 0.f) & 0xffff);
    }
    for (int i = tid; i < 15 * 64; i += 256) kvT[(33 + i / 64) * 72 + (i & 63)] = 0;
  }
  __syncthreads();
  bf16x8 kf[3][2];
#pragma unroll
  for (int dt = 0; dt < 3; ++dt)
#pragma unroll
    for (int ks = 0; ks < 2; ++ks) kf[dt][ks] = *reinterpret_cast<const bf16x8*>(&kvT[(dt * 16 + l15) * 72 + ks * 32 + g * 8]);
#pragma unroll
  for (int mt = 0; mt < 4; ++mt) {
    const int tok = tg * 256 + wid * 64 + mt * 16 + l15;
    const u16* src = phiq + ((size_t)tok * 32 + h) * 64 + g * 8;
    bf16x8 p0 = *reinterpret_cast<const bf16x8*>(src), p1 = *reinterpret_cast<const bf16x8*>(src + 32);
    f32x4 r[3];
#pragma unroll
    for (int dt = 0; dt < 3; ++dt) {
      r[dt] = (f32x4){0.f, 0.f, 0.f, 0.f};
      r[dt] = __builtin_amdgcn_mfma_f32_16x16x32_bf16(kf[dt][0], p0, r[dt], 0, 0, 0);
      r[dt] = __builtin_amdgcn_mfma_f32_16x16x32_bf16(kf[dt][1], p1, r[dt], 0, 0, 0);
    }
    float den = rbf(rbf(__shfl(r[2][0], l15)) + 1e-6f);
    float inv = 1.0f / den;
#pragma unroll
    for (int dt = 0; dt < 2; ++dt) {
      u32x2 w = (u32x2){pk2(rbf(r[dt][0]) * inv, rbf(r[dt][1]) * inv), pk2(rbf(r[dt][2]) * inv, rbf(r[dt][3]) * inv)};
      *reinterpret_cast<u32x2*>(o + (size_t)tok * D_ + h * 32 + dt * 16 + 4 * g) = w;
    }
  }
}

__global__ __launch_bounds__(256) void prep_kernel(const u16* __restrict__ x, const u16* __restrict__ wqkv,
                                                   const u16* __restrict__ w1, const u16* __restrict__ w3,
                                                   float* __restrict__ cs_qkv, float* __restrict__ cs_w1,
                                                   float* __restrict__ cs_w3, f32x2* __restrict__ st) {
  __shared__ float red[32][65];
  const int tid = threadIdx.x, b = blockIdx.x;
  if (b < 120) {
    const u16* w; float* dst; int ld, c0;
    if (b < 48) { w = wqkv; dst = cs_qkv; ld = 3072; c0 = b * 64; }
    else if (b < 64) { w = w1; dst = cs_w1; ld = 1024; c0 = (b - 48) * 64; }
    else { w = w3; dst = cs_w3; ld = F_; c0 = (b - 64) * 64; }
    const int cg = tid & 7, rg = tid >> 3;
    float s[8] = {0.f, 0.f, 0.f, 0.f, 0.f, 0.f, 0.f, 0.f};
#pragma unroll 8
    for (int r = 0; r < 32; ++r) {
      u32x4 v = *reinterpret_cast<const u32x4*>(w + (size_t)(r * 32 + rg) * ld + c0 + cg * 8);
#pragma unroll
      for (int i = 0; i < 4; ++i) { s[2 * i] += bflo(v[i]); s[2 * i + 1] += bfhi(v[i]); }
    }
#pragma unroll
    for (int i = 0; i < 8; ++i) red[rg][cg * 8 + i] = s[i];
    __syncthreads();
    if (tid < 64) {
      float t = 0.f;
#pragma unroll
      for (int r = 0; r < 32; ++r) t += red[r][tid];
      dst[c0 + tid] = t;
    }
  } else {
    const int lane = tid & 63, wid = tid >> 6;
    const int rbase = (b - 120) * 16 + wid * 4;
#pragma unroll
    for (int rr = 0; rr < 4; ++rr) {
      const int row = rbase + rr;
#pragma unroll
      for (int hf = 0; hf < 2; ++hf) {
        u32x4 v = *reinterpret_cast<const u32x4*>(x + (size_t)row * D_ + hf * 512 + lane * 8);
        float y[8];
#pragma unroll
        for (int i = 0; i < 4; ++i) { y[2 * i] = bflo(v[i]); y[2 * i + 1] = bfhi(v[i]); }
        float sm = ((y[0] + y[1]) + (y[2] + y[3])) + ((y[4] + y[5]) + (y[6] + y[7]));
        sm += __shfl_xor(sm, 1); sm += __shfl_xor(sm, 2); sm += __shfl_xor(sm, 4);
        float mu = sm * (1.0f / 64.f), m2 = 0.f;
#pragma unroll
        for (int i = 0; i < 8; ++i) { float d = y[i] - mu; m2 += d * d; }
        m2 += __shfl_xor(m2, 1); m2 += __shfl_xor(m2, 2); m2 += __shfl_xor(m2, 4);
        if ((lane & 7) == 0) st[(size_t)row * 16 + hf * 8 + (lane >> 3)] = (f32x2){sm, m2};
      }
    }
  }
}

extern "C" void kernel_launch(void* const* d_in, const int* in_sizes, int n_in, void* d_out, int out_size, void* d_ws,
                              size_t ws_size, hipStream_t stream) {
  const u16* x_in = (const u16*)d_in[0];
  const u16* wqkv = (const u16*)d_in[1];
  const u16* wphi = (const u16*)d_in[2];
  const u16* wo = (const u16*)d_in[3];
  const u16* w1 = (const u16*)d_in[5];
  const u16* w2 = (const u16*)d_in[6];
  const u16* w3 = (const u16*)d_in[7];
  const u16* w4 = (const u16*)d_in[8];
  char* ws = (char*)d_ws;
  size_t off = 0;
  auto alloc = [&](size_t bytes) { char* p = ws + off; off += (bytes + 4095) & ~(size_t)4095; return p; };
  u16* xb = (u16*)alloc((size_t)S_ * D_ * 2);
  u16* x2 = (u16*)alloc((size_t)S_ * D_ * 2);
  u16* x1 = (u16*)alloc((size_t)S_ * D_ * 2);
  u16* hs = (u16*)alloc((size_t)S_ * D_ * 2);
  u16* ob = (u16*)alloc((size_t)S_ * D_ * 2);
  u16* gb = (u16*)alloc((size_t)S_ * F_ * 2);
  u16* phiq = (u16*)alloc((size_t)S_ * 2048 * 2);
  float* kvp = (float*)alloc((size_t)32 * 16 * 2048 * 4);
  float* zp = (float*)alloc((size_t)32 * 16 * 64 * 4);
  f32x2* st_x = (f32x2*)alloc((size_t)S_ * 16 * 8);
  f32x2* st_x2 = (f32x2*)alloc((size_t)S_ * 16 * 8);
  f32x2* st_x1 = (f32x2*)alloc((size_t)S_ * 16 * 8);
  float* cs_qkv = (float*)alloc(3072 * 4);
  float* cs_w1 = (float*)alloc(1024 * 4);
  float* cs_w3 = (float*)alloc(F_ * 4);

  prep_kernel<<<248, 256, 0, stream>>>(x_in, wqkv, w1, w3, cs_qkv, cs_w1, cs_w3, st_x);
  for (int layer = 0; layer < 2; ++layer) {
    const u16* x = layer == 0 ? x_in : xb;
    u16* xo = layer == 0 ? xb : (u16*)d_out;
    gemm_kernel<192, 3, 1024, 1024, 3072, EPI_QKV><<<256, 256, 0, stream>>>(x, wqkv, cs_qkv, st_x, nullptr, nullptr, phiq, nullptr, wphi, kvp, zp);
    attn_out_kernel<<<256, 256, 0, stream>>>(phiq, kvp, zp, ob);
    gemm_kernel<64, 5, 1024, 1024, 1024, EPI_WO><<<256, 256, 0, stream>>>(ob, wo, nullptr, nullptr, x, nullptr, x2, st_x2, nullptr, nullptr, nullptr);
    gemm_kernel<64, 5, 1024, 1024, 1024, EPI_W1><<<256, 256, 0, stream>>>(x2, w1, cs_w1, st_x2, nullptr, nullptr, hs, nullptr, nullptr, nullptr, nullptr);
    gemm_kernel<64, 5, 1024, 1024, 1024, EPI_W2><<<256, 256, 0, stream>>>(hs, w2, nullptr, st_x2, x2, nullptr, x1, st_x1, nullptr, nullptr, nullptr);
    gemm_kernel<224, 3, 1024, 1024, F_, EPI_W3><<<256, 256, 0, stream>>>(x1, w3, cs_w3, st_x1, nullptr, nullptr, gb, nullptr, nullptr, nullptr, nullptr);
    gemm_kernel<64, 5, F_, F_, 1024, EPI_W4><<<256, 256, 0, stream>>>(gb, w4, nullptr, nullptr, x2, x1, xo, st_x, nullptr, nullptr, nullptr);
  }
}
```

```cpp
#include <hip/hip_runtime.h>
#include <hip/hip_bf16.h>
#include <stdint.h>
#include <type_traits>

typedef short bf16x8 __attribute__((ext_vector_type(8)));
typedef short s16x4 __attribute__((ext_vector_type(4)));
typedef float f32x4 __attribute__((ext_vector_type(4)));
typedef float f32x2 __attribute__((ext_vector_type(2)));
typedef unsigned u32x2 __attribute__((ext_vector_type(2)));
typedef unsigned u32x4 __attribute__((ext_vector_type(4)));
typedef __attribute__((address_space(3))) s16x4* lds_s16x4_p;
typedef unsigned short u16;

constexpr int S_ = 2048, D_ = 1024, H_ = 32, F_ = 3584;

__device__ __forceinline__ float bf2f(u16 v) { return __uint_as_float(((unsigned)v) << 16); }
__device__ __forceinline__ float bflo(unsigned v) { return __uint_as_float(v << 16); }
__device__ __forceinline__ float bfhi(unsigned v) { return __uint_as_float(v & 0xffff0000u); }
__device__ __forceinline__ unsigned pk2(float a, float b) {
  __hip_bfloat162 t = __float22bfloat162_rn(make_float2(a, b));
  return *reinterpret_cast<unsigned*>(&t);
}
__device__ __forceinline__ float rbf(float a) { return bflo(pk2(a, 0.f)); }
__device__ __forceinline__ float gelu_t(float x) {
  const float c1 = -2.0f * 0.7978845608028654f * 1.4426950408889634f;
  const float c2 = c1 * 0.044715f;
  float t = x * x;
  float w = x * __builtin_fmaf(t, c2, c1);
  float e = __builtin_amdgcn_exp2f(w);
  return x * __builtin_amdgcn_rcpf(1.0f + e);
}

__device__ __forceinline__ f32x2 gelu2(f32x2 x) {
  const float c1 = -2.0f * 0.7978845608028654f * 1.4426950408889634f;
  const float c2 = c1 * 0.044715f;
  f32x2 t = x * x;
  f32x2 w = x * (t * c2 + c1);
  f32x2 d = (f32x2){__builtin_amdgcn_exp2f(w[0]), __builtin_amdgcn_exp2f(w[1])} + 1.0f;
  return x * (f32x2){__builtin_amdgcn_rcpf(d[0]), __builtin_amdgcn_rcpf(d[1])};
}
__device__ __forceinline__ void x16(float v, float& a, float& b) {
  auto r = __builtin_amdgcn_permlane16_swap(__float_as_uint(v), __float_as_uint(v), false, false);
  a = __uint_as_float(r[0]); b = __uint_as_float(r[1]);
}
__device__ __forceinline__ void x32(float v, float& a, float& b) {
  auto r = __builtin_amdgcn_permlane32_swap(__float_as_uint(v), __float_as_uint(v), false, false);
  a = __uint_as_float(r[0]); b = __uint_as_float(r[1]);
}
__device__ __forceinline__ float rmax4(float v) { float a, b; x16(v, a, b); v = fmaxf(a, b); x32(v, a, b); return fmaxf(a, b); }
__device__ __forceinline__ float rmin4(float v) { float a, b; x16(v, a, b); v = fminf(a, b); x32(v, a, b); return fminf(a, b); }
__device__ __forceinline__ float rsum4(float v) { float a, b; x16(v, a, b); v = a + b; x32(v, a, b); return a + b; }

#define WAIT_V(n) asm volatile("s_waitcnt vmcnt(" #n ")" ::: "memory")
#define WAIT_L0() asm volatile("s_waitcnt lgkmcnt(0)" ::: "memory")
template <int N> __device__ __forceinline__ void wait_vm() {
  asm volatile("s_waitcnt vmcnt(%0)" ::"i"(N) : "memory");
}

__device__ __forceinline__ void glds16(unsigned voff, const void* sbase, unsigned lds) {
  unsigned keep;
  asm volatile("s_mov_b32 %0, m0\n\ts_mov_b32 m0, %3\n\ts_nop 0\n\tglobal_load_lds_dwordx4 %1, %2\n\ts_mov_b32 m0, %0"
               : "=&s"(keep) : "v"(voff), "s"(sbase), "s"(lds) : "memory");
}

__device__ __forceinline__ void mfma_acc(f32x4& c, const u32x4& a, const u32x4& b) {
  asm volatile("v_mfma_f32_16x16x32_bf16 %0, %1, %2, %0" : "+a"(c) : "v"(a), "v"(b));
}

#define STAMP(i)

enum { EPI_QKV = 0, EPI_WO = 1, EPI_W1 = 2, EPI_W2 = 3, EPI_W3 = 4, EPI_W4 = 5 };

template <int BN, int NST, int KD, int LDA, int LDB, int EPI>
__global__ __launch_bounds__(512, 2) void gemm_kernel(
    const u16* __restrict__ A, const u16* __restrict__ W,
    const float* __restrict__ cs,
    const f32x2* __restrict__ st_in,
    const u16* __restrict__ r0,
    const u16* __restrict__ r1,
    u16* __restrict__ out,
    f32x2* __restrict__ st_out,
    const u16* __restrict__ wphi,
    float* __restrict__ kvp,
    float* __restrict__ zp)
{
  constexpr int NFULL = BN / 64, HALF = (BN % 64) / 32;
  constexpr int A_BYTES = 128 * 64 * 2, B_BYTES = BN * 64 * 2, STAGE = A_BYTES + B_BYTES;
  constexpr int NT = KD / 64, NTW = 2 * NFULL + HALF, LPS = 4 + 2 * NFULL + HALF;
  constexpr int PB = BN * 2 + 16;
  constexpr int EPI_BYTES = (EPI == EPI_QKV) ? (4 * 64 * 144 + 2 * (64 * 32 + 64) * 4) : 128 * PB;
  constexpr int MAINB = NST * STAGE;
  constexpr int RS_OFF = (MAINB > EPI_BYTES ? MAINB : EPI_BYTES);
  constexpr int SMEM = RS_OFF + 128 * 16;
  constexpr bool HAS_ST = (EPI == EPI_QKV || EPI == EPI_W1 || EPI == EPI_W3 || EPI == EPI_W2);
  constexpr bool HAS_RES = (EPI == EPI_WO || EPI == EPI_W2 || EPI == EPI_W4);
  constexpr int CH = BN / 8, NIT = CH / 4;
  static_assert(SMEM <= 163840, "LDS");
  __shared__ __attribute__((aligned(16))) char smem[SMEM];

  STAMP(0);
  const int tid = threadIdx.x, lane = tid & 63;
  const int wid8 = __builtin_amdgcn_readfirstlane(tid >> 6), wid = wid8 & 3, wm = wid >> 1, wn = wid & 1;
  const int g = lane >> 4, l15 = lane & 15;
  const int xcd = blockIdx.x & 7, idx = blockIdx.x >> 3;
  const int tm = (xcd >> 1) * 4 + (idx & 3), tn = (xcd & 1) * 8 + (idx >> 2);
  const int row0 = tm * 128;
  f32x4* rs = reinterpret_cast<f32x4*>(smem + RS_OFF);

  f32x4 acc[4][NTW];
  u32x4 ra[HAS_RES ? NIT : 1], rb[(EPI == EPI_W4) ? NIT : 1];

  if (wid8 >= 4) {
    unsigned voA[4], voB[NFULL > 0 ? NFULL : 1][2], voH = 0;
#pragma unroll
    for (int i = 0; i < 4; ++i) {
      int r = (wid * 4 + i) * 8 + (lane >> 3);
      int ch = (lane & 7) ^ ((r >> 1) & 7);
      voA[i] = (unsigned)(row0 + r) * (LDA * 2) + ch * 16;
    }
#pragma unroll
    for (int b = 0; b < NFULL; ++b) {
      int colb = (EPI == EPI_QKV) ? (b * 1024 + tn * 64) : (tn * BN + b * 64);
#pragma unroll
      for (int j = 0; j < 2; ++j) {
        int r = (wid * 2 + j) * 8 + (lane >> 3);
        int f = ((r >> 1) & 1) | (((r >> 3) & 1) << 1);
        int chp = lane & 7;
        int gch = (((chp >> 1) ^ f) << 1) | (chp & 1);
        voB[b][j] = (unsigned)r * (LDB * 2) + colb * 2 + gch * 16;
      }
    }
    if constexpr (HALF) {
      int r = wid * 16 + (lane >> 2);
      int chp = lane & 3;
      int gch = (((chp >> 1) ^ ((r >> 3) & 1)) << 1) | (chp & 1);
      voH = (unsigned)r * (LDB * 2) + (tn * BN + NFULL * 64) * 2 + gch * 16;
    }
    const char* Ab = reinterpret_cast<const char*>(A);
    const char* Wb = reinterpret_cast<const char*>(W);
    const unsigned lbase = (unsigned)(uintptr_t)(__attribute__((address_space(3))) char*)smem;
    auto stage = [&](int kt, int buf) {
      const char* a = Ab + (size_t)kt * 128;
      const char* w = Wb + (size_t)kt * (64 * LDB * 2);
      const unsigned s = lbase + buf * STAGE;
#pragma unroll
      for (int i = 0; i < 4; ++i) glds16(voA[i], a, s + (wid * 4 + i) * 1024);
#pragma unroll
      for (int b = 0; b < NFULL; ++b)
#pragma unroll
        for (int j = 0; j < 2; ++j) glds16(voB[b][j], w, s + A_BYTES + b * 8192 + (wid * 2 + j) * 1024);
      if constexpr (HALF) glds16(voH, w, s + A_BYTES + NFULL * 8192 + wid * 1024);
    };
#pragma unroll
    for (int s = 0; s < NST; ++s) stage(s, s);
    wait_vm<(NST - 1) * LPS>();
    __builtin_amdgcn_s_barrier();
    int buf = 0;
    for (int kt = 0; kt < NT - NST; ++kt) {
      wait_vm<(NST - 2) * LPS>();
      __builtin_amdgcn_s_barrier();
      stage(kt + NST, buf);
      buf = buf + 1 == NST ? 0 : buf + 1;
    }
    constexpr int XL = (HAS_ST ? 4 : 0) + (HAS_RES ? NIT : 0) + (EPI == EPI_W4 ? NIT : 0);
    f32x4 stp[4];
    const int lt = tid - 256;
    if constexpr (HAS_ST) {
      const f32x4* sp = reinterpret_cast<const f32x4*>(st_in + (size_t)(row0 + (lt >> 1)) * 16 + (lt & 1) * 8);
#pragma unroll
      for (int i = 0; i < 4; ++i) stp[i] = sp[i];
    }
    if constexpr (HAS_RES) {
#pragma unroll
      for (int it = 0; it < NIT; ++it) {
        const int c = tid + 512 * it, r = c / CH, ch = c % CH;
        ra[it] = *reinterpret_cast<const u32x4*>(r0 + (size_t)(row0 + r) * D_ + tn * BN + ch * 8);
        if constexpr (EPI == EPI_W4) rb[it] = *reinterpret_cast<const u32x4*>(r1 + (size_t)(row0 + r) * D_ + tn * BN + ch * 8);
      }
    }
    asm volatile("" ::: "memory");
#pragma unroll
    for (int r = 0; r < NST - 1; ++r) {
      if (r == 0) wait_vm<(NST - 2) * LPS + XL>();
      else if (r == 1) wait_vm<(NST > 3 ? NST - 3 : 0) * LPS + XL>();
      else if (r == 2) wait_vm<(NST > 4 ? NST - 4 : 0) * LPS + XL>();
      else if (r == 3) wait_vm<(NST > 5 ? NST - 5 : 0) * LPS + XL>();
      else wait_vm<XL>();
      __builtin_amdgcn_s_barrier();
    }
    if constexpr (HAS_ST) {
      float sm = 0.f;
#pragma unroll
      for (int i = 0; i < 4; ++i) sm += stp[i][0] + stp[i][2];
      sm += __shfl_xor(sm, 1);
      float mean = sm * (1.0f / 1024.f);
      float m2 = 0.f;
#pragma unroll
      for (int i = 0; i < 4; ++i) {
        float d0 = stp[i][0] * (1.0f / 64.f) - mean, d1 = stp[i][2] * (1.0f / 64.f) - mean;
        m2 += stp[i][1] + stp[i][3] + 64.f * (d0 * d0 + d1 * d1);
      }
      m2 += __shfl_xor(m2, 1);
      float var = m2 * (1.0f / 1024.f);
      float r1_ = 1.0f / sqrtf(var + 1e-5f);
      float sc = r1_;
      if constexpr (EPI == EPI_W1) sc = r1_ / sqrtf(var * r1_ * r1_ + 1e-5f);
      if ((lt & 1) == 0) rs[lt >> 1] = (f32x4){mean, sc, r1_, 0.f};
    }
  } else {
    if constexpr (HAS_RES) {
#pragma unroll
      for (int it = 0; it < NIT; ++it) {
        const int c = tid + 512 * it, r = c / CH, ch = c % CH;
        ra[it] = *reinterpret_cast<const u32x4*>(r0 + (size_t)(row0 + r) * D_ + tn * BN + ch * 8);
        if constexpr (EPI == EPI_W4) rb[it] = *reinterpret_cast<const u32x4*>(r1 + (size_t)(row0 + r) * D_ + tn * BN + ch * 8);
      }
    }
    const int q = l15 >> 2, p = lane & 3;
    const int fB = (q >> 1) | ((g & 1) << 1);
    unsigned aoff[2], boff[2], hoff = 0;
#pragma unroll
    for (int ks = 0; ks < 2; ++ks) aoff[ks] = (wm * 64 + l15) * 128 + (((4 * ks + g) ^ (l15 >> 1)) * 16);
#pragma unroll
    for (int u = 0; u < 2; ++u) boff[u] = A_BYTES + (8 * g + q) * 128 + (((2 * wn + u) ^ fB) * 32) + p * 8;
    if constexpr (HALF) hoff = A_BYTES + NFULL * 8192 + (8 * g + q) * 64 + ((wn ^ (g & 1)) * 32) + p * 8;

    auto ldf = [&](int i, const char* s, int ks, u32x4(&af)[4], u32x4(&bfr)[NTW]) {
      if (i == 0 || i > NTW) {
        const int mt = i == 0 ? 0 : i - NTW;
        af[mt] = *reinterpret_cast<const u32x4*>(s + aoff[ks] + mt * 2048);
      } else if (i - 1 < 2 * NFULL) {
        const int b = (i - 1) >> 1, u = (i - 1) & 1;
        s16x4 lo = __builtin_amdgcn_ds_read_tr16_b64_v4i16((lds_s16x4_p)(s + boff[u] + b * 8192 + ks * 4096));
        s16x4 hi = __builtin_amdgcn_ds_read_tr16_b64_v4i16((lds_s16x4_p)(s + boff[u] + b * 8192 + ks * 4096 + 512));
        bfr[i - 1] = __builtin_shufflevector(__builtin_bit_cast(u32x2, lo), __builtin_bit_cast(u32x2, hi), 0, 1, 2, 3);
      } else {
        s16x4 lo = __builtin_amdgcn_ds_read_tr16_b64_v4i16((lds_s16x4_p)(s + hoff + ks * 2048));
        s16x4 hi = __builtin_amdgcn_ds_read_tr16_b64_v4i16((lds_s16x4_p)(s + hoff + ks * 2048 + 256));
        bfr[NTW - 1] = __builtin_shufflevector(__builtin_bit_cast(u32x2, lo), __builtin_bit_cast(u32x2, hi), 0, 1, 2, 3);
      }
    };
    constexpr int NM = 4 * NTW, NL = NTW + 4, RSP = (NM - 4) / NL > 0 ? (NM - 4) / NL : 1;
    auto phase = [&](u32x4(&afc)[4], u32x4(&bfc)[NTW], u32x4(&afn)[4], u32x4(&bfn)[NTW], int nbuf, int nks, auto rd) {
      const char* s = smem + nbuf * STAGE;
#pragma unroll
      for (int mt = 0; mt < 4; ++mt)
#pragma unroll
        for (int j = 0; j < NTW; ++j) {
          const int m = mt * NTW + j;
          if (decltype(rd)::value && m % RSP == 0 && m / RSP < NL) ldf(m / RSP, s, nks, afn, bfn);
          if (EPI == EPI_QKV && j >= 4) mfma_acc(acc[mt][j], afc[mt], bfc[j]);
          else mfma_acc(acc[mt][j], bfc[j], afc[mt]);
          __builtin_amdgcn_sched_barrier(0);
        }
    };
#pragma unroll
    for (int mt = 0; mt < 4; ++mt)
#pragma unroll
      for (int j = 0; j < NTW; ++j) acc[mt][j] = (f32x4){0.f, 0.f, 0.f, 0.f};
    u32x4 af0[4], af1[4], bf0[NTW], bf1[NTW];
    __builtin_amdgcn_s_barrier();
    asm volatile("" ::: "memory");
    STAMP(1);
#pragma unroll
    for (int i = 0; i < NL; ++i) { ldf(i, smem, 0, af0, bf0); __builtin_amdgcn_sched_barrier(0); }
    int buf = 0;
    for (int kt = 0; kt < NT - 1; ++kt) {
      phase(af0, bf0, af1, bf1, buf, 1, std::true_type{});
      WAIT_L0();
      __builtin_amdgcn_s_barrier();
      asm volatile("" ::: "memory");
      buf = buf + 1 == NST ? 0 : buf + 1;
      phase(af1, bf1, af0, bf0, buf, 0, std::true_type{});
    }
    phase(af0, bf0, af1, bf1, buf, 1, std::true_type{});
    phase(af1, bf1, af0, bf0, buf, 0, std::false_type{});
    asm volatile("s_nop 7\n\ts_nop 7\n\ts_nop 7" ::: "memory");
  }
  WAIT_L0();
  __builtin_amdgcn_s_barrier();
  asm volatile("" ::: "memory");
  STAMP(2);

  if constexpr (EPI == EPI_QKV) {
    const int h = tn * 2 + wn;
    const int q = l15 >> 2, p = lane & 3;
    f32x4 kv[4][2], zz[4];
    float* red = reinterpret_cast<float*>(smem + 4 * 64 * 144) + wn * (64 * 32 + 64);
    if (wid8 < 4) {
      bf16x8 wf[2];
#pragma unroll
      for (int ft = 0; ft < 2; ++ft)
#pragma unroll
        for (int j = 0; j < 8; ++j) {
          int d = (j < 4) ? 4 * g + j : 16 + 4 * g + (j - 4);
          wf[ft][j] = (short)wphi[(h * 32 + d) * 32 + ft * 16 + l15];
        }
      f32x4 cs4[4];
      float csv[2];
#pragma unroll
      for (int t = 0; t < 2; ++t) {
        cs4[t] = *reinterpret_cast<const f32x4*>(cs + h * 32 + t * 16 + 4 * g);
        cs4[2 + t] = *reinterpret_cast<const f32x4*>(cs + 1024 + h * 32 + t * 16 + 4 * g);
        csv[t] = cs[2048 + h * 32 + t * 16 + l15];
      }
      bf16x8 qf[4], kf[4], vf[2][2];
#pragma unroll
      for (int mt = 0; mt < 4; ++mt) {
        f32x4 st = rs[wm * 64 + mt * 16 + l15];
        unsigned pq_[4], pk_[4];
#pragma unroll
        for (int t = 0; t < 2; ++t) {
          float a[4], b[4];
#pragma unroll
          for (int i = 0; i < 4; ++i) {
            a[i] = st[1] * (acc[mt][t][i] - st[0] * cs4[t][i]);
            b[i] = st[1] * (acc[mt][2 + t][i] - st[0] * cs4[2 + t][i]);
          }
          pq_[t * 2] = pk2(a[0], a[1]); pq_[t * 2 + 1] = pk2(a[2], a[3]);
          pk_[t * 2] = pk2(b[0], b[1]); pk_[t * 2 + 1] = pk2(b[2], b[3]);
        }
        qf[mt] = __builtin_bit_cast(bf16x8, (u32x4){pq_[0], pq_[1], pq_[2], pq_[3]});
        kf[mt] = __builtin_bit_cast(bf16x8, (u32x4){pk_[0], pk_[1], pk_[2], pk_[3]});
      }
#pragma unroll
      for (int s = 0; s < 2; ++s) {
        unsigned pv[2][4];
#pragma unroll
        for (int m2 = 0; m2 < 2; ++m2) {
          int mt = s * 2 + m2;
          f32x4 s0 = rs[wm * 64 + mt * 16 + 4 * g + 0], s1 = rs[wm * 64 + mt * 16 + 4 * g + 1];
          f32x4 s2 = rs[wm * 64 + mt * 16 + 4 * g + 2], s3 = rs[wm * 64 + mt * 16 + 4 * g + 3];
#pragma unroll
          for (int t = 0; t < 2; ++t) {
            float a0 = s0[1] * (acc[mt][4 + t][0] - s0[0] * csv[t]);
            float a1 = s1[1] * (acc[mt][4 + t][1] - s1[0] * csv[t]);
            float a2 = s2[1] * (acc[mt][4 + t][2] - s2[0] * csv[t]);
            float a3 = s3[1] * (acc[mt][4 + t][3] - s3[0] * csv[t]);
            pv[t][m2 * 2] = pk2(a0, a1); pv[t][m2 * 2 + 1] = pk2(a2, a3);
          }
        }
#pragma unroll
        for (int t = 0; t < 2; ++t) vf[s][t] = __builtin_bit_cast(bf16x8, (u32x4){pv[t][0], pv[t][1], pv[t][2], pv[t][3]});
      }
      char* pkl = smem + wid * (64 * 144);
      const float L2E = 1.4426950408889634f;
#pragma unroll
      for (int which = 0; which < 2; ++which)
#pragma unroll
        for (int mt = 0; mt < 4; ++mt) {
          f32x4 z4 = (f32x4){0.f, 0.f, 0.f, 0.f};
          f32x4 p0 = __builtin_amdgcn_mfma_f32_16x16x32_bf16(wf[0], which ? kf[mt] : qf[mt], z4, 0, 0, 0);
          f32x4 p1 = __builtin_amdgcn_mfma_f32_16x16x32_bf16(wf[1], which ? kf[mt] : qf[mt], z4, 0, 0, 0);
          float v[8];
#pragma unroll
          for (int i = 0; i < 4; ++i) { v[i] = rbf(p0[i]); v[4 + i] = rbf(p1[i]); }
          float mx = v[0], mn = v[0];
#pragma unroll
          for (int i = 1; i < 8; ++i) { mx = fmaxf(mx, v[i]); mn = fminf(mn, v[i]); }
          mx = rmax4(mx); mn = rmin4(mn);
          float ep[8], en[8], sp = 0.f, sn = 0.f;
#pragma unroll
          for (int i = 0; i < 8; ++i) {
            ep[i] = __builtin_amdgcn_exp2f((v[i] - mx) * L2E);
            en[i] = __builtin_amdgcn_exp2f((mn - v[i]) * L2E);
            sp += ep[i]; sn += en[i];
          }
          sp = rsum4(sp); sn = rsum4(sn);
          float ip = __builtin_amdgcn_rcpf(sp), in_ = __builtin_amdgcn_rcpf(sn);
          u32x2 o[4];
          o[0] = (u32x2){pk2(ep[0] * ip, ep[1] * ip), pk2(ep[2] * ip, ep[3] * ip)};
          o[1] = (u32x2){pk2(ep[4] * ip, ep[5] * ip), pk2(ep[6] * ip, ep[7] * ip)};
          o[2] = (u32x2){pk2(en[0] * in_, en[1] * in_), pk2(en[2] * in_, en[3] * in_)};
          o[3] = (u32x2){pk2(en[4] * in_, en[5] * in_), pk2(en[6] * in_, en[7] * in_)};
          if (which == 0) {
            u16* dst = out + ((size_t)(row0 + wm * 64 + mt * 16 + l15) * 32 + h) * 64 + 4 * g;
#pragma unroll
            for (int c = 0; c < 4; ++c) *reinterpret_cast<u32x2*>(dst + c * 16) = o[c];
          } else {
            char* dst = pkl + (mt * 16 + l15) * 144 + g * 8;
#pragma unroll
            for (int c = 0; c < 4; ++c) *reinterpret_cast<u32x2*>(dst + c * 32) = o[c];
          }
        }
      WAIT_L0();
      __builtin_amdgcn_sched_barrier(0);
#pragma unroll
      for (int ft = 0; ft < 4; ++ft) { kv[ft][0] = kv[ft][1] = zz[ft] = (f32x4){0.f, 0.f, 0.f, 0.f}; }
      const bf16x8 ones = (bf16x8){0x3F80, 0x3F80, 0x3F80, 0x3F80, 0x3F80, 0x3F80, 0x3F80, 0x3F80};
#pragma unroll
      for (int s = 0; s < 2; ++s)
#pragma unroll
        for (int ft = 0; ft < 4; ++ft) {
          const char* src = pkl + (32 * s + 4 * g + q) * 144 + ft * 32 + p * 8;
          s16x4 lo = __builtin_amdgcn_ds_read_tr16_b64_v4i16((lds_s16x4_p)(src));
          s16x4 hi = __builtin_amdgcn_ds_read_tr16_b64_v4i16((lds_s16x4_p)(src + 16 * 144));
          bf16x8 pf = __builtin_shufflevector(lo, hi, 0, 1, 2, 3, 4, 5, 6, 7);
          kv[ft][0] = __builtin_amdgcn_mfma_f32_16x16x32_bf16(vf[s][0], pf, kv[ft][0], 0, 0, 0);
          kv[ft][1] = __builtin_amdgcn_mfma_f32_16x16x32_bf16(vf[s][1], pf, kv[ft][1], 0, 0, 0);
          zz[ft] = __builtin_amdgcn_mfma_f32_16x16x32_bf16(ones, pf, zz[ft], 0, 0, 0);
        }
      if (wm == 1) {
#pragma unroll
        for (int ft = 0; ft < 4; ++ft) {
#pragma unroll
          for (int dt = 0; dt < 2; ++dt) *reinterpret_cast<f32x4*>(red + (ft * 16 + l15) * 32 + dt * 16 + 4 * g) = kv[ft][dt];
          if (g == 0) red[64 * 32 + ft * 16 + l15] = zz[ft][0];
        }
      }
    }
    __syncthreads();
    if (wid8 < 2) {
      float* kd = kvp + (size_t)(h * 16 + tm) * 2048;
      float* zd = zp + (size_t)(h * 16 + tm) * 64;
#pragma unroll
      for (int ft = 0; ft < 4; ++ft) {
#pragma unroll
        for (int dt = 0; dt < 2; ++dt) {
          f32x4 o = *reinterpret_cast<const f32x4*>(red + (ft * 16 + l15) * 32 + dt * 16 + 4 * g);
          *reinterpret_cast<f32x4*>(kd + (ft * 16 + l15) * 32 + dt * 16 + 4 * g) = o + kv[ft][dt];
        }
        if (g == 0) zd[ft * 16 + l15] = red[64 * 32 + ft * 16 + l15] + zz[ft][0];
      }
    }
  } else {
    if (wid8 < 4) {
      f32x4 stm[4], cs4[NTW];
      if constexpr (EPI == EPI_W1 || EPI == EPI_W3) {
#pragma unroll
        for (int j = 0; j < NTW; ++j) {
          const int colj = (j < 2 * NFULL) ? ((j >> 1) * 64 + (2 * wn + (j & 1)) * 16) : (NFULL * 64 + wn * 16);
          cs4[j] = *reinterpret_cast<const f32x4*>(cs + tn * BN + colj + 4 * g);
        }
#pragma unroll
        for (int mt = 0; mt < 4; ++mt) stm[mt] = rs[wm * 64 + mt * 16 + l15];
      }
#pragma unroll
      for (int j = 0; j < NTW; ++j) {
        const int colj = (j < 2 * NFULL) ? ((j >> 1) * 64 + (2 * wn + (j & 1)) * 16) : (NFULL * 64 + wn * 16);
#pragma unroll
        for (int mt = 0; mt < 4; ++mt) {
          f32x2 v0 = (f32x2){acc[mt][j][0], acc[mt][j][1]}, v1 = (f32x2){acc[mt][j][2], acc[mt][j][3]};
          if constexpr (EPI == EPI_W1 || EPI == EPI_W3) {
            v0 = gelu2((v0 - stm[mt][0] * (f32x2){cs4[j][0], cs4[j][1]}) * stm[mt][1]);
            v1 = gelu2((v1 - stm[mt][0] * (f32x2){cs4[j][2], cs4[j][3]}) * stm[mt][1]);
          }
          *reinterpret_cast<u32x2*>(smem + (wm * 64 + mt * 16 + l15) * PB + (colj + 4 * g) * 2) = (u32x2){pk2(v0[0], v0[1]), pk2(v1[0], v1[1])};
        }
      }
    }
    __syncthreads();
#pragma unroll
    for (int it = 0; it < NIT; ++it) {
      const int c = tid + 512 * it, r = c / CH, ch = c % CH;
      u32x4 v = *reinterpret_cast<const u32x4*>(smem + r * PB + ch * 16);
      const int grow = row0 + r, gcol = tn * BN + ch * 8;
      if constexpr (EPI == EPI_W3) {
        *reinterpret_cast<u32x4*>(out + (size_t)grow * F_ + gcol) = v;
      } else if constexpr (EPI == EPI_W1) {
        const int grp = tn >> 2;
        const int sh = grp == 0 ? 1 : grp == 1 ? -1 : grp == 2 ? 2 : -2;
        const int dr = grow + sh;
        if (dr >= 0 && dr < S_) *reinterpret_cast<u32x4*>(out + (size_t)dr * D_ + gcol) = v;
        const int zr = grow - sh;
        if (zr < 0 || zr >= S_) *reinterpret_cast<u32x4*>(out + (size_t)grow * D_ + gcol) = (u32x4){0u, 0u, 0u, 0u};
      } else {
        float y[8];
        const u32x4 a = ra[it];
        if constexpr (EPI == EPI_WO) {
#pragma unroll
          for (int i = 0; i < 4; ++i) { y[2 * i] = rbf(bflo(a[i]) + bflo(v[i])); y[2 * i + 1] = rbf(bfhi(a[i]) + bfhi(v[i])); }
        } else if constexpr (EPI == EPI_W2) {
          f32x4 st = rs[r];
#pragma unroll
          for (int i = 0; i < 4; ++i) {
            y[2 * i] = rbf(rbf((bflo(a[i]) - st[0]) * st[2]) + bflo(v[i]));
            y[2 * i + 1] = rbf(rbf((bfhi(a[i]) - st[0]) * st[2]) + bfhi(v[i]));
          }
        } else {
          const u32x4 b = rb[it];
#pragma unroll
          for (int i = 0; i < 4; ++i) {
            y[2 * i] = rbf(bflo(a[i]) + rbf(bflo(b[i]) + bflo(v[i])));
            y[2 * i + 1] = rbf(bfhi(a[i]) + rbf(bfhi(b[i]) + bfhi(v[i])));
          }
        }
        *reinterpret_cast<u32x4*>(out + (size_t)grow * D_ + gcol) = (u32x4){pk2(y[0], y[1]), pk2(y[2], y[3]), pk2(y[4], y[5]), pk2(y[6], y[7])};
        float sm = ((y[0] + y[1]) + (y[2] + y[3])) + ((y[4] + y[5]) + (y[6] + y[7]));
        sm += __shfl_xor(sm, 1); sm += __shfl_xor(sm, 2); sm += __shfl_xor(sm, 4);
        float mu = sm * (1.0f / 64.f), m2 = 0.f;
#pragma unroll
        for (int i = 0; i < 8; ++i) { float d = y[i] - mu; m2 += d * d; }
        m2 += __shfl_xor(m2, 1); m2 += __shfl_xor(m2, 2); m2 += __shfl_xor(m2, 4);
        if (ch == 0) st_out[(size_t)grow * 16 + tn] = (f32x2){sm, m2};
      }
    }
  }
  STAMP(3);
}

__global__ __launch_bounds__(256) void attn_out_kernel(const u16* __restrict__ phiq, const float* __restrict__ kvp,
                                                       const float* __restrict__ zp, u16* __restrict__ o) {
  __shared__ __attribute__((aligned(16))) u16 kvT[48 * 72];
  const int tid = threadIdx.x, lane = tid & 63, wid = tid >> 6, g = lane >> 4, l15 = lane & 15;
  const int h = blockIdx.x & 31, tg = blockIdx.x >> 5;
  {
    f32x4 a0 = (f32x4){0.f, 0.f, 0.f, 0.f}, a1 = a0;
    const float* src = kvp + (size_t)h * 16 * 2048 + tid * 8;
#pragma unroll
    for (int t = 0; t < 16; ++t) {
      a0 += *reinterpret_cast<const f32x4*>(src + t * 2048);
      a1 += *reinterpret_cast<const f32x4*>(src + t * 2048 + 4);
    }
    const int f = tid >> 2, d0 = (tid & 3) * 8;
#pragma unroll
    for (int i = 0; i < 4; ++i) {
      kvT[(d0 + i) * 72 + f] = (u16)(pk2(a0[i], 0.f) & 0xffff);
      kvT[(d0 + 4 + i) * 72 + f] = (u16)(pk2(a1[i], 0.f) & 0xffff);
    }
    if (tid < 64) {
      float z = 0.f;
#pragma unroll
      for (int t = 0; t < 16; ++t) z += zp[(size_t)(h * 16 + t) * 64 + tid];
      kvT[32 * 72 + tid] = (u16)(pk2(z, 0.f) & 0xffff);
    }
    for (int i = tid; i < 15 * 64; i += 256) kvT[(33 + i / 64) * 72 + (i & 63)] = 0;
  }
  __syncthreads();
  bf16x8 kf[3][2];
#pragma unroll
  for (int dt = 0; dt < 3; ++dt)
#pragma unroll
    for (int ks = 0; ks < 2; ++ks) kf[dt][ks] = *reinterpret_cast<const bf16x8*>(&kvT[(dt * 16 + l15) * 72 + ks * 32 + g * 8]);
#pragma unroll
  for (int mt = 0; mt < 4; ++mt) {
    const int tok = tg * 256 + wid * 64 + mt * 16 + l15;
    const u16* src = phiq + ((size_t)tok * 32 + h) * 64 + g * 8;
    bf16x8 p0 = *reinterpret_cast<const bf16x8*>(src), p1 = *reinterpret_cast<const bf16x8*>(src + 32);
    f32x4 r[3];
#pragma unroll
    for (int dt = 0; dt < 3; ++dt) {
      r[dt] = (f32x4){0.f, 0.f, 0.f, 0.f};
      r[dt] = __builtin_amdgcn_mfma_f32_16x16x32_bf16(kf[dt][0], p0, r[dt], 0, 0, 0);
      r[dt] = __builtin_amdgcn_mfma_f32_16x16x32_bf16(kf[dt][1], p1, r[dt], 0, 0, 0);
    }
    float den = rbf(rbf(__shfl(r[2][0], l15)) + 1e-6f);
    float inv = 1.0f / den;
#pragma unroll
    for (int dt = 0; dt < 2; ++dt) {
      u32x2 w = (u32x2){pk2(rbf(r[dt][0]) * inv, rbf(r[dt][1]) * inv), pk2(rbf(r[dt][2]) * inv, rbf(r[dt][3]) * inv)};
      *reinterpret_cast<u32x2*>(o + (size_t)tok * D_ + h * 32 + dt * 16 + 4 * g) = w;
    }
  }
}

__global__ __launch_bounds__(256) void prep_kernel(const u16* __restrict__ x, const u16* __restrict__ wqkv,
                                                   const u16* __restrict__ w1, const u16* __restrict__ w3,
                                                   float* __restrict__ cs_qkv, float* __restrict__ cs_w1,
                                                   float* __restrict__ cs_w3, f32x2* __restrict__ st) {
  __shared__ float red[32][65];
  const int tid = threadIdx.x, b = blockIdx.x;
  if (b < 120) {
    const u16* w; float* dst; int ld, c0;
    if (b < 48) { w = wqkv; dst = cs_qkv; ld = 3072; c0 = b * 64; }
    else if (b < 64) { w = w1; dst = cs_w1; ld = 1024; c0 = (b - 48) * 64; }
    else { w = w3; dst = cs_w3; ld = F_; c0 = (b - 64) * 64; }
    const int cg = tid & 7, rg = tid >> 3;
    float s[8] = {0.f, 0.f, 0.f, 0.f, 0.f, 0.f, 0.f, 0.f};
#pragma unroll 8
    for (int r = 0; r < 32; ++r) {
      u32x4 v = *reinterpret_cast<const u32x4*>(w + (size_t)(r * 32 + rg) * ld + c0 + cg * 8);
#pragma unroll
      for (int i = 0; i < 4; ++i) { s[2 * i] += bflo(v[i]); s[2 * i + 1] += bfhi(v[i]); }
    }
#pragma unroll
    for (int i = 0; i < 8; ++i) red[rg][cg * 8 + i] = s[i];
    __syncthreads();
    if (tid < 64) {
      float t = 0.f;
#pragma unroll
      for (int r = 0; r < 32; ++r) t += red[r][tid];
      dst[c0 + tid] = t;
    }
  } else {
    const int lane = tid & 63, wid = tid >> 6;
    const int rbase = (b - 120) * 16 + wid * 4;
#pragma unroll
    for (int rr = 0; rr < 4; ++rr) {
      const int row = rbase + rr;
#pragma unroll
      for (int hf = 0; hf < 2; ++hf) {
        u32x4 v = *reinterpret_cast<const u32x4*>(x + (size_t)row * D_ + hf * 512 + lane * 8);
        float y[8];
#pragma unroll
        for (int i = 0; i < 4; ++i) { y[2 * i] = bflo(v[i]); y[2 * i + 1] = bfhi(v[i]); }
        float sm = ((y[0] + y[1]) + (y[2] + y[3])) + ((y[4] + y[5]) + (y[6] + y[7]));
        sm += __shfl_xor(sm, 1); sm += __shfl_xor(sm, 2); sm += __shfl_xor(sm, 4);
        float mu = sm * (1.0f / 64.f), m2 = 0.f;
#pragma unroll
        for (int i = 0; i < 8; ++i) { float d = y[i] - mu; m2 += d * d; }
        m2 += __shfl_xor(m2, 1); m2 += __shfl_xor(m2, 2); m2 += __shfl_xor(m2, 4);
        if ((lane & 7) == 0) st[(size_t)row * 16 + hf * 8 + (lane >> 3)] = (f32x2){sm, m2};
      }
    }
  }
}

extern "C" void kernel_launch(void* const* d_in, const int* in_sizes, int n_in, void* d_out, int out_size, void* d_ws,
                              size_t ws_size, hipStream_t stream) {
  const u16* x_in = (const u16*)d_in[0];
  const u16* wqkv = (const u16*)d_in[1];
  const u16* wphi = (const u16*)d_in[2];
  const u16* wo = (const u16*)d_in[3];
  const u16* w1 = (const u16*)d_in[5];
  const u16* w2 = (const u16*)d_in[6];
  const u16* w3 = (const u16*)d_in[7];
  const u16* w4 = (const u16*)d_in[8];
  char* ws = (char*)d_ws;
  size_t off = 0;
  auto alloc = [&](size_t bytes) { char* p = ws + off; off += (bytes + 4095) & ~(size_t)4095; return p; };
  u16* xb = (u16*)alloc((size_t)S_ * D_ * 2);
  u16* x2 = (u16*)alloc((size_t)S_ * D_ * 2);
  u16* x1 = (u16*)alloc((size_t)S_ * D_ * 2);
  u16* hs = (u16*)alloc((size_t)S_ * D_ * 2);
  u16* ob = (u16*)alloc((size_t)S_ * D_ * 2);
  u16* gb = (u16*)alloc((size_t)S_ * F_ * 2);
  u16* phiq = (u16*)alloc((size_t)S_ * 2048 * 2);
  float* kvp = (float*)alloc((size_t)32 * 16 * 2048 * 4);
  float* zp = (float*)alloc((size_t)32 * 16 * 64 * 4);
  f32x2* st_x = (f32x2*)alloc((size_t)S_ * 16 * 8);
  f32x2* st_x2 = (f32x2*)alloc((size_t)S_ * 16 * 8);
  f32x2* st_x1 = (f32x2*)alloc((size_t)S_ * 16 * 8);
  float* cs_qkv = (float*)alloc(3072 * 4);
  float* cs_w1 = (float*)alloc(1024 * 4);
  float* cs_w3 = (float*)alloc(F_ * 4);

  prep_kernel<<<248, 256, 0, stream>>>(x_in, wqkv, w1, w3, cs_qkv, cs_w1, cs_w3, st_x);
  for (int layer = 0; layer < 2; ++layer) {
    const u16* x = layer == 0 ? x_in : xb;
    u16* xo = layer == 0 ? xb : (u16*)d_out;
    gemm_kernel<192, 3, 1024, 1024, 3072, EPI_QKV><<<256, 512, 0, stream>>>(x, wqkv, cs_qkv, st_x, nullptr, nullptr, phiq, nullptr, wphi, kvp, zp);
    attn_out_kernel<<<256, 256, 0, stream>>>(phiq, kvp, zp, ob);
    gemm_kernel<64, 5, 1024, 1024, 1024, EPI_WO><<<256, 512, 0, stream>>>(ob, wo, nullptr, nullptr, x, nullptr, x2, st_x2, nullptr, nullptr, nullptr);
    gemm_kernel<64, 5, 1024, 1024, 1024, EPI_W1><<<256, 512, 0, stream>>>(x2, w1, cs_w1, st_x2, nullptr, nullptr, hs, nullptr, nullptr, nullptr, nullptr);
    gemm_kernel<64, 5, 1024, 1024, 1024, EPI_W2><<<256, 512, 0, stream>>>(hs, w2, nullptr, st_x2, x2, nullptr, x1, st_x1, nullptr, nullptr, nullptr);
    gemm_kernel<224, 3, 1024, 1024, F_, EPI_W3><<<256, 512, 0, stream>>>(x1, w3, cs_w3, st_x1, nullptr, nullptr, gb, nullptr, nullptr, nullptr, nullptr);
    gemm_kernel<64, 5, F_, F_, 1024, EPI_W4><<<256, 512, 0, stream>>>(gb, w4, nullptr, nullptr, x2, x1, xo, st_x, nullptr, nullptr, nullptr);
  }
}
```

```cpp
#include <hip/hip_runtime.h>
#include <hip/hip_bf16.h>
#include <stdint.h>
#include <type_traits>

typedef short bf16x8 __attribute__((ext_vector_type(8)));
typedef short s16x4 __attribute__((ext_vector_type(4)));
typedef float f32x4 __attribute__((ext_vector_type(4)));
typedef float f32x2 __attribute__((ext_vector_type(2)));
typedef unsigned u32x2 __attribute__((ext_vector_type(2)));
typedef unsigned u32x4 __attribute__((ext_vector_type(4)));
typedef __attribute__((address_space(3))) s16x4* lds_s16x4_p;
typedef unsigned short u16;

constexpr int S_ = 2048, D_ = 1024, H_ = 32, F_ = 3584;

__device__ __forceinline__ float bf2f(u16 v) { return __uint_as_float(((unsigned)v) << 16); }
__device__ __forceinline__ float bflo(unsigned v) { return __uint_as_float(v << 16); }
__device__ __forceinline__ float bfhi(unsigned v) { return __uint_as_float(v & 0xffff0000u); }
__device__ __forceinline__ unsigned pk2(float a, float b) {
  __hip_bfloat162 t = __float22bfloat162_rn(make_float2(a, b));
  return *reinterpret_cast<unsigned*>(&t);
}
__device__ __forceinline__ float rbf(float a) { return bflo(pk2(a, 0.f)); }
__device__ __forceinline__ float gelu_t(float x) {
  const float c1 = -2.0f * 0.7978845608028654f * 1.4426950408889634f;
  const float c2 = c1 * 0.044715f;
  float t = x * x;
  float w = x * __builtin_fmaf(t, c2, c1);
  float e = __builtin_amdgcn_exp2f(w);
  return x * __builtin_amdgcn_rcpf(1.0f + e);
}

__device__ __forceinline__ f32x2 gelu2(f32x2 x) {
  const float c1 = -2.0f * 0.7978845608028654f * 1.4426950408889634f;
  const float c2 = c1 * 0.044715f;
  f32x2 t = x * x;
  f32x2 w = x * (t * c2 + c1);
  f32x2 d = (f32x2){__builtin_amdgcn_exp2f(w[0]), __builtin_amdgcn_exp2f(w[1])} + 1.0f;
  return x * (f32x2){__builtin_amdgcn_rcpf(d[0]), __builtin_amdgcn_rcpf(d[1])};
}
__device__ __forceinline__ void x16(float v, float& a, float& b) {
  auto r = __builtin_amdgcn_permlane16_swap(__float_as_uint(v), __float_as_uint(v), false, false);
  a = __uint_as_float(r[0]); b = __uint_as_float(r[1]);
}
__device__ __forceinline__ void x32(float v, float& a, float& b) {
  auto r = __builtin_amdgcn_permlane32_swap(__float_as_uint(v), __float_as_uint(v), false, false);
  a = __uint_as_float(r[0]); b = __uint_as_float(r[1]);
}
__device__ __forceinline__ float rmax4(float v) { float a, b; x16(v, a, b); v = fmaxf(a, b); x32(v, a, b); return fmaxf(a, b); }
__device__ __forceinline__ float rmin4(float v) { float a, b; x16(v, a, b); v = fminf(a, b); x32(v, a, b); return fminf(a, b); }
__device__ __forceinline__ float rsum4(float v) { float a, b; x16(v, a, b); v = a + b; x32(v, a, b); return a + b; }

#define WAIT_V(n) asm volatile("s_waitcnt vmcnt(" #n ")" ::: "memory")
#define WAIT_L0() asm volatile("s_waitcnt lgkmcnt(0)" ::: "memory")
template <int N> __device__ __forceinline__ void wait_vm() {
  asm volatile("s_waitcnt vmcnt(%0)" ::"i"(N) : "memory");
}

__device__ __forceinline__ void glds16(unsigned voff, const void* sbase, unsigned lds) {
  unsigned keep;
  asm volatile("s_mov_b32 %0, m0\n\ts_mov_b32 m0, %3\n\ts_nop 0\n\tglobal_load_lds_dwordx4 %1, %2\n\ts_mov_b32 m0, %0"
               : "=&s"(keep) : "v"(voff), "s"(sbase), "s"(lds) : "memory");
}

__device__ __forceinline__ void mfma_acc(f32x4& c, const u32x4& a, const u32x4& b) {
  asm volatile("v_mfma_f32_16x16x32_bf16 %0, %1, %2, %0" : "+a"(c) : "v"(a), "v"(b));
}

#define STAMP(i)

enum { EPI_QKV = 0, EPI_WO = 1, EPI_W1 = 2, EPI_W2 = 3, EPI_W3 = 4, EPI_W4 = 5 };

template <int BN, int NST, int KD, int LDA, int LDB, int EPI>
__global__ __launch_bounds__(512, 2) void gemm_kernel(
    const u16* __restrict__ A, const u16* __restrict__ W,
    const float* __restrict__ cs,
    const f32x2* __restrict__ st_in,
    const u16* __restrict__ r0,
    const u16* __restrict__ r1,
    u16* __restrict__ out,
    f32x2* __restrict__ st_out,
    const u16* __restrict__ wphi,
    float* __restrict__ kvp,
    float* __restrict__ zp)
{
  constexpr int NFULL = BN / 64, HALF = (BN % 64) / 32;
  constexpr int A_BYTES = 128 * 64 * 2, B_BYTES = BN * 64 * 2, STAGE = A_BYTES + B_BYTES;
  constexpr int NT = KD / 64, NTW = 2 * NFULL + HALF, LPS = 4 + 2 * NFULL + HALF;
  constexpr int PB = BN * 2 + 16;
  constexpr int EPI_BYTES = (EPI == EPI_QKV) ? (4 * 64 * 144 + 2 * (64 * 32 + 64) * 4) : 128 * PB;
  constexpr int MAINB = NST * STAGE;
  constexpr int RS_OFF = (MAINB > EPI_BYTES ? MAINB : EPI_BYTES);
  constexpr int SMEM = RS_OFF + 128 * 16;
  constexpr bool HAS_ST = (EPI == EPI_QKV || EPI == EPI_W1 || EPI == EPI_W3 || EPI == EPI_W2);
  constexpr bool HAS_RES = (EPI == EPI_WO || EPI == EPI_W2 || EPI == EPI_W4);
  constexpr int LAG = (BN == 64) ? 1 : 0;
  constexpr int CH = BN / 8, NIT = CH / 4;
  static_assert(SMEM <= 163840, "LDS");
  __shared__ __attribute__((aligned(16))) char smem[SMEM];

  STAMP(0);
  const int tid = threadIdx.x, lane = tid & 63;
  const int wid8 = __builtin_amdgcn_readfirstlane(tid >> 6), wid = wid8 & 3, wm = wid >> 1, wn = wid & 1;
  const int g = lane >> 4, l15 = lane & 15;
  const int xcd = blockIdx.x & 7, idx = blockIdx.x >> 3;
  const int tm = (xcd >> 1) * 4 + (idx & 3), tn = (xcd & 1) * 8 + (idx >> 2);
  const int row0 = tm * 128;
  f32x4* rs = reinterpret_cast<f32x4*>(smem + RS_OFF);

  f32x4 acc[4][NTW];
  u32x4 ra[HAS_RES ? NIT : 1], rb[(EPI == EPI_W4) ? NIT : 1];

  if (wid8 >= 4) {
    unsigned voA[4], voB[NFULL > 0 ? NFULL : 1][2], voH = 0;
#pragma unroll
    for (int i = 0; i < 4; ++i) {
      int r = (wid * 4 + i) * 8 + (lane >> 3);
      int ch = (lane & 7) ^ ((r >> 1) & 7);
      voA[i] = (unsigned)(row0 + r) * (LDA * 2) + ch * 16;
    }
#pragma unroll
    for (int b = 0; b < NFULL; ++b) {
      int colb = (EPI == EPI_QKV) ? (b * 1024 + tn * 64) : (tn * BN + b * 64);
#pragma unroll
      for (int j = 0; j < 2; ++j) {
        int r = (wid * 2 + j) * 8 + (lane >> 3);
        int f = ((r >> 1) & 1) | (((r >> 3) & 1) << 1);
        int chp = lane & 7;
        int gch = (((chp >> 1) ^ f) << 1) | (chp & 1);
        voB[b][j] = (unsigned)r * (LDB * 2) + colb * 2 + gch * 16;
      }
    }
    if constexpr (HALF) {
      int r = wid * 16 + (lane >> 2);
      int chp = lane & 3;
      int gch = (((chp >> 1) ^ ((r >> 3) & 1)) << 1) | (chp & 1);
      voH = (unsigned)r * (LDB * 2) + (tn * BN + NFULL * 64) * 2 + gch * 16;
    }
    const char* Ab = reinterpret_cast<const char*>(A);
    const char* Wb = reinterpret_cast<const char*>(W);
    const unsigned lbase = (unsigned)(uintptr_t)(__attribute__((address_space(3))) char*)smem;
    auto stage = [&](int kt, int buf) {
      const char* a = Ab + (size_t)kt * 128;
      const char* w = Wb + (size_t)kt * (64 * LDB * 2);
      const unsigned s = lbase + buf * STAGE;
#pragma unroll
      for (int i = 0; i < 4; ++i) glds16(voA[i], a, s + (wid * 4 + i) * 1024);
#pragma unroll
      for (int b = 0; b < NFULL; ++b)
#pragma unroll
        for (int j = 0; j < 2; ++j) glds16(voB[b][j], w, s + A_BYTES + b * 8192 + (wid * 2 + j) * 1024);
      if constexpr (HALF) glds16(voH, w, s + A_BYTES + NFULL * 8192 + wid * 1024);
    };
#pragma unroll
    for (int s = 0; s < NST; ++s) stage(s, s);
    wait_vm<(NST - 1 - LAG) * LPS>();
    __builtin_amdgcn_s_barrier();
    int buf = 0;
    for (int kt = 0; kt < NT - NST; ++kt) {
      wait_vm<(NST - 2 - LAG) * LPS>();
      __builtin_amdgcn_s_barrier();
      stage(kt + NST, buf);
      buf = buf + 1 == NST ? 0 : buf + 1;
    }
    constexpr int XL = (HAS_ST ? 4 : 0) + (HAS_RES ? NIT : 0) + (EPI == EPI_W4 ? NIT : 0);
    f32x4 stp[4];
    const int lt = tid - 256;
    if constexpr (HAS_ST) {
      const f32x4* sp = reinterpret_cast<const f32x4*>(st_in + (size_t)(row0 + (lt >> 1)) * 16 + (lt & 1) * 8);
#pragma unroll
      for (int i = 0; i < 4; ++i) stp[i] = sp[i];
    }
    if constexpr (HAS_RES) {
#pragma unroll
      for (int it = 0; it < NIT; ++it) {
        const int c = tid + 512 * it, r = c / CH, ch = c % CH;
        ra[it] = *reinterpret_cast<const u32x4*>(r0 + (size_t)(row0 + r) * D_ + tn * BN + ch * 8);
        if constexpr (EPI == EPI_W4) rb[it] = *reinterpret_cast<const u32x4*>(r1 + (size_t)(row0 + r) * D_ + tn * BN + ch * 8);
      }
    }
    asm volatile("" ::: "memory");
#pragma unroll
    for (int r = 0; r < NST - 1; ++r) {
      constexpr int NSL = NST - LAG;
      if (r == 0) wait_vm<(NSL - 2) * LPS + XL>();
      else if (r == 1) wait_vm<(NSL > 3 ? NSL - 3 : 0) * LPS + XL>();
      else if (r == 2) wait_vm<(NSL > 4 ? NSL - 4 : 0) * LPS + XL>();
      else if (r == 3) wait_vm<(NSL > 5 ? NSL - 5 : 0) * LPS + XL>();
      else wait_vm<XL>();
      __builtin_amdgcn_s_barrier();
    }
    if constexpr (HAS_ST) {
      float sm = 0.f;
#pragma unroll
      for (int i = 0; i < 4; ++i) sm += stp[i][0] + stp[i][2];
      sm += __shfl_xor(sm, 1);
      float mean = sm * (1.0f / 1024.f);
      float m2 = 0.f;
#pragma unroll
      for (int i = 0; i < 4; ++i) {
        float d0 = stp[i][0] * (1.0f / 64.f) - mean, d1 = stp[i][2] * (1.0f / 64.f) - mean;
        m2 += stp[i][1] + stp[i][3] + 64.f * (d0 * d0 + d1 * d1);
      }
      m2 += __shfl_xor(m2, 1);
      float var = m2 * (1.0f / 1024.f);
      float r1_ = 1.0f / sqrtf(var + 1e-5f);
      float sc = r1_;
      if constexpr (EPI == EPI_W1) sc = r1_ / sqrtf(var * r1_ * r1_ + 1e-5f);
      if ((lt & 1) == 0) rs[lt >> 1] = (f32x4){mean, sc, r1_, 0.f};
    }
  } else {
    if constexpr (HAS_RES) {
#pragma unroll
      for (int it = 0; it < NIT; ++it) {
        const int c = tid + 512 * it, r = c / CH, ch = c % CH;
        ra[it] = *reinterpret_cast<const u32x4*>(r0 + (size_t)(row0 + r) * D_ + tn * BN + ch * 8);
        if constexpr (EPI == EPI_W4) rb[it] = *reinterpret_cast<const u32x4*>(r1 + (size_t)(row0 + r) * D_ + tn * BN + ch * 8);
      }
    }
    const int q = l15 >> 2, p = lane & 3;
    const int fB = (q >> 1) | ((g & 1) << 1);
    unsigned aoff[2], boff[2], hoff = 0;
#pragma unroll
    for (int ks = 0; ks < 2; ++ks) aoff[ks] = (wm * 64 + l15) * 128 + (((4 * ks + g) ^ (l15 >> 1)) * 16);
#pragma unroll
    for (int u = 0; u < 2; ++u) boff[u] = A_BYTES + (8 * g + q) * 128 + (((2 * wn + u) ^ fB) * 32) + p * 8;
    if constexpr (HALF) hoff = A_BYTES + NFULL * 8192 + (8 * g + q) * 64 + ((wn ^ (g & 1)) * 32) + p * 8;

    auto ldf = [&](int i, const char* s, int ks, u32x4(&af)[4], u32x4(&bfr)[NTW]) {
      if (i == 0 || i > NTW) {
        const int mt = i == 0 ? 0 : i - NTW;
        af[mt] = *reinterpret_cast<const u32x4*>(s + aoff[ks] + mt * 2048);
      } else if (i - 1 < 2 * NFULL) {
        const int b = (i - 1) >> 1, u = (i - 1) & 1;
        s16x4 lo = __builtin_amdgcn_ds_read_tr16_b64_v4i16((lds_s16x4_p)(s + boff[u] + b * 8192 + ks * 4096));
        s16x4 hi = __builtin_amdgcn_ds_read_tr16_b64_v4i16((lds_s16x4_p)(s + boff[u] + b * 8192 + ks * 4096 + 512));
        bfr[i - 1] = __builtin_shufflevector(__builtin_bit_cast(u32x2, lo), __builtin_bit_cast(u32x2, hi), 0, 1, 2, 3);
      } else {
        s16x4 lo = __builtin_amdgcn_ds_read_tr16_b64_v4i16((lds_s16x4_p)(s + hoff + ks * 2048));
        s16x4 hi = __builtin_amdgcn_ds_read_tr16_b64_v4i16((lds_s16x4_p)(s + hoff + ks * 2048 + 256));
        bfr[NTW - 1] = __builtin_shufflevector(__builtin_bit_cast(u32x2, lo), __builtin_bit_cast(u32x2, hi), 0, 1, 2, 3);
      }
    };
    constexpr int NM = 4 * NTW, NL = NTW + 4;
    auto phase = [&](u32x4(&afc)[4], u32x4(&bfc)[NTW], u32x4(&afn)[4], u32x4(&bfn)[NTW], int nbuf, int nks, auto rd, auto bar) {
      const char* s = smem + nbuf * STAGE;
      constexpr int BAR = decltype(bar)::value, R0 = BAR < 0 ? 0 : BAR + 1;
      constexpr int RSP = (NM - R0 - 2) / NL > 0 ? (NM - R0 - 2) / NL : 1;
      static_assert(R0 + (NL - 1) * RSP < NM, "reads");
#pragma unroll
      for (int mt = 0; mt < 4; ++mt)
#pragma unroll
        for (int j = 0; j < NTW; ++j) {
          const int m = mt * NTW + j;
          if (m == BAR) {
            WAIT_L0();
            __builtin_amdgcn_s_barrier();
            asm volatile("" ::: "memory");
          }
          if (decltype(rd)::value && m >= R0 && (m - R0) % RSP == 0 && (m - R0) / RSP < NL) ldf((m - R0) / RSP, s, nks, afn, bfn);
          if (EPI == EPI_QKV && j >= 4) mfma_acc(acc[mt][j], afc[mt], bfc[j]);
          else mfma_acc(acc[mt][j], bfc[j], afc[mt]);
          __builtin_amdgcn_sched_barrier(0);
        }
    };
    typedef std::integral_constant<int, -1> nobar;
#pragma unroll
    for (int mt = 0; mt < 4; ++mt)
#pragma unroll
      for (int j = 0; j < NTW; ++j) {
        acc[mt][j] = (f32x4){0.f, 0.f, 0.f, 0.f};
        asm volatile("" : "+a"(acc[mt][j]));
      }
    u32x4 af0[4], af1[4], bf0[NTW], bf1[NTW];
    __builtin_amdgcn_s_barrier();
    asm volatile("" ::: "memory");
    STAMP(1);
#pragma unroll
    for (int i = 0; i < NL; ++i) { ldf(i, smem, 0, af0, bf0); __builtin_amdgcn_sched_barrier(0); }
    int buf = 0;
    if constexpr (LAG) {
      phase(af0, bf0, af1, bf1, 0, 1, std::true_type{}, nobar{});
      for (int kt = 0; kt < NT - 1; ++kt) {
        buf = buf + 1 == NST ? 0 : buf + 1;
        phase(af1, bf1, af0, bf0, buf, 0, std::true_type{}, nobar{});
        asm volatile("" ::: "memory");
        __builtin_amdgcn_s_barrier();
        asm volatile("" ::: "memory");
        phase(af0, bf0, af1, bf1, buf, 1, std::true_type{}, nobar{});
      }
      phase(af1, bf1, af0, bf0, buf, 0, std::false_type{}, nobar{});
    } else {
      for (int kt = 0; kt < NT - 1; ++kt) {
        phase(af0, bf0, af1, bf1, buf, 1, std::true_type{}, nobar{});
        buf = buf + 1 == NST ? 0 : buf + 1;
        phase(af1, bf1, af0, bf0, buf, 0, std::true_type{}, std::integral_constant<int, 4>{});
      }
      phase(af0, bf0, af1, bf1, buf, 1, std::true_type{}, nobar{});
      phase(af1, bf1, af0, bf0, buf, 0, std::false_type{}, nobar{});
    }
    asm volatile("s_nop 7\n\ts_nop 7\n\ts_nop 7" ::: "memory");
  }
  WAIT_L0();
  __builtin_amdgcn_s_barrier();
  asm volatile("" ::: "memory");
  STAMP(2);

  if constexpr (EPI == EPI_QKV) {
    const int h = tn * 2 + wn;
    const int q = l15 >> 2, p = lane & 3;
    f32x4 kv[4][2], zz[4];
    float* red = reinterpret_cast<float*>(smem + 4 * 64 * 144) + wn * (64 * 32 + 64);
    if (wid8 < 4) {
      bf16x8 wf[2];
#pragma unroll
      for (int ft = 0; ft < 2; ++ft)
#pragma unroll
        for (int j = 0; j < 8; ++j) {
          int d = (j < 4) ? 4 * g + j : 16 + 4 * g + (j - 4);
          wf[ft][j] = (short)wphi[(h * 32 + d) * 32 + ft * 16 + l15];
        }
      f32x4 cs4[4];
      float csv[2];
#pragma unroll
      for (int t = 0; t < 2; ++t) {
        cs4[t] = *reinterpret_cast<const f32x4*>(cs + h * 32 + t * 16 + 4 * g);
        cs4[2 + t] = *reinterpret_cast<const f32x4*>(cs + 1024 + h * 32 + t * 16 + 4 * g);
        csv[t] = cs[2048 + h * 32 + t * 16 + l15];
      }
      bf16x8 qf[4], kf[4], vf[2][2];
#pragma unroll
      for (int mt = 0; mt < 4; ++mt) {
        f32x4 st = rs[wm * 64 + mt * 16 + l15];
        unsigned pq_[4], pk_[4];
#pragma unroll
        for (int t = 0; t < 2; ++t) {
          float a[4], b[4];
#pragma unroll
          for (int i = 0; i < 4; ++i) {
            a[i] = st[1] * (acc[mt][t][i] - st[0] * cs4[t][i]);
            b[i] = st[1] * (acc[mt][2 + t][i] - st[0] * cs4[2 + t][i]);
          }
          pq_[t * 2] = pk2(a[0], a[1]); pq_[t * 2 + 1] = pk2(a[2], a[3]);
          pk_[t * 2] = pk2(b[0], b[1]); pk_[t * 2 + 1] = pk2(b[2], b[3]);
        }
        qf[mt] = __builtin_bit_cast(bf16x8, (u32x4){pq_[0], pq_[1], pq_[2], pq_[3]});
        kf[mt] = __builtin_bit_cast(bf16x8, (u32x4){pk_[0], pk_[1], pk_[2], pk_[3]});
      }
#pragma unroll
      for (int s = 0; s < 2; ++s) {
        unsigned pv[2][4];
#pragma unroll
        for (int m2 = 0; m2 < 2; ++m2) {
          int mt = s * 2 + m2;
          f32x4 s0 = rs[wm * 64 + mt * 16 + 4 * g + 0], s1 = rs[wm * 64 + mt * 16 + 4 * g + 1];
          f32x4 s2 = rs[wm * 64 + mt * 16 + 4 * g + 2], s3 = rs[wm * 64 + mt * 16 + 4 * g + 3];
#pragma unroll
          for (int t = 0; t < 2; ++t) {
            float a0 = s0[1] * (acc[mt][4 + t][0] - s0[0] * csv[t]);
            float a1 = s1[1] * (acc[mt][4 + t][1] - s1[0] * csv[t]);
            float a2 = s2[1] * (acc[mt][4 + t][2] - s2[0] * csv[t]);
            float a3 = s3[1] * (acc[mt][4 + t][3] - s3[0] * csv[t]);
            pv[t][m2 * 2] = pk2(a0, a1); pv[t][m2 * 2 + 1] = pk2(a2, a3);
          }
        }
#pragma unroll
        for (int t = 0; t < 2; ++t) vf[s][t] = __builtin_bit_cast(bf16x8, (u32x4){pv[t][0], pv[t][1], pv[t][2], pv[t][3]});
      }
      char* pkl = smem + wid * (64 * 144);
      const float L2E = 1.4426950408889634f;
#pragma unroll
      for (int which = 0; which < 2; ++which)
#pragma unroll
        for (int mt = 0; mt < 4; ++mt) {
          f32x4 z4 = (f32x4){0.f, 0.f, 0.f, 0.f};
          f32x4 p0 = __builtin_amdgcn_mfma_f32_16x16x32_bf16(wf[0], which ? kf[mt] : qf[mt], z4, 0, 0, 0);
          f32x4 p1 = __builtin_amdgcn_mfma_f32_16x16x32_bf16(wf[1], which ? kf[mt] : qf[mt], z4, 0, 0, 0);
          float v[8];
#pragma unroll
          for (int i = 0; i < 4; ++i) { v[i] = rbf(p0[i]); v[4 + i] = rbf(p1[i]); }
          float mx = v[0], mn = v[0];
#pragma unroll
          for (int i = 1; i < 8; ++i) { mx = fmaxf(mx, v[i]); mn = fminf(mn, v[i]); }
          mx = rmax4(mx); mn = rmin4(mn);
          float ep[8], en[8], sp = 0.f, sn = 0.f;
#pragma unroll
          for (int i = 0; i < 8; ++i) {
            ep[i] = __builtin_amdgcn_exp2f((v[i] - mx) * L2E);
            en[i] = __builtin_amdgcn_exp2f((mn - v[i]) * L2E);
            sp += ep[i]; sn += en[i];
          }
          sp = rsum4(sp); sn = rsum4(sn);
          float ip = __builtin_amdgcn_rcpf(sp), in_ = __builtin_amdgcn_rcpf(sn);
          u32x2 o[4];
          o[0] = (u32x2){pk2(ep[0] * ip, ep[1] * ip), pk2(ep[2] * ip, ep[3] * ip)};
          o[1] = (u32x2){pk2(ep[4] * ip, ep[5] * ip), pk2(ep[6] * ip, ep[7] * ip)};
          o[2] = (u32x2){pk2(en[0] * in_, en[1] * in_), pk2(en[2] * in_, en[3] * in_)};
          o[3] = (u32x2){pk2(en[4] * in_, en[5] * in_), pk2(en[6] * in_, en[7] * in_)};
          if (which == 0) {
            u16* dst = out + ((size_t)(row0 + wm * 64 + mt * 16 + l15) * 32 + h) * 64 + 4 * g;
#pragma unroll
            for (int c = 0; c < 4; ++c) *reinterpret_cast<u32x2*>(dst + c * 16) = o[c];
          } else {
            char* dst = pkl + (mt * 16 + l15) * 144 + g * 8;
#pragma unroll
            for (int c = 0; c < 4; ++c) *reinterpret_cast<u32x2*>(dst + c * 32) = o[c];
          }
        }
      WAIT_L0();
      __builtin_amdgcn_sched_barrier(0);
#pragma unroll
      for (int ft = 0; ft < 4; ++ft) { kv[ft][0] = kv[ft][1] = zz[ft] = (f32x4){0.f, 0.f, 0.f, 0.f}; }
      const bf16x8 ones = (bf16x8){0x3F80, 0x3F80, 0x3F80, 0x3F80, 0x3F80, 0x3F80, 0x3F80, 0x3F80};
#pragma unroll
      for (int s = 0; s < 2; ++s)
#pragma unroll
        for (int ft = 0; ft < 4; ++ft) {
          const char* src = pkl + (32 * s + 4 * g + q) * 144 + ft * 32 + p * 8;
          s16x4 lo = __builtin_amdgcn_ds_read_tr16_b64_v4i16((lds_s16x4_p)(src));
          s16x4 hi = __builtin_amdgcn_ds_read_tr16_b64_v4i16((lds_s16x4_p)(src + 16 * 144));
          bf16x8 pf = __builtin_shufflevector(lo, hi, 0, 1, 2, 3, 4, 5, 6, 7);
          kv[ft][0] = __builtin_amdgcn_mfma_f32_16x16x32_bf16(vf[s][0], pf, kv[ft][0], 0, 0, 0);
          kv[ft][1] = __builtin_amdgcn_mfma_f32_16x16x32_bf16(vf[s][1], pf, kv[ft][1], 0, 0, 0);
          zz[ft] = __builtin_amdgcn_mfma_f32_16x16x32_bf16(ones, pf, zz[ft], 0, 0, 0);
        }
      if (wm == 1) {
#pragma unroll
        for (int ft = 0; ft < 4; ++ft) {
#pragma unroll
          for (int dt = 0; dt < 2; ++dt) *reinterpret_cast<f32x4*>(red + (ft * 16 + l15) * 32 + dt * 16 + 4 * g) = kv[ft][dt];
          if (g == 0) red[64 * 32 + ft * 16 + l15] = zz[ft][0];
        }
      }
    }
    __syncthreads();
    if (wid8 < 2) {
      float* kd = kvp + (size_t)(h * 16 + tm) * 2048;
      float* zd = zp + (size_t)(h * 16 + tm) * 64;
#pragma unroll
      for (int ft = 0; ft < 4; ++ft) {
#pragma unroll
        for (int dt = 0; dt < 2; ++dt) {
          f32x4 o = *reinterpret_cast<const f32x4*>(red + (ft * 16 + l15) * 32 + dt * 16 + 4 * g);
          *reinterpret_cast<f32x4*>(kd + (ft * 16 + l15) * 32 + dt * 16 + 4 * g) = o + kv[ft][dt];
        }
        if (g == 0) zd[ft * 16 + l15] = red[64 * 32 + ft * 16 + l15] + zz[ft][0];
      }
    }
  } else {
    if (wid8 < 4) {
      f32x4 stm[4], cs4[NTW];
      if constexpr (EPI == EPI_W1 || EPI == EPI_W3) {
#pragma unroll
        for (int j = 0; j < NTW; ++j) {
          const int colj = (j < 2 * NFULL) ? ((j >> 1) * 64 + (2 * wn + (j & 1)) * 16) : (NFULL * 64 + wn * 16);
          cs4[j] = *reinterpret_cast<const f32x4*>(cs + tn * BN + colj + 4 * g);
        }
#pragma unroll
        for (int mt = 0; mt < 4; ++mt) stm[mt] = rs[wm * 64 + mt * 16 + l15];
      }
#pragma unroll
      for (int j = 0; j < NTW; ++j) {
        const int colj = (j < 2 * NFULL) ? ((j >> 1) * 64 + (2 * wn + (j & 1)) * 16) : (NFULL * 64 + wn * 16);
#pragma unroll
        for (int mt = 0; mt < 4; ++mt) {
          f32x2 v0 = (f32x2){acc[mt][j][0], acc[mt][j][1]}, v1 = (f32x2){acc[mt][j][2], acc[mt][j][3]};
          if constexpr (EPI == EPI_W1 || EPI == EPI_W3) {
            v0 = gelu2((v0 - stm[mt][0] * (f32x2){cs4[j][0], cs4[j][1]}) * stm[mt][1]);
            v1 = gelu2((v1 - stm[mt][0] * (f32x2){cs4[j][2], cs4[j][3]}) * stm[mt][1]);
          }
          *reinterpret_cast<u32x2*>(smem + (wm * 64 + mt * 16 + l15) * PB + (colj + 4 * g) * 2) = (u32x2){pk2(v0[0], v0[1]), pk2(v1[0], v1[1])};
        }
      }
    }
    __syncthreads();
#pragma unroll
    for (int it = 0; it < NIT; ++it) {
      const int c = tid + 512 * it, r = c / CH, ch = c % CH;
      u32x4 v = *reinterpret_cast<const u32x4*>(smem + r * PB + ch * 16);
      const int grow = row0 + r, gcol = tn * BN + ch * 8;
      if constexpr (EPI == EPI_W3) {
        *reinterpret_cast<u32x4*>(out + (size_t)grow * F_ + gcol) = v;
      } else if constexpr (EPI == EPI_W1) {
        const int grp = tn >> 2;
        const int sh = grp == 0 ? 1 : grp == 1 ? -1 : grp == 2 ? 2 : -2;
        const int dr = grow + sh;
        if (dr >= 0 && dr < S_) *reinterpret_cast<u32x4*>(out + (size_t)dr * D_ + gcol) = v;
        const int zr = grow - sh;
        if (zr < 0 || zr >= S_) *reinterpret_cast<u32x4*>(out + (size_t)grow * D_ + gcol) = (u32x4){0u, 0u, 0u, 0u};
      } else {
        float y[8];
        const u32x4 a = ra[it];
        if constexpr (EPI == EPI_WO) {
#pragma unroll
          for (int i = 0; i < 4; ++i) { y[2 * i] = rbf(bflo(a[i]) + bflo(v[i])); y[2 * i + 1] = rbf(bfhi(a[i]) + bfhi(v[i])); }
        } else if constexpr (EPI == EPI_W2) {
          f32x4 st = rs[r];
#pragma unroll
          for (int i = 0; i < 4; ++i) {
            y[2 * i] = rbf(rbf((bflo(a[i]) - st[0]) * st[2]) + bflo(v[i]));
            y[2 * i + 1] = rbf(rbf((bfhi(a[i]) - st[0]) * st[2]) + bfhi(v[i]));
          }
        } else {
          const u32x4 b = rb[it];
#pragma unroll
          for (int i = 0; i < 4; ++i) {
            y[2 * i] = rbf(bflo(a[i]) + rbf(bflo(b[i]) + bflo(v[i])));
            y[2 * i + 1] = rbf(bfhi(a[i]) + rbf(bfhi(b[i]) + bfhi(v[i])));
          }
        }
        *reinterpret_cast<u32x4*>(out + (size_t)grow * D_ + gcol) = (u32x4){pk2(y[0], y[1]), pk2(y[2], y[3]), pk2(y[4], y[5]), pk2(y[6], y[7])};
        float sm = ((y[0] + y[1]) + (y[2] + y[3])) + ((y[4] + y[5]) + (y[6] + y[7]));
        sm += __shfl_xor(sm, 1); sm += __shfl_xor(sm, 2); sm += __shfl_xor(sm, 4);
        float mu = sm * (1.0f / 64.f), m2 = 0.f;
#pragma unroll
        for (int i = 0; i < 8; ++i) { float d = y[i] - mu; m2 += d * d; }
        m2 += __shfl_xor(m2, 1); m2 += __shfl_xor(m2, 2); m2 += __shfl_xor(m2, 4);
        if (ch == 0) st_out[(size_t)grow * 16 + tn] = (f32x2){sm, m2};
      }
    }
  }
  STAMP(3);
}

__global__ __launch_bounds__(256) void attn_out_kernel(const u16* __restrict__ phiq, const float* __restrict__ kvp,
                                                       const float* __restrict__ zp, u16* __restrict__ o) {
  __shared__ __attribute__((aligned(16))) u16 kvT[48 * 72];
  const int tid = threadIdx.x, lane = tid & 63, wid = tid >> 6, g = lane >> 4, l15 = lane & 15;
  const int h = blockIdx.x & 31, tg = blockIdx.x >> 5;
  {
    f32x4 a0 = (f32x4){0.f, 0.f, 0.f, 0.f}, a1 = a0;
    const float* src = kvp + (size_t)h * 16 * 2048 + tid * 8;
#pragma unroll
    for (int t = 0; t < 16; ++t) {
      a0 += *reinterpret_cast<const f32x4*>(src + t * 2048);
      a1 += *reinterpret_cast<const f32x4*>(src + t * 2048 + 4);
    }
    const int f = tid >> 2, d0 = (tid & 3) * 8;
#pragma unroll
    for (int i = 0; i < 4; ++i) {
      kvT[(d0 + i) * 72 + f] = (u16)(pk2(a0[i], 0.f) & 0xffff);
      kvT[(d0 + 4 + i) * 72 + f] = (u16)(pk2(a1[i], 0.f) & 0xffff);
    }
    if (tid < 64) {
      float z = 0.f;
#pragma unroll
      for (int t = 0; t < 16; ++t) z += zp[(size_t)(h * 16 + t) * 64 + tid];
      kvT[32 * 72 + tid] = (u16)(pk2(z, 0.f) & 0xffff);
    }
    for (int i = tid; i < 15 * 64; i += 256) kvT[(33 + i / 64) * 72 + (i & 63)] = 0;
  }
  __syncthreads();
  bf16x8 kf[3][2];
#pragma unroll
  for (int dt = 0; dt < 3; ++dt)
#pragma unroll
    for (int ks = 0; ks < 2; ++ks) kf[dt][ks] = *reinterpret_cast<const bf16x8*>(&kvT[(dt * 16 + l15) * 72 + ks * 32 + g * 8]);
#pragma unroll
  for (int mt = 0; mt < 4; ++mt) {
    const int tok = tg * 256 + wid * 64 + mt * 16 + l15;
    const u16* src = phiq + ((size_t)tok * 32 + h) * 64 + g * 8;
    bf16x8 p0 = *reinterpret_cast<const bf16x8*>(src), p1 = *reinterpret_cast<const bf16x8*>(src + 32);
    f32x4 r[3];
#pragma unroll
    for (int dt = 0; dt < 3; ++dt) {
      r[dt] = (f32x4){0.f, 0.f, 0.f, 0.f};
      r[dt] = __builtin_amdgcn_mfma_f32_16x16x32_bf16(kf[dt][0], p0, r[dt], 0, 0, 0);
      r[dt] = __builtin_amdgcn_mfma_f32_16x16x32_bf16(kf[dt][1], p1, r[dt], 0, 0, 0);
    }
    float den = rbf(rbf(__shfl(r[2][0], l15)) + 1e-6f);
    float inv = 1.0f / den;
#pragma unroll
    for (int dt = 0; dt < 2; ++dt) {
      u32x2 w = (u32x2){pk2(rbf(r[dt][0]) * inv, rbf(r[dt][1]) * inv), pk2(rbf(r[dt][2]) * inv, rbf(r[dt][3]) * inv)};
      *reinterpret_cast<u32x2*>(o + (size_t)tok * D_ + h * 32 + dt * 16 + 4 * g) = w;
    }
  }
}

__global__ __launch_bounds__(256) void prep_kernel(const u16* __restrict__ x, const u16* __restrict__ wqkv,
                                                   const u16* __restrict__ w1, const u16* __restrict__ w3,
                                                   float* __restrict__ cs_qkv, float* __restrict__ cs_w1,
                                                   float* __restrict__ cs_w3, f32x2* __restrict__ st,
                                                   const u16* __restrict__ wo, const u16* __restrict__ w2,
                                                   const u16* __restrict__ w4) {
  __shared__ float red[32][65];
  const int tid = threadIdx.x, b = blockIdx.x;
  if (b >= 248) {
    const int pb = b - 248;
    const u16* w = pb < 32 ? wo + (size_t)pb * 32768 : pb < 64 ? w2 + (size_t)(pb - 32) * 32768 : w4 + (size_t)(pb - 64) * 32768;
    u32x4 v[16];
#pragma unroll
    for (int r = 0; r < 16; ++r) v[r] = *reinterpret_cast<const u32x4*>(w + r * 2048 + tid * 8);
#pragma unroll
    for (int r = 0; r < 16; ++r) asm volatile("" ::"v"(v[r]));
  } else if (b < 120) {
    const u16* w; float* dst; int ld, c0;
    if (b < 48) { w = wqkv; dst = cs_qkv; ld = 3072; c0 = b * 64; }
    else if (b < 64) { w = w1; dst = cs_w1; ld = 1024; c0 = (b - 48) * 64; }
    else { w = w3; dst = cs_w3; ld = F_; c0 = (b - 64) * 64; }
    const int cg = tid & 7, rg = tid >> 3;
    float s[8] = {0.f, 0.f, 0.f, 0.f, 0.f, 0.f, 0.f, 0.f};
#pragma unroll 8
    for (int r = 0; r < 32; ++r) {
      u32x4 v = *reinterpret_cast<const u32x4*>(w + (size_t)(r * 32 + rg) * ld + c0 + cg * 8);
#pragma unroll
      for (int i = 0; i < 4; ++i) { s[2 * i] += bflo(v[i]); s[2 * i + 1] += bfhi(v[i]); }
    }
#pragma unroll
    for (int i = 0; i < 8; ++i) red[rg][cg * 8 + i] = s[i];
    __syncthreads();
    if (tid < 64) {
      float t = 0.f;
#pragma unroll
      for (int r = 0; r < 32; ++r) t += red[r][tid];
      dst[c0 + tid] = t;
    }
  } else {
    const int lane = tid & 63, wid = tid >> 6;
    const int rbase = (b - 120) * 16 + wid * 4;
#pragma unroll
    for (int rr = 0; rr < 4; ++rr) {
      const int row = rbase + rr;
#pragma unroll
      for (int hf = 0; hf < 2; ++hf) {
        u32x4 v = *reinterpret_cast<const u32x4*>(x + (size_t)row * D_ + hf * 512 + lane * 8);
        float y[8];
#pragma unroll
        for (int i = 0; i < 4; ++i) { y[2 * i] = bflo(v[i]); y[2 * i + 1] = bfhi(v[i]); }
        float sm = ((y[0] + y[1]) + (y[2] + y[3])) + ((y[4] + y[5]) + (y[6] + y[7]));
        sm += __shfl_xor(sm, 1); sm += __shfl_xor(sm, 2); sm += __shfl_xor(sm, 4);
        float mu = sm * (1.0f / 64.f), m2 = 0.f;
#pragma unroll
        for (int i = 0; i < 8; ++i) { float d = y[i] - mu; m2 += d * d; }
        m2 += __shfl_xor(m2, 1); m2 += __shfl_xor(m2, 2); m2 += __shfl_xor(m2, 4);
        if ((lane & 7) == 0) st[(size_t)row * 16 + hf * 8 + (lane >> 3)] = (f32x2){sm, m2};
      }
    }
  }
}

extern "C" void kernel_launch(void* const* d_in, const int* in_sizes, int n_in, void* d_out, int out_size, void* d_ws,
                              size_t ws_size, hipStream_t stream) {
  const u16* x_in = (const u16*)d_in[0];
  const u16* wqkv = (const u16*)d_in[1];
  const u16* wphi = (const u16*)d_in[2];
  const u16* wo = (const u16*)d_in[3];
  const u16* w1 = (const u16*)d_in[5];
  const u16* w2 = (const u16*)d_in[6];
  const u16* w3 = (const u16*)d_in[7];
  const u16* w4 = (const u16*)d_in[8];
  char* ws = (char*)d_ws;
  size_t off = 0;
  auto alloc = [&](size_t bytes) { char* p = ws + off; off += (bytes + 4095) & ~(size_t)4095; return p; };
  u16* xb = (u16*)alloc((size_t)S_ * D_ * 2);
  u16* x2 = (u16*)alloc((size_t)S_ * D_ * 2);
  u16* x1 = (u16*)alloc((size_t)S_ * D_ * 2);
  u16* hs = (u16*)alloc((size_t)S_ * D_ * 2);
  u16* ob = (u16*)alloc((size_t)S_ * D_ * 2);
  u16* gb = (u16*)alloc((size_t)S_ * F_ * 2);
  u16* phiq = (u16*)alloc((size_t)S_ * 2048 * 2);
  float* kvp = (float*)alloc((size_t)32 * 16 * 2048 * 4);
  float* zp = (float*)alloc((size_t)32 * 16 * 64 * 4);
  f32x2* st_x = (f32x2*)alloc((size_t)S_ * 16 * 8);
  f32x2* st_x2 = (f32x2*)alloc((size_t)S_ * 16 * 8);
  f32x2* st_x1 = (f32x2*)alloc((size_t)S_ * 16 * 8);
  float* cs_qkv = (float*)alloc(3072 * 4);
  float* cs_w1 = (float*)alloc(1024 * 4);
  float* cs_w3 = (float*)alloc(F_ * 4);

  prep_kernel<<<248 + 176, 256, 0, stream>>>(x_in, wqkv, w1, w3, cs_qkv, cs_w1, cs_w3, st_x, wo, w2, w4);
  for (int layer = 0; layer < 2; ++layer) {
    const u16* x = layer == 0 ? x_in : xb;
    u16* xo = layer == 0 ? xb : (u16*)d_out;
    gemm_kernel<192, 3, 1024, 1024, 3072, EPI_QKV><<<256, 512, 0, stream>>>(x, wqkv, cs_qkv, st_x, nullptr, nullptr, phiq, nullptr, wphi, kvp, zp);
    attn_out_kernel<<<256, 256, 0, stream>>>(phiq, kvp, zp, ob);
    gemm_kernel<64, 5, 1024, 1024, 1024, EPI_WO><<<256, 512, 0, stream>>>(ob, wo, nullptr, nullptr, x, nullptr, x2, st_x2, nullptr, nullptr, nullptr);
    gemm_kernel<64, 5, 1024, 1024, 1024, EPI_W1><<<256, 512, 0, stream>>>(x2, w1, cs_w1, st_x2, nullptr, nullptr, hs, nullptr, nullptr, nullptr, nullptr);
    gemm_kernel<64, 5, 1024, 1024, 1024, EPI_W2><<<256, 512, 0, stream>>>(hs, w2, nullptr, st_x2, x2, nullptr, x1, st_x1, nullptr, nullptr, nullptr);
    gemm_kernel<224, 3, 1024, 1024, F_, EPI_W3><<<256, 512, 0, stream>>>(x1, w3, cs_w3, st_x1, nullptr, nullptr, gb, nullptr, nullptr, nullptr, nullptr);
    gemm_kernel<64, 5, F_, F_, 1024, EPI_W4><<<256, 512, 0, stream>>>(gb, w4, nullptr, nullptr, x2, x1, xo, st_x, nullptr, nullptr, nullptr);
  }
}
```

```cpp
#include <hip/hip_runtime.h>
#include <hip/hip_bf16.h>
#include <stdint.h>
#include <type_traits>

typedef short bf16x8 __attribute__((ext_vector_type(8)));
typedef short s16x4 __attribute__((ext_vector_type(4)));
typedef float f32x4 __attribute__((ext_vector_type(4)));
typedef float f32x2 __attribute__((ext_vector_type(2)));
typedef unsigned u32x2 __attribute__((ext_vector_type(2)));
typedef unsigned u32x4 __attribute__((ext_vector_type(4)));
typedef __attribute__((address_space(3))) s16x4* lds_s16x4_p;
typedef unsigned short u16;

constexpr int S_ = 2048, D_ = 1024, H_ = 32, F_ = 3584;

__device__ __forceinline__ float bf2f(u16 v) { return __uint_as_float(((unsigned)v) << 16); }
__device__ __forceinline__ float bflo(unsigned v) { return __uint_as_float(v << 16); }
__device__ __forceinline__ float bfhi(unsigned v) { return __uint_as_float(v & 0xffff0000u); }
typedef __bf16 bf16x2_t __attribute__((ext_vector_type(2)));
__device__ __forceinline__ unsigned pk2(float a, float b) {
  return __builtin_bit_cast(unsigned, __builtin_convertvector((f32x2){a, b}, bf16x2_t));
}
__device__ __forceinline__ unsigned pk2v(f32x2 v) { return __builtin_bit_cast(unsigned, __builtin_convertvector(v, bf16x2_t)); }
__device__ __forceinline__ float max2_(float a, float b) { float r; asm("v_max_f32 %0, %1, %2" : "=v"(r) : "v"(a), "v"(b)); return r; }
__device__ __forceinline__ float min2_(float a, float b) { float r; asm("v_min_f32 %0, %1, %2" : "=v"(r) : "v"(a), "v"(b)); return r; }
__device__ __forceinline__ float max3_(float a, float b, float c) { float r; asm("v_max3_f32 %0, %1, %2, %3" : "=v"(r) : "v"(a), "v"(b), "v"(c)); return r; }
__device__ __forceinline__ float min3_(float a, float b, float c) { float r; asm("v_min3_f32 %0, %1, %2, %3" : "=v"(r) : "v"(a), "v"(b), "v"(c)); return r; }
__device__ __forceinline__ float rbf(float a) { return bflo(pk2(a, 0.f)); }
__device__ __forceinline__ float gelu_t(float x) {
  const float c1 = -2.0f * 0.7978845608028654f * 1.4426950408889634f;
  const float c2 = c1 * 0.044715f;
  float t = x * x;
  float w = x * __builtin_fmaf(t, c2, c1);
  float e = __builtin_amdgcn_exp2f(w);
  return x * __builtin_amdgcn_rcpf(1.0f + e);
}

__device__ __forceinline__ f32x2 gelu2(f32x2 x) {
  const float c1 = -2.0f * 0.7978845608028654f * 1.4426950408889634f;
  const float c2 = c1 * 0.044715f;
  f32x2 t = x * x;
  f32x2 w = x * (t * c2 + c1);
  f32x2 d = (f32x2){__builtin_amdgcn_exp2f(w[0]), __builtin_amdgcn_exp2f(w[1])} + 1.0f;
  return x * (f32x2){__builtin_amdgcn_rcpf(d[0]), __builtin_amdgcn_rcpf(d[1])};
}
__device__ __forceinline__ void x16(float v, float& a, float& b) {
  auto r = __builtin_amdgcn_permlane16_swap(__float_as_uint(v), __float_as_uint(v), false, false);
  a = __uint_as_float(r[0]); b = __uint_as_float(r[1]);
}
__device__ __forceinline__ void x32(float v, float& a, float& b) {
  auto r = __builtin_amdgcn_permlane32_swap(__float_as_uint(v), __float_as_uint(v), false, false);
  a = __uint_as_float(r[0]); b = __uint_as_float(r[1]);
}
__device__ __forceinline__ float rmax4(float v) { float a, b; x16(v, a, b); v = max2_(a, b); x32(v, a, b); return max2_(a, b); }
__device__ __forceinline__ float rmin4(float v) { float a, b; x16(v, a, b); v = min2_(a, b); x32(v, a, b); return min2_(a, b); }
__device__ __forceinline__ float rsum4(float v) { float a, b; x16(v, a, b); v = a + b; x32(v, a, b); return a + b; }

__device__ __forceinline__ float rsum8(float v) {
  v += __uint_as_float(__builtin_amdgcn_mov_dpp(__float_as_uint(v), 0xB1, 0xF, 0xF, true));
  v += __uint_as_float(__builtin_amdgcn_mov_dpp(__float_as_uint(v), 0x4E, 0xF, 0xF, true));
  v += __uint_as_float(__builtin_amdgcn_mov_dpp(__float_as_uint(v), 0x141, 0xF, 0xF, true));
  return v;
}
#define WAIT_V(n) asm volatile("s_waitcnt vmcnt(" #n ")" ::: "memory")
#define WAIT_L0() asm volatile("s_waitcnt lgkmcnt(0)" ::: "memory")
template <int N> __device__ __forceinline__ void wait_vm() {
  asm volatile("s_waitcnt vmcnt(%0)" ::"i"(N) : "memory");
}

__device__ __forceinline__ void glds16(unsigned voff, const void* sbase, unsigned lds) {
  unsigned keep;
  asm volatile("s_mov_b32 %0, m0\n\ts_mov_b32 m0, %3\n\ts_nop 0\n\tglobal_load_lds_dwordx4 %1, %2\n\ts_mov_b32 m0, %0"
               : "=&s"(keep) : "v"(voff), "s"(sbase), "s"(lds) : "memory");
}

__device__ __forceinline__ void mfma_acc(f32x4& c, const u32x4& a, const u32x4& b) {
  asm volatile("v_mfma_f32_16x16x32_bf16 %0, %1, %2, %0" : "+a"(c) : "v"(a), "v"(b));
}

#define STAMP(i)

enum { EPI_QKV = 0, EPI_WO = 1, EPI_W1 = 2, EPI_W2 = 3, EPI_W3 = 4, EPI_W4 = 5 };

template <int BN, int NST, int KD, int LDA, int LDB, int EPI>
__global__ __launch_bounds__(512, 2) void gemm_kernel(
    const u16* __restrict__ A, const u16* __restrict__ W,
    const float* __restrict__ cs,
    const f32x2* __restrict__ st_in,
    const u16* __restrict__ r0,
    const u16* __restrict__ r1,
    u16* __restrict__ out,
    f32x2* __restrict__ st_out,
    const u16* __restrict__ wphi,
    float* __restrict__ kvp,
    float* __restrict__ zp)
{
  constexpr int NFULL = BN / 64, HALF = (BN % 64) / 32;
  constexpr int A_BYTES = 128 * 64 * 2, B_BYTES = BN * 64 * 2, STAGE = A_BYTES + B_BYTES;
  constexpr int NT = KD / 64, NTW = 2 * NFULL + HALF, LPS = 4 + 2 * NFULL + HALF;
  constexpr int PB = BN * 2 + 16;
  constexpr int EPI_BYTES = (EPI == EPI_QKV) ? (4 * 64 * 144 + 2 * (64 * 32 + 64) * 4) : 128 * PB;
  constexpr int MAINB = NST * STAGE;
  constexpr int RS_OFF = (MAINB > EPI_BYTES ? MAINB : EPI_BYTES);
  constexpr int CS_OFF = RS_OFF + 128 * 16;
  constexpr int WP_OFF = CS_OFF + 1024;
  constexpr int SMEM = WP_OFF + (EPI == EPI_QKV ? 4096 : 0);
  constexpr bool HAS_CS = (EPI == EPI_QKV || EPI == EPI_W1 || EPI == EPI_W3);
  constexpr bool HAS_ST = (EPI == EPI_QKV || EPI == EPI_W1 || EPI == EPI_W3 || EPI == EPI_W2);
  constexpr bool HAS_RES = (EPI == EPI_WO || EPI == EPI_W2 || EPI == EPI_W4);
  constexpr int LAG = (BN == 64) ? 1 : 0;
  constexpr int CH = BN / 8, NIT = CH / 4;
  static_assert(SMEM <= 163840, "LDS");
  __shared__ __attribute__((aligned(16))) char smem[SMEM];

  STAMP(0);
  const int tid = threadIdx.x, lane = tid & 63;
  const int wid8 = __builtin_amdgcn_readfirstlane(tid >> 6), wid = wid8 & 3, wm = wid >> 1, wn = wid & 1;
  const int g = lane >> 4, l15 = lane & 15;
  const int xcd = blockIdx.x & 7, idx = blockIdx.x >> 3;
  const int tm = (xcd >> 1) * 4 + (idx & 3), tn = (xcd & 1) * 8 + (idx >> 2);
  const int row0 = tm * 128;
  f32x4* rs = reinterpret_cast<f32x4*>(smem + RS_OFF);

  f32x4 acc[4][NTW];
  u32x4 ra[HAS_RES ? NIT : 1], rb[(EPI == EPI_W4) ? NIT : 1];

  if (wid8 >= 4) {
    unsigned voA[4], voB[NFULL > 0 ? NFULL : 1][2], voH = 0;
#pragma unroll
    for (int i = 0; i < 4; ++i) {
      int r = (wid * 4 + i) * 8 + (lane >> 3);
      int ch = (lane & 7) ^ ((r >> 1) & 7);
      voA[i] = (unsigned)(row0 + r) * (LDA * 2) + ch * 16;
    }
#pragma unroll
    for (int b = 0; b < NFULL; ++b) {
      int colb = (EPI == EPI_QKV) ? (b * 1024 + tn * 64) : (tn * BN + b * 64);
#pragma unroll
      for (int j = 0; j < 2; ++j) {
        int r = (wid * 2 + j) * 8 + (lane >> 3);
        int f = ((r >> 1) & 1) | (((r >> 3) & 1) << 1);
        int chp = lane & 7;
        int gch = (((chp >> 1) ^ f) << 1) | (chp & 1);
        voB[b][j] = (unsigned)r * (LDB * 2) + colb * 2 + gch * 16;
      }
    }
    if constexpr (HALF) {
      int r = wid * 16 + (lane >> 2);
      int chp = lane & 3;
      int gch = (((chp >> 1) ^ ((r >> 3) & 1)) << 1) | (chp & 1);
      voH = (unsigned)r * (LDB * 2) + (tn * BN + NFULL * 64) * 2 + gch * 16;
    }
    const char* Ab = reinterpret_cast<const char*>(A);
    const char* Wb = reinterpret_cast<const char*>(W);
    const unsigned lbase = (unsigned)(uintptr_t)(__attribute__((address_space(3))) char*)smem;
    auto stage = [&](int kt, int buf) {
      const char* a = Ab + (size_t)kt * 128;
      const char* w = Wb + (size_t)kt * (64 * LDB * 2);
      const unsigned s = lbase + buf * STAGE;
#pragma unroll
      for (int i = 0; i < 4; ++i) glds16(voA[i], a, s + (wid * 4 + i) * 1024);
#pragma unroll
      for (int b = 0; b < NFULL; ++b)
#pragma unroll
        for (int j = 0; j < 2; ++j) glds16(voB[b][j], w, s + A_BYTES + b * 8192 + (wid * 2 + j) * 1024);
      if constexpr (HALF) glds16(voH, w, s + A_BYTES + NFULL * 8192 + wid * 1024);
    };
#pragma unroll
    for (int s = 0; s < NST; ++s) stage(s, s);
    constexpr int XL = 0;
    f32x4 stp[4];
    const int lt = tid - 256;
    f32x4 csx;
    u32x4 wpx;
    if constexpr (HAS_CS) {
      const int c4 = lt < BN / 4 ? lt : 0;
      csx = *reinterpret_cast<const f32x4*>(cs + (EPI == EPI_QKV ? (c4 >> 4) * 1024 + tn * 64 + (c4 & 15) * 4 : tn * BN + c4 * 4));
    }
    if constexpr (EPI == EPI_QKV) wpx = *reinterpret_cast<const u32x4*>(wphi + tn * 2048 + lt * 8);
    if constexpr (HAS_ST) {
      const f32x4* sp = reinterpret_cast<const f32x4*>(st_in + (size_t)(row0 + (lt >> 1)) * 16 + (lt & 1) * 8);
#pragma unroll
      for (int i = 0; i < 4; ++i) stp[i] = sp[i];
    }
    if constexpr (HAS_RES) {
#pragma unroll
      for (int it = 0; it < NIT; ++it) {
        const int c = tid + 512 * it, r = c / CH, ch = c % CH;
        ra[it] = *reinterpret_cast<const u32x4*>(r0 + (size_t)(row0 + r) * D_ + tn * BN + ch * 8);
        if constexpr (EPI == EPI_W4) rb[it] = *reinterpret_cast<const u32x4*>(r1 + (size_t)(row0 + r) * D_ + tn * BN + ch * 8);
      }
    }
    asm volatile("" ::: "memory");
    wait_vm<(NST - 1 - LAG) * LPS>();
    __builtin_amdgcn_s_barrier();
    int buf = 0;
    for (int kt = 0; kt < NT - NST; ++kt) {
      wait_vm<(NST - 2 - LAG) * LPS>();
      __builtin_amdgcn_s_barrier();
      stage(kt + NST, buf);
      buf = buf + 1 == NST ? 0 : buf + 1;
    }
    asm volatile("" ::: "memory");
#pragma unroll
    for (int r = 0; r < NST - 1; ++r) {
      constexpr int NSL = NST - LAG;
      if (r == 0) wait_vm<(NSL - 2) * LPS + XL>();
      else if (r == 1) wait_vm<(NSL > 3 ? NSL - 3 : 0) * LPS + XL>();
      else if (r == 2) wait_vm<(NSL > 4 ? NSL - 4 : 0) * LPS + XL>();
      else if (r == 3) wait_vm<(NSL > 5 ? NSL - 5 : 0) * LPS + XL>();
      else wait_vm<XL>();
      __builtin_amdgcn_s_barrier();
    }
    if constexpr (HAS_ST) {
#pragma unroll
      for (int i = 0; i < 4; ++i) asm volatile("" : "+v"(stp[i]));
      float sm = 0.f;
#pragma unroll
      for (int i = 0; i < 4; ++i) sm += stp[i][0] + stp[i][2];
      sm += __shfl_xor(sm, 1);
      float mean = sm * (1.0f / 1024.f);
      float m2 = 0.f;
#pragma unroll
      for (int i = 0; i < 4; ++i) {
        float d0 = stp[i][0] * (1.0f / 64.f) - mean, d1 = stp[i][2] * (1.0f / 64.f) - mean;
        m2 += stp[i][1] + stp[i][3] + 64.f * (d0 * d0 + d1 * d1);
      }
      m2 += __shfl_xor(m2, 1);
      float var = m2 * (1.0f / 1024.f);
      float r1_ = 1.0f / sqrtf(var + 1e-5f);
      float sc = r1_;
      if constexpr (EPI == EPI_W1) sc = r1_ / sqrtf(var * r1_ * r1_ + 1e-5f);
      if ((lt & 1) == 0) rs[lt >> 1] = (f32x4){mean, sc, r1_, 0.f};
    }
    if constexpr (HAS_CS) {
      if (lt < BN / 4) *reinterpret_cast<f32x4*>(smem + CS_OFF + lt * 16) = csx;
    }
    if constexpr (EPI == EPI_QKV) *reinterpret_cast<u32x4*>(smem + WP_OFF + lt * 16) = wpx;
  } else {
    if constexpr (HAS_RES) {
#pragma unroll
      for (int it = 0; it < NIT; ++it) {
        const int c = tid + 512 * it, r = c / CH, ch = c % CH;
        ra[it] = *reinterpret_cast<const u32x4*>(r0 + (size_t)(row0 + r) * D_ + tn * BN + ch * 8);
        if constexpr (EPI == EPI_W4) rb[it] = *reinterpret_cast<const u32x4*>(r1 + (size_t)(row0 + r) * D_ + tn * BN + ch * 8);
      }
    }
    const int q = l15 >> 2, p = lane & 3;
    const int fB = (q >> 1) | ((g & 1) << 1);
    unsigned aoff[2], boff[2], hoff = 0;
#pragma unroll
    for (int ks = 0; ks < 2; ++ks) aoff[ks] = (wm * 64 + l15) * 128 + (((4 * ks + g) ^ (l15 >> 1)) * 16);
#pragma unroll
    for (int u = 0; u < 2; ++u) boff[u] = A_BYTES + (8 * g + q) * 128 + (((2 * wn + u) ^ fB) * 32) + p * 8;
    if constexpr (HALF) hoff = A_BYTES + NFULL * 8192 + (8 * g + q) * 64 + ((wn ^ (g & 1)) * 32) + p * 8;

    auto ldf = [&](int i, const char* s, int ks, u32x4(&af)[4], u32x4(&bfr)[NTW]) {
      if (i == 0 || i > NTW) {
        const int mt = i == 0 ? 0 : i - NTW;
        af[mt] = *reinterpret_cast<const u32x4*>(s + aoff[ks] + mt * 2048);
      } else if (i - 1 < 2 * NFULL) {
        const int b = (i - 1) >> 1, u = (i - 1) & 1;
        s16x4 lo = __builtin_amdgcn_ds_read_tr16_b64_v4i16((lds_s16x4_p)(s + boff[u] + b * 8192 + ks * 4096));
        s16x4 hi = __builtin_amdgcn_ds_read_tr16_b64_v4i16((lds_s16x4_p)(s + boff[u] + b * 8192 + ks * 4096 + 512));
        bfr[i - 1] = __builtin_shufflevector(__builtin_bit_cast(u32x2, lo), __builtin_bit_cast(u32x2, hi), 0, 1, 2, 3);
      } else {
        s16x4 lo = __builtin_amdgcn_ds_read_tr16_b64_v4i16((lds_s16x4_p)(s + hoff + ks * 2048));
        s16x4 hi = __builtin_amdgcn_ds_read_tr16_b64_v4i16((lds_s16x4_p)(s + hoff + ks * 2048 + 256));
        bfr[NTW - 1] = __builtin_shufflevector(__builtin_bit_cast(u32x2, lo), __builtin_bit_cast(u32x2, hi), 0, 1, 2, 3);
      }
    };
    constexpr int NM = 4 * NTW, NL = NTW + 4;
    auto phase = [&](u32x4(&afc)[4], u32x4(&bfc)[NTW], u32x4(&afn)[4], u32x4(&bfn)[NTW], int nbuf, int nks, auto rd, auto bar) {
      const char* s = smem + nbuf * STAGE;
      constexpr int BAR = decltype(bar)::value, R0 = BAR < 0 ? 0 : BAR + 1;
      constexpr int RSP = (NM - R0 - 2) / NL > 0 ? (NM - R0 - 2) / NL : 1;
      static_assert(R0 + (NL - 1) * RSP < NM, "reads");
#pragma unroll
      for (int mt = 0; mt < 4; ++mt)
#pragma unroll
        for (int j = 0; j < NTW; ++j) {
          const int m = mt * NTW + j;
          if (m == BAR) {
            WAIT_L0();
            __builtin_amdgcn_s_barrier();
            asm volatile("" ::: "memory");
          }
          if (decltype(rd)::value && m >= R0 && (m - R0) % RSP == 0 && (m - R0) / RSP < NL) ldf((m - R0) / RSP, s, nks, afn, bfn);
          if (EPI == EPI_QKV && j >= 4) mfma_acc(acc[mt][j], afc[mt], bfc[j]);
          else mfma_acc(acc[mt][j], bfc[j], afc[mt]);
          __builtin_amdgcn_sched_barrier(0);
        }
    };
    typedef std::integral_constant<int, -1> nobar;
#pragma unroll
    for (int mt = 0; mt < 4; ++mt)
#pragma unroll
      for (int j = 0; j < NTW; ++j) {
        acc[mt][j] = (f32x4){0.f, 0.f, 0.f, 0.f};
        asm volatile("" : "+a"(acc[mt][j]));
      }
    u32x4 af0[4], af1[4], bf0[NTW], bf1[NTW];
    __builtin_amdgcn_s_barrier();
    asm volatile("" ::: "memory");
    STAMP(1);
#pragma unroll
    for (int i = 0; i < NL; ++i) { ldf(i, smem, 0, af0, bf0); __builtin_amdgcn_sched_barrier(0); }
    int buf = 0;
    if constexpr (LAG) {
      phase(af0, bf0, af1, bf1, 0, 1, std::true_type{}, nobar{});
      for (int kt = 0; kt < NT - 1; ++kt) {
        buf = buf + 1 == NST ? 0 : buf + 1;
        phase(af1, bf1, af0, bf0, buf, 0, std::true_type{}, nobar{});
        asm volatile("" ::: "memory");
        __builtin_amdgcn_s_barrier();
        asm volatile("" ::: "memory");
        phase(af0, bf0, af1, bf1, buf, 1, std::true_type{}, nobar{});
      }
      phase(af1, bf1, af0, bf0, buf, 0, std::false_type{}, nobar{});
    } else {
      for (int kt = 0; kt < NT - 1; ++kt) {
        phase(af0, bf0, af1, bf1, buf, 1, std::true_type{}, nobar{});
        buf = buf + 1 == NST ? 0 : buf + 1;
        phase(af1, bf1, af0, bf0, buf, 0, std::true_type{}, std::integral_constant<int, 4>{});
      }
      phase(af0, bf0, af1, bf1, buf, 1, std::true_type{}, nobar{});
      phase(af1, bf1, af0, bf0, buf, 0, std::false_type{}, nobar{});
    }
    asm volatile("s_nop 7\n\ts_nop 7\n\ts_nop 7" ::: "memory");
  }
  WAIT_L0();
  __builtin_amdgcn_s_barrier();
  asm volatile("" ::: "memory");
  STAMP(2);

  if constexpr (EPI == EPI_QKV) {
    const int h = tn * 2 + wn;
    const int q = l15 >> 2, p = lane & 3;
    f32x4 kv[4][2], zz[4];
    float* red = reinterpret_cast<float*>(smem + 4 * 64 * 144) + wn * (64 * 32 + 64);
    if (wid8 < 4) {
      bf16x8 wf[2];
#pragma unroll
      for (int ft = 0; ft < 2; ++ft) {
        const char* src = smem + WP_OFF + wn * 2048 + (4 * g + q) * 64 + ft * 32 + p * 8;
        s16x4 lo = __builtin_amdgcn_ds_read_tr16_b64_v4i16((lds_s16x4_p)(src));
        s16x4 hi = __builtin_amdgcn_ds_read_tr16_b64_v4i16((lds_s16x4_p)(src + 16 * 64));
        wf[ft] = __builtin_shufflevector(lo, hi, 0, 1, 2, 3, 4, 5, 6, 7);
      }
      const float* csl = reinterpret_cast<const float*>(smem + CS_OFF);
      f32x4 cs4[4];
      float csv[2];
#pragma unroll
      for (int t = 0; t < 2; ++t) {
        cs4[t] = *reinterpret_cast<const f32x4*>(csl + wn * 32 + t * 16 + 4 * g);
        cs4[2 + t] = *reinterpret_cast<const f32x4*>(csl + 64 + wn * 32 + t * 16 + 4 * g);
        csv[t] = csl[128 + wn * 32 + t * 16 + l15];
      }
      bf16x8 qf[4], kf[4], vf[2][2];
#pragma unroll
      for (int mt = 0; mt < 4; ++mt) {
        f32x4 st = rs[wm * 64 + mt * 16 + l15];
        unsigned pq_[4], pk_[4];
#pragma unroll
        for (int t = 0; t < 2; ++t) {
          float a[4], b[4];
#pragma unroll
          for (int i = 0; i < 4; ++i) {
            a[i] = st[1] * (acc[mt][t][i] - st[0] * cs4[t][i]);
            b[i] = st[1] * (acc[mt][2 + t][i] - st[0] * cs4[2 + t][i]);
          }
          pq_[t * 2] = pk2(a[0], a[1]); pq_[t * 2 + 1] = pk2(a[2], a[3]);
          pk_[t * 2] = pk2(b[0], b[1]); pk_[t * 2 + 1] = pk2(b[2], b[3]);
        }
        qf[mt] = __builtin_bit_cast(bf16x8, (u32x4){pq_[0], pq_[1], pq_[2], pq_[3]});
        kf[mt] = __builtin_bit_cast(bf16x8, (u32x4){pk_[0], pk_[1], pk_[2], pk_[3]});
      }
#pragma unroll
      for (int s = 0; s < 2; ++s) {
        unsigned pv[2][4];
#pragma unroll
        for (int m2 = 0; m2 < 2; ++m2) {
          int mt = s * 2 + m2;
          f32x4 s0 = rs[wm * 64 + mt * 16 + 4 * g + 0], s1 = rs[wm * 64 + mt * 16 + 4 * g + 1];
          f32x4 s2 = rs[wm * 64 + mt * 16 + 4 * g + 2], s3 = rs[wm * 64 + mt * 16 + 4 * g + 3];
#pragma unroll
          for (int t = 0; t < 2; ++t) {
            float a0 = s0[1] * (acc[mt][4 + t][0] - s0[0] * csv[t]);
            float a1 = s1[1] * (acc[mt][4 + t][1] - s1[0] * csv[t]);
            float a2 = s2[1] * (acc[mt][4 + t][2] - s2[0] * csv[t]);
            float a3 = s3[1] * (acc[mt][4 + t][3] - s3[0] * csv[t]);
            pv[t][m2 * 2] = pk2(a0, a1); pv[t][m2 * 2 + 1] = pk2(a2, a3);
          }
        }
#pragma unroll
        for (int t = 0; t < 2; ++t) vf[s][t] = __builtin_bit_cast(bf16x8, (u32x4){pv[t][0], pv[t][1], pv[t][2], pv[t][3]});
      }
      char* pkl = smem + wid * (64 * 144);
      const float L2E = 1.4426950408889634f;
#pragma unroll
      for (int which = 0; which < 2; ++which)
#pragma unroll
        for (int mt = 0; mt < 4; ++mt) {
          f32x4 z4 = (f32x4){0.f, 0.f, 0.f, 0.f};
          f32x4 p0 = __builtin_amdgcn_mfma_f32_16x16x32_bf16(wf[0], which ? kf[mt] : qf[mt], z4, 0, 0, 0);
          f32x4 p1 = __builtin_amdgcn_mfma_f32_16x16x32_bf16(wf[1], which ? kf[mt] : qf[mt], z4, 0, 0, 0);
          f32x2 v[4];
          {
            const unsigned r0_ = pk2(p0[0], p0[1]), r1_ = pk2(p0[2], p0[3]), r2_ = pk2(p1[0], p1[1]), r3_ = pk2(p1[2], p1[3]);
            v[0] = (f32x2){bflo(r0_), bfhi(r0_)}; v[1] = (f32x2){bflo(r1_), bfhi(r1_)};
            v[2] = (f32x2){bflo(r2_), bfhi(r2_)}; v[3] = (f32x2){bflo(r3_), bfhi(r3_)};
          }
          float mx = max2_(max3_(max3_(v[0][0], v[0][1], v[1][0]), v[1][1], v[2][0]), max3_(v[2][1], v[3][0], v[3][1]));
          float mn = min2_(min3_(min3_(v[0][0], v[0][1], v[1][0]), v[1][1], v[2][0]), min3_(v[2][1], v[3][0], v[3][1]));
          mx = rmax4(mx); mn = rmin4(mn);
          const float mxl = -mx * L2E, mnl = mn * L2E;
          f32x2 ep[4], en[4];
#pragma unroll
          for (int i = 0; i < 4; ++i) {
            const f32x2 ap = v[i] * L2E + mxl, an = v[i] * (-L2E) + mnl;
            ep[i] = (f32x2){__builtin_amdgcn_exp2f(ap[0]), __builtin_amdgcn_exp2f(ap[1])};
            en[i] = (f32x2){__builtin_amdgcn_exp2f(an[0]), __builtin_amdgcn_exp2f(an[1])};
          }
          const f32x2 sp2 = (ep[0] + ep[1]) + (ep[2] + ep[3]), sn2 = (en[0] + en[1]) + (en[2] + en[3]);
          const float ip = __builtin_amdgcn_rcpf(rsum4(sp2[0] + sp2[1])), in_ = __builtin_amdgcn_rcpf(rsum4(sn2[0] + sn2[1]));
          u32x2 o[4];
          o[0] = (u32x2){pk2v(ep[0] * ip), pk2v(ep[1] * ip)};
          o[1] = (u32x2){pk2v(ep[2] * ip), pk2v(ep[3] * ip)};
          o[2] = (u32x2){pk2v(en[0] * in_), pk2v(en[1] * in_)};
          o[3] = (u32x2){pk2v(en[2] * in_), pk2v(en[3] * in_)};
          if (which == 0) {
            u16* dst = out + ((size_t)(row0 + wm * 64 + mt * 16 + l15) * 32 + h) * 64 + 4 * g;
#pragma unroll
            for (int c = 0; c < 4; ++c) *reinterpret_cast<u32x2*>(dst + c * 16) = o[c];
          } else {
            char* dst = pkl + (mt * 16 + l15) * 144 + g * 8;
#pragma unroll
            for (int c = 0; c < 4; ++c) *reinterpret_cast<u32x2*>(dst + c * 32) = o[c];
          }
        }
      WAIT_L0();
      __builtin_amdgcn_sched_barrier(0);
#pragma unroll
      for (int ft = 0; ft < 4; ++ft) { kv[ft][0] = kv[ft][1] = zz[ft] = (f32x4){0.f, 0.f, 0.f, 0.f}; }
      const bf16x8 ones = (bf16x8){0x3F80, 0x3F80, 0x3F80, 0x3F80, 0x3F80, 0x3F80, 0x3F80, 0x3F80};
#pragma unroll
      for (int s = 0; s < 2; ++s)
#pragma unroll
        for (int ft = 0; ft < 4; ++ft) {
          const char* src = pkl + (32 * s + 4 * g + q) * 144 + ft * 32 + p * 8;
          s16x4 lo = __builtin_amdgcn_ds_read_tr16_b64_v4i16((lds_s16x4_p)(src));
          s16x4 hi = __builtin_amdgcn_ds_read_tr16_b64_v4i16((lds_s16x4_p)(src + 16 * 144));
          bf16x8 pf = __builtin_shufflevector(lo, hi, 0, 1, 2, 3, 4, 5, 6, 7);
          kv[ft][0] = __builtin_amdgcn_mfma_f32_16x16x32_bf16(vf[s][0], pf, kv[ft][0], 0, 0, 0);
          kv[ft][1] = __builtin_amdgcn_mfma_f32_16x16x32_bf16(vf[s][1], pf, kv[ft][1], 0, 0, 0);
          zz[ft] = __builtin_amdgcn_mfma_f32_16x16x32_bf16(ones, pf, zz[ft], 0, 0, 0);
        }
      if (wm == 1) {
#pragma unroll
        for (int ft = 0; ft < 4; ++ft) {
#pragma unroll
          for (int dt = 0; dt < 2; ++dt) *reinterpret_cast<f32x4*>(red + (ft * 16 + l15) * 32 + dt * 16 + 4 * g) = kv[ft][dt];
          if (g == 0) red[64 * 32 + ft * 16 + l15] = zz[ft][0];
        }
      }
    }
    __syncthreads();
    if (wid8 < 2) {
      float* kd = kvp + (size_t)(h * 16 + tm) * 2048;
      float* zd = zp + (size_t)(h * 16 + tm) * 64;
#pragma unroll
      for (int ft = 0; ft < 4; ++ft) {
#pragma unroll
        for (int dt = 0; dt < 2; ++dt) {
          f32x4 o = *reinterpret_cast<const f32x4*>(red + (ft * 16 + l15) * 32 + dt * 16 + 4 * g);
          *reinterpret_cast<f32x4*>(kd + (ft * 16 + l15) * 32 + dt * 16 + 4 * g) = o + kv[ft][dt];
        }
        if (g == 0) zd[ft * 16 + l15] = red[64 * 32 + ft * 16 + l15] + zz[ft][0];
      }
    }
  } else {
    if (wid8 < 4) {
      f32x4 stm[4], cs4[NTW];
      if constexpr (EPI == EPI_W1 || EPI == EPI_W3) {
#pragma unroll
        for (int j = 0; j < NTW; ++j) {
          const int colj = (j < 2 * NFULL) ? ((j >> 1) * 64 + (2 * wn + (j & 1)) * 16) : (NFULL * 64 + wn * 16);
          cs4[j] = *reinterpret_cast<const f32x4*>(smem + CS_OFF + (colj + 4 * g) * 4);
        }
#pragma unroll
        for (int mt = 0; mt < 4; ++mt) stm[mt] = rs[wm * 64 + mt * 16 + l15];
      }
#pragma unroll
      for (int j = 0; j < NTW; ++j) {
        const int colj = (j < 2 * NFULL) ? ((j >> 1) * 64 + (2 * wn + (j & 1)) * 16) : (NFULL * 64 + wn * 16);
#pragma unroll
        for (int mt = 0; mt < 4; ++mt) {
          f32x2 v0 = (f32x2){acc[mt][j][0], acc[mt][j][1]}, v1 = (f32x2){acc[mt][j][2], acc[mt][j][3]};
          if constexpr (EPI == EPI_W1 || EPI == EPI_W3) {
            v0 = gelu2((v0 - stm[mt][0] * (f32x2){cs4[j][0], cs4[j][1]}) * stm[mt][1]);
            v1 = gelu2((v1 - stm[mt][0] * (f32x2){cs4[j][2], cs4[j][3]}) * stm[mt][1]);
          }
          *reinterpret_cast<u32x2*>(smem + (wm * 64 + mt * 16 + l15) * PB + (colj + 4 * g) * 2) = (u32x2){pk2(v0[0], v0[1]), pk2(v1[0], v1[1])};
        }
      }
    }
    __syncthreads();
#pragma unroll
    for (int it = 0; it < NIT; ++it) {
      const int c = tid + 512 * it, r = c / CH, ch = c % CH;
      u32x4 v = *reinterpret_cast<const u32x4*>(smem + r * PB + ch * 16);
      const int grow = row0 + r, gcol = tn * BN + ch * 8;
      if constexpr (EPI == EPI_W3) {
        *reinterpret_cast<u32x4*>(out + (size_t)grow * F_ + gcol) = v;
      } else if constexpr (EPI == EPI_W1) {
        const int grp = tn >> 2;
        const int sh = grp == 0 ? 1 : grp == 1 ? -1 : grp == 2 ? 2 : -2;
        const int dr = grow + sh;
        if (dr >= 0 && dr < S_) *reinterpret_cast<u32x4*>(out + (size_t)dr * D_ + gcol) = v;
        const int zr = grow - sh;
        if (zr < 0 || zr >= S_) *reinterpret_cast<u32x4*>(out + (size_t)grow * D_ + gcol) = (u32x4){0u, 0u, 0u, 0u};
      } else {
        float y[8];
        const u32x4 a = ra[it];
        if constexpr (EPI == EPI_WO) {
#pragma unroll
          for (int i = 0; i < 4; ++i) { y[2 * i] = rbf(bflo(a[i]) + bflo(v[i])); y[2 * i + 1] = rbf(bfhi(a[i]) + bfhi(v[i])); }
        } else if constexpr (EPI == EPI_W2) {
          f32x4 st = rs[r];
#pragma unroll
          for (int i = 0; i < 4; ++i) {
            y[2 * i] = rbf(rbf((bflo(a[i]) - st[0]) * st[2]) + bflo(v[i]));
            y[2 * i + 1] = rbf(rbf((bfhi(a[i]) - st[0]) * st[2]) + bfhi(v[i]));
          }
        } else {
          const u32x4 b = rb[it];
#pragma unroll
          for (int i = 0; i < 4; ++i) {
            y[2 * i] = rbf(bflo(a[i]) + rbf(bflo(b[i]) + bflo(v[i])));
            y[2 * i + 1] = rbf(bfhi(a[i]) + rbf(bfhi(b[i]) + bfhi(v[i])));
          }
        }
        *reinterpret_cast<u32x4*>(out + (size_t)grow * D_ + gcol) = (u32x4){pk2(y[0], y[1]), pk2(y[2], y[3]), pk2(y[4], y[5]), pk2(y[6], y[7])};
        float sm = ((y[0] + y[1]) + (y[2] + y[3])) + ((y[4] + y[5]) + (y[6] + y[7]));
        sm = rsum8(sm);
        float mu = sm * (1.0f / 64.f), m2 = 0.f;
#pragma unroll
        for (int i = 0; i < 8; ++i) { float d = y[i] - mu; m2 += d * d; }
        m2 = rsum8(m2);
        if (ch == 0) st_out[(size_t)grow * 16 + tn] = (f32x2){sm, m2};
      }
    }
  }
  STAMP(3);
}

__global__ __launch_bounds__(256, 1) void attn_out_kernel(const u16* __restrict__ phiq, const float* __restrict__ kvp,
                                                          const float* __restrict__ zp, u16* __restrict__ o) {
  __shared__ __attribute__((aligned(16))) u16 kvT[48 * 72];
  const int tid = threadIdx.x, lane = tid & 63, wid = tid >> 6, g = lane >> 4, l15 = lane & 15;
  const int h = blockIdx.x & 31, tg = blockIdx.x >> 5;
  f32x4 kvv[16][2];
  float zv[16];
  bf16x8 pq[4][2];
  {
    const float* src = kvp + (size_t)h * 16 * 2048 + tid * 8;
#pragma unroll
    for (int t = 0; t < 16; ++t) {
      kvv[t][0] = *reinterpret_cast<const f32x4*>(src + t * 2048);
      kvv[t][1] = *reinterpret_cast<const f32x4*>(src + t * 2048 + 4);
    }
#pragma unroll
    for (int t = 0; t < 16; ++t) zv[t] = zp[(size_t)(h * 16 + t) * 64 + lane];
#pragma unroll
    for (int mt = 0; mt < 4; ++mt) {
      const u16* ps = phiq + ((size_t)(tg * 256 + wid * 64 + mt * 16 + l15) * 32 + h) * 64 + g * 8;
      pq[mt][0] = *reinterpret_cast<const bf16x8*>(ps);
      pq[mt][1] = *reinterpret_cast<const bf16x8*>(ps + 32);
    }
  }
  __builtin_amdgcn_sched_barrier(0);
  {
    for (int i = tid; i < 15 * 64; i += 256) kvT[(33 + i / 64) * 72 + (i & 63)] = 0;
    f32x4 a0 = kvv[0][0], a1 = kvv[0][1];
#pragma unroll
    for (int t = 1; t < 16; ++t) { a0 += kvv[t][0]; a1 += kvv[t][1]; }
    const int f = tid >> 2, d0 = (tid & 3) * 8;
#pragma unroll
    for (int i = 0; i < 4; ++i) {
      kvT[(d0 + i) * 72 + f] = (u16)(pk2(a0[i], 0.f) & 0xffff);
      kvT[(d0 + 4 + i) * 72 + f] = (u16)(pk2(a1[i], 0.f) & 0xffff);
    }
    if (tid < 64) {
      float z = 0.f;
#pragma unroll
      for (int t = 0; t < 16; ++t) z += zv[t];
      kvT[32 * 72 + tid] = (u16)(pk2(z, 0.f) & 0xffff);
    }
  }
  __syncthreads();
  bf16x8 kf[3][2];
#pragma unroll
  for (int dt = 0; dt < 3; ++dt)
#pragma unroll
    for (int ks = 0; ks < 2; ++ks) kf[dt][ks] = *reinterpret_cast<const bf16x8*>(&kvT[(dt * 16 + l15) * 72 + ks * 32 + g * 8]);
#pragma unroll
  for (int mt = 0; mt < 4; ++mt) {
    const int tok = tg * 256 + wid * 64 + mt * 16 + l15;
    const bf16x8 p0 = pq[mt][0], p1 = pq[mt][1];
    f32x4 r[3];
#pragma unroll
    for (int dt = 0; dt < 3; ++dt) {
      r[dt] = (f32x4){0.f, 0.f, 0.f, 0.f};
      r[dt] = __builtin_amdgcn_mfma_f32_16x16x32_bf16(kf[dt][0], p0, r[dt], 0, 0, 0);
      r[dt] = __builtin_amdgcn_mfma_f32_16x16x32_bf16(kf[dt][1], p1, r[dt], 0, 0, 0);
    }
    float den = rbf(rbf(__shfl(r[2][0], l15)) + 1e-6f);
    float inv = 1.0f / den;
#pragma unroll
    for (int dt = 0; dt < 2; ++dt) {
      u32x2 w = (u32x2){pk2(rbf(r[dt][0]) * inv, rbf(r[dt][1]) * inv), pk2(rbf(r[dt][2]) * inv, rbf(r[dt][3]) * inv)};
      *reinterpret_cast<u32x2*>(o + (size_t)tok * D_ + h * 32 + dt * 16 + 4 * g) = w;
    }
  }
}

__global__ __launch_bounds__(256) void prep_kernel(const u16* __restrict__ x, const u16* __restrict__ wqkv,
                                                   const u16* __restrict__ w1, const u16* __restrict__ w3,
                                                   float* __restrict__ cs_qkv, float* __restrict__ cs_w1,
                                                   float* __restrict__ cs_w3, f32x2* __restrict__ st,
                                                   const u16* __restrict__ wo, const u16* __restrict__ w2,
                                                   const u16* __restrict__ w4) {
  __shared__ float red[32][65];
  const int tid = threadIdx.x, b = blockIdx.x;
  if (b >= 248) {
    const int pb = b - 248;
    const u16* w = pb < 32 ? wo + (size_t)pb * 32768 : pb < 64 ? w2 + (size_t)(pb - 32) * 32768 : w4 + (size_t)(pb - 64) * 32768;
    u32x4 v[16];
#pragma unroll
    for (int r = 0; r < 16; ++r) v[r] = *reinterpret_cast<const u32x4*>(w + r * 2048 + tid * 8);
#pragma unroll
    for (int r = 0; r < 16; ++r) asm volatile("" ::"v"(v[r]));
  } else if (b < 120) {
    const u16* w; float* dst; int ld, c0;
    if (b < 48) { w = wqkv; dst = cs_qkv; ld = 3072; c0 = b * 64; }
    else if (b < 64) { w = w1; dst = cs_w1; ld = 1024; c0 = (b - 48) * 64; }
    else { w = w3; dst = cs_w3; ld = F_; c0 = (b - 64) * 64; }
    const int cg = tid & 7, rg = tid >> 3;
    float s[8] = {0.f, 0.f, 0.f, 0.f, 0.f, 0.f, 0.f, 0.f};
#pragma unroll 8
    for (int r = 0; r < 32; ++r) {
      u32x4 v = *reinterpret_cast<const u32x4*>(w + (size_t)(r * 32 + rg) * ld + c0 + cg * 8);
#pragma unroll
      for (int i = 0; i < 4; ++i) { s[2 * i] += bflo(v[i]); s[2 * i + 1] += bfhi(v[i]); }
    }
#pragma unroll
    for (int i = 0; i < 8; ++i) red[rg][cg * 8 + i] = s[i];
    __syncthreads();
    if (tid < 64) {
      float t = 0.f;
#pragma unroll
      for (int r = 0; r < 32; ++r) t += red[r][tid];
      dst[c0 + tid] = t;
    }
  } else {
    const int lane = tid & 63, wid = tid >> 6;
    const int rbase = (b - 120) * 16 + wid * 4;
#pragma unroll
    for (int rr = 0; rr < 4; ++rr) {
      const int row = rbase + rr;
#pragma unroll
      for (int hf = 0; hf < 2; ++hf) {
        u32x4 v = *reinterpret_cast<const u32x4*>(x + (size_t)row * D_ + hf * 512 + lane * 8);
        float y[8];
#pragma unroll
        for (int i = 0; i < 4; ++i) { y[2 * i] = bflo(v[i]); y[2 * i + 1] = bfhi(v[i]); }
        float sm = ((y[0] + y[1]) + (y[2] + y[3])) + ((y[4] + y[5]) + (y[6] + y[7]));
        sm = rsum8(sm);
        float mu = sm * (1.0f / 64.f), m2 = 0.f;
#pragma unroll
        for (int i = 0; i < 8; ++i) { float d = y[i] - mu; m2 += d * d; }
        m2 = rsum8(m2);
        if ((lane & 7) == 0) st[(size_t)row * 16 + hf * 8 + (lane >> 3)] = (f32x2){sm, m2};
      }
    }
  }
}

extern "C" void kernel_launch(void* const* d_in, const int* in_sizes, int n_in, void* d_out, int out_size, void* d_ws,
                              size_t ws_size, hipStream_t stream) {
  const u16* x_in = (const u16*)d_in[0];
  const u16* wqkv = (const u16*)d_in[1];
  const u16* wphi = (const u16*)d_in[2];
  const u16* wo = (const u16*)d_in[3];
  const u16* w1 = (const u16*)d_in[5];
  const u16* w2 = (const u16*)d_in[6];
  const u16* w3 = (const u16*)d_in[7];
  const u16* w4 = (const u16*)d_in[8];
  char* ws = (char*)d_ws;
  size_t off = 0;
  auto alloc = [&](size_t bytes) { char* p = ws + off; off += (bytes + 4095) & ~(size_t)4095; return p; };
  u16* xb = (u16*)alloc((size_t)S_ * D_ * 2);
  u16* x2 = (u16*)alloc((size_t)S_ * D_ * 2);
  u16* x1 = (u16*)alloc((size_t)S_ * D_ * 2);
  u16* hs = (u16*)alloc((size_t)S_ * D_ * 2);
  u16* ob = (u16*)alloc((size_t)S_ * D_ * 2);
  u16* gb = (u16*)alloc((size_t)S_ * F_ * 2);
  u16* phiq = (u16*)alloc((size_t)S_ * 2048 * 2);
  float* kvp = (float*)alloc((size_t)32 * 16 * 2048 * 4);
  float* zp = (float*)alloc((size_t)32 * 16 * 64 * 4);
  f32x2* st_x = (f32x2*)alloc((size_t)S_ * 16 * 8);
  f32x2* st_x2 = (f32x2*)alloc((size_t)S_ * 16 * 8);
  f32x2* st_x1 = (f32x2*)alloc((size_t)S_ * 16 * 8);
  float* cs_qkv = (float*)alloc(3072 * 4);
  float* cs_w1 = (float*)alloc(1024 * 4);
  float* cs_w3 = (float*)alloc(F_ * 4);

  prep_kernel<<<248 + 176, 256, 0, stream>>>(x_in, wqkv, w1, w3, cs_qkv, cs_w1, cs_w3, st_x, wo, w2, w4);
  for (int layer = 0; layer < 2; ++layer) {
    const u16* x = layer == 0 ? x_in : xb;
    u16* xo = layer == 0 ? xb : (u16*)d_out;
    gemm_kernel<192, 3, 1024, 1024, 3072, EPI_QKV><<<256, 512, 0, stream>>>(x, wqkv, cs_qkv, st_x, nullptr, nullptr, phiq, nullptr, wphi, kvp, zp);
    attn_out_kernel<<<256, 256, 0, stream>>>(phiq, kvp, zp, ob);
    gemm_kernel<64, 5, 1024, 1024, 1024, EPI_WO><<<256, 512, 0, stream>>>(ob, wo, nullptr, nullptr, x, nullptr, x2, st_x2, nullptr, nullptr, nullptr);
    gemm_kernel<64, 5, 1024, 1024, 1024, EPI_W1><<<256, 512, 0, stream>>>(x2, w1, cs_w1, st_x2, nullptr, nullptr, hs, nullptr, nullptr, nullptr, nullptr);
    gemm_kernel<64, 5, 1024, 1024, 1024, EPI_W2><<<256, 512, 0, stream>>>(hs, w2, nullptr, st_x2, x2, nullptr, x1, st_x1, nullptr, nullptr, nullptr);
    gemm_kernel<224, 3, 1024, 1024, F_, EPI_W3><<<256, 512, 0, stream>>>(x1, w3, cs_w3, st_x1, nullptr, nullptr, gb, nullptr, nullptr, nullptr, nullptr);
    gemm_kernel<64, 5, F_, F_, 1024, EPI_W4><<<256, 512, 0, stream>>>(gb, w4, nullptr, nullptr, x2, x1, xo, st_x, nullptr, nullptr, nullptr);
  }
}
```

```cpp
#include <hip/hip_runtime.h>
#include <hip/hip_bf16.h>
#include <stdint.h>
#include <type_traits>

typedef short bf16x8 __attribute__((ext_vector_type(8)));
typedef short s16x4 __attribute__((ext_vector_type(4)));
typedef float f32x4 __attribute__((ext_vector_type(4)));
typedef float f32x2 __attribute__((ext_vector_type(2)));
typedef unsigned u32x2 __attribute__((ext_vector_type(2)));
typedef unsigned u32x4 __attribute__((ext_vector_type(4)));
typedef __attribute__((address_space(3))) s16x4* lds_s16x4_p;
typedef unsigned short u16;

constexpr int S_ = 2048, D_ = 1024, H_ = 32, F_ = 3584;

__device__ __forceinline__ float bf2f(u16 v) { return __uint_as_float(((unsigned)v) << 16); }
__device__ __forceinline__ float bflo(unsigned v) { return __uint_as_float(v << 16); }
__device__ __forceinline__ float bfhi(unsigned v) { return __uint_as_float(v & 0xffff0000u); }
typedef __bf16 bf16x2_t __attribute__((ext_vector_type(2)));
__device__ __forceinline__ unsigned pk2(float a, float b) {
  return __builtin_bit_cast(unsigned, __builtin_convertvector((f32x2){a, b}, bf16x2_t));
}
__device__ __forceinline__ unsigned pk2v(f32x2 v) { return __builtin_bit_cast(unsigned, __builtin_convertvector(v, bf16x2_t)); }
__device__ __forceinline__ float max2_(float a, float b) { float r; asm("v_max_f32 %0, %1, %2" : "=v"(r) : "v"(a), "v"(b)); return r; }
__device__ __forceinline__ float min2_(float a, float b) { float r; asm("v_min_f32 %0, %1, %2" : "=v"(r) : "v"(a), "v"(b)); return r; }
__device__ __forceinline__ float max3_(float a, float b, float c) { float r; asm("v_max3_f32 %0, %1, %2, %3" : "=v"(r) : "v"(a), "v"(b), "v"(c)); return r; }
__device__ __forceinline__ float min3_(float a, float b, float c) { float r; asm("v_min3_f32 %0, %1, %2, %3" : "=v"(r) : "v"(a), "v"(b), "v"(c)); return r; }
__device__ __forceinline__ float rbf(float a) { return bflo(pk2(a, 0.f)); }
__device__ __forceinline__ float gelu_t(float x) {
  const float c1 = -2.0f * 0.7978845608028654f * 1.4426950408889634f;
  const float c2 = c1 * 0.044715f;
  float t = x * x;
  float w = x * __builtin_fmaf(t, c2, c1);
  float e = __builtin_amdgcn_exp2f(w);
  return x * __builtin_amdgcn_rcpf(1.0f + e);
}

__device__ __forceinline__ f32x2 gelu2(f32x2 x) {
  const float c1 = -2.0f * 0.7978845608028654f * 1.4426950408889634f;
  const float c2 = c1 * 0.044715f;
  f32x2 t = x * x;
  f32x2 w = x * (t * c2 + c1);
  f32x2 d = (f32x2){__builtin_amdgcn_exp2f(w[0]), __builtin_amdgcn_exp2f(w[1])} + 1.0f;
  return x * (f32x2){__builtin_amdgcn_rcpf(d[0]), __builtin_amdgcn_rcpf(d[1])};
}
__device__ __forceinline__ void x16(float v, float& a, float& b) {
  auto r = __builtin_amdgcn_permlane16_swap(__float_as_uint(v), __float_as_uint(v), false, false);
  a = __uint_as_float(r[0]); b = __uint_as_float(r[1]);
}
__device__ __forceinline__ void x32(float v, float& a, float& b) {
  auto r = __builtin_amdgcn_permlane32_swap(__float_as_uint(v), __float_as_uint(v), false, false);
  a = __uint_as_float(r[0]); b = __uint_as_float(r[1]);
}
__device__ __forceinline__ float rmax4(float v) { float a, b; x16(v, a, b); v = max2_(a, b); x32(v, a, b); return max2_(a, b); }
__device__ __forceinline__ float rmin4(float v) { float a, b; x16(v, a, b); v = min2_(a, b); x32(v, a, b); return min2_(a, b); }
__device__ __forceinline__ float rsum4(float v) { float a, b; x16(v, a, b); v = a + b; x32(v, a, b); return a + b; }

__device__ __forceinline__ float rsum8(float v) {
  v += __uint_as_float(__builtin_amdgcn_mov_dpp(__float_as_uint(v), 0xB1, 0xF, 0xF, true));
  v += __uint_as_float(__builtin_amdgcn_mov_dpp(__float_as_uint(v), 0x4E, 0xF, 0xF, true));
  v += __uint_as_float(__builtin_amdgcn_mov_dpp(__float_as_uint(v), 0x141, 0xF, 0xF, true));
  return v;
}
#define WAIT_V(n) asm volatile("s_waitcnt vmcnt(" #n ")" ::: "memory")
#define WAIT_L0() asm volatile("s_waitcnt lgkmcnt(0)" ::: "memory")
template <int N> __device__ __forceinline__ void wait_vm() {
  asm volatile("s_waitcnt vmcnt(%0)" ::"i"(N) : "memory");
}

__device__ __forceinline__ void glds16(unsigned voff, const void* sbase, unsigned lds) {
  unsigned keep;
  asm volatile("s_mov_b32 %0, m0\n\ts_mov_b32 m0, %3\n\ts_nop 0\n\tglobal_load_lds_dwordx4 %1, %2\n\ts_mov_b32 m0, %0"
               : "=&s"(keep) : "v"(voff), "s"(sbase), "s"(lds) : "memory");
}

__device__ __forceinline__ void mfma_acc(f32x4& c, const u32x4& a, const u32x4& b) {
  asm volatile("v_mfma_f32_16x16x32_bf16 %0, %1, %2, %0" : "+a"(c) : "v"(a), "v"(b));
}

#define STAMP(i)

enum { EPI_QKV = 0, EPI_WO = 1, EPI_W1 = 2, EPI_W2 = 3, EPI_W3 = 4, EPI_W4 = 5 };

template <int BN, int NST, int KD, int LDA, int LDB, int EPI>
__global__ __launch_bounds__(512, 2) void gemm_kernel(
    const u16* __restrict__ A, const u16* __restrict__ W,
    const u16* __restrict__ Wc,
    const float* __restrict__ cs,
    const f32x2* __restrict__ st_in,
    const u16* __restrict__ r0,
    const u16* __restrict__ r1,
    u16* __restrict__ out,
    f32x2* __restrict__ st_out,
    const u16* __restrict__ wphi,
    float* __restrict__ kvp,
    float* __restrict__ zp)
{
  constexpr int NFULL = BN / 64, HALF = (BN % 64) / 32;
  constexpr int A_BYTES = 128 * 64 * 2, B_BYTES = BN * 64 * 2, STAGE = A_BYTES + B_BYTES;
  constexpr int NT = KD / 64, NTW = 2 * NFULL + HALF, LPS = 4 + 2 * NFULL + HALF;
  constexpr int PB = BN * 2 + 16;
  constexpr int EPI_BYTES = (EPI == EPI_QKV) ? (4 * 64 * 144 + 2 * (64 * 32 + 64) * 4) : 128 * PB;
  constexpr int MAINB = NST * STAGE;
  constexpr int RS_OFF = (MAINB > EPI_BYTES ? MAINB : EPI_BYTES);
  constexpr int CS_OFF = RS_OFF + 128 * 16;
  constexpr int WP_OFF = CS_OFF + 1024;
  constexpr int SMEM = WP_OFF + (EPI == EPI_QKV ? 4096 : 0);
  constexpr bool HAS_CS = (EPI == EPI_QKV || EPI == EPI_W1 || EPI == EPI_W3);
  constexpr bool HAS_ST = (EPI == EPI_QKV || EPI == EPI_W1 || EPI == EPI_W3 || EPI == EPI_W2);
  constexpr bool HAS_RES = (EPI == EPI_WO || EPI == EPI_W2 || EPI == EPI_W4);
  constexpr int LAG = (BN == 64) ? 1 : 0;
  constexpr int CH = BN / 8, NIT = CH / 4;
  static_assert(SMEM <= 163840, "LDS");
  __shared__ __attribute__((aligned(16))) char smem[SMEM];

  STAMP(0);
  const int tid = threadIdx.x, lane = tid & 63;
  const int wid8 = __builtin_amdgcn_readfirstlane(tid >> 6), wid = wid8 & 3, wm = wid >> 1, wn = wid & 1;
  const int g = lane >> 4, l15 = lane & 15;
  const int xcd = blockIdx.x & 7, idx = blockIdx.x >> 3;
  const int tm = (xcd >> 1) * 4 + (idx & 3), tn = (xcd & 1) * 8 + (idx >> 2);
  const int row0 = tm * 128;
  f32x4* rs = reinterpret_cast<f32x4*>(smem + RS_OFF);

  f32x4 acc[4][NTW];
  u32x4 ra[HAS_RES ? NIT : 1], rb[(EPI == EPI_W4) ? NIT : 1];

  if (wid8 >= 4) {
    unsigned voA[4], voB[NFULL > 0 ? NFULL : 1][2], voH = 0;
    const bool pk_ = (HALF == 0) && ((tn & 7) == 3);
    const unsigned pitchB = pk_ ? NFULL * 128 : LDB * 2;
#pragma unroll
    for (int i = 0; i < 4; ++i) {
      int r = (wid * 4 + i) * 8 + (lane >> 3);
      int ch = (lane & 7) ^ ((r >> 1) & 7);
      voA[i] = (unsigned)(row0 + r) * (LDA * 2) + ch * 16;
    }
#pragma unroll
    for (int b = 0; b < NFULL; ++b) {
      int colb = (EPI == EPI_QKV) ? (b * 1024 + tn * 64) : (tn * BN + b * 64);
#pragma unroll
      for (int j = 0; j < 2; ++j) {
        int r = (wid * 2 + j) * 8 + (lane >> 3);
        int f = ((r >> 1) & 1) | (((r >> 3) & 1) << 1);
        int chp = lane & 7;
        int gch = (((chp >> 1) ^ f) << 1) | (chp & 1);
        voB[b][j] = (unsigned)r * pitchB + (pk_ ? b * 128 : colb * 2) + gch * 16;
      }
    }
    if constexpr (HALF) {
      int r = wid * 16 + (lane >> 2);
      int chp = lane & 3;
      int gch = (((chp >> 1) ^ ((r >> 3) & 1)) << 1) | (chp & 1);
      voH = (unsigned)r * (LDB * 2) + (tn * BN + NFULL * 64) * 2 + gch * 16;
    }
    const char* Ab = reinterpret_cast<const char*>(A);
    const char* Wb = pk_ ? reinterpret_cast<const char*>(Wc) + (size_t)(tn >> 3) * (KD * NFULL * 128) : reinterpret_cast<const char*>(W);
    const unsigned lbase = (unsigned)(uintptr_t)(__attribute__((address_space(3))) char*)smem;
    auto stage = [&](int kt, int buf) {
      const char* a = Ab + (size_t)kt * 128;
      const char* w = Wb + (size_t)(kt * (64 * pitchB));
      const unsigned s = lbase + buf * STAGE;
#pragma unroll
      for (int i = 0; i < 4; ++i) glds16(voA[i], a, s + (wid * 4 + i) * 1024);
#pragma unroll
      for (int b = 0; b < NFULL; ++b)
#pragma unroll
        for (int j = 0; j < 2; ++j) glds16(voB[b][j], w, s + A_BYTES + b * 8192 + (wid * 2 + j) * 1024);
      if constexpr (HALF) glds16(voH, w, s + A_BYTES + NFULL * 8192 + wid * 1024);
    };
#pragma unroll
    for (int s = 0; s < NST; ++s) stage(s, s);
    constexpr int XL = 0;
    f32x4 stp[4];
    const int lt = tid - 256;
    f32x4 csx;
    u32x4 wpx;
    if constexpr (HAS_CS) {
      const int c4 = lt < BN / 4 ? lt : 0;
      csx = *reinterpret_cast<const f32x4*>(cs + (EPI == EPI_QKV ? (c4 >> 4) * 1024 + tn * 64 + (c4 & 15) * 4 : tn * BN + c4 * 4));
    }
    if constexpr (EPI == EPI_QKV) wpx = *reinterpret_cast<const u32x4*>(wphi + tn * 2048 + lt * 8);
    if constexpr (HAS_ST) {
      const f32x4* sp = reinterpret_cast<const f32x4*>(st_in + (size_t)(row0 + (lt >> 1)) * 16 + (lt & 1) * 8);
#pragma unroll
      for (int i = 0; i < 4; ++i) stp[i] = sp[i];
    }
    if constexpr (HAS_RES) {
#pragma unroll
      for (int it = 0; it < NIT; ++it) {
        const int c = tid + 512 * it, r = c / CH, ch = c % CH;
        ra[it] = *reinterpret_cast<const u32x4*>(r0 + (size_t)(row0 + r) * D_ + tn * BN + ch * 8);
        if constexpr (EPI == EPI_W4) rb[it] = *reinterpret_cast<const u32x4*>(r1 + (size_t)(row0 + r) * D_ + tn * BN + ch * 8);
      }
    }
    asm volatile("" ::: "memory");
    wait_vm<(NST - 1 - LAG) * LPS>();
    __builtin_amdgcn_s_barrier();
    int buf = 0;
    for (int kt = 0; kt < NT - NST; ++kt) {
      wait_vm<(NST - 2 - LAG) * LPS>();
      __builtin_amdgcn_s_barrier();
      stage(kt + NST, buf);
      buf = buf + 1 == NST ? 0 : buf + 1;
    }
    asm volatile("" ::: "memory");
#pragma unroll
    for (int r = 0; r < NST - 1; ++r) {
      constexpr int NSL = NST - LAG;
      if (r == 0) wait_vm<(NSL - 2) * LPS + XL>();
      else if (r == 1) wait_vm<(NSL > 3 ? NSL - 3 : 0) * LPS + XL>();
      else if (r == 2) wait_vm<(NSL > 4 ? NSL - 4 : 0) * LPS + XL>();
      else if (r == 3) wait_vm<(NSL > 5 ? NSL - 5 : 0) * LPS + XL>();
      else wait_vm<XL>();
      __builtin_amdgcn_s_barrier();
    }
    if constexpr (HAS_ST) {
#pragma unroll
      for (int i = 0; i < 4; ++i) asm volatile("" : "+v"(stp[i]));
      float sm = 0.f;
#pragma unroll
      for (int i = 0; i < 4; ++i) sm += stp[i][0] + stp[i][2];
      sm += __shfl_xor(sm, 1);
      float mean = sm * (1.0f / 1024.f);
      float m2 = 0.f;
#pragma unroll
      for (int i = 0; i < 4; ++i) {
        float d0 = stp[i][0] * (1.0f / 64.f) - mean, d1 = stp[i][2] * (1.0f / 64.f) - mean;
        m2 += stp[i][1] + stp[i][3] + 64.f * (d0 * d0 + d1 * d1);
      }
      m2 += __shfl_xor(m2, 1);
      float var = m2 * (1.0f / 1024.f);
      float r1_ = 1.0f / sqrtf(var + 1e-5f);
      float sc = r1_;
      if constexpr (EPI == EPI_W1) sc = r1_ / sqrtf(var * r1_ * r1_ + 1e-5f);
      if ((lt & 1) == 0) rs[lt >> 1] = (f32x4){mean, sc, r1_, 0.f};
    }
    if constexpr (HAS_CS) {
      if (lt < BN / 4) *reinterpret_cast<f32x4*>(smem + CS_OFF + lt * 16) = csx;
    }
    if constexpr (EPI == EPI_QKV) *reinterpret_cast<u32x4*>(smem + WP_OFF + lt * 16) = wpx;
  } else {
    if constexpr (HAS_RES) {
#pragma unroll
      for (int it = 0; it < NIT; ++it) {
        const int c = tid + 512 * it, r = c / CH, ch = c % CH;
        ra[it] = *reinterpret_cast<const u32x4*>(r0 + (size_t)(row0 + r) * D_ + tn * BN + ch * 8);
        if constexpr (EPI == EPI_W4) rb[it] = *reinterpret_cast<const u32x4*>(r1 + (size_t)(row0 + r) * D_ + tn * BN + ch * 8);
      }
    }
    const int q = l15 >> 2, p = lane & 3;
    const int fB = (q >> 1) | ((g & 1) << 1);
    unsigned aoff[2], boff[2], hoff = 0;
#pragma unroll
    for (int ks = 0; ks < 2; ++ks) aoff[ks] = (wm * 64 + l15) * 128 + (((4 * ks + g) ^ (l15 >> 1)) * 16);
#pragma unroll
    for (int u = 0; u < 2; ++u) boff[u] = A_BYTES + (8 * g + q) * 128 + (((2 * wn + u) ^ fB) * 32) + p * 8;
    if constexpr (HALF) hoff = A_BYTES + NFULL * 8192 + (8 * g + q) * 64 + ((wn ^ (g & 1)) * 32) + p * 8;

    auto ldf = [&](int i, const char* s, int ks, u32x4(&af)[4], u32x4(&bfr)[NTW]) {
      if (i == 0 || i > NTW) {
        const int mt = i == 0 ? 0 : i - NTW;
        af[mt] = *reinterpret_cast<const u32x4*>(s + aoff[ks] + mt * 2048);
      } else if (i - 1 < 2 * NFULL) {
        const int b = (i - 1) >> 1, u = (i - 1) & 1;
        s16x4 lo = __builtin_amdgcn_ds_read_tr16_b64_v4i16((lds_s16x4_p)(s + boff[u] + b * 8192 + ks * 4096));
        s16x4 hi = __builtin_amdgcn_ds_read_tr16_b64_v4i16((lds_s16x4_p)(s + boff[u] + b * 8192 + ks * 4096 + 512));
        bfr[i - 1] = __builtin_shufflevector(__builtin_bit_cast(u32x2, lo), __builtin_bit_cast(u32x2, hi), 0, 1, 2, 3);
      } else {
        s16x4 lo = __builtin_amdgcn_ds_read_tr16_b64_v4i16((lds_s16x4_p)(s + hoff + ks * 2048));
        s16x4 hi = __builtin_amdgcn_ds_read_tr16_b64_v4i16((lds_s16x4_p)(s + hoff + ks * 2048 + 256));
        bfr[NTW - 1] = __builtin_shufflevector(__builtin_bit_cast(u32x2, lo), __builtin_bit_cast(u32x2, hi), 0, 1, 2, 3);
      }
    };
    constexpr int NM = 4 * NTW, NL = NTW + 4;
    auto phase = [&](u32x4(&afc)[4], u32x4(&bfc)[NTW], u32x4(&afn)[4], u32x4(&bfn)[NTW], int nbuf, int nks, auto rd, auto bar) {
      const char* s = smem + nbuf * STAGE;
      constexpr int BAR = decltype(bar)::value, R0 = BAR < 0 ? 0 : BAR + 1;
      constexpr int RSP = (NM - R0 - 2) / NL > 0 ? (NM - R0 - 2) / NL : 1;
      static_assert(R0 + (NL - 1) * RSP < NM, "reads");
#pragma unroll
      for (int mt = 0; mt < 4; ++mt)
#pragma unroll
        for (int j = 0; j < NTW; ++j) {
          const int m = mt * NTW + j;
          if (m == BAR) {
            WAIT_L0();
            __builtin_amdgcn_s_barrier();
            asm volatile("" ::: "memory");
          }
          if (decltype(rd)::value && m >= R0 && (m - R0) % RSP == 0 && (m - R0) / RSP < NL) ldf((m - R0) / RSP, s, nks, afn, bfn);
          if (EPI == EPI_QKV && j >= 4) mfma_acc(acc[mt][j], afc[mt], bfc[j]);
          else mfma_acc(acc[mt][j], bfc[j], afc[mt]);
          __builtin_amdgcn_sched_barrier(0);
        }
    };
    typedef std::integral_constant<int, -1> nobar;
#pragma unroll
    for (int mt = 0; mt < 4; ++mt)
#pragma unroll
      for (int j = 0; j < NTW; ++j) {
        acc[mt][j] = (f32x4){0.f, 0.f, 0.f, 0.f};
        asm volatile("" : "+a"(acc[mt][j]));
      }
    u32x4 af0[4], af1[4], bf0[NTW], bf1[NTW];
    __builtin_amdgcn_s_barrier();
    asm volatile("" ::: "memory");
    STAMP(1);
#pragma unroll
    for (int i = 0; i < NL; ++i) { ldf(i, smem, 0, af0, bf0); __builtin_amdgcn_sched_barrier(0); }
    int buf = 0;
    if constexpr (LAG) {
      phase(af0, bf0, af1, bf1, 0, 1, std::true_type{}, nobar{});
      for (int kt = 0; kt < NT - 1; ++kt) {
        buf = buf + 1 == NST ? 0 : buf + 1;
        phase(af1, bf1, af0, bf0, buf, 0, std::true_type{}, nobar{});
        asm volatile("" ::: "memory");
        __builtin_amdgcn_s_barrier();
        asm volatile("" ::: "memory");
        phase(af0, bf0, af1, bf1, buf, 1, std::true_type{}, nobar{});
      }
      phase(af1, bf1, af0, bf0, buf, 0, std::false_type{}, nobar{});
    } else {
      for (int kt = 0; kt < NT - 1; ++kt) {
        phase(af0, bf0, af1, bf1, buf, 1, std::true_type{}, nobar{});
        buf = buf + 1 == NST ? 0 : buf + 1;
        phase(af1, bf1, af0, bf0, buf, 0, std::true_type{}, std::integral_constant<int, 4>{});
      }
      phase(af0, bf0, af1, bf1, buf, 1, std::true_type{}, nobar{});
      phase(af1, bf1, af0, bf0, buf, 0, std::false_type{}, nobar{});
    }
    asm volatile("s_nop 7\n\ts_nop 7\n\ts_nop 7" ::: "memory");
  }
  WAIT_L0();
  __builtin_amdgcn_s_barrier();
  asm volatile("" ::: "memory");
  STAMP(2);

  if constexpr (EPI == EPI_QKV) {
    const int h = tn * 2 + wn;
    const int q = l15 >> 2, p = lane & 3;
    f32x4 kv[4][2], zz[4];
    float* red = reinterpret_cast<float*>(smem + 4 * 64 * 144) + wn * (64 * 32 + 64);
    if (wid8 < 4) {
      bf16x8 wf[2];
#pragma unroll
      for (int ft = 0; ft < 2; ++ft) {
        const char* src = smem + WP_OFF + wn * 2048 + (4 * g + q) * 64 + ft * 32 + p * 8;
        s16x4 lo = __builtin_amdgcn_ds_read_tr16_b64_v4i16((lds_s16x4_p)(src));
        s16x4 hi = __builtin_amdgcn_ds_read_tr16_b64_v4i16((lds_s16x4_p)(src + 16 * 64));
        wf[ft] = __builtin_shufflevector(lo, hi, 0, 1, 2, 3, 4, 5, 6, 7);
      }
      const float* csl = reinterpret_cast<const float*>(smem + CS_OFF);
      f32x4 cs4[4];
      float csv[2];
#pragma unroll
      for (int t = 0; t < 2; ++t) {
        cs4[t] = *reinterpret_cast<const f32x4*>(csl + wn * 32 + t * 16 + 4 * g);
        cs4[2 + t] = *reinterpret_cast<const f32x4*>(csl + 64 + wn * 32 + t * 16 + 4 * g);
        csv[t] = csl[128 + wn * 32 + t * 16 + l15];
      }
      bf16x8 qf[4], kf[4], vf[2][2];
#pragma unroll
      for (int mt = 0; mt < 4; ++mt) {
        f32x4 st = rs[wm * 64 + mt * 16 + l15];
        unsigned pq_[4], pk_[4];
#pragma unroll
        for (int t = 0; t < 2; ++t) {
          float a[4], b[4];
#pragma unroll
          for (int i = 0; i < 4; ++i) {
            a[i] = st[1] * (acc[mt][t][i] - st[0] * cs4[t][i]);
            b[i] = st[1] * (acc[mt][2 + t][i] - st[0] * cs4[2 + t][i]);
          }
          pq_[t * 2] = pk2(a[0], a[1]); pq_[t * 2 + 1] = pk2(a[2], a[3]);
          pk_[t * 2] = pk2(b[0], b[1]); pk_[t * 2 + 1] = pk2(b[2], b[3]);
        }
        qf[mt] = __builtin_bit_cast(bf16x8, (u32x4){pq_[0], pq_[1], pq_[2], pq_[3]});
        kf[mt] = __builtin_bit_cast(bf16x8, (u32x4){pk_[0], pk_[1], pk_[2], pk_[3]});
      }
#pragma unroll
      for (int s = 0; s < 2; ++s) {
        unsigned pv[2][4];
#pragma unroll
        for (int m2 = 0; m2 < 2; ++m2) {
          int mt = s * 2 + m2;
          f32x4 s0 = rs[wm * 64 + mt * 16 + 4 * g + 0], s1 = rs[wm * 64 + mt * 16 + 4 * g + 1];
          f32x4 s2 = rs[wm * 64 + mt * 16 + 4 * g + 2], s3 = rs[wm * 64 + mt * 16 + 4 * g + 3];
#pragma unroll
          for (int t = 0; t < 2; ++t) {
            float a0 = s0[1] * (acc[mt][4 + t][0] - s0[0] * csv[t]);
            float a1 = s1[1] * (acc[mt][4 + t][1] - s1[0] * csv[t]);
            float a2 = s2[1] * (acc[mt][4 + t][2] - s2[0] * csv[t]);
            float a3 = s3[1] * (acc[mt][4 + t][3] - s3[0] * csv[t]);
            pv[t][m2 * 2] = pk2(a0, a1); pv[t][m2 * 2 + 1] = pk2(a2, a3);
          }
        }
#pragma unroll
        for (int t = 0; t < 2; ++t) vf[s][t] = __builtin_bit_cast(bf16x8, (u32x4){pv[t][0], pv[t][1], pv[t][2], pv[t][3]});
      }
      char* pkl = smem + wid * (64 * 144);
      const float L2E = 1.4426950408889634f;
#pragma unroll
      for (int which = 0; which < 2; ++which)
#pragma unroll
        for (int mt = 0; mt < 4; ++mt) {
          f32x4 z4 = (f32x4){0.f, 0.f, 0.f, 0.f};
          f32x4 p0 = __builtin_amdgcn_mfma_f32_16x16x32_bf16(wf[0], which ? kf[mt] : qf[mt], z4, 0, 0, 0);
          f32x4 p1 = __builtin_amdgcn_mfma_f32_16x16x32_bf16(wf[1], which ? kf[mt] : qf[mt], z4, 0, 0, 0);
          f32x2 v[4];
          {
            const unsigned r0_ = pk2(p0[0], p0[1]), r1_ = pk2(p0[2], p0[3]), r2_ = pk2(p1[0], p1[1]), r3_ = pk2(p1[2], p1[3]);
            v[0] = (f32x2){bflo(r0_), bfhi(r0_)}; v[1] = (f32x2){bflo(r1_), bfhi(r1_)};
            v[2] = (f32x2){bflo(r2_), bfhi(r2_)}; v[3] = (f32x2){bflo(r3_), bfhi(r3_)};
          }
          float mx = max2_(max3_(max3_(v[0][0], v[0][1], v[1][0]), v[1][1], v[2][0]), max3_(v[2][1], v[3][0], v[3][1]));
          float mn = min2_(min3_(min3_(v[0][0], v[0][1], v[1][0]), v[1][1], v[2][0]), min3_(v[2][1], v[3][0], v[3][1]));
          mx = rmax4(mx); mn = rmin4(mn);
          const float mxl = -mx * L2E, mnl = mn * L2E;
          f32x2 ep[4], en[4];
#pragma unroll
          for (int i = 0; i < 4; ++i) {
            const f32x2 ap = v[i] * L2E + mxl, an = v[i] * (-L2E) + mnl;
            ep[i] = (f32x2){__builtin_amdgcn_exp2f(ap[0]), __builtin_amdgcn_exp2f(ap[1])};
            en[i] = (f32x2){__builtin_amdgcn_exp2f(an[0]), __builtin_amdgcn_exp2f(an[1])};
          }
          const f32x2 sp2 = (ep[0] + ep[1]) + (ep[2] + ep[3]), sn2 = (en[0] + en[1]) + (en[2] + en[3]);
          const float ip = __builtin_amdgcn_rcpf(rsum4(sp2[0] + sp2[1])), in_ = __builtin_amdgcn_rcpf(rsum4(sn2[0] + sn2[1]));
          u32x2 o[4];
          o[0] = (u32x2){pk2v(ep[0] * ip), pk2v(ep[1] * ip)};
          o[1] = (u32x2){pk2v(ep[2] * ip), pk2v(ep[3] * ip)};
          o[2] = (u32x2){pk2v(en[0] * in_), pk2v(en[1] * in_)};
          o[3] = (u32x2){pk2v(en[2] * in_), pk2v(en[3] * in_)};
          if (which == 0) {
            u16* dst = out + ((size_t)(row0 + wm * 64 + mt * 16 + l15) * 32 + h) * 64 + 4 * g;
#pragma unroll
            for (int c = 0; c < 4; ++c) *reinterpret_cast<u32x2*>(dst + c * 16) = o[c];
          } else {
            char* dst = pkl + (mt * 16 + l15) * 144 + g * 8;
#pragma unroll
            for (int c = 0; c < 4; ++c) *reinterpret_cast<u32x2*>(dst + c * 32) = o[c];
          }
        }
      WAIT_L0();
      __builtin_amdgcn_sched_barrier(0);
#pragma unroll
      for (int ft = 0; ft < 4; ++ft) { kv[ft][0] = kv[ft][1] = zz[ft] = (f32x4){0.f, 0.f, 0.f, 0.f}; }
      const bf16x8 ones = (bf16x8){0x3F80, 0x3F80, 0x3F80, 0x3F80, 0x3F80, 0x3F80, 0x3F80, 0x3F80};
#pragma unroll
      for (int s = 0; s < 2; ++s)
#pragma unroll
        for (int ft = 0; ft < 4; ++ft) {
          const char* src = pkl + (32 * s + 4 * g + q) * 144 + ft * 32 + p * 8;
          s16x4 lo = __builtin_amdgcn_ds_read_tr16_b64_v4i16((lds_s16x4_p)(src));
          s16x4 hi = __builtin_amdgcn_ds_read_tr16_b64_v4i16((lds_s16x4_p)(src + 16 * 144));
          bf16x8 pf = __builtin_shufflevector(lo, hi, 0, 1, 2, 3, 4, 5, 6, 7);
          kv[ft][0] = __builtin_amdgcn_mfma_f32_16x16x32_bf16(vf[s][0], pf, kv[ft][0], 0, 0, 0);
          kv[ft][1] = __builtin_amdgcn_mfma_f32_16x16x32_bf16(vf[s][1], pf, kv[ft][1], 0, 0, 0);
          zz[ft] = __builtin_amdgcn_mfma_f32_16x16x32_bf16(ones, pf, zz[ft], 0, 0, 0);
        }
      if (wm == 1) {
#pragma unroll
        for (int ft = 0; ft < 4; ++ft) {
#pragma unroll
          for (int dt = 0; dt < 2; ++dt) *reinterpret_cast<f32x4*>(red + (ft * 16 + l15) * 32 + dt * 16 + 4 * g) = kv[ft][dt];
          if (g == 0) red[64 * 32 + ft * 16 + l15] = zz[ft][0];
        }
      }
    }
    __syncthreads();
    if (wid8 < 2) {
      float* kd = kvp + (size_t)(h * 16 + tm) * 2048;
      float* zd = zp + (size_t)(h * 16 + tm) * 64;
#pragma unroll
      for (int ft = 0; ft < 4; ++ft) {
#pragma unroll
        for (int dt = 0; dt < 2; ++dt) {
          f32x4 o = *reinterpret_cast<const f32x4*>(red + (ft * 16 + l15) * 32 + dt * 16 + 4 * g);
          *reinterpret_cast<f32x4*>(kd + (ft * 16 + l15) * 32 + dt * 16 + 4 * g) = o + kv[ft][dt];
        }
        if (g == 0) zd[ft * 16 + l15] = red[64 * 32 + ft * 16 + l15] + zz[ft][0];
      }
    }
  } else {
    if (wid8 < 4) {
      f32x4 stm[4], cs4[NTW];
      if constexpr (EPI == EPI_W1 || EPI == EPI_W3) {
#pragma unroll
        for (int j = 0; j < NTW; ++j) {
          const int colj = (j < 2 * NFULL) ? ((j >> 1) * 64 + (2 * wn + (j & 1)) * 16) : (NFULL * 64 + wn * 16);
          cs4[j] = *reinterpret_cast<const f32x4*>(smem + CS_OFF + (colj + 4 * g) * 4);
        }
#pragma unroll
        for (int mt = 0; mt < 4; ++mt) stm[mt] = rs[wm * 64 + mt * 16 + l15];
      }
#pragma unroll
      for (int j = 0; j < NTW; ++j) {
        const int colj = (j < 2 * NFULL) ? ((j >> 1) * 64 + (2 * wn + (j & 1)) * 16) : (NFULL * 64 + wn * 16);
#pragma unroll
        for (int mt = 0; mt < 4; ++mt) {
          f32x2 v0 = (f32x2){acc[mt][j][0], acc[mt][j][1]}, v1 = (f32x2){acc[mt][j][2], acc[mt][j][3]};
          if constexpr (EPI == EPI_W1 || EPI == EPI_W3) {
            v0 = gelu2((v0 - stm[mt][0] * (f32x2){cs4[j][0], cs4[j][1]}) * stm[mt][1]);
            v1 = gelu2((v1 - stm[mt][0] * (f32x2){cs4[j][2], cs4[j][3]}) * stm[mt][1]);
          }
          *reinterpret_cast<u32x2*>(smem + (wm * 64 + mt * 16 + l15) * PB + (colj + 4 * g) * 2) = (u32x2){pk2(v0[0], v0[1]), pk2(v1[0], v1[1])};
        }
      }
    }
    __syncthreads();
#pragma unroll
    for (int it = 0; it < NIT; ++it) {
      const int c = tid + 512 * it, r = c / CH, ch = c % CH;
      u32x4 v = *reinterpret_cast<const u32x4*>(smem + r * PB + ch * 16);
      const int grow = row0 + r, gcol = tn * BN + ch * 8;
      if constexpr (EPI == EPI_W3) {
        *reinterpret_cast<u32x4*>(out + (size_t)grow * F_ + gcol) = v;
      } else if constexpr (EPI == EPI_W1) {
        const int grp = tn >> 2;
        const int sh = grp == 0 ? 1 : grp == 1 ? -1 : grp == 2 ? 2 : -2;
        const int dr = grow + sh;
        if (dr >= 0 && dr < S_) *reinterpret_cast<u32x4*>(out + (size_t)dr * D_ + gcol) = v;
        const int zr = grow - sh;
        if (zr < 0 || zr >= S_) *reinterpret_cast<u32x4*>(out + (size_t)grow * D_ + gcol) = (u32x4){0u, 0u, 0u, 0u};
      } else {
        float y[8];
        const u32x4 a = ra[it];
        if constexpr (EPI == EPI_WO) {
#pragma unroll
          for (int i = 0; i < 4; ++i) { y[2 * i] = rbf(bflo(a[i]) + bflo(v[i])); y[2 * i + 1] = rbf(bfhi(a[i]) + bfhi(v[i])); }
        } else if constexpr (EPI == EPI_W2) {
          f32x4 st = rs[r];
#pragma unroll
          for (int i = 0; i < 4; ++i) {
            y[2 * i] = rbf(rbf((bflo(a[i]) - st[0]) * st[2]) + bflo(v[i]));
            y[2 * i + 1] = rbf(rbf((bfhi(a[i]) - st[0]) * st[2]) + bfhi(v[i]));
          }
        } else {
          const u32x4 b = rb[it];
#pragma unroll
          for (int i = 0; i < 4; ++i) {
            y[2 * i] = rbf(bflo(a[i]) + rbf(bflo(b[i]) + bflo(v[i])));
            y[2 * i + 1] = rbf(bfhi(a[i]) + rbf(bfhi(b[i]) + bfhi(v[i])));
          }
        }
        *reinterpret_cast<u32x4*>(out + (size_t)grow * D_ + gcol) = (u32x4){pk2(y[0], y[1]), pk2(y[2], y[3]), pk2(y[4], y[5]), pk2(y[6], y[7])};
        float sm = ((y[0] + y[1]) + (y[2] + y[3])) + ((y[4] + y[5]) + (y[6] + y[7]));
        sm = rsum8(sm);
        float mu = sm * (1.0f / 64.f), m2 = 0.f;
#pragma unroll
        for (int i = 0; i < 8; ++i) { float d = y[i] - mu; m2 += d * d; }
        m2 = rsum8(m2);
        if (ch == 0) st_out[(size_t)grow * 16 + tn] = (f32x2){sm, m2};
      }
    }
  }
  STAMP(3);
}

__global__ __launch_bounds__(256, 1) void attn_out_kernel(const u16* __restrict__ phiq, const float* __restrict__ kvp,
                                                          const float* __restrict__ zp, u16* __restrict__ o) {
  __shared__ __attribute__((aligned(16))) u16 kvT[48 * 72];
  const int tid = threadIdx.x, lane = tid & 63, wid = tid >> 6, g = lane >> 4, l15 = lane & 15;
  const int h = blockIdx.x & 31, tg = blockIdx.x >> 5;
  f32x4 kvv[16][2];
  float zv[16];
  bf16x8 pq[4][2];
  {
    const float* src = kvp + (size_t)h * 16 * 2048 + tid * 8;
#pragma unroll
    for (int t = 0; t < 16; ++t) {
      kvv[t][0] = *reinterpret_cast<const f32x4*>(src + t * 2048);
      kvv[t][1] = *reinterpret_cast<const f32x4*>(src + t * 2048 + 4);
    }
#pragma unroll
    for (int t = 0; t < 16; ++t) zv[t] = zp[(size_t)(h * 16 + t) * 64 + lane];
#pragma unroll
    for (int mt = 0; mt < 4; ++mt) {
      const u16* ps = phiq + ((size_t)(tg * 256 + wid * 64 + mt * 16 + l15) * 32 + h) * 64 + g * 8;
      pq[mt][0] = *reinterpret_cast<const bf16x8*>(ps);
      pq[mt][1] = *reinterpret_cast<const bf16x8*>(ps + 32);
    }
  }
  __builtin_amdgcn_sched_barrier(0);
  {
    for (int i = tid; i < 15 * 64; i += 256) kvT[(33 + i / 64) * 72 + (i & 63)] = 0;
    f32x4 a0 = kvv[0][0], a1 = kvv[0][1];
#pragma unroll
    for (int t = 1; t < 16; ++t) { a0 += kvv[t][0]; a1 += kvv[t][1]; }
    const int f = tid >> 2, d0 = (tid & 3) * 8;
#pragma unroll
    for (int i = 0; i < 4; ++i) {
      kvT[(d0 + i) * 72 + f] = (u16)(pk2(a0[i], 0.f) & 0xffff);
      kvT[(d0 + 4 + i) * 72 + f] = (u16)(pk2(a1[i], 0.f) & 0xffff);
    }
    if (tid < 64) {
      float z = 0.f;
#pragma unroll
      for (int t = 0; t < 16; ++t) z += zv[t];
      kvT[32 * 72 + tid] = (u16)(pk2(z, 0.f) & 0xffff);
    }
  }
  __syncthreads();
  bf16x8 kf[3][2];
#pragma unroll
  for (int dt = 0; dt < 3; ++dt)
#pragma unroll
    for (int ks = 0; ks < 2; ++ks) kf[dt][ks] = *reinterpret_cast<const bf16x8*>(&kvT[(dt * 16 + l15) * 72 + ks * 32 + g * 8]);
#pragma unroll
  for (int mt = 0; mt < 4; ++mt) {
    const int tok = tg * 256 + wid * 64 + mt * 16 + l15;
    const bf16x8 p0 = pq[mt][0], p1 = pq[mt][1];
    f32x4 r[3];
#pragma unroll
    for (int dt = 0; dt < 3; ++dt) {
      r[dt] = (f32x4){0.f, 0.f, 0.f, 0.f};
      r[dt] = __builtin_amdgcn_mfma_f32_16x16x32_bf16(kf[dt][0], p0, r[dt], 0, 0, 0);
      r[dt] = __builtin_amdgcn_mfma_f32_16x16x32_bf16(kf[dt][1], p1, r[dt], 0, 0, 0);
    }
    float den = rbf(rbf(__shfl(r[2][0], l15)) + 1e-6f);
    float inv = 1.0f / den;
#pragma unroll
    for (int dt = 0; dt < 2; ++dt) {
      u32x2 w = (u32x2){pk2(rbf(r[dt][0]) * inv, rbf(r[dt][1]) * inv), pk2(rbf(r[dt][2]) * inv, rbf(r[dt][3]) * inv)};
      *reinterpret_cast<u32x2*>(o + (size_t)tok * D_ + h * 32 + dt * 16 + 4 * g) = w;
    }
  }
}

constexpr int WC_QKV = 0, WC_WO = WC_QKV + 2 * 1024 * 384, WC_W1 = WC_WO + 2 * 1024 * 128, WC_W2 = WC_W1 + 2 * 1024 * 128,
              WC_W4 = WC_W2 + 2 * 1024 * 128, WC_END = WC_W4 + 2 * F_ * 128;
__global__ __launch_bounds__(256) void prep_kernel(const u16* __restrict__ x, const u16* __restrict__ wqkv,
                                                   const u16* __restrict__ w1, const u16* __restrict__ w3,
                                                   float* __restrict__ cs_qkv, float* __restrict__ cs_w1,
                                                   float* __restrict__ cs_w3, f32x2* __restrict__ st,
                                                   const u16* __restrict__ wo, const u16* __restrict__ w2,
                                                   const u16* __restrict__ w4, char* __restrict__ wc) {
  __shared__ float red[32][65];
  const int tid = threadIdx.x, b = blockIdx.x;
  if (b >= 248) {
    const int pb = b - 248;
    const u16* w = pb < 32 ? wo + (size_t)pb * 32768 : pb < 64 ? w2 + (size_t)(pb - 32) * 32768 : w4 + (size_t)(pb - 64) * 32768;
    u32x4 v[16];
#pragma unroll
    for (int r = 0; r < 16; ++r) v[r] = *reinterpret_cast<const u32x4*>(w + r * 2048 + tid * 8);
    if (((tid >> 3) & 7) == 3) {
      const int kd = pb < 64 ? 1024 : F_, row = (pb < 32 ? pb : pb < 64 ? pb - 32 : pb - 64) * 32 + (tid >> 7);
      char* dst = wc + (pb < 32 ? WC_WO : pb < 64 ? WC_W2 : WC_W4) + (size_t)((tid >> 6) & 1) * kd * 128 + (size_t)row * 128 + (tid & 7) * 16;
#pragma unroll
      for (int r = 0; r < 16; ++r) *reinterpret_cast<u32x4*>(dst + r * 256) = v[r];
    }
#pragma unroll
    for (int r = 0; r < 16; ++r) asm volatile("" ::"v"(v[r]));
  } else if (b < 120) {
    const u16* w; float* dst; int ld, c0;
    if (b < 48) { w = wqkv; dst = cs_qkv; ld = 3072; c0 = b * 64; }
    else if (b < 64) { w = w1; dst = cs_w1; ld = 1024; c0 = (b - 48) * 64; }
    else { w = w3; dst = cs_w3; ld = F_; c0 = (b - 64) * 64; }
    const int cg = tid & 7, rg = tid >> 3;
    float s[8] = {0.f, 0.f, 0.f, 0.f, 0.f, 0.f, 0.f, 0.f};
    const bool cp = b < 64 && (b & 7) == 3;
    const int cpp = b < 48 ? 384 : 128;
    char* cpd = wc + (b < 48 ? WC_QKV + ((b >> 3) & 1) * (1024 * 384) + (b >> 4) * 128 : WC_W1 + ((b >> 3) & 1) * (1024 * 128)) + cg * 16;
#pragma unroll 8
    for (int r = 0; r < 32; ++r) {
      u32x4 v = *reinterpret_cast<const u32x4*>(w + (size_t)(r * 32 + rg) * ld + c0 + cg * 8);
      if (cp) *reinterpret_cast<u32x4*>(cpd + (r * 32 + rg) * cpp) = v;
#pragma unroll
      for (int i = 0; i < 4; ++i) { s[2 * i] += bflo(v[i]); s[2 * i + 1] += bfhi(v[i]); }
    }
#pragma unroll
    for (int i = 0; i < 8; ++i) red[rg][cg * 8 + i] = s[i];
    __syncthreads();
    if (tid < 64) {
      float t = 0.f;
#pragma unroll
      for (int r = 0; r < 32; ++r) t += red[r][tid];
      dst[c0 + tid] = t;
    }
  } else {
    const int lane = tid & 63, wid = tid >> 6;
    const int rbase = (b - 120) * 16 + wid * 4;
#pragma unroll
    for (int rr = 0; rr < 4; ++rr) {
      const int row = rbase + rr;
#pragma unroll
      for (int hf = 0; hf < 2; ++hf) {
        u32x4 v = *reinterpret_cast<const u32x4*>(x + (size_t)row * D_ + hf * 512 + lane * 8);
        float y[8];
#pragma unroll
        for (int i = 0; i < 4; ++i) { y[2 * i] = bflo(v[i]); y[2 * i + 1] = bfhi(v[i]); }
        float sm = ((y[0] + y[1]) + (y[2] + y[3])) + ((y[4] + y[5]) + (y[6] + y[7]));
        sm = rsum8(sm);
        float mu = sm * (1.0f / 64.f), m2 = 0.f;
#pragma unroll
        for (int i = 0; i < 8; ++i) { float d = y[i] - mu; m2 += d * d; }
        m2 = rsum8(m2);
        if ((lane & 7) == 0) st[(size_t)row * 16 + hf * 8 + (lane >> 3)] = (f32x2){sm, m2};
      }
    }
  }
}

extern "C" void kernel_launch(void* const* d_in, const int* in_sizes, int n_in, void* d_out, int out_size, void* d_ws,
                              size_t ws_size, hipStream_t stream) {
  const u16* x_in = (const u16*)d_in[0];
  const u16* wqkv = (const u16*)d_in[1];
  const u16* wphi = (const u16*)d_in[2];
  const u16* wo = (const u16*)d_in[3];
  const u16* w1 = (const u16*)d_in[5];
  const u16* w2 = (const u16*)d_in[6];
  const u16* w3 = (const u16*)d_in[7];
  const u16* w4 = (const u16*)d_in[8];
  char* ws = (char*)d_ws;
  size_t off = 0;
  auto alloc = [&](size_t bytes) { char* p = ws + off; off += (bytes + 4095) & ~(size_t)4095; return p; };
  u16* xb = (u16*)alloc((size_t)S_ * D_ * 2);
  u16* x2 = (u16*)alloc((size_t)S_ * D_ * 2);
  u16* x1 = (u16*)alloc((size_t)S_ * D_ * 2);
  u16* hs = (u16*)alloc((size_t)S_ * D_ * 2);
  u16* ob = (u16*)alloc((size_t)S_ * D_ * 2);
  u16* gb = (u16*)alloc((size_t)S_ * F_ * 2);
  u16* phiq = (u16*)alloc((size_t)S_ * 2048 * 2);
  float* kvp = (float*)alloc((size_t)32 * 16 * 2048 * 4);
  float* zp = (float*)alloc((size_t)32 * 16 * 64 * 4);
  f32x2* st_x = (f32x2*)alloc((size_t)S_ * 16 * 8);
  f32x2* st_x2 = (f32x2*)alloc((size_t)S_ * 16 * 8);
  f32x2* st_x1 = (f32x2*)alloc((size_t)S_ * 16 * 8);
  float* cs_qkv = (float*)alloc(3072 * 4);
  float* cs_w1 = (float*)alloc(1024 * 4);
  float* cs_w3 = (float*)alloc(F_ * 4);
  char* wc = alloc(WC_END);

  prep_kernel<<<248 + 176, 256, 0, stream>>>(x_in, wqkv, w1, w3, cs_qkv, cs_w1, cs_w3, st_x, wo, w2, w4, wc);
  for (int layer = 0; layer < 2; ++layer) {
    const u16* x = layer == 0 ? x_in : xb;
    u16* xo = layer == 0 ? xb : (u16*)d_out;
    gemm_kernel<192, 3, 1024, 1024, 3072, EPI_QKV><<<256, 512, 0, stream>>>(x, wqkv, (const u16*)(wc + WC_QKV), cs_qkv, st_x, nullptr, nullptr, phiq, nullptr, wphi, kvp, zp);
    attn_out_kernel<<<256, 256, 0, stream>>>(phiq, kvp, zp, ob);
    gemm_kernel<64, 5, 1024, 1024, 1024, EPI_WO><<<256, 512, 0, stream>>>(ob, wo, (const u16*)(wc + WC_WO), nullptr, nullptr, x, nullptr, x2, st_x2, nullptr, nullptr, nullptr);
    gemm_kernel<64, 5, 1024, 1024, 1024, EPI_W1><<<256, 512, 0, stream>>>(x2, w1, (const u16*)(wc + WC_W1), cs_w1, st_x2, nullptr, nullptr, hs, nullptr, nullptr, nullptr, nullptr);
    gemm_kernel<64, 5, 1024, 1024, 1024, EPI_W2><<<256, 512, 0, stream>>>(hs, w2, (const u16*)(wc + WC_W2), nullptr, st_x2, x2, nullptr, x1, st_x1, nullptr, nullptr, nullptr);
    gemm_kernel<224, 3, 1024, 1024, F_, EPI_W3><<<256, 512, 0, stream>>>(x1, w3, nullptr, cs_w3, st_x1, nullptr, nullptr, gb, nullptr, nullptr, nullptr, nullptr);
    gemm_kernel<64, 5, F_, F_, 1024, EPI_W4><<<256, 512, 0, stream>>>(gb, w4, (const u16*)(wc + WC_W4), nullptr, nullptr, x2, x1, xo, st_x, nullptr, nullptr, nullptr);
  }
}
```

```cpp
#include <hip/hip_runtime.h>
#include <hip/hip_bf16.h>
#include <stdint.h>
#include <stdio.h>
#include <type_traits>

typedef short bf16x8 __attribute__((ext_vector_type(8)));
typedef short s16x4 __attribute__((ext_vector_type(4)));
typedef float f32x4 __attribute__((ext_vector_type(4)));
typedef float f32x2 __attribute__((ext_vector_type(2)));
typedef unsigned u32x2 __attribute__((ext_vector_type(2)));
typedef unsigned u32x4 __attribute__((ext_vector_type(4)));
typedef __attribute__((address_space(3))) s16x4* lds_s16x4_p;
typedef unsigned short u16;

constexpr int S_ = 2048, D_ = 1024, H_ = 32, F_ = 3584;

__device__ __forceinline__ float bf2f(u16 v) { return __uint_as_float(((unsigned)v) << 16); }
__device__ __forceinline__ float bflo(unsigned v) { return __uint_as_float(v << 16); }
__device__ __forceinline__ float bfhi(unsigned v) { return __uint_as_float(v & 0xffff0000u); }
typedef __bf16 bf16x2_t __attribute__((ext_vector_type(2)));
__device__ __forceinline__ unsigned pk2(float a, float b) {
  return __builtin_bit_cast(unsigned, __builtin_convertvector((f32x2){a, b}, bf16x2_t));
}
__device__ __forceinline__ unsigned pk2v(f32x2 v) { return __builtin_bit_cast(unsigned, __builtin_convertvector(v, bf16x2_t)); }
__device__ __forceinline__ float max2_(float a, float b) { float r; asm("v_max_f32 %0, %1, %2" : "=v"(r) : "v"(a), "v"(b)); return r; }
__device__ __forceinline__ float min2_(float a, float b) { float r; asm("v_min_f32 %0, %1, %2" : "=v"(r) : "v"(a), "v"(b)); return r; }
__device__ __forceinline__ float max3_(float a, float b, float c) { float r; asm("v_max3_f32 %0, %1, %2, %3" : "=v"(r) : "v"(a), "v"(b), "v"(c)); return r; }
__device__ __forceinline__ float min3_(float a, float b, float c) { float r; asm("v_min3_f32 %0, %1, %2, %3" : "=v"(r) : "v"(a), "v"(b), "v"(c)); return r; }
__device__ __forceinline__ float rbf(float a) { return bflo(pk2(a, 0.f)); }
__device__ __forceinline__ float gelu_t(float x) {
  const float c1 = -2.0f * 0.7978845608028654f * 1.4426950408889634f;
  const float c2 = c1 * 0.044715f;
  float t = x * x;
  float w = x * __builtin_fmaf(t, c2, c1);
  float e = __builtin_amdgcn_exp2f(w);
  return x * __builtin_amdgcn_rcpf(1.0f + e);
}

__device__ __forceinline__ f32x2 gelu2(f32x2 x) {
  const float c1 = -2.0f * 0.7978845608028654f * 1.4426950408889634f;
  const float c2 = c1 * 0.044715f;
  f32x2 t = x * x;
  f32x2 w = x * (t * c2 + c1);
  f32x2 d = (f32x2){__builtin_amdgcn_exp2f(w[0]), __builtin_amdgcn_exp2f(w[1])} + 1.0f;
  return x * (f32x2){__builtin_amdgcn_rcpf(d[0]), __builtin_amdgcn_rcpf(d[1])};
}
__device__ __forceinline__ void x16(float v, float& a, float& b) {
  auto r = __builtin_amdgcn_permlane16_swap(__float_as_uint(v), __float_as_uint(v), false, false);
  a = __uint_as_float(r[0]); b = __uint_as_float(r[1]);
}
__device__ __forceinline__ void x32(float v, float& a, float& b) {
  auto r = __builtin_amdgcn_permlane32_swap(__float_as_uint(v), __float_as_uint(v), false, false);
  a = __uint_as_float(r[0]); b = __uint_as_float(r[1]);
}
__device__ __forceinline__ float rmax4(float v) { float a, b; x16(v, a, b); v = max2_(a, b); x32(v, a, b); return max2_(a, b); }
__device__ __forceinline__ float rmin4(float v) { float a, b; x16(v, a, b); v = min2_(a, b); x32(v, a, b); return min2_(a, b); }
__device__ __forceinline__ float rsum4(float v) { float a, b; x16(v, a, b); v = a + b; x32(v, a, b); return a + b; }

__device__ __forceinline__ float rsum8(float v) {
  v += __uint_as_float(__builtin_amdgcn_mov_dpp(__float_as_uint(v), 0xB1, 0xF, 0xF, true));
  v += __uint_as_float(__builtin_amdgcn_mov_dpp(__float_as_uint(v), 0x4E, 0xF, 0xF, true));
  v += __uint_as_float(__builtin_amdgcn_mov_dpp(__float_as_uint(v), 0x141, 0xF, 0xF, true));
  return v;
}
#define WAIT_V(n) asm volatile("s_waitcnt vmcnt(" #n ")" ::: "memory")
#define WAIT_L0() asm volatile("s_waitcnt lgkmcnt(0)" ::: "memory")
template <int N> __device__ __forceinline__ void wait_vm() {
  asm volatile("s_waitcnt vmcnt(%0)" ::"i"(N) : "memory");
}

__device__ __forceinline__ void glds16(unsigned voff, const void* sbase, unsigned lds) {
  unsigned keep;
  asm volatile("s_mov_b32 %0, m0\n\ts_mov_b32 m0, %3\n\ts_nop 0\n\tglobal_load_lds_dwordx4 %1, %2\n\ts_mov_b32 m0, %0"
               : "=&s"(keep) : "v"(voff), "s"(sbase), "s"(lds) : "memory");
}

__device__ __forceinline__ void mfma_acc(f32x4& c, const u32x4& a, const u32x4& b) {
  asm volatile("v_mfma_f32_16x16x32_bf16 %0, %1, %2, %0" : "+a"(c) : "v"(a), "v"(b));
}

#define STAMP(i)

enum { EPI_QKV = 0, EPI_WO = 1, EPI_W1 = 2, EPI_W2 = 3, EPI_W3 = 4, EPI_W4 = 5 };

template <int BN, int NST, int KD, int LDA, int LDB, int EPI>
__global__ __launch_bounds__(512, 2) void gemm_kernel(
    const u16* __restrict__ A, const u16* __restrict__ W,
    const float* __restrict__ cs,
    const f32x2* __restrict__ st_in,
    const u16* __restrict__ r0,
    const u16* __restrict__ r1,
    u16* __restrict__ out,
    f32x2* __restrict__ st_out,
    const u16* __restrict__ wphi,
    float* __restrict__ kvp,
    float* __restrict__ zp)
{
  constexpr int NFULL = BN / 64, HALF = (BN % 64) / 32;
  constexpr int A_BYTES = 128 * 64 * 2, B_BYTES = BN * 64 * 2, STAGE = A_BYTES + B_BYTES;
  constexpr int NT = KD / 64, NTW = 2 * NFULL + HALF, LPS = 4 + 2 * NFULL + HALF;
  constexpr int PB = BN * 2 + 16;
  constexpr int EPI_BYTES = (EPI == EPI_QKV) ? (4 * 64 * 144 + 2 * (64 * 32 + 64) * 4) : 128 * PB;
  constexpr int MAINB = NST * STAGE;
  constexpr int RS_OFF = (MAINB > EPI_BYTES ? MAINB : EPI_BYTES);
  constexpr int CS_OFF = RS_OFF + 128 * 16;
  constexpr int WP_OFF = CS_OFF + 1024;
  constexpr int SMEM = WP_OFF + (EPI == EPI_QKV ? 4096 : 0);
  constexpr bool HAS_CS = (EPI == EPI_QKV || EPI == EPI_W1 || EPI == EPI_W3);
  constexpr bool HAS_ST = (EPI == EPI_QKV || EPI == EPI_W1 || EPI == EPI_W3 || EPI == EPI_W2);
  constexpr bool HAS_RES = (EPI == EPI_WO || EPI == EPI_W2 || EPI == EPI_W4);
  constexpr int LAG = (BN == 64) ? 1 : 0;
  constexpr int CH = BN / 8, NIT = CH / 4;
  static_assert(SMEM <= 163840, "LDS");
  __shared__ __attribute__((aligned(16))) char smem[SMEM];

  STAMP(0);
  const int tid = threadIdx.x, lane = tid & 63;
  const int wid8 = __builtin_amdgcn_readfirstlane(tid >> 6), wid = wid8 & 3, wm = wid >> 1, wn = wid & 1;
  const int g = lane >> 4, l15 = lane & 15;
  const int xcd = blockIdx.x & 7, idx = blockIdx.x >> 3;
  const int tm = (xcd >> 1) * 4 + (idx & 3), tn = (xcd & 1) * 8 + (idx >> 2);
  const int row0 = tm * 128;
  f32x4* rs = reinterpret_cast<f32x4*>(smem + RS_OFF);

  f32x4 acc[4][NTW];
  u32x4 ra[HAS_RES ? NIT : 1], rb[(EPI == EPI_W4) ? NIT : 1];

  if (wid8 >= 4) {
    unsigned voA[4], voB[NFULL > 0 ? NFULL : 1][2], voH = 0;
#pragma unroll
    for (int i = 0; i < 4; ++i) {
      int r = (wid * 4 + i) * 8 + (lane >> 3);
      int ch = (lane & 7) ^ ((r >> 1) & 7);
      voA[i] = (unsigned)(row0 + r) * (LDA * 2) + ch * 16;
    }
#pragma unroll
    for (int b = 0; b < NFULL; ++b) {
      int colb = (EPI == EPI_QKV) ? (b * 1024 + tn * 64) : (tn * BN + b * 64);
#pragma unroll
      for (int j = 0; j < 2; ++j) {
        int r = (wid * 2 + j) * 8 + (lane >> 3);
        int f = ((r >> 1) & 1) | (((r >> 3) & 1) << 1);
        int chp = lane & 7;
        int gch = (((chp >> 1) ^ f) << 1) | (chp & 1);
        voB[b][j] = (unsigned)r * (LDB * 2) + colb * 2 + gch * 16;
      }
    }
    if constexpr (HALF) {
      int r = wid * 16 + (lane >> 2);
      int chp = lane & 3;
      int gch = (((chp >> 1) ^ ((r >> 3) & 1)) << 1) | (chp & 1);
      voH = (unsigned)r * (LDB * 2) + (tn * BN + NFULL * 64) * 2 + gch * 16;
    }
    const char* Ab = reinterpret_cast<const char*>(A);
    const char* Wb = reinterpret_cast<const char*>(W);
    const unsigned lbase = (unsigned)(uintptr_t)(__attribute__((address_space(3))) char*)smem;
    auto stage = [&](int kt, int buf) {
      const char* a = Ab + (size_t)kt * 128;
      const char* w = Wb + (size_t)kt * (64 * LDB * 2);
      const unsigned s = lbase + buf * STAGE;
#pragma unroll
      for (int i = 0; i < 4; ++i) glds16(voA[i], a, s + (wid * 4 + i) * 1024);
#pragma unroll
      for (int b = 0; b < NFULL; ++b)
#pragma unroll
        for (int j = 0; j < 2; ++j) glds16(voB[b][j], w, s + A_BYTES + b * 8192 + (wid * 2 + j) * 1024);
      if constexpr (HALF) glds16(voH, w, s + A_BYTES + NFULL * 8192 + wid * 1024);
    };
#pragma unroll
    for (int s = 0; s < NST; ++s) stage(s, s);
    constexpr int XL = 0;
    f32x4 stp[4];
    const int lt = tid - 256;
    f32x4 csx;
    u32x4 wpx;
    if constexpr (HAS_CS) {
      const int c4 = lt < BN / 4 ? lt : 0;
      csx = *reinterpret_cast<const f32x4*>(cs + (EPI == EPI_QKV ? (c4 >> 4) * 1024 + tn * 64 + (c4 & 15) * 4 : tn * BN + c4 * 4));
    }
    if constexpr (EPI == EPI_QKV) wpx = *reinterpret_cast<const u32x4*>(wphi + tn * 2048 + lt * 8);
    if constexpr (HAS_ST) {
      const f32x4* sp = reinterpret_cast<const f32x4*>(st_in + (size_t)(row0 + (lt >> 1)) * 16 + (lt & 1) * 8);
#pragma unroll
      for (int i = 0; i < 4; ++i) stp[i] = sp[i];
    }
    if constexpr (HAS_RES) {
#pragma unroll
      for (int it = 0; it < NIT; ++it) {
        const int c = tid + 512 * it, r = c / CH, ch = c % CH;
        ra[it] = *reinterpret_cast<const u32x4*>(r0 + (size_t)(row0 + r) * D_ + tn * BN + ch * 8);
        if constexpr (EPI == EPI_W4) rb[it] = *reinterpret_cast<const u32x4*>(r1 + (size_t)(row0 + r) * D_ + tn * BN + ch * 8);
      }
    }
    asm volatile("" ::: "memory");
    wait_vm<(NST - 1 - LAG) * LPS>();
    __builtin_amdgcn_s_barrier();
    int buf = 0;
    for (int kt = 0; kt < NT - NST; ++kt) {
      wait_vm<(NST - 2 - LAG) * LPS>();
      __builtin_amdgcn_s_barrier();
      stage(kt + NST, buf);
      buf = buf + 1 == NST ? 0 : buf + 1;
    }
    asm volatile("" ::: "memory");
#pragma unroll
    for (int r = 0; r < NST - 1; ++r) {
      constexpr int NSL = NST - LAG;
      if (r == 0) wait_vm<(NSL - 2) * LPS + XL>();
      else if (r == 1) wait_vm<(NSL > 3 ? NSL - 3 : 0) * LPS + XL>();
      else if (r == 2) wait_vm<(NSL > 4 ? NSL - 4 : 0) * LPS + XL>();
      else if (r == 3) wait_vm<(NSL > 5 ? NSL - 5 : 0) * LPS + XL>();
      else wait_vm<XL>();
      __builtin_amdgcn_s_barrier();
    }
    if constexpr (HAS_ST) {
#pragma unroll
      for (int i = 0; i < 4; ++i) asm volatile("" : "+v"(stp[i]));
      float sm = 0.f;
#pragma unroll
      for (int i = 0; i < 4; ++i) sm += stp[i][0] + stp[i][2];
      sm += __shfl_xor(sm, 1);
      float mean = sm * (1.0f / 1024.f);
      float m2 = 0.f;
#pragma unroll
      for (int i = 0; i < 4; ++i) {
        float d0 = stp[i][0] * (1.0f / 64.f) - mean, d1 = stp[i][2] * (1.0f / 64.f) - mean;
        m2 += stp[i][1] + stp[i][3] + 64.f * (d0 * d0 + d1 * d1);
      }
      m2 += __shfl_xor(m2, 1);
      float var = m2 * (1.0f / 1024.f);
      float r1_ = 1.0f / sqrtf(var + 1e-5f);
      float sc = r1_;
      if constexpr (EPI == EPI_W1) sc = r1_ / sqrtf(var * r1_ * r1_ + 1e-5f);
      if ((lt & 1) == 0) rs[lt >> 1] = (f32x4){mean, sc, r1_, 0.f};
    }
    if constexpr (HAS_CS) {
      if (lt < BN / 4) *reinterpret_cast<f32x4*>(smem + CS_OFF + lt * 16) = csx;
    }
    if constexpr (EPI == EPI_QKV) *reinterpret_cast<u32x4*>(smem + WP_OFF + lt * 16) = wpx;
  } else {
    if constexpr (HAS_RES) {
#pragma unroll
      for (int it = 0; it < NIT; ++it) {
        const int c = tid + 512 * it, r = c / CH, ch = c % CH;
        ra[it] = *reinterpret_cast<const u32x4*>(r0 + (size_t)(row0 + r) * D_ + tn * BN + ch * 8);
        if constexpr (EPI == EPI_W4) rb[it] = *reinterpret_cast<const u32x4*>(r1 + (size_t)(row0 + r) * D_ + tn * BN + ch * 8);
      }
    }
    const int q = l15 >> 2, p = lane & 3;
    const int fB = (q >> 1) | ((g & 1) << 1);
    unsigned aoff[2], boff[2], hoff = 0;
#pragma unroll
    for (int ks = 0; ks < 2; ++ks) aoff[ks] = (wm * 64 + l15) * 128 + (((4 * ks + g) ^ (l15 >> 1)) * 16);
#pragma unroll
    for (int u = 0; u < 2; ++u) boff[u] = A_BYTES + (8 * g + q) * 128 + (((2 * wn + u) ^ fB) * 32) + p * 8;
    if constexpr (HALF) hoff = A_BYTES + NFULL * 8192 + (8 * g + q) * 64 + ((wn ^ (g & 1)) * 32) + p * 8;

    auto ldf = [&](int i, const char* s, int ks, u32x4(&af)[4], u32x4(&bfr)[NTW]) {
      if (i == 0 || i > NTW) {
        const int mt = i == 0 ? 0 : i - NTW;
        af[mt] = *reinterpret_cast<const u32x4*>(s + aoff[ks] + mt * 2048);
      } else if (i - 1 < 2 * NFULL) {
        const int b = (i - 1) >> 1, u = (i - 1) & 1;
        s16x4 lo = __builtin_amdgcn_ds_read_tr16_b64_v4i16((lds_s16x4_p)(s + boff[u] + b * 8192 + ks * 4096));
        s16x4 hi = __builtin_amdgcn_ds_read_tr16_b64_v4i16((lds_s16x4_p)(s + boff[u] + b * 8192 + ks * 4096 + 512));
        bfr[i - 1] = __builtin_shufflevector(__builtin_bit_cast(u32x2, lo), __builtin_bit_cast(u32x2, hi), 0, 1, 2, 3);
      } else {
        s16x4 lo = __builtin_amdgcn_ds_read_tr16_b64_v4i16((lds_s16x4_p)(s + hoff + ks * 2048));
        s16x4 hi = __builtin_amdgcn_ds_read_tr16_b64_v4i16((lds_s16x4_p)(s + hoff + ks * 2048 + 256));
        bfr[NTW - 1] = __builtin_shufflevector(__builtin_bit_cast(u32x2, lo), __builtin_bit_cast(u32x2, hi), 0, 1, 2, 3);
      }
    };
    constexpr int NM = 4 * NTW, NL = NTW + 4;
    auto phase = [&](u32x4(&afc)[4], u32x4(&bfc)[NTW], u32x4(&afn)[4], u32x4(&bfn)[NTW], int nbuf, int nks, auto rd, auto bar) {
      const char* s = smem + nbuf * STAGE;
      constexpr int BAR = decltype(bar)::value, R0 = BAR < 0 ? 0 : BAR + 1;
      constexpr int RSP = (NM - R0 - 2) / NL > 0 ? (NM - R0 - 2) / NL : 1;
      static_assert(R0 + (NL - 1) * RSP < NM, "reads");
#pragma unroll
      for (int mt = 0; mt < 4; ++mt)
#pragma unroll
        for (int j = 0; j < NTW; ++j) {
          const int m = mt * NTW + j;
          if (m == BAR) {
            WAIT_L0();
            __builtin_amdgcn_s_barrier();
            asm volatile("" ::: "memory");
          }
          if (decltype(rd)::value && m >= R0 && (m - R0) % RSP == 0 && (m - R0) / RSP < NL) ldf((m - R0) / RSP, s, nks, afn, bfn);
          if (EPI == EPI_QKV && j >= 4) mfma_acc(acc[mt][j], afc[mt], bfc[j]);
          else mfma_acc(acc[mt][j], bfc[j], afc[mt]);
          __builtin_amdgcn_sched_barrier(0);
        }
    };
    typedef std::integral_constant<int, -1> nobar;
#pragma unroll
    for (int mt = 0; mt < 4; ++mt)
#pragma unroll
      for (int j = 0; j < NTW; ++j) {
        acc[mt][j] = (f32x4){0.f, 0.f, 0.f, 0.f};
        asm volatile("" : "+a"(acc[mt][j]));
      }
    u32x4 af0[4], af1[4], bf0[NTW], bf1[NTW];
    __builtin_amdgcn_s_barrier();
    asm volatile("" ::: "memory");
    STAMP(1);
#pragma unroll
    for (int i = 0; i < NL; ++i) { ldf(i, smem, 0, af0, bf0); __builtin_amdgcn_sched_barrier(0); }
    int buf = 0;
    if constexpr (LAG) {
      phase(af0, bf0, af1, bf1, 0, 1, std::true_type{}, nobar{});
      for (int kt = 0; kt < NT - 1; ++kt) {
        buf = buf + 1 == NST ? 0 : buf + 1;
        phase(af1, bf1, af0, bf0, buf, 0, std::true_type{}, nobar{});
        asm volatile("" ::: "memory");
        __builtin_amdgcn_s_barrier();
        asm volatile("" ::: "memory");
        phase(af0, bf0, af1, bf1, buf, 1, std::true_type{}, nobar{});
      }
      phase(af1, bf1, af0, bf0, buf, 0, std::false_type{}, nobar{});
    } else {
      for (int kt = 0; kt < NT - 1; ++kt) {
        phase(af0, bf0, af1, bf1, buf, 1, std::true_type{}, nobar{});
        buf = buf + 1 == NST ? 0 : buf + 1;
        phase(af1, bf1, af0, bf0, buf, 0, std::true_type{}, std::integral_constant<int, 4>{});
      }
      phase(af0, bf0, af1, bf1, buf, 1, std::true_type{}, nobar{});
      phase(af1, bf1, af0, bf0, buf, 0, std::false_type{}, nobar{});
    }
    asm volatile("s_nop 7\n\ts_nop 7\n\ts_nop 7" ::: "memory");
  }
  WAIT_L0();
  __builtin_amdgcn_s_barrier();
  asm volatile("" ::: "memory");
  STAMP(2);

  if constexpr (EPI == EPI_QKV) {
    const int h = tn * 2 + wn;
    const int q = l15 >> 2, p = lane & 3;
    f32x4 kv[4][2], zz[4];
    float* red = reinterpret_cast<float*>(smem + 4 * 64 * 144) + wn * (64 * 32 + 64);
    if (wid8 < 4) {
      bf16x8 wf[2];
#pragma unroll
      for (int ft = 0; ft < 2; ++ft) {
        const char* src = smem + WP_OFF + wn * 2048 + (4 * g + q) * 64 + ft * 32 + p * 8;
        s16x4 lo = __builtin_amdgcn_ds_read_tr16_b64_v4i16((lds_s16x4_p)(src));
        s16x4 hi = __builtin_amdgcn_ds_read_tr16_b64_v4i16((lds_s16x4_p)(src + 16 * 64));
        wf[ft] = __builtin_shufflevector(lo, hi, 0, 1, 2, 3, 4, 5, 6, 7);
      }
      const float* csl = reinterpret_cast<const float*>(smem + CS_OFF);
      f32x4 cs4[4];
      float csv[2];
#pragma unroll
      for (int t = 0; t < 2; ++t) {
        cs4[t] = *reinterpret_cast<const f32x4*>(csl + wn * 32 + t * 16 + 4 * g);
        cs4[2 + t] = *reinterpret_cast<const f32x4*>(csl + 64 + wn * 32 + t * 16 + 4 * g);
        csv[t] = csl[128 + wn * 32 + t * 16 + l15];
      }
      bf16x8 qf[4], kf[4], vf[2][2];
#pragma unroll
      for (int mt = 0; mt < 4; ++mt) {
        f32x4 st = rs[wm * 64 + mt * 16 + l15];
        unsigned pq_[4], pk_[4];
#pragma unroll
        for (int t = 0; t < 2; ++t) {
          float a[4], b[4];
#pragma unroll
          for (int i = 0; i < 4; ++i) {
            a[i] = st[1] * (acc[mt][t][i] - st[0] * cs4[t][i]);
            b[i] = st[1] * (acc[mt][2 + t][i] - st[0] * cs4[2 + t][i]);
          }
          pq_[t * 2] = pk2(a[0], a[1]); pq_[t * 2 + 1] = pk2(a[2], a[3]);
          pk_[t * 2] = pk2(b[0], b[1]); pk_[t * 2 + 1] = pk2(b[2], b[3]);
        }
        qf[mt] = __builtin_bit_cast(bf16x8, (u32x4){pq_[0], pq_[1], pq_[2], pq_[3]});
        kf[mt] = __builtin_bit_cast(bf16x8, (u32x4){pk_[0], pk_[1], pk_[2], pk_[3]});
      }
#pragma unroll
      for (int s = 0; s < 2; ++s) {
        unsigned pv[2][4];
#pragma unroll
        for (int m2 = 0; m2 < 2; ++m2) {
          int mt = s * 2 + m2;
          f32x4 s0 = rs[wm * 64 + mt * 16 + 4 * g + 0], s1 = rs[wm * 64 + mt * 16 + 4 * g + 1];
          f32x4 s2 = rs[wm * 64 + mt * 16 + 4 * g + 2], s3 = rs[wm * 64 + mt * 16 + 4 * g + 3];
#pragma unroll
          for (int t = 0; t < 2; ++t) {
            float a0 = s0[1] * (acc[mt][4 + t][0] - s0[0] * csv[t]);
            float a1 = s1[1] * (acc[mt][4 + t][1] - s1[0] * csv[t]);
            float a2 = s2[1] * (acc[mt][4 + t][2] - s2[0] * csv[t]);
            float a3 = s3[1] * (acc[mt][4 + t][3] - s3[0] * csv[t]);
            pv[t][m2 * 2] = pk2(a0, a1); pv[t][m2 * 2 + 1] = pk2(a2, a3);
          }
        }
#pragma unroll
        for (int t = 0; t < 2; ++t) vf[s][t] = __builtin_bit_cast(bf16x8, (u32x4){pv[t][0], pv[t][1], pv[t][2], pv[t][3]});
      }
      char* pkl = smem + wid * (64 * 144);
      const float L2E = 1.4426950408889634f;
#pragma unroll
      for (int which = 0; which < 2; ++which)
#pragma unroll
        for (int mt = 0; mt < 4; ++mt) {
          f32x4 z4 = (f32x4){0.f, 0.f, 0.f, 0.f};
          f32x4 p0 = __builtin_amdgcn_mfma_f32_16x16x32_bf16(wf[0], which ? kf[mt] : qf[mt], z4, 0, 0, 0);
          f32x4 p1 = __builtin_amdgcn_mfma_f32_16x16x32_bf16(wf[1], which ? kf[mt] : qf[mt], z4, 0, 0, 0);
          f32x2 v[4];
          {
            const unsigned r0_ = pk2(p0[0], p0[1]), r1_ = pk2(p0[2], p0[3]), r2_ = pk2(p1[0], p1[1]), r3_ = pk2(p1[2], p1[3]);
            v[0] = (f32x2){bflo(r0_), bfhi(r0_)}; v[1] = (f32x2){bflo(r1_), bfhi(r1_)};
            v[2] = (f32x2){bflo(r2_), bfhi(r2_)}; v[3] = (f32x2){bflo(r3_), bfhi(r3_)};
          }
          float mx = max2_(max3_(max3_(v[0][0], v[0][1], v[1][0]), v[1][1], v[2][0]), max3_(v[2][1], v[3][0], v[3][1]));
          float mn = min2_(min3_(min3_(v[0][0], v[0][1], v[1][0]), v[1][1], v[2][0]), min3_(v[2][1], v[3][0], v[3][1]));
          mx = rmax4(mx); mn = rmin4(mn);
          const float mxl = -mx * L2E, mnl = mn * L2E;
          f32x2 ep[4], en[4];
#pragma unroll
          for (int i = 0; i < 4; ++i) {
            const f32x2 ap = v[i] * L2E + mxl, an = v[i] * (-L2E) + mnl;
            ep[i] = (f32x2){__builtin_amdgcn_exp2f(ap[0]), __builtin_amdgcn_exp2f(ap[1])};
            en[i] = (f32x2){__builtin_amdgcn_exp2f(an[0]), __builtin_amdgcn_exp2f(an[1])};
          }
          const f32x2 sp2 = (ep[0] + ep[1]) + (ep[2] + ep[3]), sn2 = (en[0] + en[1]) + (en[2] + en[3]);
          const float ip = __builtin_amdgcn_rcpf(rsum4(sp2[0] + sp2[1])), in_ = __builtin_amdgcn_rcpf(rsum4(sn2[0] + sn2[1]));
          u32x2 o[4];
          o[0] = (u32x2){pk2v(ep[0] * ip), pk2v(ep[1] * ip)};
          o[1] = (u32x2){pk2v(ep[2] * ip), pk2v(ep[3] * ip)};
          o[2] = (u32x2){pk2v(en[0] * in_), pk2v(en[1] * in_)};
          o[3] = (u32x2){pk2v(en[2] * in_), pk2v(en[3] * in_)};
          if (which == 0) {
            u16* dst = out + ((size_t)(row0 + wm * 64 + mt * 16 + l15) * 32 + h) * 64 + 4 * g;
#pragma unroll
            for (int c = 0; c < 4; ++c) *reinterpret_cast<u32x2*>(dst + c * 16) = o[c];
          } else {
            char* dst = pkl + (mt * 16 + l15) * 144 + g * 8;
#pragma unroll
            for (int c = 0; c < 4; ++c) *reinterpret_cast<u32x2*>(dst + c * 32) = o[c];
          }
        }
      WAIT_L0();
      __builtin_amdgcn_sched_barrier(0);
#pragma unroll
      for (int ft = 0; ft < 4; ++ft) { kv[ft][0] = kv[ft][1] = zz[ft] = (f32x4){0.f, 0.f, 0.f, 0.f}; }
      const bf16x8 ones = (bf16x8){0x3F80, 0x3F80, 0x3F80, 0x3F80, 0x3F80, 0x3F80, 0x3F80, 0x3F80};
#pragma unroll
      for (int s = 0; s < 2; ++s)
#pragma unroll
        for (int ft = 0; ft < 4; ++ft) {
          const char* src = pkl + (32 * s + 4 * g + q) * 144 + ft * 32 + p * 8;
          s16x4 lo = __builtin_amdgcn_ds_read_tr16_b64_v4i16((lds_s16x4_p)(src));
          s16x4 hi = __builtin_amdgcn_ds_read_tr16_b64_v4i16((lds_s16x4_p)(src + 16 * 144));
          bf16x8 pf = __builtin_shufflevector(lo, hi, 0, 1, 2, 3, 4, 5, 6, 7);
          kv[ft][0] = __builtin_amdgcn_mfma_f32_16x16x32_bf16(vf[s][0], pf, kv[ft][0], 0, 0, 0);
          kv[ft][1] = __builtin_amdgcn_mfma_f32_16x16x32_bf16(vf[s][1], pf, kv[ft][1], 0, 0, 0);
          zz[ft] = __builtin_amdgcn_mfma_f32_16x16x32_bf16(ones, pf, zz[ft], 0, 0, 0);
        }
      if (wm == 1) {
#pragma unroll
        for (int ft = 0; ft < 4; ++ft) {
#pragma unroll
          for (int dt = 0; dt < 2; ++dt) *reinterpret_cast<f32x4*>(red + (ft * 16 + l15) * 32 + dt * 16 + 4 * g) = kv[ft][dt];
          if (g == 0) red[64 * 32 + ft * 16 + l15] = zz[ft][0];
        }
      }
    }
    __syncthreads();
    if (wid8 < 2) {
      float* kd = kvp + (size_t)(h * 16 + tm) * 2048;
      float* zd = zp + (size_t)(h * 16 + tm) * 64;
#pragma unroll
      for (int ft = 0; ft < 4; ++ft) {
#pragma unroll
        for (int dt = 0; dt < 2; ++dt) {
          f32x4 o = *reinterpret_cast<const f32x4*>(red + (ft * 16 + l15) * 32 + dt * 16 + 4 * g);
          *reinterpret_cast<f32x4*>(kd + (ft * 16 + l15) * 32 + dt * 16 + 4 * g) = o + kv[ft][dt];
        }
        if (g == 0) zd[ft * 16 + l15] = red[64 * 32 + ft * 16 + l15] + zz[ft][0];
      }
    }
  } else {
    if (wid8 < 4) {
      f32x4 stm[4], cs4[NTW];
      if constexpr (EPI == EPI_W1 || EPI == EPI_W3) {
#pragma unroll
        for (int j = 0; j < NTW; ++j) {
          const int colj = (j < 2 * NFULL) ? ((j >> 1) * 64 + (2 * wn + (j & 1)) * 16) : (NFULL * 64 + wn * 16);
          cs4[j] = *reinterpret_cast<const f32x4*>(smem + CS_OFF + (colj + 4 * g) * 4);
        }
#pragma unroll
        for (int mt = 0; mt < 4; ++mt) stm[mt] = rs[wm * 64 + mt * 16 + l15];
      }
#pragma unroll
      for (int j = 0; j < NTW; ++j) {
        const int colj = (j < 2 * NFULL) ? ((j >> 1) * 64 + (2 * wn + (j & 1)) * 16) : (NFULL * 64 + wn * 16);
#pragma unroll
        for (int mt = 0; mt < 4; ++mt) {
          f32x2 v0 = (f32x2){acc[mt][j][0], acc[mt][j][1]}, v1 = (f32x2){acc[mt][j][2], acc[mt][j][3]};
          if constexpr (EPI == EPI_W1 || EPI == EPI_W3) {
            v0 = gelu2((v0 - stm[mt][0] * (f32x2){cs4[j][0], cs4[j][1]}) * stm[mt][1]);
            v1 = gelu2((v1 - stm[mt][0] * (f32x2){cs4[j][2], cs4[j][3]}) * stm[mt][1]);
          }
          *reinterpret_cast<u32x2*>(smem + (wm * 64 + mt * 16 + l15) * PB + (colj + 4 * g) * 2) = (u32x2){pk2(v0[0], v0[1]), pk2(v1[0], v1[1])};
        }
      }
    }
    __syncthreads();
#pragma unroll
    for (int it = 0; it < NIT; ++it) {
      const int c = tid + 512 * it, r = c / CH, ch = c % CH;
      u32x4 v = *reinterpret_cast<const u32x4*>(smem + r * PB + ch * 16);
      const int grow = row0 + r, gcol = tn * BN + ch * 8;
      if constexpr (EPI == EPI_W3) {
        *reinterpret_cast<u32x4*>(out + (size_t)grow * F_ + gcol) = v;
      } else if constexpr (EPI == EPI_W1) {
        const int grp = tn >> 2;
        const int sh = grp == 0 ? 1 : grp == 1 ? -1 : grp == 2 ? 2 : -2;
        const int dr = grow + sh;
        if (dr >= 0 && dr < S_) *reinterpret_cast<u32x4*>(out + (size_t)dr * D_ + gcol) = v;
        const int zr = grow - sh;
        if (zr < 0 || zr >= S_) *reinterpret_cast<u32x4*>(out + (size_t)grow * D_ + gcol) = (u32x4){0u, 0u, 0u, 0u};
      } else {
        float y[8];
        const u32x4 a = ra[it];
        if constexpr (EPI == EPI_WO) {
#pragma unroll
          for (int i = 0; i < 4; ++i) { y[2 * i] = rbf(bflo(a[i]) + bflo(v[i])); y[2 * i + 1] = rbf(bfhi(a[i]) + bfhi(v[i])); }
        } else if constexpr (EPI == EPI_W2) {
          f32x4 st = rs[r];
#pragma unroll
          for (int i = 0; i < 4; ++i) {
            y[2 * i] = rbf(rbf((bflo(a[i]) - st[0]) * st[2]) + bflo(v[i]));
            y[2 * i + 1] = rbf(rbf((bfhi(a[i]) - st[0]) * st[2]) + bfhi(v[i]));
          }
        } else {
          const u32x4 b = rb[it];
#pragma unroll
          for (int i = 0; i < 4; ++i) {
            y[2 * i] = rbf(bflo(a[i]) + rbf(bflo(b[i]) + bflo(v[i])));
            y[2 * i + 1] = rbf(bfhi(a[i]) + rbf(bfhi(b[i]) + bfhi(v[i])));
          }
        }
        *reinterpret_cast<u32x4*>(out + (size_t)grow * D_ + gcol) = (u32x4){pk2(y[0], y[1]), pk2(y[2], y[3]), pk2(y[4], y[5]), pk2(y[6], y[7])};
        float sm = ((y[0] + y[1]) + (y[2] + y[3])) + ((y[4] + y[5]) + (y[6] + y[7]));
        sm = rsum8(sm);
        float mu = sm * (1.0f / 64.f), m2 = 0.f;
#pragma unroll
        for (int i = 0; i < 8; ++i) { float d = y[i] - mu; m2 += d * d; }
        m2 = rsum8(m2);
        if (ch == 0) st_out[(size_t)grow * 16 + tn] = (f32x2){sm, m2};
      }
    }
  }
  STAMP(3);
}

__global__ __launch_bounds__(256, 1) void attn_out_kernel(const u16* __restrict__ phiq, const float* __restrict__ kvp,
                                                          const float* __restrict__ zp, u16* __restrict__ o) {
  __shared__ __attribute__((aligned(16))) u16 kvT[48 * 72];
  const int tid = threadIdx.x, lane = tid & 63, wid = tid >> 6, g = lane >> 4, l15 = lane & 15;
  const int h = blockIdx.x & 31, tg = blockIdx.x >> 5;
  f32x4 kvv[16][2];
  float zv[16];
  bf16x8 pq[4][2];
  {
    const float* src = kvp + (size_t)h * 16 * 2048 + tid * 8;
#pragma unroll
    for (int t = 0; t < 16; ++t) {
      kvv[t][0] = *reinterpret_cast<const f32x4*>(src + t * 2048);
      kvv[t][1] = *reinterpret_cast<const f32x4*>(src + t * 2048 + 4);
    }
#pragma unroll
    for (int t = 0; t < 16; ++t) zv[t] = zp[(size_t)(h * 16 + t) * 64 + lane];
#pragma unroll
    for (int mt = 0; mt < 4; ++mt) {
      const u16* ps = phiq + ((size_t)(tg * 256 + wid * 64 + mt * 16 + l15) * 32 + h) * 64 + g * 8;
      pq[mt][0] = *reinterpret_cast<const bf16x8*>(ps);
      pq[mt][1] = *reinterpret_cast<const bf16x8*>(ps + 32);
    }
  }
  __builtin_amdgcn_sched_barrier(0);
  {
    for (int i = tid; i < 15 * 64; i += 256) kvT[(33 + i / 64) * 72 + (i & 63)] = 0;
    f32x4 a0 = kvv[0][0], a1 = kvv[0][1];
#pragma unroll
    for (int t = 1; t < 16; ++t) { a0 += kvv[t][0]; a1 += kvv[t][1]; }
    const int f = tid >> 2, d0 = (tid & 3) * 8;
#pragma unroll
    for (int i = 0; i < 4; ++i) {
      kvT[(d0 + i) * 72 + f] = (u16)(pk2(a0[i], 0.f) & 0xffff);
      kvT[(d0 + 4 + i) * 72 + f] = (u16)(pk2(a1[i], 0.f) & 0xffff);
    }
    if (tid < 64) {
      float z = 0.f;
#pragma unroll
      for (int t = 0; t < 16; ++t) z += zv[t];
      kvT[32 * 72 + tid] = (u16)(pk2(z, 0.f) & 0xffff);
    }
  }
  __syncthreads();
  bf16x8 kf[3][2];
#pragma unroll
  for (int dt = 0; dt < 3; ++dt)
#pragma unroll
    for (int ks = 0; ks < 2; ++ks) kf[dt][ks] = *reinterpret_cast<const bf16x8*>(&kvT[(dt * 16 + l15) * 72 + ks * 32 + g * 8]);
#pragma unroll
  for (int mt = 0; mt < 4; ++mt) {
    const int tok = tg * 256 + wid * 64 + mt * 16 + l15;
    const bf16x8 p0 = pq[mt][0], p1 = pq[mt][1];
    f32x4 r[3];
#pragma unroll
    for (int dt = 0; dt < 3; ++dt) {
      r[dt] = (f32x4){0.f, 0.f, 0.f, 0.f};
      r[dt] = __builtin_amdgcn_mfma_f32_16x16x32_bf16(kf[dt][0], p0, r[dt], 0, 0, 0);
      r[dt] = __builtin_amdgcn_mfma_f32_16x16x32_bf16(kf[dt][1], p1, r[dt], 0, 0, 0);
    }
    float den = rbf(rbf(__shfl(r[2][0], l15)) + 1e-6f);
    float inv = 1.0f / den;
#pragma unroll
    for (int dt = 0; dt < 2; ++dt) {
      u32x2 w = (u32x2){pk2(rbf(r[dt][0]) * inv, rbf(r[dt][1]) * inv), pk2(rbf(r[dt][2]) * inv, rbf(r[dt][3]) * inv)};
      *reinterpret_cast<u32x2*>(o + (size_t)tok * D_ + h * 32 + dt * 16 + 4 * g) = w;
    }
  }
}

__global__ __launch_bounds__(256) void prep_kernel(const u16* __restrict__ x, const u16* __restrict__ wqkv,
                                                   const u16* __restrict__ w1, const u16* __restrict__ w3,
                                                   float* __restrict__ cs_qkv, float* __restrict__ cs_w1,
                                                   float* __restrict__ cs_w3, f32x2* __restrict__ st,
                                                   const u16* __restrict__ wo, const u16* __restrict__ w2,
                                                   const u16* __restrict__ w4) {
  __shared__ float red[32][65];
  const int tid = threadIdx.x, b = blockIdx.x;
  if (b >= 248) {
    const int pb = b - 248;
    const u16* w = pb < 32 ? wo + (size_t)pb * 32768 : pb < 64 ? w2 + (size_t)(pb - 32) * 32768 : w4 + (size_t)(pb - 64) * 32768;
    u32x4 v[16];
#pragma unroll
    for (int r = 0; r < 16; ++r) v[r] = *reinterpret_cast<const u32x4*>(w + r * 2048 + tid * 8);
#pragma unroll
    for (int r = 0; r < 16; ++r) asm volatile("" ::"v"(v[r]));
  } else if (b < 120) {
    const u16* w; float* dst; int ld, c0;
    if (b < 48) { w = wqkv; dst = cs_qkv; ld = 3072; c0 = b * 64; }
    else if (b < 64) { w = w1; dst = cs_w1; ld = 1024; c0 = (b - 48) * 64; }
    else { w = w3; dst = cs_w3; ld = F_; c0 = (b - 64) * 64; }
    const int cg = tid & 7, rg = tid >> 3;
    float s[8] = {0.f, 0.f, 0.f, 0.f, 0.f, 0.f, 0.f, 0.f};
#pragma unroll 8
    for (int r = 0; r < 32; ++r) {
      u32x4 v = *reinterpret_cast<const u32x4*>(w + (size_t)(r * 32 + rg) * ld + c0 + cg * 8);
#pragma unroll
      for (int i = 0; i < 4; ++i) { s[2 * i] += bflo(v[i]); s[2 * i + 1] += bfhi(v[i]); }
    }
#pragma unroll
    for (int i = 0; i < 8; ++i) red[rg][cg * 8 + i] = s[i];
    __syncthreads();
    if (tid < 64) {
      float t = 0.f;
#pragma unroll
      for (int r = 0; r < 32; ++r) t += red[r][tid];
      dst[c0 + tid] = t;
    }
  } else {
    const int lane = tid & 63, wid = tid >> 6;
    const int rbase = (b - 120) * 16 + wid * 4;
#pragma unroll
    for (int rr = 0; rr < 4; ++rr) {
      const int row = rbase + rr;
#pragma unroll
      for (int hf = 0; hf < 2; ++hf) {
        u32x4 v = *reinterpret_cast<const u32x4*>(x + (size_t)row * D_ + hf * 512 + lane * 8);
        float y[8];
#pragma unroll
        for (int i = 0; i < 4; ++i) { y[2 * i] = bflo(v[i]); y[2 * i + 1] = bfhi(v[i]); }
        float sm = ((y[0] + y[1]) + (y[2] + y[3])) + ((y[4] + y[5]) + (y[6] + y[7]));
        sm = rsum8(sm);
        float mu = sm * (1.0f / 64.f), m2 = 0.f;
#pragma unroll
        for (int i = 0; i < 8; ++i) { float d = y[i] - mu; m2 += d * d; }
        m2 = rsum8(m2);
        if ((lane & 7) == 0) st[(size_t)row * 16 + hf * 8 + (lane >> 3)] = (f32x2){sm, m2};
      }
    }
  }
}

extern "C" void kernel_launch(void* const* d_in, const int* in_sizes, int n_in, void* d_out, int out_size, void* d_ws,
                              size_t ws_size, hipStream_t stream) {
  const u16* x_in = (const u16*)d_in[0];
  const u16* wqkv = (const u16*)d_in[1];
  const u16* wphi = (const u16*)d_in[2];
  const u16* wo = (const u16*)d_in[3];
  const u16* w1 = (const u16*)d_in[5];
  const u16* w2 = (const u16*)d_in[6];
  const u16* w3 = (const u16*)d_in[7];
  const u16* w4 = (const u16*)d_in[8];
  char* ws = (char*)d_ws; for (int i = 0; i < 9; ++i) fprintf(stderr, "in%d %p\n", i, d_in[i]); fprintf(stderr, "out %p ws %p\n", d_out, d_ws);
  size_t off = 0;
  auto alloc = [&](size_t bytes) { char* p = ws + off; off += (bytes + 4095) & ~(size_t)4095; return p; };
  u16* xb = (u16*)alloc((size_t)S_ * D_ * 2);
  u16* x2 = (u16*)alloc((size_t)S_ * D_ * 2);
  u16* x1 = (u16*)alloc((size_t)S_ * D_ * 2);
  u16* hs = (u16*)alloc((size_t)S_ * D_ * 2);
  u16* ob = (u16*)alloc((size_t)S_ * D_ * 2);
  u16* gb = (u16*)alloc((size_t)S_ * F_ * 2);
  u16* phiq = (u16*)alloc((size_t)S_ * 2048 * 2);
  float* kvp = (float*)alloc((size_t)32 * 16 * 2048 * 4);
  float* zp = (float*)alloc((size_t)32 * 16 * 64 * 4);
  f32x2* st_x = (f32x2*)alloc((size_t)S_ * 16 * 8);
  f32x2* st_x2 = (f32x2*)alloc((size_t)S_ * 16 * 8);
  f32x2* st_x1 = (f32x2*)alloc((size_t)S_ * 16 * 8);
  float* cs_qkv = (float*)alloc(3072 * 4);
  float* cs_w1 = (float*)alloc(1024 * 4);
  float* cs_w3 = (float*)alloc(F_ * 4);

  prep_kernel<<<248 + 176, 256, 0, stream>>>(x_in, wqkv, w1, w3, cs_qkv, cs_w1, cs_w3, st_x, wo, w2, w4);
  for (int layer = 0; layer < 2; ++layer) {
    const u16* x = layer == 0 ? x_in : xb;
    u16* xo = layer == 0 ? xb : (u16*)d_out;
    gemm_kernel<192, 3, 1024, 1024, 3072, EPI_QKV><<<256, 512, 0, stream>>>(x, wqkv, cs_qkv, st_x, nullptr, nullptr, phiq, nullptr, wphi, kvp, zp);
    attn_out_kernel<<<256, 256, 0, stream>>>(phiq, kvp, zp, ob);
    gemm_kernel<64, 5, 1024, 1024, 1024, EPI_WO><<<256, 512, 0, stream>>>(ob, wo, nullptr, nullptr, x, nullptr, x2, st_x2, nullptr, nullptr, nullptr);
    gemm_kernel<64, 5, 1024, 1024, 1024, EPI_W1><<<256, 512, 0, stream>>>(x2, w1, cs_w1, st_x2, nullptr, nullptr, hs, nullptr, nullptr, nullptr, nullptr);
    gemm_kernel<64, 5, 1024, 1024, 1024, EPI_W2><<<256, 512, 0, stream>>>(hs, w2, nullptr, st_x2, x2, nullptr, x1, st_x1, nullptr, nullptr, nullptr);
    gemm_kernel<224, 3, 1024, 1024, F_, EPI_W3><<<256, 512, 0, stream>>>(x1, w3, cs_w3, st_x1, nullptr, nullptr, gb, nullptr, nullptr, nullptr, nullptr);
    gemm_kernel<64, 5, F_, F_, 1024, EPI_W4><<<256, 512, 0, stream>>>(gb, w4, nullptr, nullptr, x2, x1, xo, st_x, nullptr, nullptr, nullptr);
  }
}
```

```cpp
#include <hip/hip_runtime.h>
#include <hip/hip_bf16.h>
#include <stdint.h>
#include <type_traits>

typedef short bf16x8 __attribute__((ext_vector_type(8)));
typedef short s16x4 __attribute__((ext_vector_type(4)));
typedef float f32x4 __attribute__((ext_vector_type(4)));
typedef float f32x2 __attribute__((ext_vector_type(2)));
typedef unsigned u32x2 __attribute__((ext_vector_type(2)));
typedef unsigned u32x4 __attribute__((ext_vector_type(4)));
typedef __attribute__((address_space(3))) s16x4* lds_s16x4_p;
typedef unsigned short u16;

constexpr int S_ = 2048, D_ = 1024, H_ = 32, F_ = 3584;

__device__ __forceinline__ float bf2f(u16 v) { return __uint_as_float(((unsigned)v) << 16); }
__device__ __forceinline__ float bflo(unsigned v) { return __uint_as_float(v << 16); }
__device__ __forceinline__ float bfhi(unsigned v) { return __uint_as_float(v & 0xffff0000u); }
typedef __bf16 bf16x2_t __attribute__((ext_vector_type(2)));
__device__ __forceinline__ unsigned pk2(float a, float b) {
  return __builtin_bit_cast(unsigned, __builtin_convertvector((f32x2){a, b}, bf16x2_t));
}
__device__ __forceinline__ unsigned pk2v(f32x2 v) { return __builtin_bit_cast(unsigned, __builtin_convertvector(v, bf16x2_t)); }
__device__ __forceinline__ float max2_(float a, float b) { float r; asm("v_max_f32 %0, %1, %2" : "=v"(r) : "v"(a), "v"(b)); return r; }
__device__ __forceinline__ float min2_(float a, float b) { float r; asm("v_min_f32 %0, %1, %2" : "=v"(r) : "v"(a), "v"(b)); return r; }
__device__ __forceinline__ float max3_(float a, float b, float c) { float r; asm("v_max3_f32 %0, %1, %2, %3" : "=v"(r) : "v"(a), "v"(b), "v"(c)); return r; }
__device__ __forceinline__ float min3_(float a, float b, float c) { float r; asm("v_min3_f32 %0, %1, %2, %3" : "=v"(r) : "v"(a), "v"(b), "v"(c)); return r; }
__device__ __forceinline__ float rbf(float a) { return bflo(pk2(a, 0.f)); }
__device__ __forceinline__ float gelu_t(float x) {
  const float c1 = -2.0f * 0.7978845608028654f * 1.4426950408889634f;
  const float c2 = c1 * 0.044715f;
  float t = x * x;
  float w = x * __builtin_fmaf(t, c2, c1);
  float e = __builtin_amdgcn_exp2f(w);
  return x * __builtin_amdgcn_rcpf(1.0f + e);
}

__device__ __forceinline__ f32x2 gelu2(f32x2 x) {
  const float c1 = -2.0f * 0.7978845608028654f * 1.4426950408889634f;
  const float c2 = c1 * 0.044715f;
  f32x2 t = x * x;
  f32x2 w = x * (t * c2 + c1);
  f32x2 d = (f32x2){__builtin_amdgcn_exp2f(w[0]), __builtin_amdgcn_exp2f(w[1])} + 1.0f;
  return x * (f32x2){__builtin_amdgcn_rcpf(d[0]), __builtin_amdgcn_rcpf(d[1])};
}
__device__ __forceinline__ void x16(float v, float& a, float& b) {
  auto r = __builtin_amdgcn_permlane16_swap(__float_as_uint(v), __float_as_uint(v), false, false);
  a = __uint_as_float(r[0]); b = __uint_as_float(r[1]);
}
__device__ __forceinline__ void x32(float v, float& a, float& b) {
  auto r = __builtin_amdgcn_permlane32_swap(__float_as_uint(v), __float_as_uint(v), false, false);
  a = __uint_as_float(r[0]); b = __uint_as_float(r[1]);
}
__device__ __forceinline__ float rmax4(float v) { float a, b; x16(v, a, b); v = max2_(a, b); x32(v, a, b); return max2_(a, b); }
__device__ __forceinline__ float rmin4(float v) { float a, b; x16(v, a, b); v = min2_(a, b); x32(v, a, b); return min2_(a, b); }
__device__ __forceinline__ float rsum4(float v) { float a, b; x16(v, a, b); v = a + b; x32(v, a, b); return a + b; }

__device__ __forceinline__ float rsum8(float v) {
  v += __uint_as_float(__builtin_amdgcn_mov_dpp(__float_as_uint(v), 0xB1, 0xF, 0xF, true));
  v += __uint_as_float(__builtin_amdgcn_mov_dpp(__float_as_uint(v), 0x4E, 0xF, 0xF, true));
  v += __uint_as_float(__builtin_amdgcn_mov_dpp(__float_as_uint(v), 0x141, 0xF, 0xF, true));
  return v;
}
#define WAIT_V(n) asm volatile("s_waitcnt vmcnt(" #n ")" ::: "memory")
#define WAIT_L0() asm volatile("s_waitcnt lgkmcnt(0)" ::: "memory")
template <int N> __device__ __forceinline__ void wait_vm() {
  asm volatile("s_waitcnt vmcnt(%0)" ::"i"(N) : "memory");
}

__device__ __forceinline__ void glds16(unsigned voff, const void* sbase, unsigned lds) {
  unsigned keep;
  asm volatile("s_mov_b32 %0, m0\n\ts_mov_b32 m0, %3\n\ts_nop 0\n\tglobal_load_lds_dwordx4 %1, %2\n\ts_mov_b32 m0, %0"
               : "=&s"(keep) : "v"(voff), "s"(sbase), "s"(lds) : "memory");
}

__device__ __forceinline__ void mfma_acc(f32x4& c, const u32x4& a, const u32x4& b) {
  asm volatile("v_mfma_f32_16x16x32_bf16 %0, %1, %2, %0" : "+a"(c) : "v"(a), "v"(b));
}

#define STAMP(i)

enum { EPI_QKV = 0, EPI_WO = 1, EPI_W1 = 2, EPI_W2 = 3, EPI_W3 = 4, EPI_W4 = 5 };

template <int BN, int NST, int KD, int LDA, int LDB, int EPI>
__global__ __launch_bounds__(512, 2) void gemm_kernel(
    const u16* __restrict__ A, const u16* __restrict__ W,
    const u16* __restrict__ Wc,
    const float* __restrict__ cs,
    const f32x2* __restrict__ st_in,
    const u16* __restrict__ r0,
    const u16* __restrict__ r1,
    u16* __restrict__ out,
    f32x2* __restrict__ st_out,
    const u16* __restrict__ wphi,
    float* __restrict__ kvp,
    float* __restrict__ zp)
{
  constexpr int NFULL = BN / 64, HALF = (BN % 64) / 32;
  constexpr int A_BYTES = 128 * 64 * 2, B_BYTES = BN * 64 * 2, STAGE = A_BYTES + B_BYTES;
  constexpr int NT = KD / 64, NTW = 2 * NFULL + HALF, LPS = 4 + 2 * NFULL + HALF;
  constexpr int PB = BN * 2 + 16;
  constexpr int EPI_BYTES = (EPI == EPI_QKV) ? (4 * 64 * 144 + 2 * (64 * 32 + 64) * 4) : 128 * PB;
  constexpr int MAINB = NST * STAGE;
  constexpr int RS_OFF = (MAINB > EPI_BYTES ? MAINB : EPI_BYTES);
  constexpr int CS_OFF = RS_OFF + 128 * 16;
  constexpr int WP_OFF = CS_OFF + 1024;
  constexpr int SMEM = WP_OFF + (EPI == EPI_QKV ? 4096 : 0);
  constexpr bool HAS_CS = (EPI == EPI_QKV || EPI == EPI_W1 || EPI == EPI_W3);
  constexpr bool HAS_ST = (EPI == EPI_QKV || EPI == EPI_W1 || EPI == EPI_W3 || EPI == EPI_W2);
  constexpr bool HAS_RES = (EPI == EPI_WO || EPI == EPI_W2 || EPI == EPI_W4);
  constexpr int LAG = (BN == 64) ? 1 : 0;
  constexpr int CH = BN / 8, NIT = CH / 4;
  static_assert(SMEM <= 163840, "LDS");
  __shared__ __attribute__((aligned(16))) char smem[SMEM];

  STAMP(0);
  const int tid = threadIdx.x, lane = tid & 63;
  const int wid8 = __builtin_amdgcn_readfirstlane(tid >> 6), wid = wid8 & 3, wm = wid >> 1, wn = wid & 1;
  const int g = lane >> 4, l15 = lane & 15;
  const int xcd = blockIdx.x & 7, idx = blockIdx.x >> 3;
  const int tm = (xcd >> 1) * 4 + (idx & 3), tn = (xcd & 1) * 8 + (idx >> 2);
  const int row0 = tm * 128;
  f32x4* rs = reinterpret_cast<f32x4*>(smem + RS_OFF);

  f32x4 acc[4][NTW];
  u32x4 ra[HAS_RES ? NIT : 1], rb[(EPI == EPI_W4) ? NIT : 1];

  if (wid8 >= 4) {
    unsigned voA[4], voB[NFULL > 0 ? NFULL : 1][2], voH = 0;
    const bool pk_ = (HALF == 0) && ((tn & 7) == 3);
    const unsigned pitchB = pk_ ? NFULL * 128 : LDB * 2;
#pragma unroll
    for (int i = 0; i < 4; ++i) {
      int r = (wid * 4 + i) * 8 + (lane >> 3);
      int ch = (lane & 7) ^ ((r >> 1) & 7);
      voA[i] = (unsigned)(row0 + r) * (LDA * 2) + ch * 16;
    }
#pragma unroll
    for (int b = 0; b < NFULL; ++b) {
      int colb = (EPI == EPI_QKV) ? (b * 1024 + tn * 64) : (tn * BN + b * 64);
#pragma unroll
      for (int j = 0; j < 2; ++j) {
        int r = (wid * 2 + j) * 8 + (lane >> 3);
        int f = ((r >> 1) & 1) | (((r >> 3) & 1) << 1);
        int chp = lane & 7;
        int gch = (((chp >> 1) ^ f) << 1) | (chp & 1);
        voB[b][j] = (unsigned)r * pitchB + (pk_ ? b * 128 : colb * 2) + gch * 16;
      }
    }
    if constexpr (HALF) {
      int r = wid * 16 + (lane >> 2);
      int chp = lane & 3;
      int gch = (((chp >> 1) ^ ((r >> 3) & 1)) << 1) | (chp & 1);
      voH = (unsigned)r * (LDB * 2) + (tn * BN + NFULL * 64) * 2 + gch * 16;
    }
    const char* Ab = reinterpret_cast<const char*>(A);
    const char* Wb = pk_ ? reinterpret_cast<const char*>(Wc) + (size_t)(tn >> 3) * (KD * NFULL * 128) : reinterpret_cast<const char*>(W);
    const unsigned lbase = (unsigned)(uintptr_t)(__attribute__((address_space(3))) char*)smem;
    auto stage = [&](int kt, int buf) {
      const char* a = Ab + (size_t)kt * 128;
      const char* w = Wb + (size_t)(kt * (64 * pitchB));
      const unsigned s = lbase + buf * STAGE;
#pragma unroll
      for (int i = 0; i < 4; ++i) glds16(voA[i], a, s + (wid * 4 + i) * 1024);
#pragma unroll
      for (int b = 0; b < NFULL; ++b)
#pragma unroll
        for (int j = 0; j < 2; ++j) glds16(voB[b][j], w, s + A_BYTES + b * 8192 + (wid * 2 + j) * 1024);
      if constexpr (HALF) glds16(voH, w, s + A_BYTES + NFULL * 8192 + wid * 1024);
    };
#pragma unroll
    for (int s = 0; s < NST; ++s) stage(s, s);
    constexpr int XL = 0;
    f32x4 stp[4];
    const int lt = tid - 256;
    f32x4 csx;
    u32x4 wpx;
    if constexpr (HAS_CS) {
      const int c4 = lt < BN / 4 ? lt : 0;
      csx = *reinterpret_cast<const f32x4*>(cs + (EPI == EPI_QKV ? (c4 >> 4) * 1024 + tn * 64 + (c4 & 15) * 4 : tn * BN + c4 * 4));
    }
    if constexpr (EPI == EPI_QKV) wpx = *reinterpret_cast<const u32x4*>(wphi + tn * 2048 + lt * 8);
    if constexpr (HAS_ST) {
      const f32x4* sp = reinterpret_cast<const f32x4*>(st_in + (size_t)(row0 + (lt >> 1)) * 16 + (lt & 1) * 8);
#pragma unroll
      for (int i = 0; i < 4; ++i) stp[i] = sp[i];
    }
    if constexpr (HAS_RES) {
#pragma unroll
      for (int it = 0; it < NIT; ++it) {
        const int c = tid + 512 * it, r = c / CH, ch = c % CH;
        ra[it] = *reinterpret_cast<const u32x4*>(r0 + (size_t)(row0 + r) * D_ + tn * BN + ch * 8);
        if constexpr (EPI == EPI_W4) rb[it] = *reinterpret_cast<const u32x4*>(r1 + (size_t)(row0 + r) * D_ + tn * BN + ch * 8);
      }
    }
    asm volatile("" ::: "memory");
    wait_vm<(NST - 1 - LAG) * LPS>();
    __builtin_amdgcn_s_barrier();
    int buf = 0;
    for (int kt = 0; kt < NT - NST; ++kt) {
      wait_vm<(NST - 2 - LAG) * LPS>();
      __builtin_amdgcn_s_barrier();
      stage(kt + NST, buf);
      buf = buf + 1 == NST ? 0 : buf + 1;
    }
    asm volatile("" ::: "memory");
#pragma unroll
    for (int r = 0; r < NST - 1; ++r) {
      constexpr int NSL = NST - LAG;
      if (r == 0) wait_vm<(NSL - 2) * LPS + XL>();
      else if (r == 1) wait_vm<(NSL > 3 ? NSL - 3 : 0) * LPS + XL>();
      else if (r == 2) wait_vm<(NSL > 4 ? NSL - 4 : 0) * LPS + XL>();
      else if (r == 3) wait_vm<(NSL > 5 ? NSL - 5 : 0) * LPS + XL>();
      else wait_vm<XL>();
      __builtin_amdgcn_s_barrier();
    }
    if constexpr (HAS_ST) {
#pragma unroll
      for (int i = 0; i < 4; ++i) asm volatile("" : "+v"(stp[i]));
      float sm = 0.f;
#pragma unroll
      for (int i = 0; i < 4; ++i) sm += stp[i][0] + stp[i][2];
      sm += __shfl_xor(sm, 1);
      float mean = sm * (1.0f / 1024.f);
      float m2 = 0.f;
#pragma unroll
      for (int i = 0; i < 4; ++i) {
        float d0 = stp[i][0] * (1.0f / 64.f) - mean, d1 = stp[i][2] * (1.0f / 64.f) - mean;
        m2 += stp[i][1] + stp[i][3] + 64.f * (d0 * d0 + d1 * d1);
      }
      m2 += __shfl_xor(m2, 1);
      float var = m2 * (1.0f / 1024.f);
      float r1_ = 1.0f / sqrtf(var + 1e-5f);
      float sc = r1_;
      if constexpr (EPI == EPI_W1) sc = r1_ / sqrtf(var * r1_ * r1_ + 1e-5f);
      if ((lt & 1) == 0) rs[lt >> 1] = (f32x4){mean, sc, r1_, 0.f};
    }
    if constexpr (HAS_CS) {
      if (lt < BN / 4) *reinterpret_cast<f32x4*>(smem + CS_OFF + lt * 16) = csx;
    }
    if constexpr (EPI == EPI_QKV) *reinterpret_cast<u32x4*>(smem + WP_OFF + lt * 16) = wpx;
  } else {
    if constexpr (HAS_RES) {
#pragma unroll
      for (int it = 0; it < NIT; ++it) {
        const int c = tid + 512 * it, r = c / CH, ch = c % CH;
        ra[it] = *reinterpret_cast<const u32x4*>(r0 + (size_t)(row0 + r) * D_ + tn * BN + ch * 8);
        if constexpr (EPI == EPI_W4) rb[it] = *reinterpret_cast<const u32x4*>(r1 + (size_t)(row0 + r) * D_ + tn * BN + ch * 8);
      }
    }
    const int q = l15 >> 2, p = lane & 3;
    const int fB = (q >> 1) | ((g & 1) << 1);
    unsigned aoff[2], boff[2], hoff = 0;
#pragma unroll
    for (int ks = 0; ks < 2; ++ks) aoff[ks] = (wm * 64 + l15) * 128 + (((4 * ks + g) ^ (l15 >> 1)) * 16);
#pragma unroll
    for (int u = 0; u < 2; ++u) boff[u] = A_BYTES + (8 * g + q) * 128 + (((2 * wn + u) ^ fB) * 32) + p * 8;
    if constexpr (HALF) hoff = A_BYTES + NFULL * 8192 + (8 * g + q) * 64 + ((wn ^ (g & 1)) * 32) + p * 8;

    auto ldf = [&](int i, const char* s, int ks, u32x4(&af)[4], u32x4(&bfr)[NTW]) {
      if (i == 0 || i > NTW) {
        const int mt = i == 0 ? 0 : i - NTW;
        af[mt] = *reinterpret_cast<const u32x4*>(s + aoff[ks] + mt * 2048);
      } else if (i - 1 < 2 * NFULL) {
        const int b = (i - 1) >> 1, u = (i - 1) & 1;
        s16x4 lo = __builtin_amdgcn_ds_read_tr16_b64_v4i16((lds_s16x4_p)(s + boff[u] + b * 8192 + ks * 4096));
        s16x4 hi = __builtin_amdgcn_ds_read_tr16_b64_v4i16((lds_s16x4_p)(s + boff[u] + b * 8192 + ks * 4096 + 512));
        bfr[i - 1] = __builtin_shufflevector(__builtin_bit_cast(u32x2, lo), __builtin_bit_cast(u32x2, hi), 0, 1, 2, 3);
      } else {
        s16x4 lo = __builtin_amdgcn_ds_read_tr16_b64_v4i16((lds_s16x4_p)(s + hoff + ks * 2048));
        s16x4 hi = __builtin_amdgcn_ds_read_tr16_b64_v4i16((lds_s16x4_p)(s + hoff + ks * 2048 + 256));
        bfr[NTW - 1] = __builtin_shufflevector(__builtin_bit_cast(u32x2, lo), __builtin_bit_cast(u32x2, hi), 0, 1, 2, 3);
      }
    };
    constexpr int NM = 4 * NTW, NL = NTW + 4;
    auto phase = [&](u32x4(&afc)[4], u32x4(&bfc)[NTW], u32x4(&afn)[4], u32x4(&bfn)[NTW], int nbuf, int nks, auto rd, auto bar) {
      const char* s = smem + nbuf * STAGE;
      constexpr int BAR = decltype(bar)::value, R0 = BAR < 0 ? 0 : BAR + 1;
      constexpr int RSP = (NM - R0 - 2) / NL > 0 ? (NM - R0 - 2) / NL : 1;
      static_assert(R0 + (NL - 1) * RSP < NM, "reads");
#pragma unroll
      for (int mt = 0; mt < 4; ++mt)
#pragma unroll
        for (int j = 0; j < NTW; ++j) {
          const int m = mt * NTW + j;
          if (m == BAR) {
            WAIT_L0();
            __builtin_amdgcn_s_barrier();
            asm volatile("" ::: "memory");
          }
          if (decltype(rd)::value && m >= R0 && (m - R0) % RSP == 0 && (m - R0) / RSP < NL) ldf((m - R0) / RSP, s, nks, afn, bfn);
          if (EPI == EPI_QKV && j >= 4) mfma_acc(acc[mt][j], afc[mt], bfc[j]);
          else mfma_acc(acc[mt][j], bfc[j], afc[mt]);
          __builtin_amdgcn_sched_barrier(0);
        }
    };
    typedef std::integral_constant<int, -1> nobar;
#pragma unroll
    for (int mt = 0; mt < 4; ++mt)
#pragma unroll
      for (int j = 0; j < NTW; ++j) {
        acc[mt][j] = (f32x4){0.f, 0.f, 0.f, 0.f};
        asm volatile("" : "+a"(acc[mt][j]));
      }
    u32x4 af0[4], af1[4], bf0[NTW], bf1[NTW];
    __builtin_amdgcn_s_barrier();
    asm volatile("" ::: "memory");
    STAMP(1);
#pragma unroll
    for (int i = 0; i < NL; ++i) { ldf(i, smem, 0, af0, bf0); __builtin_amdgcn_sched_barrier(0); }
    int buf = 0;
    if constexpr (LAG) {
      phase(af0, bf0, af1, bf1, 0, 1, std::true_type{}, nobar{});
      for (int kt = 0; kt < NT - 1; ++kt) {
        buf = buf + 1 == NST ? 0 : buf + 1;
        phase(af1, bf1, af0, bf0, buf, 0, std::true_type{}, nobar{});
        asm volatile("" ::: "memory");
        __builtin_amdgcn_s_barrier();
        asm volatile("" ::: "memory");
        phase(af0, bf0, af1, bf1, buf, 1, std::true_type{}, nobar{});
      }
      phase(af1, bf1, af0, bf0, buf, 0, std::false_type{}, nobar{});
    } else {
      for (int kt = 0; kt < NT - 1; ++kt) {
        phase(af0, bf0, af1, bf1, buf, 1, std::true_type{}, nobar{});
        buf = buf + 1 == NST ? 0 : buf + 1;
        phase(af1, bf1, af0, bf0, buf, 0, std::true_type{}, std::integral_constant<int, 4>{});
      }
      phase(af0, bf0, af1, bf1, buf, 1, std::true_type{}, nobar{});
      phase(af1, bf1, af0, bf0, buf, 0, std::false_type{}, nobar{});
    }
    asm volatile("s_nop 7\n\ts_nop 7\n\ts_nop 7" ::: "memory");
  }
  WAIT_L0();
  __builtin_amdgcn_s_barrier();
  asm volatile("" ::: "memory");
  STAMP(2);

  if constexpr (EPI == EPI_QKV) {
    const int h = tn * 2 + wn;
    const int q = l15 >> 2, p = lane & 3;
    f32x4 kv[4][2], zz[4];
    float* red = reinterpret_cast<float*>(smem + 4 * 64 * 144) + wn * (64 * 32 + 64);
    if (wid8 < 4) {
      bf16x8 wf[2];
#pragma unroll
      for (int ft = 0; ft < 2; ++ft) {
        const char* src = smem + WP_OFF + wn * 2048 + (4 * g + q) * 64 + ft * 32 + p * 8;
        s16x4 lo = __builtin_amdgcn_ds_read_tr16_b64_v4i16((lds_s16x4_p)(src));
        s16x4 hi = __builtin_amdgcn_ds_read_tr16_b64_v4i16((lds_s16x4_p)(src + 16 * 64));
        wf[ft] = __builtin_shufflevector(lo, hi, 0, 1, 2, 3, 4, 5, 6, 7);
      }
      const float* csl = reinterpret_cast<const float*>(smem + CS_OFF);
      f32x4 cs4[4];
      float csv[2];
#pragma unroll
      for (int t = 0; t < 2; ++t) {
        cs4[t] = *reinterpret_cast<const f32x4*>(csl + wn * 32 + t * 16 + 4 * g);
        cs4[2 + t] = *reinterpret_cast<const f32x4*>(csl + 64 + wn * 32 + t * 16 + 4 * g);
        csv[t] = csl[128 + wn * 32 + t * 16 + l15];
      }
      bf16x8 qf[4], kf[4], vf[2][2];
#pragma unroll
      for (int mt = 0; mt < 4; ++mt) {
        f32x4 st = rs[wm * 64 + mt * 16 + l15];
        unsigned pq_[4], pk_[4];
#pragma unroll
        for (int t = 0; t < 2; ++t) {
          float a[4], b[4];
#pragma unroll
          for (int i = 0; i < 4; ++i) {
            a[i] = st[1] * (acc[mt][t][i] - st[0] * cs4[t][i]);
            b[i] = st[1] * (acc[mt][2 + t][i] - st[0] * cs4[2 + t][i]);
          }
          pq_[t * 2] = pk2(a[0], a[1]); pq_[t * 2 + 1] = pk2(a[2], a[3]);
          pk_[t * 2] = pk2(b[0], b[1]); pk_[t * 2 + 1] = pk2(b[2], b[3]);
        }
        qf[mt] = __builtin_bit_cast(bf16x8, (u32x4){pq_[0], pq_[1], pq_[2], pq_[3]});
        kf[mt] = __builtin_bit_cast(bf16x8, (u32x4){pk_[0], pk_[1], pk_[2], pk_[3]});
      }
#pragma unroll
      for (int s = 0; s < 2; ++s) {
        unsigned pv[2][4];
#pragma unroll
        for (int m2 = 0; m2 < 2; ++m2) {
          int mt = s * 2 + m2;
          f32x4 s0 = rs[wm * 64 + mt * 16 + 4 * g + 0], s1 = rs[wm * 64 + mt * 16 + 4 * g + 1];
          f32x4 s2 = rs[wm * 64 + mt * 16 + 4 * g + 2], s3 = rs[wm * 64 + mt * 16 + 4 * g + 3];
#pragma unroll
          for (int t = 0; t < 2; ++t) {
            float a0 = s0[1] * (acc[mt][4 + t][0] - s0[0] * csv[t]);
            float a1 = s1[1] * (acc[mt][4 + t][1] - s1[0] * csv[t]);
            float a2 = s2[1] * (acc[mt][4 + t][2] - s2[0] * csv[t]);
            float a3 = s3[1] * (acc[mt][4 + t][3] - s3[0] * csv[t]);
            pv[t][m2 * 2] = pk2(a0, a1); pv[t][m2 * 2 + 1] = pk2(a2, a3);
          }
        }
#pragma unroll
        for (int t = 0; t < 2; ++t) vf[s][t] = __builtin_bit_cast(bf16x8, (u32x4){pv[t][0], pv[t][1], pv[t][2], pv[t][3]});
      }
      char* pkl = smem + wid * (64 * 144);
      const float L2E = 1.4426950408889634f;
#pragma unroll
      for (int which = 0; which < 2; ++which)
#pragma unroll
        for (int mt = 0; mt < 4; ++mt) {
          f32x4 z4 = (f32x4){0.f, 0.f, 0.f, 0.f};
          f32x4 p0 = __builtin_amdgcn_mfma_f32_16x16x32_bf16(wf[0], which ? kf[mt] : qf[mt], z4, 0, 0, 0);
          f32x4 p1 = __builtin_amdgcn_mfma_f32_16x16x32_bf16(wf[1], which ? kf[mt] : qf[mt], z4, 0, 0, 0);
          f32x2 v[4];
          {
            const unsigned r0_ = pk2(p0[0], p0[1]), r1_ = pk2(p0[2], p0[3]), r2_ = pk2(p1[0], p1[1]), r3_ = pk2(p1[2], p1[3]);
            v[0] = (f32x2){bflo(r0_), bfhi(r0_)}; v[1] = (f32x2){bflo(r1_), bfhi(r1_)};
            v[2] = (f32x2){bflo(r2_), bfhi(r2_)}; v[3] = (f32x2){bflo(r3_), bfhi(r3_)};
          }
          float mx = max2_(max3_(max3_(v[0][0], v[0][1], v[1][0]), v[1][1], v[2][0]), max3_(v[2][1], v[3][0], v[3][1]));
          float mn = min2_(min3_(min3_(v[0][0], v[0][1], v[1][0]), v[1][1], v[2][0]), min3_(v[2][1], v[3][0], v[3][1]));
          mx = rmax4(mx); mn = rmin4(mn);
          const float mxl = -mx * L2E, mnl = mn * L2E;
          f32x2 ep[4], en[4];
#pragma unroll
          for (int i = 0; i < 4; ++i) {
            const f32x2 ap = v[i] * L2E + mxl, an = v[i] * (-L2E) + mnl;
            ep[i] = (f32x2){__builtin_amdgcn_exp2f(ap[0]), __builtin_amdgcn_exp2f(ap[1])};
            en[i] = (f32x2){__builtin_amdgcn_exp2f(an[0]), __builtin_amdgcn_exp2f(an[1])};
          }
          const f32x2 sp2 = (ep[0] + ep[1]) + (ep[2] + ep[3]), sn2 = (en[0] + en[1]) + (en[2] + en[3]);
          const float ip = __builtin_amdgcn_rcpf(rsum4(sp2[0] + sp2[1])), in_ = __builtin_amdgcn_rcpf(rsum4(sn2[0] + sn2[1]));
          u32x2 o[4];
          o[0] = (u32x2){pk2v(ep[0] * ip), pk2v(ep[1] * ip)};
          o[1] = (u32x2){pk2v(ep[2] * ip), pk2v(ep[3] * ip)};
          o[2] = (u32x2){pk2v(en[0] * in_), pk2v(en[1] * in_)};
          o[3] = (u32x2){pk2v(en[2] * in_), pk2v(en[3] * in_)};
          if (which == 0) {
            u16* dst = out + ((size_t)(row0 + wm * 64 + mt * 16 + l15) * 32 + h) * 64 + 4 * g;
#pragma unroll
            for (int c = 0; c < 4; ++c) *reinterpret_cast<u32x2*>(dst + c * 16) = o[c];
          } else {
            char* dst = pkl + (mt * 16 + l15) * 144 + g * 8;
#pragma unroll
            for (int c = 0; c < 4; ++c) *reinterpret_cast<u32x2*>(dst + c * 32) = o[c];
          }
        }
      WAIT_L0();
      __builtin_amdgcn_sched_barrier(0);
#pragma unroll
      for (int ft = 0; ft < 4; ++ft) { kv[ft][0] = kv[ft][1] = zz[ft] = (f32x4){0.f, 0.f, 0.f, 0.f}; }
      const bf16x8 ones = (bf16x8){0x3F80, 0x3F80, 0x3F80, 0x3F80, 0x3F80, 0x3F80, 0x3F80, 0x3F80};
#pragma unroll
      for (int s = 0; s < 2; ++s)
#pragma unroll
        for (int ft = 0; ft < 4; ++ft) {
          const char* src = pkl + (32 * s + 4 * g + q) * 144 + ft * 32 + p * 8;
          s16x4 lo = __builtin_amdgcn_ds_read_tr16_b64_v4i16((lds_s16x4_p)(src));
          s16x4 hi = __builtin_amdgcn_ds_read_tr16_b64_v4i16((lds_s16x4_p)(src + 16 * 144));
          bf16x8 pf = __builtin_shufflevector(lo, hi, 0, 1, 2, 3, 4, 5, 6, 7);
          kv[ft][0] = __builtin_amdgcn_mfma_f32_16x16x32_bf16(vf[s][0], pf, kv[ft][0], 0, 0, 0);
          kv[ft][1] = __builtin_amdgcn_mfma_f32_16x16x32_bf16(vf[s][1], pf, kv[ft][1], 0, 0, 0);
          zz[ft] = __builtin_amdgcn_mfma_f32_16x16x32_bf16(ones, pf, zz[ft], 0, 0, 0);
        }
      if (wm == 1) {
#pragma unroll
        for (int ft = 0; ft < 4; ++ft) {
#pragma unroll
          for (int dt = 0; dt < 2; ++dt) *reinterpret_cast<f32x4*>(red + (ft * 16 + l15) * 32 + dt * 16 + 4 * g) = kv[ft][dt];
          if (g == 0) red[64 * 32 + ft * 16 + l15] = zz[ft][0];
        }
      }
    }
    __syncthreads();
    if (wid8 < 2) {
      float* kd = kvp + (size_t)(h * 16 + tm) * 2048;
      float* zd = zp + (size_t)(h * 16 + tm) * 64;
#pragma unroll
      for (int ft = 0; ft < 4; ++ft) {
#pragma unroll
        for (int dt = 0; dt < 2; ++dt) {
          f32x4 o = *reinterpret_cast<const f32x4*>(red + (ft * 16 + l15) * 32 + dt * 16 + 4 * g);
          *reinterpret_cast<f32x4*>(kd + (ft * 16 + l15) * 32 + dt * 16 + 4 * g) = o + kv[ft][dt];
        }
        if (g == 0) zd[ft * 16 + l15] = red[64 * 32 + ft * 16 + l15] + zz[ft][0];
      }
    }
  } else {
    if (wid8 < 4) {
      f32x4 stm[4], cs4[NTW];
      if constexpr (EPI == EPI_W1 || EPI == EPI_W3) {
#pragma unroll
        for (int j = 0; j < NTW; ++j) {
          const int colj = (j < 2 * NFULL) ? ((j >> 1) * 64 + (2 * wn + (j & 1)) * 16) : (NFULL * 64 + wn * 16);
          cs4[j] = *reinterpret_cast<const f32x4*>(smem + CS_OFF + (colj + 4 * g) * 4);
        }
#pragma unroll
        for (int mt = 0; mt < 4; ++mt) stm[mt] = rs[wm * 64 + mt * 16 + l15];
      }
#pragma unroll
      for (int j = 0; j < NTW; ++j) {
        const int colj = (j < 2 * NFULL) ? ((j >> 1) * 64 + (2 * wn + (j & 1)) * 16) : (NFULL * 64 + wn * 16);
#pragma unroll
        for (int mt = 0; mt < 4; ++mt) {
          f32x2 v0 = (f32x2){acc[mt][j][0], acc[mt][j][1]}, v1 = (f32x2){acc[mt][j][2], acc[mt][j][3]};
          if constexpr (EPI == EPI_W1 || EPI == EPI_W3) {
            v0 = gelu2((v0 - stm[mt][0] * (f32x2){cs4[j][0], cs4[j][1]}) * stm[mt][1]);
            v1 = gelu2((v1 - stm[mt][0] * (f32x2){cs4[j][2], cs4[j][3]}) * stm[mt][1]);
          }
          *reinterpret_cast<u32x2*>(smem + (wm * 64 + mt * 16 + l15) * PB + (colj + 4 * g) * 2) = (u32x2){pk2(v0[0], v0[1]), pk2(v1[0], v1[1])};
        }
      }
    }
    __syncthreads();
#pragma unroll
    for (int it = 0; it < NIT; ++it) {
      const int c = tid + 512 * it, r = c / CH, ch = c % CH;
      u32x4 v = *reinterpret_cast<const u32x4*>(smem + r * PB + ch * 16);
      const int grow = row0 + r, gcol = tn * BN + ch * 8;
      if constexpr (EPI == EPI_W3) {
        *reinterpret_cast<u32x4*>(out + (size_t)grow * F_ + gcol) = v;
      } else if constexpr (EPI == EPI_W1) {
        const int grp = tn >> 2;
        const int sh = grp == 0 ? 1 : grp == 1 ? -1 : grp == 2 ? 2 : -2;
        const int dr = grow + sh;
        if (dr >= 0 && dr < S_) *reinterpret_cast<u32x4*>(out + (size_t)dr * D_ + gcol) = v;
        const int zr = grow - sh;
        if (zr < 0 || zr >= S_) *reinterpret_cast<u32x4*>(out + (size_t)grow * D_ + gcol) = (u32x4){0u, 0u, 0u, 0u};
      } else {
        float y[8];
        const u32x4 a = ra[it];
        if constexpr (EPI == EPI_WO) {
#pragma unroll
          for (int i = 0; i < 4; ++i) { y[2 * i] = rbf(bflo(a[i]) + bflo(v[i])); y[2 * i + 1] = rbf(bfhi(a[i]) + bfhi(v[i])); }
        } else if constexpr (EPI == EPI_W2) {
          f32x4 st = rs[r];
#pragma unroll
          for (int i = 0; i < 4; ++i) {
            y[2 * i] = rbf(rbf((bflo(a[i]) - st[0]) * st[2]) + bflo(v[i]));
            y[2 * i + 1] = rbf(rbf((bfhi(a[i]) - st[0]) * st[2]) + bfhi(v[i]));
          }
        } else {
          const u32x4 b = rb[it];
#pragma unroll
          for (int i = 0; i < 4; ++i) {
            y[2 * i] = rbf(bflo(a[i]) + rbf(bflo(b[i]) + bflo(v[i])));
            y[2 * i + 1] = rbf(bfhi(a[i]) + rbf(bfhi(b[i]) + bfhi(v[i])));
          }
        }
        *reinterpret_cast<u32x4*>(out + (size_t)grow * D_ + gcol) = (u32x4){pk2(y[0], y[1]), pk2(y[2], y[3]), pk2(y[4], y[5]), pk2(y[6], y[7])};
        float sm = ((y[0] + y[1]) + (y[2] + y[3])) + ((y[4] + y[5]) + (y[6] + y[7]));
        sm = rsum8(sm);
        float mu = sm * (1.0f / 64.f), m2 = 0.f;
#pragma unroll
        for (int i = 0; i < 8; ++i) { float d = y[i] - mu; m2 += d * d; }
        m2 = rsum8(m2);
        if (ch == 0) st_out[(size_t)grow * 16 + tn] = (f32x2){sm, m2};
      }
    }
  }
  STAMP(3);
}

__global__ __launch_bounds__(256, 1) void attn_out_kernel(const u16* __restrict__ phiq, const float* __restrict__ kvp,
                                                          const float* __restrict__ zp, u16* __restrict__ o) {
  __shared__ __attribute__((aligned(16))) u16 kvT[48 * 72];
  const int tid = threadIdx.x, lane = tid & 63, wid = tid >> 6, g = lane >> 4, l15 = lane & 15;
  const int h = blockIdx.x & 31, tg = blockIdx.x >> 5;
  f32x4 kvv[16][2];
  float zv[16];
  bf16x8 pq[4][2];
  {
    const float* src = kvp + (size_t)h * 16 * 2048 + tid * 8;
#pragma unroll
    for (int t = 0; t < 16; ++t) {
      kvv[t][0] = *reinterpret_cast<const f32x4*>(src + t * 2048);
      kvv[t][1] = *reinterpret_cast<const f32x4*>(src + t * 2048 + 4);
    }
#pragma unroll
    for (int t = 0; t < 16; ++t) zv[t] = zp[(size_t)(h * 16 + t) * 64 + lane];
#pragma unroll
    for (int mt = 0; mt < 4; ++mt) {
      const u16* ps = phiq + ((size_t)(tg * 256 + wid * 64 + mt * 16 + l15) * 32 + h) * 64 + g * 8;
      pq[mt][0] = *reinterpret_cast<const bf16x8*>(ps);
      pq[mt][1] = *reinterpret_cast<const bf16x8*>(ps + 32);
    }
  }
  __builtin_amdgcn_sched_barrier(0);
  {
    for (int i = tid; i < 15 * 64; i += 256) kvT[(33 + i / 64) * 72 + (i & 63)] = 0;
    f32x4 a0 = kvv[0][0], a1 = kvv[0][1];
#pragma unroll
    for (int t = 1; t < 16; ++t) { a0 += kvv[t][0]; a1 += kvv[t][1]; }
    const int f = tid >> 2, d0 = (tid & 3) * 8;
#pragma unroll
    for (int i = 0; i < 4; ++i) {
      kvT[(d0 + i) * 72 + f] = (u16)(pk2(a0[i], 0.f) & 0xffff);
      kvT[(d0 + 4 + i) * 72 + f] = (u16)(pk2(a1[i], 0.f) & 0xffff);
    }
    if (tid < 64) {
      float z = 0.f;
#pragma unroll
      for (int t = 0; t < 16; ++t) z += zv[t];
      kvT[32 * 72 + tid] = (u16)(pk2(z, 0.f) & 0xffff);
    }
  }
  __syncthreads();
  bf16x8 kf[3][2];
#pragma unroll
  for (int dt = 0; dt < 3; ++dt)
#pragma unroll
    for (int ks = 0; ks < 2; ++ks) kf[dt][ks] = *reinterpret_cast<const bf16x8*>(&kvT[(dt * 16 + l15) * 72 + ks * 32 + g * 8]);
#pragma unroll
  for (int mt = 0; mt < 4; ++mt) {
    const int tok = tg * 256 + wid * 64 + mt * 16 + l15;
    const bf16x8 p0 = pq[mt][0], p1 = pq[mt][1];
    f32x4 r[3];
#pragma unroll
    for (int dt = 0; dt < 3; ++dt) {
      r[dt] = (f32x4){0.f, 0.f, 0.f, 0.f};
      r[dt] = __builtin_amdgcn_mfma_f32_16x16x32_bf16(kf[dt][0], p0, r[dt], 0, 0, 0);
      r[dt] = __builtin_amdgcn_mfma_f32_16x16x32_bf16(kf[dt][1], p1, r[dt], 0, 0, 0);
    }
    float den = rbf(rbf(__shfl(r[2][0], l15)) + 1e-6f);
    float inv = 1.0f / den;
#pragma unroll
    for (int dt = 0; dt < 2; ++dt) {
      u32x2 w = (u32x2){pk2(rbf(r[dt][0]) * inv, rbf(r[dt][1]) * inv), pk2(rbf(r[dt][2]) * inv, rbf(r[dt][3]) * inv)};
      *reinterpret_cast<u32x2*>(o + (size_t)tok * D_ + h * 32 + dt * 16 + 4 * g) = w;
    }
  }
}

constexpr int WC_QKV = 0, WC_WO = WC_QKV + 2 * 1024 * 384, WC_W1 = WC_WO + 2 * 1024 * 128, WC_W2 = WC_W1 + 2 * 1024 * 128,
              WC_W4 = WC_W2 + 2 * 1024 * 128, WC_END = WC_W4 + 2 * F_ * 128;
__global__ __launch_bounds__(256) void prep_kernel(const u16* __restrict__ x, const u16* __restrict__ wqkv,
                                                   const u16* __restrict__ w1, const u16* __restrict__ w3,
                                                   float* __restrict__ cs_qkv, float* __restrict__ cs_w1,
                                                   float* __restrict__ cs_w3, f32x2* __restrict__ st,
                                                   const u16* __restrict__ wo, const u16* __restrict__ w2,
                                                   const u16* __restrict__ w4, char* __restrict__ wc) {
  __shared__ float red[32][65];
  const int tid = threadIdx.x, b = blockIdx.x;
  if (b >= 248) {
    const int pb = b - 248;
    const u16* w = pb < 32 ? wo + (size_t)pb * 32768 : pb < 64 ? w2 + (size_t)(pb - 32) * 32768 : w4 + (size_t)(pb - 64) * 32768;
    u32x4 v[16];
#pragma unroll
    for (int r = 0; r < 16; ++r) v[r] = *reinterpret_cast<const u32x4*>(w + r * 2048 + tid * 8);
    if (((tid >> 3) & 7) == 3) {
      const int kd = pb < 64 ? 1024 : F_, row = (pb < 32 ? pb : pb < 64 ? pb - 32 : pb - 64) * 32 + (tid >> 7);
      char* dst = wc + (pb < 32 ? WC_WO : pb < 64 ? WC_W2 : WC_W4) + (size_t)((tid >> 6) & 1) * kd * 128 + (size_t)row * 128 + (tid & 7) * 16;
#pragma unroll
      for (int r = 0; r < 16; ++r) *reinterpret_cast<u32x4*>(dst + r * 256) = v[r];
    }
#pragma unroll
    for (int r = 0; r < 16; ++r) asm volatile("" ::"v"(v[r]));
  } else if (b < 120) {
    const u16* w; float* dst; int ld, c0;
    if (b < 48) { w = wqkv; dst = cs_qkv; ld = 3072; c0 = b * 64; }
    else if (b < 64) { w = w1; dst = cs_w1; ld = 1024; c0 = (b - 48) * 64; }
    else { w = w3; dst = cs_w3; ld = F_; c0 = (b - 64) * 64; }
    const int cg = tid & 7, rg = tid >> 3;
    float s[8] = {0.f, 0.f, 0.f, 0.f, 0.f, 0.f, 0.f, 0.f};
    const bool cp = b < 64 && (b & 7) == 3;
    const int cpp = b < 48 ? 384 : 128;
    char* cpd = wc + (b < 48 ? WC_QKV + ((b >> 3) & 1) * (1024 * 384) + (b >> 4) * 128 : WC_W1 + ((b >> 3) & 1) * (1024 * 128)) + cg * 16;
#pragma unroll
    for (int r0 = 0; r0 < 32; r0 += 16) {
      u32x4 v[16];
#pragma unroll
      for (int r = 0; r < 16; ++r) v[r] = *reinterpret_cast<const u32x4*>(w + (size_t)((r0 + r) * 32 + rg) * ld + c0 + cg * 8);
      if (cp) {
#pragma unroll
        for (int r = 0; r < 16; ++r) *reinterpret_cast<u32x4*>(cpd + ((r0 + r) * 32 + rg) * cpp) = v[r];
      }
#pragma unroll
      for (int r = 0; r < 16; ++r)
#pragma unroll
        for (int i = 0; i < 4; ++i) { s[2 * i] += bflo(v[r][i]); s[2 * i + 1] += bfhi(v[r][i]); }
    }
#pragma unroll
    for (int i = 0; i < 8; ++i) red[rg][cg * 8 + i] = s[i];
    __syncthreads();
    if (tid < 64) {
      float t = 0.f;
#pragma unroll
      for (int r = 0; r < 32; ++r) t += red[r][tid];
      dst[c0 + tid] = t;
    }
  } else {
    const int lane = tid & 63, wid = tid >> 6;
    const int rbase = (b - 120) * 16 + wid * 4;
    u32x4 xv[8];
#pragma unroll
    for (int i = 0; i < 8; ++i) xv[i] = *reinterpret_cast<const u32x4*>(x + (size_t)rbase * D_ + i * 512 + lane * 8);
    __builtin_amdgcn_sched_barrier(0);
#pragma unroll
    for (int rr = 0; rr < 4; ++rr) {
      const int row = rbase + rr;
#pragma unroll
      for (int hf = 0; hf < 2; ++hf) {
        const u32x4 v = xv[rr * 2 + hf];
        float y[8];
#pragma unroll
        for (int i = 0; i < 4; ++i) { y[2 * i] = bflo(v[i]); y[2 * i + 1] = bfhi(v[i]); }
        float sm = ((y[0] + y[1]) + (y[2] + y[3])) + ((y[4] + y[5]) + (y[6] + y[7]));
        sm = rsum8(sm);
        float mu = sm * (1.0f / 64.f), m2 = 0.f;
#pragma unroll
        for (int i = 0; i < 8; ++i) { float d = y[i] - mu; m2 += d * d; }
        m2 = rsum8(m2);
        if ((lane & 7) == 0) st[(size_t)row * 16 + hf * 8 + (lane >> 3)] = (f32x2){sm, m2};
      }
    }
  }
}

extern "C" void kernel_launch(void* const* d_in, const int* in_sizes, int n_in, void* d_out, int out_size, void* d_ws,
                              size_t ws_size, hipStream_t stream) {
  const u16* x_in = (const u16*)d_in[0];
  const u16* wqkv = (const u16*)d_in[1];
  const u16* wphi = (const u16*)d_in[2];
  const u16* wo = (const u16*)d_in[3];
  const u16* w1 = (const u16*)d_in[5];
  const u16* w2 = (const u16*)d_in[6];
  const u16* w3 = (const u16*)d_in[7];
  const u16* w4 = (const u16*)d_in[8];
  char* ws = (char*)d_ws;
  size_t off = 0;
  auto alloc = [&](size_t bytes) { char* p = ws + off; off += (bytes + 4095) & ~(size_t)4095; return p; };
  u16* xb = (u16*)alloc((size_t)S_ * D_ * 2);
  u16* x2 = (u16*)alloc((size_t)S_ * D_ * 2);
  u16* x1 = (u16*)alloc((size_t)S_ * D_ * 2);
  u16* hs = (u16*)alloc((size_t)S_ * D_ * 2);
  u16* ob = (u16*)alloc((size_t)S_ * D_ * 2);
  u16* gb = (u16*)alloc((size_t)S_ * F_ * 2);
  u16* phiq = (u16*)alloc((size_t)S_ * 2048 * 2);
  float* kvp = (float*)alloc((size_t)32 * 16 * 2048 * 4);
  float* zp = (float*)alloc((size_t)32 * 16 * 64 * 4);
  f32x2* st_x = (f32x2*)alloc((size_t)S_ * 16 * 8);
  f32x2* st_x2 = (f32x2*)alloc((size_t)S_ * 16 * 8);
  f32x2* st_x1 = (f32x2*)alloc((size_t)S_ * 16 * 8);
  float* cs_qkv = (float*)alloc(3072 * 4);
  float* cs_w1 = (float*)alloc(1024 * 4);
  float* cs_w3 = (float*)alloc(F_ * 4);
  char* wc = alloc(WC_END);

  prep_kernel<<<248 + 176, 256, 0, stream>>>(x_in, wqkv, w1, w3, cs_qkv, cs_w1, cs_w3, st_x, wo, w2, w4, wc);
  for (int layer = 0; layer < 2; ++layer) {
    const u16* x = layer == 0 ? x_in : xb;
    u16* xo = layer == 0 ? xb : (u16*)d_out;
    gemm_kernel<192, 3, 1024, 1024, 3072, EPI_QKV><<<256, 512, 0, stream>>>(x, wqkv, (const u16*)(wc + WC_QKV), cs_qkv, st_x, nullptr, nullptr, phiq, nullptr, wphi, kvp, zp);
    attn_out_kernel<<<256, 256, 0, stream>>>(phiq, kvp, zp, ob);
    gemm_kernel<64, 5, 1024, 1024, 1024, EPI_WO><<<256, 512, 0, stream>>>(ob, wo, (const u16*)(wc + WC_WO), nullptr, nullptr, x, nullptr, x2, st_x2, nullptr, nullptr, nullptr);
    gemm_kernel<64, 5, 1024, 1024, 1024, EPI_W1><<<256, 512, 0, stream>>>(x2, w1, (const u16*)(wc + WC_W1), cs_w1, st_x2, nullptr, nullptr, hs, nullptr, nullptr, nullptr, nullptr);
    gemm_kernel<64, 5, 1024, 1024, 1024, EPI_W2><<<256, 512, 0, stream>>>(hs, w2, (const u16*)(wc + WC_W2), nullptr, st_x2, x2, nullptr, x1, st_x1, nullptr, nullptr, nullptr);
    gemm_kernel<224, 3, 1024, 1024, F_, EPI_W3><<<256, 512, 0, stream>>>(x1, w3, nullptr, cs_w3, st_x1, nullptr, nullptr, gb, nullptr, nullptr, nullptr, nullptr);
    gemm_kernel<64, 5, F_, F_, 1024, EPI_W4><<<256, 512, 0, stream>>>(gb, w4, (const u16*)(wc + WC_W4), nullptr, nullptr, x2, x1, xo, st_x, nullptr, nullptr, nullptr);
  }
}
```

```cpp
#include <hip/hip_runtime.h>
#include <hip/hip_bf16.h>
#include <stdint.h>
#include <type_traits>

typedef short bf16x8 __attribute__((ext_vector_type(8)));
typedef short s16x4 __attribute__((ext_vector_type(4)));
typedef float f32x4 __attribute__((ext_vector_type(4)));
typedef float f32x2 __attribute__((ext_vector_type(2)));
typedef unsigned u32x2 __attribute__((ext_vector_type(2)));
typedef unsigned u32x4 __attribute__((ext_vector_type(4)));
typedef __attribute__((address_space(3))) s16x4* lds_s16x4_p;
typedef unsigned short u16;

constexpr int S_ = 2048, D_ = 1024, H_ = 32, F_ = 3584;

__device__ __forceinline__ float bf2f(u16 v) { return __uint_as_float(((unsigned)v) << 16); }
__device__ __forceinline__ float bflo(unsigned v) { return __uint_as_float(v << 16); }
__device__ __forceinline__ float bfhi(unsigned v) { return __uint_as_float(v & 0xffff0000u); }
typedef __bf16 bf16x2_t __attribute__((ext_vector_type(2)));
__device__ __forceinline__ unsigned pk2(float a, float b) {
  return __builtin_bit_cast(unsigned, __builtin_convertvector((f32x2){a, b}, bf16x2_t));
}
__device__ __forceinline__ unsigned pk2v(f32x2 v) { return __builtin_bit_cast(unsigned, __builtin_convertvector(v, bf16x2_t)); }
__device__ __forceinline__ float max2_(float a, float b) { float r; asm("v_max_f32 %0, %1, %2" : "=v"(r) : "v"(a), "v"(b)); return r; }
__device__ __forceinline__ float min2_(float a, float b) { float r; asm("v_min_f32 %0, %1, %2" : "=v"(r) : "v"(a), "v"(b)); return r; }
__device__ __forceinline__ float max3_(float a, float b, float c) { float r; asm("v_max3_f32 %0, %1, %2, %3" : "=v"(r) : "v"(a), "v"(b), "v"(c)); return r; }
__device__ __forceinline__ float min3_(float a, float b, float c) { float r; asm("v_min3_f32 %0, %1, %2, %3" : "=v"(r) : "v"(a), "v"(b), "v"(c)); return r; }
__device__ __forceinline__ float rbf(float a) { return bflo(pk2(a, 0.f)); }
__device__ __forceinline__ float gelu_t(float x) {
  const float c1 = -2.0f * 0.7978845608028654f * 1.4426950408889634f;
  const float c2 = c1 * 0.044715f;
  float t = x * x;
  float w = x * __builtin_fmaf(t, c2, c1);
  float e = __builtin_amdgcn_exp2f(w);
  return x * __builtin_amdgcn_rcpf(1.0f + e);
}

__device__ __forceinline__ f32x2 gelu2(f32x2 x) {
  const float c1 = -2.0f * 0.7978845608028654f * 1.4426950408889634f;
  const float c2 = c1 * 0.044715f;
  f32x2 t = x * x;
  f32x2 w = x * (t * c2 + c1);
  f32x2 d = (f32x2){__builtin_amdgcn_exp2f(w[0]), __builtin_amdgcn_exp2f(w[1])} + 1.0f;
  return x * (f32x2){__builtin_amdgcn_rcpf(d[0]), __builtin_amdgcn_rcpf(d[1])};
}
__device__ __forceinline__ void x16(float v, float& a, float& b) {
  auto r = __builtin_amdgcn_permlane16_swap(__float_as_uint(v), __float_as_uint(v), false, false);
  a = __uint_as_float(r[0]); b = __uint_as_float(r[1]);
}
__device__ __forceinline__ void x32(float v, float& a, float& b) {
  auto r = __builtin_amdgcn_permlane32_swap(__float_as_uint(v), __float_as_uint(v), false, false);
  a = __uint_as_float(r[0]); b = __uint_as_float(r[1]);
}
__device__ __forceinline__ float rmax4(float v) { float a, b; x16(v, a, b); v = max2_(a, b); x32(v, a, b); return max2_(a, b); }
__device__ __forceinline__ float rmin4(float v) { float a, b; x16(v, a, b); v = min2_(a, b); x32(v, a, b); return min2_(a, b); }
__device__ __forceinline__ float rsum4(float v) { float a, b; x16(v, a, b); v = a + b; x32(v, a, b); return a + b; }

__device__ __forceinline__ float rsum8(float v) {
  v += __uint_as_float(__builtin_amdgcn_mov_dpp(__float_as_uint(v), 0xB1, 0xF, 0xF, true));
  v += __uint_as_float(__builtin_amdgcn_mov_dpp(__float_as_uint(v), 0x4E, 0xF, 0xF, true));
  v += __uint_as_float(__builtin_amdgcn_mov_dpp(__float_as_uint(v), 0x141, 0xF, 0xF, true));
  return v;
}
#define WAIT_V(n) asm volatile("s_waitcnt vmcnt(" #n ")" ::: "memory")
#define WAIT_L0() asm volatile("s_waitcnt lgkmcnt(0)" ::: "memory")
template <int N> __device__ __forceinline__ void wait_vm() {
  asm volatile("s_waitcnt vmcnt(%0)" ::"i"(N) : "memory");
}

__device__ __forceinline__ void glds16(unsigned voff, const void* sbase, unsigned lds) {
  unsigned keep;
  asm volatile("s_mov_b32 %0, m0\n\ts_mov_b32 m0, %3\n\ts_nop 0\n\tglobal_load_lds_dwordx4 %1, %2\n\ts_mov_b32 m0, %0"
               : "=&s"(keep) : "v"(voff), "s"(sbase), "s"(lds) : "memory");
}

__device__ __forceinline__ void mfma_acc(f32x4& c, const u32x4& a, const u32x4& b) {
  asm volatile("v_mfma_f32_16x16x32_bf16 %0, %1, %2, %0" : "+a"(c) : "v"(a), "v"(b));
}

#define STAMP(i)

enum { EPI_QKV = 0, EPI_WO = 1, EPI_W1 = 2, EPI_W2 = 3, EPI_W3 = 4, EPI_W4 = 5 };

template <int BN, int NST, int KD, int LDA, int LDB, int EPI>
__global__ __launch_bounds__(512, 2) void gemm_kernel(
    const u16* __restrict__ A, const u16* __restrict__ W,
    const u16* __restrict__ Wc,
    const float* __restrict__ cs,
    const f32x2* __restrict__ st_in,
    const u16* __restrict__ r0,
    const u16* __restrict__ r1,
    u16* __restrict__ out,
    f32x2* __restrict__ st_out,
    const u16* __restrict__ wphi,
    float* __restrict__ kvp,
    float* __restrict__ zp)
{
  constexpr int NFULL = BN / 64, HALF = (BN % 64) / 32;
  constexpr int A_BYTES = 128 * 64 * 2, B_BYTES = BN * 64 * 2, STAGE = A_BYTES + B_BYTES;
  constexpr int NT = KD / 64, NTW = 2 * NFULL + HALF, LPS = 4 + 2 * NFULL + HALF;
  constexpr int PB = BN * 2 + 16;
  constexpr int EPI_BYTES = (EPI == EPI_QKV) ? (4 * 64 * 144 + 2 * (64 * 32 + 64) * 4) : 128 * PB;
  constexpr int MAINB = NST * STAGE;
  constexpr int RS_OFF = (MAINB > EPI_BYTES ? MAINB : EPI_BYTES);
  constexpr int CS_OFF = RS_OFF + 128 * 16;
  constexpr int WP_OFF = CS_OFF + 1024;
  constexpr int SMEM = WP_OFF + (EPI == EPI_QKV ? 4096 : 0);
  constexpr bool HAS_CS = (EPI == EPI_QKV || EPI == EPI_W1 || EPI == EPI_W3);
  constexpr bool HAS_ST = (EPI == EPI_QKV || EPI == EPI_W1 || EPI == EPI_W3 || EPI == EPI_W2);
  constexpr bool HAS_RES = (EPI == EPI_WO || EPI == EPI_W2 || EPI == EPI_W4);
  constexpr int LAG = (BN == 64) ? 1 : 0;
  constexpr int CH = BN / 8, NIT = CH / 4;
  static_assert(SMEM <= 163840, "LDS");
  __shared__ __attribute__((aligned(16))) char smem[SMEM];

  STAMP(0);
  const int tid = threadIdx.x, lane = tid & 63;
  const int wid8 = __builtin_amdgcn_readfirstlane(tid >> 6), wid = wid8 & 3, wm = wid >> 1, wn = wid & 1;
  const int g = lane >> 4, l15 = lane & 15;
  const int xcd = blockIdx.x & 7, idx = blockIdx.x >> 3;
  const int tm = (xcd >> 1) * 4 + (idx & 3), tn = (xcd & 1) * 8 + (idx >> 2);
  const int row0 = tm * 128;
  f32x4* rs = reinterpret_cast<f32x4*>(smem + RS_OFF);

  f32x4 acc[4][NTW];
  u32x4 ra[HAS_RES ? NIT : 1], rb[(EPI == EPI_W4) ? NIT : 1];

  if (wid8 >= 4) {
    unsigned voA[4], voB[NFULL > 0 ? NFULL : 1][2], voH = 0;
    const bool pk_ = (HALF == 0) && ((tn & 7) == 3);
    const unsigned pitchB = pk_ ? NFULL * 128 : LDB * 2;
#pragma unroll
    for (int i = 0; i < 4; ++i) {
      int r = (wid * 4 + i) * 8 + (lane >> 3);
      int ch = (lane & 7) ^ ((r >> 1) & 7);
      voA[i] = (unsigned)(row0 + r) * (LDA * 2) + ch * 16;
    }
#pragma unroll
    for (int b = 0; b < NFULL; ++b) {
      int colb = (EPI == EPI_QKV) ? (b * 1024 + tn * 64) : (tn * BN + b * 64);
#pragma unroll
      for (int j = 0; j < 2; ++j) {
        int r = (wid * 2 + j) * 8 + (lane >> 3);
        int f = ((r >> 1) & 1) | (((r >> 3) & 1) << 1);
        int chp = lane & 7;
        int gch = (((chp >> 1) ^ f) << 1) | (chp & 1);
        voB[b][j] = (unsigned)r * pitchB + (pk_ ? b * 128 : colb * 2) + gch * 16;
      }
    }
    if constexpr (HALF) {
      int r = wid * 16 + (lane >> 2);
      int chp = lane & 3;
      int gch = (((chp >> 1) ^ ((r >> 3) & 1)) << 1) | (chp & 1);
      voH = (unsigned)r * (LDB * 2) + (tn * BN + NFULL * 64) * 2 + gch * 16;
    }
    const char* Ab = reinterpret_cast<const char*>(A);
    const char* Wb = pk_ ? reinterpret_cast<const char*>(Wc) + (size_t)(tn >> 3) * (KD * NFULL * 128) : reinterpret_cast<const char*>(W);
    const unsigned lbase = (unsigned)(uintptr_t)(__attribute__((address_space(3))) char*)smem;
    auto stage = [&](int kt, int buf) {
      const char* a = Ab + (size_t)kt * 128;
      const char* w = Wb + (size_t)(kt * (64 * pitchB));
      const unsigned s = lbase + buf * STAGE;
#pragma unroll
      for (int i = 0; i < 4; ++i) glds16(voA[i], a, s + (wid * 4 + i) * 1024);
#pragma unroll
      for (int b = 0; b < NFULL; ++b)
#pragma unroll
        for (int j = 0; j < 2; ++j) glds16(voB[b][j], w, s + A_BYTES + b * 8192 + (wid * 2 + j) * 1024);
      if constexpr (HALF) glds16(voH, w, s + A_BYTES + NFULL * 8192 + wid * 1024);
    };
#pragma unroll
    for (int s = 0; s < NST; ++s) stage(s, s);
    constexpr int XL = 0;
    f32x4 stp[4];
    const int lt = tid - 256;
    u32x4 wpx;
    if constexpr (EPI == EPI_QKV) wpx = *reinterpret_cast<const u32x4*>(wphi + tn * 2048 + lt * 8);
    if constexpr (HAS_ST) {
      const f32x4* sp = reinterpret_cast<const f32x4*>(st_in + (size_t)(row0 + (lt >> 1)) * 16 + (lt & 1) * 8);
#pragma unroll
      for (int i = 0; i < 4; ++i) stp[i] = sp[i];
    }
    if constexpr (HAS_RES) {
#pragma unroll
      for (int it = 0; it < NIT; ++it) {
        const int c = tid + 512 * it, r = c / CH, ch = c % CH;
        ra[it] = *reinterpret_cast<const u32x4*>(r0 + (size_t)(row0 + r) * D_ + tn * BN + ch * 8);
        if constexpr (EPI == EPI_W4) rb[it] = *reinterpret_cast<const u32x4*>(r1 + (size_t)(row0 + r) * D_ + tn * BN + ch * 8);
      }
    }
    asm volatile("" ::: "memory");
    wait_vm<(NST - 1 - LAG) * LPS>();
    __builtin_amdgcn_s_barrier();
    int buf = 0;
    for (int kt = 0; kt < NT - NST; ++kt) {
      wait_vm<(NST - 2 - LAG) * LPS>();
      __builtin_amdgcn_s_barrier();
      stage(kt + NST, buf);
      buf = buf + 1 == NST ? 0 : buf + 1;
    }
    asm volatile("" ::: "memory");
#pragma unroll
    for (int r = 0; r < NST - 1; ++r) {
      constexpr int NSL = NST - LAG;
      if (r == 0) wait_vm<(NSL - 2) * LPS + XL>();
      else if (r == 1) wait_vm<(NSL > 3 ? NSL - 3 : 0) * LPS + XL>();
      else if (r == 2) wait_vm<(NSL > 4 ? NSL - 4 : 0) * LPS + XL>();
      else if (r == 3) wait_vm<(NSL > 5 ? NSL - 5 : 0) * LPS + XL>();
      else wait_vm<XL>();
      __builtin_amdgcn_s_barrier();
    }
    if constexpr (HAS_ST) {
#pragma unroll
      for (int i = 0; i < 4; ++i) asm volatile("" : "+v"(stp[i]));
      float sm = 0.f;
#pragma unroll
      for (int i = 0; i < 4; ++i) sm += stp[i][0] + stp[i][2];
      sm += __shfl_xor(sm, 1);
      float mean = sm * (1.0f / 1024.f);
      float m2 = 0.f;
#pragma unroll
      for (int i = 0; i < 4; ++i) {
        float d0 = stp[i][0] * (1.0f / 64.f) - mean, d1 = stp[i][2] * (1.0f / 64.f) - mean;
        m2 += stp[i][1] + stp[i][3] + 64.f * (d0 * d0 + d1 * d1);
      }
      m2 += __shfl_xor(m2, 1);
      float var = m2 * (1.0f / 1024.f);
      float r1_ = 1.0f / sqrtf(var + 1e-5f);
      float sc = r1_;
      if constexpr (EPI == EPI_W1) sc = r1_ / sqrtf(var * r1_ * r1_ + 1e-5f);
      if ((lt & 1) == 0) rs[lt >> 1] = (f32x4){mean, sc, r1_, 0.f};
    }
    if constexpr (EPI == EPI_QKV) *reinterpret_cast<u32x4*>(smem + WP_OFF + lt * 16) = wpx;
  } else {
    if constexpr (HAS_RES) {
#pragma unroll
      for (int it = 0; it < NIT; ++it) {
        const int c = tid + 512 * it, r = c / CH, ch = c % CH;
        ra[it] = *reinterpret_cast<const u32x4*>(r0 + (size_t)(row0 + r) * D_ + tn * BN + ch * 8);
        if constexpr (EPI == EPI_W4) rb[it] = *reinterpret_cast<const u32x4*>(r1 + (size_t)(row0 + r) * D_ + tn * BN + ch * 8);
      }
    }
    if constexpr (HAS_CS) {
      if (tid < BN / 4) {
        const float* cp = cs + (EPI == EPI_QKV ? (tid >> 4) * 1024 + tn * 64 + (tid & 15) * 4 : tn * BN + tid * 4);
        f32x4 c[16];
#pragma unroll
        for (int i = 0; i < 16; ++i) c[i] = *reinterpret_cast<const f32x4*>(cp + i * LDB);
#pragma unroll
        for (int i = 8; i > 0; i >>= 1)
#pragma unroll
          for (int j = 0; j < i; ++j) c[j] += c[j + i];
        *reinterpret_cast<f32x4*>(smem + CS_OFF + tid * 16) = c[0];
      }
    }
    const int q = l15 >> 2, p = lane & 3;
    const int fB = (q >> 1) | ((g & 1) << 1);
    unsigned aoff[2], boff[2], hoff = 0;
#pragma unroll
    for (int ks = 0; ks < 2; ++ks) aoff[ks] = (wm * 64 + l15) * 128 + (((4 * ks + g) ^ (l15 >> 1)) * 16);
#pragma unroll
    for (int u = 0; u < 2; ++u) boff[u] = A_BYTES + (8 * g + q) * 128 + (((2 * wn + u) ^ fB) * 32) + p * 8;
    if constexpr (HALF) hoff = A_BYTES + NFULL * 8192 + (8 * g + q) * 64 + ((wn ^ (g & 1)) * 32) + p * 8;

    auto ldf = [&](int i, const char* s, int ks, u32x4(&af)[4], u32x4(&bfr)[NTW]) {
      if (i == 0 || i > NTW) {
        const int mt = i == 0 ? 0 : i - NTW;
        af[mt] = *reinterpret_cast<const u32x4*>(s + aoff[ks] + mt * 2048);
      } else if (i - 1 < 2 * NFULL) {
        const int b = (i - 1) >> 1, u = (i - 1) & 1;
        s16x4 lo = __builtin_amdgcn_ds_read_tr16_b64_v4i16((lds_s16x4_p)(s + boff[u] + b * 8192 + ks * 4096));
        s16x4 hi = __builtin_amdgcn_ds_read_tr16_b64_v4i16((lds_s16x4_p)(s + boff[u] + b * 8192 + ks * 4096 + 512));
        bfr[i - 1] = __builtin_shufflevector(__builtin_bit_cast(u32x2, lo), __builtin_bit_cast(u32x2, hi), 0, 1, 2, 3);
      } else {
        s16x4 lo = __builtin_amdgcn_ds_read_tr16_b64_v4i16((lds_s16x4_p)(s + hoff + ks * 2048));
        s16x4 hi = __builtin_amdgcn_ds_read_tr16_b64_v4i16((lds_s16x4_p)(s + hoff + ks * 2048 + 256));
        bfr[NTW - 1] = __builtin_shufflevector(__builtin_bit_cast(u32x2, lo), __builtin_bit_cast(u32x2, hi), 0, 1, 2, 3);
      }
    };
    constexpr int NM = 4 * NTW, NL = NTW + 4;
    auto phase = [&](u32x4(&afc)[4], u32x4(&bfc)[NTW], u32x4(&afn)[4], u32x4(&bfn)[NTW], int nbuf, int nks, auto rd, auto bar) {
      const char* s = smem + nbuf * STAGE;
      constexpr int BAR = decltype(bar)::value, R0 = BAR < 0 ? 0 : BAR + 1;
      constexpr int RSP = (NM - R0 - 2) / NL > 0 ? (NM - R0 - 2) / NL : 1;
      static_assert(R0 + (NL - 1) * RSP < NM, "reads");
#pragma unroll
      for (int mt = 0; mt < 4; ++mt)
#pragma unroll
        for (int j = 0; j < NTW; ++j) {
          const int m = mt * NTW + j;
          if (m == BAR) {
            WAIT_L0();
            __builtin_amdgcn_s_barrier();
            asm volatile("" ::: "memory");
          }
          if (decltype(rd)::value && m >= R0 && (m - R0) % RSP == 0 && (m - R0) / RSP < NL) ldf((m - R0) / RSP, s, nks, afn, bfn);
          if (EPI == EPI_QKV && j >= 4) mfma_acc(acc[mt][j], afc[mt], bfc[j]);
          else mfma_acc(acc[mt][j], bfc[j], afc[mt]);
          __builtin_amdgcn_sched_barrier(0);
        }
    };
    typedef std::integral_constant<int, -1> nobar;
#pragma unroll
    for (int mt = 0; mt < 4; ++mt)
#pragma unroll
      for (int j = 0; j < NTW; ++j) {
        acc[mt][j] = (f32x4){0.f, 0.f, 0.f, 0.f};
        asm volatile("" : "+a"(acc[mt][j]));
      }
    u32x4 af0[4], af1[4], bf0[NTW], bf1[NTW];
    __builtin_amdgcn_s_barrier();
    asm volatile("" ::: "memory");
    STAMP(1);
#pragma unroll
    for (int i = 0; i < NL; ++i) { ldf(i, smem, 0, af0, bf0); __builtin_amdgcn_sched_barrier(0); }
    int buf = 0;
    if constexpr (LAG) {
      phase(af0, bf0, af1, bf1, 0, 1, std::true_type{}, nobar{});
      for (int kt = 0; kt < NT - 1; ++kt) {
        buf = buf + 1 == NST ? 0 : buf + 1;
        phase(af1, bf1, af0, bf0, buf, 0, std::true_type{}, nobar{});
        asm volatile("" ::: "memory");
        __builtin_amdgcn_s_barrier();
        asm volatile("" ::: "memory");
        phase(af0, bf0, af1, bf1, buf, 1, std::true_type{}, nobar{});
      }
      phase(af1, bf1, af0, bf0, buf, 0, std::false_type{}, nobar{});
    } else {
      for (int kt = 0; kt < NT - 1; ++kt) {
        phase(af0, bf0, af1, bf1, buf, 1, std::true_type{}, nobar{});
        buf = buf + 1 == NST ? 0 : buf + 1;
        phase(af1, bf1, af0, bf0, buf, 0, std::true_type{}, std::integral_constant<int, 4>{});
      }
      phase(af0, bf0, af1, bf1, buf, 1, std::true_type{}, nobar{});
      phase(af1, bf1, af0, bf0, buf, 0, std::false_type{}, nobar{});
    }
    asm volatile("s_nop 7\n\ts_nop 7\n\ts_nop 7" ::: "memory");
  }
  WAIT_L0();
  __builtin_amdgcn_s_barrier();
  asm volatile("" ::: "memory");
  STAMP(2);

  if constexpr (EPI == EPI_QKV) {
    const int h = tn * 2 + wn;
    const int q = l15 >> 2, p = lane & 3;
    f32x4 kv[4][2], zz[4];
    float* red = reinterpret_cast<float*>(smem + 4 * 64 * 144) + wn * (64 * 32 + 64);
    if (wid8 < 4) {
      bf16x8 wf[2];
#pragma unroll
      for (int ft = 0; ft < 2; ++ft) {
        const char* src = smem + WP_OFF + wn * 2048 + (4 * g + q) * 64 + ft * 32 + p * 8;
        s16x4 lo = __builtin_amdgcn_ds_read_tr16_b64_v4i16((lds_s16x4_p)(src));
        s16x4 hi = __builtin_amdgcn_ds_read_tr16_b64_v4i16((lds_s16x4_p)(src + 16 * 64));
        wf[ft] = __builtin_shufflevector(lo, hi, 0, 1, 2, 3, 4, 5, 6, 7);
      }
      const float* csl = reinterpret_cast<const float*>(smem + CS_OFF);
      f32x4 cs4[4];
      float csv[2];
#pragma unroll
      for (int t = 0; t < 2; ++t) {
        cs4[t] = *reinterpret_cast<const f32x4*>(csl + wn * 32 + t * 16 + 4 * g);
        cs4[2 + t] = *reinterpret_cast<const f32x4*>(csl + 64 + wn * 32 + t * 16 + 4 * g);
        csv[t] = csl[128 + wn * 32 + t * 16 + l15];
      }
      bf16x8 qf[4], kf[4], vf[2][2];
#pragma unroll
      for (int mt = 0; mt < 4; ++mt) {
        f32x4 st = rs[wm * 64 + mt * 16 + l15];
        unsigned pq_[4], pk_[4];
#pragma unroll
        for (int t = 0; t < 2; ++t) {
          float a[4], b[4];
#pragma unroll
          for (int i = 0; i < 4; ++i) {
            a[i] = st[1] * (acc[mt][t][i] - st[0] * cs4[t][i]);
            b[i] = st[1] * (acc[mt][2 + t][i] - st[0] * cs4[2 + t][i]);
          }
          pq_[t * 2] = pk2(a[0], a[1]); pq_[t * 2 + 1] = pk2(a[2], a[3]);
          pk_[t * 2] = pk2(b[0], b[1]); pk_[t * 2 + 1] = pk2(b[2], b[3]);
        }
        qf[mt] = __builtin_bit_cast(bf16x8, (u32x4){pq_[0], pq_[1], pq_[2], pq_[3]});
        kf[mt] = __builtin_bit_cast(bf16x8, (u32x4){pk_[0], pk_[1], pk_[2], pk_[3]});
      }
#pragma unroll
      for (int s = 0; s < 2; ++s) {
        unsigned pv[2][4];
#pragma unroll
        for (int m2 = 0; m2 < 2; ++m2) {
          int mt = s * 2 + m2;
          f32x4 s0 = rs[wm * 64 + mt * 16 + 4 * g + 0], s1 = rs[wm * 64 + mt * 16 + 4 * g + 1];
          f32x4 s2 = rs[wm * 64 + mt * 16 + 4 * g + 2], s3 = rs[wm * 64 + mt * 16 + 4 * g + 3];
#pragma unroll
          for (int t = 0; t < 2; ++t) {
            float a0 = s0[1] * (acc[mt][4 + t][0] - s0[0] * csv[t]);
            float a1 = s1[1] * (acc[mt][4 + t][1] - s1[0] * csv[t]);
            float a2 = s2[1] * (acc[mt][4 + t][2] - s2[0] * csv[t]);
            float a3 = s3[1] * (acc[mt][4 + t][3] - s3[0] * csv[t]);
            pv[t][m2 * 2] = pk2(a0, a1); pv[t][m2 * 2 + 1] = pk2(a2, a3);
          }
        }
#pragma unroll
        for (int t = 0; t < 2; ++t) vf[s][t] = __builtin_bit_cast(bf16x8, (u32x4){pv[t][0], pv[t][1], pv[t][2], pv[t][3]});
      }
      char* pkl = smem + wid * (64 * 144);
      const float L2E = 1.4426950408889634f;
#pragma unroll
      for (int which = 0; which < 2; ++which)
#pragma unroll
        for (int mt = 0; mt < 4; ++mt) {
          f32x4 z4 = (f32x4){0.f, 0.f, 0.f, 0.f};
          f32x4 p0 = __builtin_amdgcn_mfma_f32_16x16x32_bf16(wf[0], which ? kf[mt] : qf[mt], z4, 0, 0, 0);
          f32x4 p1 = __builtin_amdgcn_mfma_f32_16x16x32_bf16(wf[1], which ? kf[mt] : qf[mt], z4, 0, 0, 0);
          f32x2 v[4];
          {
            const unsigned r0_ = pk2(p0[0], p0[1]), r1_ = pk2(p0[2], p0[3]), r2_ = pk2(p1[0], p1[1]), r3_ = pk2(p1[2], p1[3]);
            v[0] = (f32x2){bflo(r0_), bfhi(r0_)}; v[1] = (f32x2){bflo(r1_), bfhi(r1_)};
            v[2] = (f32x2){bflo(r2_), bfhi(r2_)}; v[3] = (f32x2){bflo(r3_), bfhi(r3_)};
          }
          float mx = max2_(max3_(max3_(v[0][0], v[0][1], v[1][0]), v[1][1], v[2][0]), max3_(v[2][1], v[3][0], v[3][1]));
          float mn = min2_(min3_(min3_(v[0][0], v[0][1], v[1][0]), v[1][1], v[2][0]), min3_(v[2][1], v[3][0], v[3][1]));
          mx = rmax4(mx); mn = rmin4(mn);
          const float mxl = -mx * L2E, mnl = mn * L2E;
          f32x2 ep[4], en[4];
#pragma unroll
          for (int i = 0; i < 4; ++i) {
            const f32x2 ap = v[i] * L2E + mxl, an = v[i] * (-L2E) + mnl;
            ep[i] = (f32x2){__builtin_amdgcn_exp2f(ap[0]), __builtin_amdgcn_exp2f(ap[1])};
            en[i] = (f32x2){__builtin_amdgcn_exp2f(an[0]), __builtin_amdgcn_exp2f(an[1])};
          }
          const f32x2 sp2 = (ep[0] + ep[1]) + (ep[2] + ep[3]), sn2 = (en[0] + en[1]) + (en[2] + en[3]);
          const float ip = __builtin_amdgcn_rcpf(rsum4(sp2[0] + sp2[1])), in_ = __builtin_amdgcn_rcpf(rsum4(sn2[0] + sn2[1]));
          u32x2 o[4];
          o[0] = (u32x2){pk2v(ep[0] * ip), pk2v(ep[1] * ip)};
          o[1] = (u32x2){pk2v(ep[2] * ip), pk2v(ep[3] * ip)};
          o[2] = (u32x2){pk2v(en[0] * in_), pk2v(en[1] * in_)};
          o[3] = (u32x2){pk2v(en[2] * in_), pk2v(en[3] * in_)};
          if (which == 0) {
            u16* dst = out + ((size_t)(row0 + wm * 64 + mt * 16 + l15) * 32 + h) * 64 + 4 * g;
#pragma unroll
            for (int c = 0; c < 4; ++c) *reinterpret_cast<u32x2*>(dst + c * 16) = o[c];
          } else {
            char* dst = pkl + (mt * 16 + l15) * 144 + g * 8;
#pragma unroll
            for (int c = 0; c < 4; ++c) *reinterpret_cast<u32x2*>(dst + c * 32) = o[c];
          }
        }
      WAIT_L0();
      __builtin_amdgcn_sched_barrier(0);
#pragma unroll
      for (int ft = 0; ft < 4; ++ft) { kv[ft][0] = kv[ft][1] = zz[ft] = (f32x4){0.f, 0.f, 0.f, 0.f}; }
      const bf16x8 ones = (bf16x8){0x3F80, 0x3F80, 0x3F80, 0x3F80, 0x3F80, 0x3F80, 0x3F80, 0x3F80};
#pragma unroll
      for (int s = 0; s < 2; ++s)
#pragma unroll
        for (int ft = 0; ft < 4; ++ft) {
          const char* src = pkl + (32 * s + 4 * g + q) * 144 + ft * 32 + p * 8;
          s16x4 lo = __builtin_amdgcn_ds_read_tr16_b64_v4i16((lds_s16x4_p)(src));
          s16x4 hi = __builtin_amdgcn_ds_read_tr16_b64_v4i16((lds_s16x4_p)(src + 16 * 144));
          bf16x8 pf = __builtin_shufflevector(lo, hi, 0, 1, 2, 3, 4, 5, 6, 7);
          kv[ft][0] = __builtin_amdgcn_mfma_f32_16x16x32_bf16(vf[s][0], pf, kv[ft][0], 0, 0, 0);
          kv[ft][1] = __builtin_amdgcn_mfma_f32_16x16x32_bf16(vf[s][1], pf, kv[ft][1], 0, 0, 0);
          zz[ft] = __builtin_amdgcn_mfma_f32_16x16x32_bf16(ones, pf, zz[ft], 0, 0, 0);
        }
      if (wm == 1) {
#pragma unroll
        for (int ft = 0; ft < 4; ++ft) {
#pragma unroll
          for (int dt = 0; dt < 2; ++dt) *reinterpret_cast<f32x4*>(red + (ft * 16 + l15) * 32 + dt * 16 + 4 * g) = kv[ft][dt];
          if (g == 0) red[64 * 32 + ft * 16 + l15] = zz[ft][0];
        }
      }
    }
    __syncthreads();
    if (wid8 < 2) {
      float* kd = kvp + (size_t)(h * 16 + tm) * 2048;
      float* zd = zp + (size_t)(h * 16 + tm) * 64;
#pragma unroll
      for (int ft = 0; ft < 4; ++ft) {
#pragma unroll
        for (int dt = 0; dt < 2; ++dt) {
          f32x4 o = *reinterpret_cast<const f32x4*>(red + (ft * 16 + l15) * 32 + dt * 16 + 4 * g);
          *reinterpret_cast<f32x4*>(kd + (ft * 16 + l15) * 32 + dt * 16 + 4 * g) = o + kv[ft][dt];
        }
        if (g == 0) zd[ft * 16 + l15] = red[64 * 32 + ft * 16 + l15] + zz[ft][0];
      }
    }
  } else {
    if (wid8 < 4) {
      f32x4 stm[4], cs4[NTW];
      if constexpr (EPI == EPI_W1 || EPI == EPI_W3) {
#pragma unroll
        for (int j = 0; j < NTW; ++j) {
          const int colj = (j < 2 * NFULL) ? ((j >> 1) * 64 + (2 * wn + (j & 1)) * 16) : (NFULL * 64 + wn * 16);
          cs4[j] = *reinterpret_cast<const f32x4*>(smem + CS_OFF + (colj + 4 * g) * 4);
        }
#pragma unroll
        for (int mt = 0; mt < 4; ++mt) stm[mt] = rs[wm * 64 + mt * 16 + l15];
      }
#pragma unroll
      for (int j = 0; j < NTW; ++j) {
        const int colj = (j < 2 * NFULL) ? ((j >> 1) * 64 + (2 * wn + (j & 1)) * 16) : (NFULL * 64 + wn * 16);
#pragma unroll
        for (int mt = 0; mt < 4; ++mt) {
          f32x2 v0 = (f32x2){acc[mt][j][0], acc[mt][j][1]}, v1 = (f32x2){acc[mt][j][2], acc[mt][j][3]};
          if constexpr (EPI == EPI_W1 || EPI == EPI_W3) {
            v0 = gelu2((v0 - stm[mt][0] * (f32x2){cs4[j][0], cs4[j][1]}) * stm[mt][1]);
            v1 = gelu2((v1 - stm[mt][0] * (f32x2){cs4[j][2], cs4[j][3]}) * stm[mt][1]);
          }
          *reinterpret_cast<u32x2*>(smem + (wm * 64 + mt * 16 + l15) * PB + (colj + 4 * g) * 2) = (u32x2){pk2(v0[0], v0[1]), pk2(v1[0], v1[1])};
        }
      }
    }
    __syncthreads();
#pragma unroll
    for (int it = 0; it < NIT; ++it) {
      const int c = tid + 512 * it, r = c / CH, ch = c % CH;
      u32x4 v = *reinterpret_cast<const u32x4*>(smem + r * PB + ch * 16);
      const int grow = row0 + r, gcol = tn * BN + ch * 8;
      if constexpr (EPI == EPI_W3) {
        *reinterpret_cast<u32x4*>(out + (size_t)grow * F_ + gcol) = v;
      } else if constexpr (EPI == EPI_W1) {
        const int grp = tn >> 2;
        const int sh = grp == 0 ? 1 : grp == 1 ? -1 : grp == 2 ? 2 : -2;
        const int dr = grow + sh;
        if (dr >= 0 && dr < S_) *reinterpret_cast<u32x4*>(out + (size_t)dr * D_ + gcol) = v;
        const int zr = grow - sh;
        if (zr < 0 || zr >= S_) *reinterpret_cast<u32x4*>(out + (size_t)grow * D_ + gcol) = (u32x4){0u, 0u, 0u, 0u};
      } else {
        float y[8];
        const u32x4 a = ra[it];
        if constexpr (EPI == EPI_WO) {
#pragma unroll
          for (int i = 0; i < 4; ++i) { y[2 * i] = rbf(bflo(a[i]) + bflo(v[i])); y[2 * i + 1] = rbf(bfhi(a[i]) + bfhi(v[i])); }
        } else if constexpr (EPI == EPI_W2) {
          f32x4 st = rs[r];
#pragma unroll
          for (int i = 0; i < 4; ++i) {
            y[2 * i] = rbf(rbf((bflo(a[i]) - st[0]) * st[2]) + bflo(v[i]));
            y[2 * i + 1] = rbf(rbf((bfhi(a[i]) - st[0]) * st[2]) + bfhi(v[i]));
          }
        } else {
          const u32x4 b = rb[it];
#pragma unroll
          for (int i = 0; i < 4; ++i) {
            y[2 * i] = rbf(bflo(a[i]) + rbf(bflo(b[i]) + bflo(v[i])));
            y[2 * i + 1] = rbf(bfhi(a[i]) + rbf(bfhi(b[i]) + bfhi(v[i])));
          }
        }
        *reinterpret_cast<u32x4*>(out + (size_t)grow * D_ + gcol) = (u32x4){pk2(y[0], y[1]), pk2(y[2], y[3]), pk2(y[4], y[5]), pk2(y[6], y[7])};
        float sm = ((y[0] + y[1]) + (y[2] + y[3])) + ((y[4] + y[5]) + (y[6] + y[7]));
        sm = rsum8(sm);
        float mu = sm * (1.0f / 64.f), m2 = 0.f;
#pragma unroll
        for (int i = 0; i < 8; ++i) { float d = y[i] - mu; m2 += d * d; }
        m2 = rsum8(m2);
        if (ch == 0) st_out[(size_t)grow * 16 + tn] = (f32x2){sm, m2};
      }
    }
  }
  STAMP(3);
}

__global__ __launch_bounds__(256, 1) void attn_out_kernel(const u16* __restrict__ phiq, const float* __restrict__ kvp,
                                                          const float* __restrict__ zp, u16* __restrict__ o) {
  __shared__ __attribute__((aligned(16))) u16 kvT[48 * 72];
  const int tid = threadIdx.x, lane = tid & 63, wid = tid >> 6, g = lane >> 4, l15 = lane & 15;
  const int h = blockIdx.x & 31, tg = blockIdx.x >> 5;
  f32x4 kvv[16][2];
  float zv[16];
  bf16x8 pq[4][2];
  {
    const float* src = kvp + (size_t)h * 16 * 2048 + tid * 8;
#pragma unroll
    for (int t = 0; t < 16; ++t) {
      kvv[t][0] = *reinterpret_cast<const f32x4*>(src + t * 2048);
      kvv[t][1] = *reinterpret_cast<const f32x4*>(src + t * 2048 + 4);
    }
#pragma unroll
    for (int t = 0; t < 16; ++t) zv[t] = zp[(size_t)(h * 16 + t) * 64 + lane];
#pragma unroll
    for (int mt = 0; mt < 4; ++mt) {
      const u16* ps = phiq + ((size_t)(tg * 256 + wid * 64 + mt * 16 + l15) * 32 + h) * 64 + g * 8;
      pq[mt][0] = *reinterpret_cast<const bf16x8*>(ps);
      pq[mt][1] = *reinterpret_cast<const bf16x8*>(ps + 32);
    }
  }
  __builtin_amdgcn_sched_barrier(0);
  {
    for (int i = tid; i < 15 * 64; i += 256) kvT[(33 + i / 64) * 72 + (i & 63)] = 0;
    f32x4 a0 = kvv[0][0], a1 = kvv[0][1];
#pragma unroll
    for (int t = 1; t < 16; ++t) { a0 += kvv[t][0]; a1 += kvv[t][1]; }
    const int f = tid >> 2, d0 = (tid & 3) * 8;
#pragma unroll
    for (int i = 0; i < 4; ++i) {
      kvT[(d0 + i) * 72 + f] = (u16)(pk2(a0[i], 0.f) & 0xffff);
      kvT[(d0 + 4 + i) * 72 + f] = (u16)(pk2(a1[i], 0.f) & 0xffff);
    }
    if (tid < 64) {
      float z = 0.f;
#pragma unroll
      for (int t = 0; t < 16; ++t) z += zv[t];
      kvT[32 * 72 + tid] = (u16)(pk2(z, 0.f) & 0xffff);
    }
  }
  __syncthreads();
  bf16x8 kf[3][2];
#pragma unroll
  for (int dt = 0; dt < 3; ++dt)
#pragma unroll
    for (int ks = 0; ks < 2; ++ks) kf[dt][ks] = *reinterpret_cast<const bf16x8*>(&kvT[(dt * 16 + l15) * 72 + ks * 32 + g * 8]);
#pragma unroll
  for (int mt = 0; mt < 4; ++mt) {
    const int tok = tg * 256 + wid * 64 + mt * 16 + l15;
    const bf16x8 p0 = pq[mt][0], p1 = pq[mt][1];
    f32x4 r[3];
#pragma unroll
    for (int dt = 0; dt < 3; ++dt) {
      r[dt] = (f32x4){0.f, 0.f, 0.f, 0.f};
      r[dt] = __builtin_amdgcn_mfma_f32_16x16x32_bf16(kf[dt][0], p0, r[dt], 0, 0, 0);
      r[dt] = __builtin_amdgcn_mfma_f32_16x16x32_bf16(kf[dt][1], p1, r[dt], 0, 0, 0);
    }
    float den = rbf(rbf(__shfl(r[2][0], l15)) + 1e-6f);
    float inv = 1.0f / den;
#pragma unroll
    for (int dt = 0; dt < 2; ++dt) {
      u32x2 w = (u32x2){pk2(rbf(r[dt][0]) * inv, rbf(r[dt][1]) * inv), pk2(rbf(r[dt][2]) * inv, rbf(r[dt][3]) * inv)};
      *reinterpret_cast<u32x2*>(o + (size_t)tok * D_ + h * 32 + dt * 16 + 4 * g) = w;
    }
  }
}

constexpr int WC_QKV = 0, WC_WO = WC_QKV + 2 * 1024 * 384, WC_W1 = WC_WO + 2 * 1024 * 128, WC_W2 = WC_W1 + 2 * 1024 * 128,
              WC_W4 = WC_W2 + 2 * 1024 * 128, WC_END = WC_W4 + 2 * F_ * 128;
__global__ __launch_bounds__(256) void prep_kernel(const u16* __restrict__ x, const u16* __restrict__ wqkv,
                                                   const u16* __restrict__ w1, const u16* __restrict__ w3,
                                                   float* __restrict__ cs_qkv, float* __restrict__ cs_w1,
                                                   float* __restrict__ cs_w3, f32x2* __restrict__ st,
                                                   const u16* __restrict__ wo, const u16* __restrict__ w2,
                                                   const u16* __restrict__ w4, char* __restrict__ wc) {
  __shared__ __attribute__((aligned(16))) float red[4][512];
  const int tid = threadIdx.x, b = blockIdx.x;
  if (b >= 368) {
    const int pb = b - 368;
    const u16* w = pb < 32 ? wo + (size_t)pb * 32768 : pb < 64 ? w2 + (size_t)(pb - 32) * 32768 : w4 + (size_t)(pb - 64) * 32768;
    u32x4 v[16];
#pragma unroll
    for (int r = 0; r < 16; ++r) v[r] = *reinterpret_cast<const u32x4*>(w + r * 2048 + tid * 8);
    if (((tid >> 3) & 7) == 3) {
      const int kd = pb < 64 ? 1024 : F_, row = (pb < 32 ? pb : pb < 64 ? pb - 32 : pb - 64) * 32 + (tid >> 7);
      char* dst = wc + (pb < 32 ? WC_WO : pb < 64 ? WC_W2 : WC_W4) + (size_t)((tid >> 6) & 1) * kd * 128 + (size_t)row * 128 + (tid & 7) * 16;
#pragma unroll
      for (int r = 0; r < 16; ++r) *reinterpret_cast<u32x4*>(dst + r * 256) = v[r];
    }
#pragma unroll
    for (int r = 0; r < 16; ++r) asm volatile("" ::"v"(v[r]));
  } else if (b < 240) {
    const u16* w; float* dst; int ld, ch, rg;
    if (b < 96) { w = wqkv; dst = cs_qkv; ld = 3072; ch = b % 6; rg = b / 6; }
    else if (b < 128) { w = w1; dst = cs_w1; ld = 1024; ch = (b - 96) & 1; rg = (b - 96) >> 1; }
    else { w = w3; dst = cs_w3; ld = F_; ch = (b - 128) % 7; rg = (b - 128) / 7; }
    const int lane = tid & 63, wid = tid >> 6, row = rg * 64 + wid * 16;
    u32x4 v[16];
#pragma unroll
    for (int r = 0; r < 16; ++r) v[r] = *reinterpret_cast<const u32x4*>(w + (size_t)(row + r) * ld + ch * 512 + lane * 8);
    if (b < 128 && (lane >> 3) == 3) {
      const int cpp = b < 96 ? 384 : 128;
      char* cpd = wc + (b < 96 ? WC_QKV + (ch & 1) * (1024 * 384) + (ch >> 1) * 128 : WC_W1 + ch * (1024 * 128)) + row * cpp + (lane & 7) * 16;
#pragma unroll
      for (int r = 0; r < 16; ++r) *reinterpret_cast<u32x4*>(cpd + r * cpp) = v[r];
    }
    f32x4 s0 = (f32x4){0.f, 0.f, 0.f, 0.f}, s1 = s0;
#pragma unroll
    for (int r = 0; r < 16; ++r) {
      s0 += (f32x4){bflo(v[r][0]), bfhi(v[r][0]), bflo(v[r][1]), bfhi(v[r][1])};
      s1 += (f32x4){bflo(v[r][2]), bfhi(v[r][2]), bflo(v[r][3]), bfhi(v[r][3])};
    }
    *reinterpret_cast<f32x4*>(&red[wid][lane * 8]) = s0;
    *reinterpret_cast<f32x4*>(&red[wid][lane * 8 + 4]) = s1;
    __syncthreads();
    f32x2 t = *reinterpret_cast<const f32x2*>(&red[0][tid * 2]);
#pragma unroll
    for (int i = 1; i < 4; ++i) t += *reinterpret_cast<const f32x2*>(&red[i][tid * 2]);
    *reinterpret_cast<f32x2*>(dst + rg * ld + ch * 512 + tid * 2) = t;
  } else {
    const int lane = tid & 63, wid = tid >> 6;
    const int rbase = (b - 240) * 16 + wid * 4;
    u32x4 xv[8];
#pragma unroll
    for (int i = 0; i < 8; ++i) xv[i] = *reinterpret_cast<const u32x4*>(x + (size_t)rbase * D_ + i * 512 + lane * 8);
    __builtin_amdgcn_sched_barrier(0);
#pragma unroll
    for (int rr = 0; rr < 4; ++rr) {
      const int row = rbase + rr;
#pragma unroll
      for (int hf = 0; hf < 2; ++hf) {
        const u32x4 v = xv[rr * 2 + hf];
        float y[8];
#pragma unroll
        for (int i = 0; i < 4; ++i) { y[2 * i] = bflo(v[i]); y[2 * i + 1] = bfhi(v[i]); }
        float sm = ((y[0] + y[1]) + (y[2] + y[3])) + ((y[4] + y[5]) + (y[6] + y[7]));
        sm = rsum8(sm);
        float mu = sm * (1.0f / 64.f), m2 = 0.f;
#pragma unroll
        for (int i = 0; i < 8; ++i) { float d = y[i] - mu; m2 += d * d; }
        m2 = rsum8(m2);
        if ((lane & 7) == 0) st[(size_t)row * 16 + hf * 8 + (lane >> 3)] = (f32x2){sm, m2};
      }
    }
  }
}

extern "C" void kernel_launch(void* const* d_in, const int* in_sizes, int n_in, void* d_out, int out_size, void* d_ws,
                              size_t ws_size, hipStream_t stream) {
  const u16* x_in = (const u16*)d_in[0];
  const u16* wqkv = (const u16*)d_in[1];
  const u16* wphi = (const u16*)d_in[2];
  const u16* wo = (const u16*)d_in[3];
  const u16* w1 = (const u16*)d_in[5];
  const u16* w2 = (const u16*)d_in[6];
  const u16* w3 = (const u16*)d_in[7];
  const u16* w4 = (const u16*)d_in[8];
  char* ws = (char*)d_ws;
  size_t off = 0;
  auto alloc = [&](size_t bytes) { char* p = ws + off; off += (bytes + 4095) & ~(size_t)4095; return p; };
  u16* xb = (u16*)alloc((size_t)S_ * D_ * 2);
  u16* x2 = (u16*)alloc((size_t)S_ * D_ * 2);
  u16* x1 = (u16*)alloc((size_t)S_ * D_ * 2);
  u16* hs = (u16*)alloc((size_t)S_ * D_ * 2);
  u16* ob = (u16*)alloc((size_t)S_ * D_ * 2);
  u16* gb = (u16*)alloc((size_t)S_ * F_ * 2);
  u16* phiq = (u16*)alloc((size_t)S_ * 2048 * 2);
  float* kvp = (float*)alloc((size_t)32 * 16 * 2048 * 4);
  float* zp = (float*)alloc((size_t)32 * 16 * 64 * 4);
  f32x2* st_x = (f32x2*)alloc((size_t)S_ * 16 * 8);
  f32x2* st_x2 = (f32x2*)alloc((size_t)S_ * 16 * 8);
  f32x2* st_x1 = (f32x2*)alloc((size_t)S_ * 16 * 8);
  float* cs_qkv = (float*)alloc(16 * 3072 * 4);
  float* cs_w1 = (float*)alloc(16 * 1024 * 4);
  float* cs_w3 = (float*)alloc(16 * F_ * 4);
  char* wc = alloc(WC_END);

  prep_kernel<<<368 + 176, 256, 0, stream>>>(x_in, wqkv, w1, w3, cs_qkv, cs_w1, cs_w3, st_x, wo, w2, w4, wc);
  for (int layer = 0; layer < 2; ++layer) {
    const u16* x = layer == 0 ? x_in : xb;
    u16* xo = layer == 0 ? xb : (u16*)d_out;
    gemm_kernel<192, 3, 1024, 1024, 3072, EPI_QKV><<<256, 512, 0, stream>>>(x, wqkv, (const u16*)(wc + WC_QKV), cs_qkv, st_x, nullptr, nullptr, phiq, nullptr, wphi, kvp, zp);
    attn_out_kernel<<<256, 256, 0, stream>>>(phiq, kvp, zp, ob);
    gemm_kernel<64, 5, 1024, 1024, 1024, EPI_WO><<<256, 512, 0, stream>>>(ob, wo, (const u16*)(wc + WC_WO), nullptr, nullptr, x, nullptr, x2, st_x2, nullptr, nullptr, nullptr);
    gemm_kernel<64, 5, 1024, 1024, 1024, EPI_W1><<<256, 512, 0, stream>>>(x2, w1, (const u16*)(wc + WC_W1), cs_w1, st_x2, nullptr, nullptr, hs, nullptr, nullptr, nullptr, nullptr);
    gemm_kernel<64, 5, 1024, 1024, 1024, EPI_W2><<<256, 512, 0, stream>>>(hs, w2, (const u16*)(wc + WC_W2), nullptr, st_x2, x2, nullptr, x1, st_x1, nullptr, nullptr, nullptr);
    gemm_kernel<224, 3, 1024, 1024, F_, EPI_W3><<<256, 512, 0, stream>>>(x1, w3, nullptr, cs_w3, st_x1, nullptr, nullptr, gb, nullptr, nullptr, nullptr, nullptr);
    gemm_kernel<64, 5, F_, F_, 1024, EPI_W4><<<256, 512, 0, stream>>>(gb, w4, (const u16*)(wc + WC_W4), nullptr, nullptr, x2, x1, xo, st_x, nullptr, nullptr, nullptr);
  }
}
```

```cpp
#include <hip/hip_runtime.h>
#include <hip/hip_bf16.h>
#include <stdint.h>
#include <type_traits>

typedef short bf16x8 __attribute__((ext_vector_type(8)));
typedef short s16x4 __attribute__((ext_vector_type(4)));
typedef float f32x4 __attribute__((ext_vector_type(4)));
typedef float f32x2 __attribute__((ext_vector_type(2)));
typedef unsigned u32x2 __attribute__((ext_vector_type(2)));
typedef unsigned u32x4 __attribute__((ext_vector_type(4)));
typedef __attribute__((address_space(3))) s16x4* lds_s16x4_p;
typedef unsigned short u16;

constexpr int S_ = 2048, D_ = 1024, H_ = 32, F_ = 3584;

__device__ __forceinline__ float bf2f(u16 v) { return __uint_as_float(((unsigned)v) << 16); }
__device__ __forceinline__ float bflo(unsigned v) { return __uint_as_float(v << 16); }
__device__ __forceinline__ float bfhi(unsigned v) { return __uint_as_float(v & 0xffff0000u); }
typedef __bf16 bf16x2_t __attribute__((ext_vector_type(2)));
__device__ __forceinline__ unsigned pk2(float a, float b) {
  return __builtin_bit_cast(unsigned, __builtin_convertvector((f32x2){a, b}, bf16x2_t));
}
__device__ __forceinline__ unsigned pk2v(f32x2 v) { return __builtin_bit_cast(unsigned, __builtin_convertvector(v, bf16x2_t)); }
__device__ __forceinline__ float max2_(float a, float b) { float r; asm("v_max_f32 %0, %1, %2" : "=v"(r) : "v"(a), "v"(b)); return r; }
__device__ __forceinline__ float min2_(float a, float b) { float r; asm("v_min_f32 %0, %1, %2" : "=v"(r) : "v"(a), "v"(b)); return r; }
__device__ __forceinline__ float max3_(float a, float b, float c) { float r; asm("v_max3_f32 %0, %1, %2, %3" : "=v"(r) : "v"(a), "v"(b), "v"(c)); return r; }
__device__ __forceinline__ float min3_(float a, float b, float c) { float r; asm("v_min3_f32 %0, %1, %2, %3" : "=v"(r) : "v"(a), "v"(b), "v"(c)); return r; }
__device__ __forceinline__ float rbf(float a) { return bflo(pk2(a, 0.f)); }
__device__ __forceinline__ float gelu_t(float x) {
  const float c1 = -2.0f * 0.7978845608028654f * 1.4426950408889634f;
  const float c2 = c1 * 0.044715f;
  float t = x * x;
  float w = x * __builtin_fmaf(t, c2, c1);
  float e = __builtin_amdgcn_exp2f(w);
  return x * __builtin_amdgcn_rcpf(1.0f + e);
}

__device__ __forceinline__ f32x2 gelu2(f32x2 x) {
  const float c1 = -2.0f * 0.7978845608028654f * 1.4426950408889634f;
  const float c2 = c1 * 0.044715f;
  f32x2 t = x * x;
  f32x2 w = x * (t * c2 + c1);
  f32x2 d = (f32x2){__builtin_amdgcn_exp2f(w[0]), __builtin_amdgcn_exp2f(w[1])} + 1.0f;
  return x * (f32x2){__builtin_amdgcn_rcpf(d[0]), __builtin_amdgcn_rcpf(d[1])};
}
__device__ __forceinline__ void x16(float v, float& a, float& b) {
  auto r = __builtin_amdgcn_permlane16_swap(__float_as_uint(v), __float_as_uint(v), false, false);
  a = __uint_as_float(r[0]); b = __uint_as_float(r[1]);
}
__device__ __forceinline__ void x32(float v, float& a, float& b) {
  auto r = __builtin_amdgcn_permlane32_swap(__float_as_uint(v), __float_as_uint(v), false, false);
  a = __uint_as_float(r[0]); b = __uint_as_float(r[1]);
}
__device__ __forceinline__ float rmax4(float v) { float a, b; x16(v, a, b); v = max2_(a, b); x32(v, a, b); return max2_(a, b); }
__device__ __forceinline__ float rmin4(float v) { float a, b; x16(v, a, b); v = min2_(a, b); x32(v, a, b); return min2_(a, b); }
__device__ __forceinline__ float rsum4(float v) { float a, b; x16(v, a, b); v = a + b; x32(v, a, b); return a + b; }

__device__ __forceinline__ float rsum8(float v) {
  v += __uint_as_float(__builtin_amdgcn_mov_dpp(__float_as_uint(v), 0xB1, 0xF, 0xF, true));
  v += __uint_as_float(__builtin_amdgcn_mov_dpp(__float_as_uint(v), 0x4E, 0xF, 0xF, true));
  v += __uint_as_float(__builtin_amdgcn_mov_dpp(__float_as_uint(v), 0x141, 0xF, 0xF, true));
  return v;
}
#define WAIT_V(n) asm volatile("s_waitcnt vmcnt(" #n ")" ::: "memory")
#define WAIT_L0() asm volatile("s_waitcnt lgkmcnt(0)" ::: "memory")
template <int N> __device__ __forceinline__ void wait_vm() {
  asm volatile("s_waitcnt vmcnt(%0)" ::"i"(N) : "memory");
}

__device__ __forceinline__ void glds16(unsigned voff, const void* sbase, unsigned lds) {
  unsigned keep;
  asm volatile("s_mov_b32 %0, m0\n\ts_mov_b32 m0, %3\n\ts_nop 0\n\tglobal_load_lds_dwordx4 %1, %2\n\ts_mov_b32 m0, %0"
               : "=&s"(keep) : "v"(voff), "s"(sbase), "s"(lds) : "memory");
}

__device__ __forceinline__ void mfma_acc(f32x4& c, const u32x4& a, const u32x4& b) {
  asm volatile("v_mfma_f32_16x16x32_bf16 %0, %1, %2, %0" : "+a"(c) : "v"(a), "v"(b));
}

#define STAMP(i)

enum { EPI_QKV = 0, EPI_WO = 1, EPI_W1 = 2, EPI_W2 = 3, EPI_W3 = 4, EPI_W4 = 5 };

template <int BN, int NST, int KD, int LDA, int LDB, int EPI>
__global__ __launch_bounds__(512, 2) void gemm_kernel(
    const u16* __restrict__ A, const u16* __restrict__ W,
    const u16* __restrict__ Wc,
    const float* __restrict__ cs,
    const f32x2* __restrict__ st_in,
    const u16* __restrict__ r0,
    const u16* __restrict__ r1,
    u16* __restrict__ out,
    f32x2* __restrict__ st_out,
    const u16* __restrict__ wphi,
    float* __restrict__ kvp,
    float* __restrict__ zp)
{
  constexpr int NFULL = BN / 64, HALF = (BN % 64) / 32;
  constexpr int A_BYTES = 128 * 64 * 2, B_BYTES = BN * 64 * 2, STAGE = A_BYTES + B_BYTES;
  constexpr int NT = KD / 64, NTW = 2 * NFULL + HALF, LPS = 4 + 2 * NFULL + HALF;
  constexpr int PB = BN * 2 + 16;
  constexpr int EPI_BYTES = (EPI == EPI_QKV) ? (4 * 64 * 144 + 2 * (64 * 32 + 64) * 4) : 128 * PB;
  constexpr int MAINB = NST * STAGE;
  constexpr int RS_OFF = (MAINB > EPI_BYTES ? MAINB : EPI_BYTES);
  constexpr int CS_OFF = RS_OFF + 128 * 16;
  constexpr int WP_OFF = CS_OFF + 1024;
  constexpr int SMEM = WP_OFF + (EPI == EPI_QKV ? 4096 : 0);
  constexpr bool HAS_CS = (EPI == EPI_QKV || EPI == EPI_W1 || EPI == EPI_W3);
  constexpr bool HAS_ST = (EPI == EPI_QKV || EPI == EPI_W1 || EPI == EPI_W3 || EPI == EPI_W2);
  constexpr bool HAS_RES = (EPI == EPI_WO || EPI == EPI_W2 || EPI == EPI_W4);
  constexpr int LAG = (BN == 64) ? 1 : 0;
  constexpr int CH = BN / 8, NIT = CH / 4;
  static_assert(SMEM <= 163840, "LDS");
  __shared__ __attribute__((aligned(16))) char smem[SMEM];

  STAMP(0);
  const int tid = threadIdx.x, lane = tid & 63;
  const int wid8 = __builtin_amdgcn_readfirstlane(tid >> 6), wid = wid8 & 3, wm = wid >> 1, wn = wid & 1;
  const int g = lane >> 4, l15 = lane & 15;
  const int xcd = blockIdx.x & 7, idx = blockIdx.x >> 3;
  const int tm = (xcd >> 1) * 4 + (idx & 3), tn = (xcd & 1) * 8 + (idx >> 2);
  const int row0 = tm * 128;
  f32x4* rs = reinterpret_cast<f32x4*>(smem + RS_OFF);

  f32x4 acc[4][NTW];
  u32x4 ra[HAS_RES ? NIT : 1], rb[(EPI == EPI_W4) ? NIT : 1];

  if (wid8 >= 4) {
    unsigned voA[4], voB[NFULL > 0 ? NFULL : 1][2], voH = 0;
    const bool pk_ = (HALF == 0) && ((tn & 7) == 3);
    const unsigned pitchB = pk_ ? NFULL * 128 : LDB * 2;
#pragma unroll
    for (int i = 0; i < 4; ++i) {
      int r = (wid * 4 + i) * 8 + (lane >> 3);
      int ch = (lane & 7) ^ ((r >> 1) & 7);
      voA[i] = (unsigned)(row0 + r) * (LDA * 2) + ch * 16;
    }
#pragma unroll
    for (int b = 0; b < NFULL; ++b) {
      int colb = (EPI == EPI_QKV) ? (b * 1024 + tn * 64) : (tn * BN + b * 64);
#pragma unroll
      for (int j = 0; j < 2; ++j) {
        int r = (wid * 2 + j) * 8 + (lane >> 3);
        int f = ((r >> 1) & 1) | (((r >> 3) & 1) << 1);
        int chp = lane & 7;
        int gch = (((chp >> 1) ^ f) << 1) | (chp & 1);
        voB[b][j] = (unsigned)r * pitchB + (pk_ ? b * 128 : colb * 2) + gch * 16;
      }
    }
    if constexpr (HALF) {
      int r = wid * 16 + (lane >> 2);
      int chp = lane & 3;
      int gch = (((chp >> 1) ^ ((r >> 3) & 1)) << 1) | (chp & 1);
      voH = (unsigned)r * (LDB * 2) + (tn * BN + NFULL * 64) * 2 + gch * 16;
    }
    const char* Ab = reinterpret_cast<const char*>(A);
    const char* Wb = pk_ ? reinterpret_cast<const char*>(Wc) + (size_t)(tn >> 3) * (KD * NFULL * 128) : reinterpret_cast<const char*>(W);
    const unsigned lbase = (unsigned)(uintptr_t)(__attribute__((address_space(3))) char*)smem;
    auto stage = [&](int kt, int buf) {
      const char* a = Ab + (size_t)kt * 128;
      const char* w = Wb + (size_t)(kt * (64 * pitchB));
      const unsigned s = lbase + buf * STAGE;
#pragma unroll
      for (int i = 0; i < 4; ++i) glds16(voA[i], a, s + (wid * 4 + i) * 1024);
#pragma unroll
      for (int b = 0; b < NFULL; ++b)
#pragma unroll
        for (int j = 0; j < 2; ++j) glds16(voB[b][j], w, s + A_BYTES + b * 8192 + (wid * 2 + j) * 1024);
      if constexpr (HALF) glds16(voH, w, s + A_BYTES + NFULL * 8192 + wid * 1024);
    };
#pragma unroll
    for (int s = 0; s < NST; ++s) stage(s, s);
    constexpr int XL = HAS_CS ? 16 : 0;
    f32x4 stp[4];
    const int lt = tid - 256;
    u32x4 wpx;
    if constexpr (EPI == EPI_QKV) wpx = *reinterpret_cast<const u32x4*>(wphi + tn * 2048 + lt * 8);
    if constexpr (HAS_ST) {
      const f32x4* sp = reinterpret_cast<const f32x4*>(st_in + (size_t)(row0 + (lt >> 1)) * 16 + (lt & 1) * 8);
#pragma unroll
      for (int i = 0; i < 4; ++i) stp[i] = sp[i];
    }
    if constexpr (HAS_RES) {
#pragma unroll
      for (int it = 0; it < NIT; ++it) {
        const int c = tid + 512 * it, r = c / CH, ch = c % CH;
        ra[it] = *reinterpret_cast<const u32x4*>(r0 + (size_t)(row0 + r) * D_ + tn * BN + ch * 8);
        if constexpr (EPI == EPI_W4) rb[it] = *reinterpret_cast<const u32x4*>(r1 + (size_t)(row0 + r) * D_ + tn * BN + ch * 8);
      }
    }
    asm volatile("" ::: "memory");
    wait_vm<(NST - 1 - LAG) * LPS>();
    __builtin_amdgcn_s_barrier();
    int buf = 0;
    for (int kt = 0; kt < NT - NST; ++kt) {
      wait_vm<(NST - 2 - LAG) * LPS>();
      __builtin_amdgcn_s_barrier();
      stage(kt + NST, buf);
      buf = buf + 1 == NST ? 0 : buf + 1;
    }
    asm volatile("" ::: "memory");
    f32x4 csp[HAS_CS ? 16 : 1];
    if constexpr (HAS_CS) {
      const int c4 = lt < BN / 4 ? lt : 0;
      const float* cp = cs + (EPI == EPI_QKV ? (c4 >> 4) * 1024 + tn * 64 + (c4 & 15) * 4 : tn * BN + c4 * 4);
#pragma unroll
      for (int i = 0; i < 16; ++i) csp[i] = *reinterpret_cast<const f32x4*>(cp + i * LDB);
    }
    asm volatile("" ::: "memory");
#pragma unroll
    for (int r = 0; r < NST - 1; ++r) {
      constexpr int NSL = NST - LAG;
      if (r == 0) wait_vm<(NSL - 2) * LPS + XL>();
      else if (r == 1) wait_vm<(NSL > 3 ? NSL - 3 : 0) * LPS + XL>();
      else if (r == 2) wait_vm<(NSL > 4 ? NSL - 4 : 0) * LPS + XL>();
      else if (r == 3) wait_vm<(NSL > 5 ? NSL - 5 : 0) * LPS + XL>();
      else wait_vm<XL>();
      __builtin_amdgcn_s_barrier();
    }
    if constexpr (HAS_ST) {
#pragma unroll
      for (int i = 0; i < 4; ++i) asm volatile("" : "+v"(stp[i]));
      float sm = 0.f;
#pragma unroll
      for (int i = 0; i < 4; ++i) sm += stp[i][0] + stp[i][2];
      sm += __shfl_xor(sm, 1);
      float mean = sm * (1.0f / 1024.f);
      float m2 = 0.f;
#pragma unroll
      for (int i = 0; i < 4; ++i) {
        float d0 = stp[i][0] * (1.0f / 64.f) - mean, d1 = stp[i][2] * (1.0f / 64.f) - mean;
        m2 += stp[i][1] + stp[i][3] + 64.f * (d0 * d0 + d1 * d1);
      }
      m2 += __shfl_xor(m2, 1);
      float var = m2 * (1.0f / 1024.f);
      float r1_ = 1.0f / sqrtf(var + 1e-5f);
      float sc = r1_;
      if constexpr (EPI == EPI_W1) sc = r1_ / sqrtf(var * r1_ * r1_ + 1e-5f);
      if ((lt & 1) == 0) rs[lt >> 1] = (f32x4){mean, sc, r1_, 0.f};
    }
    if constexpr (HAS_CS) {
#pragma unroll
      for (int i = 0; i < 16; ++i) asm volatile("" : "+v"(csp[i]));
#pragma unroll
      for (int i = 8; i > 0; i >>= 1)
#pragma unroll
        for (int j = 0; j < i; ++j) csp[j] += csp[j + i];
      if (lt < BN / 4) *reinterpret_cast<f32x4*>(smem + CS_OFF + lt * 16) = csp[0];
    }
    if constexpr (EPI == EPI_QKV) *reinterpret_cast<u32x4*>(smem + WP_OFF + lt * 16) = wpx;
  } else {
    if constexpr (HAS_RES) {
#pragma unroll
      for (int it = 0; it < NIT; ++it) {
        const int c = tid + 512 * it, r = c / CH, ch = c % CH;
        ra[it] = *reinterpret_cast<const u32x4*>(r0 + (size_t)(row0 + r) * D_ + tn * BN + ch * 8);
        if constexpr (EPI == EPI_W4) rb[it] = *reinterpret_cast<const u32x4*>(r1 + (size_t)(row0 + r) * D_ + tn * BN + ch * 8);
      }
    }
    const int q = l15 >> 2, p = lane & 3;
    const int fB = (q >> 1) | ((g & 1) << 1);
    unsigned aoff[2], boff[2], hoff = 0;
#pragma unroll
    for (int ks = 0; ks < 2; ++ks) aoff[ks] = (wm * 64 + l15) * 128 + (((4 * ks + g) ^ (l15 >> 1)) * 16);
#pragma unroll
    for (int u = 0; u < 2; ++u) boff[u] = A_BYTES + (8 * g + q) * 128 + (((2 * wn + u) ^ fB) * 32) + p * 8;
    if constexpr (HALF) hoff = A_BYTES + NFULL * 8192 + (8 * g + q) * 64 + ((wn ^ (g & 1)) * 32) + p * 8;

    auto ldf = [&](int i, const char* s, int ks, u32x4(&af)[4], u32x4(&bfr)[NTW]) {
      if (i == 0 || i > NTW) {
        const int mt = i == 0 ? 0 : i - NTW;
        af[mt] = *reinterpret_cast<const u32x4*>(s + aoff[ks] + mt * 2048);
      } else if (i - 1 < 2 * NFULL) {
        const int b = (i - 1) >> 1, u = (i - 1) & 1;
        s16x4 lo = __builtin_amdgcn_ds_read_tr16_b64_v4i16((lds_s16x4_p)(s + boff[u] + b * 8192 + ks * 4096));
        s16x4 hi = __builtin_amdgcn_ds_read_tr16_b64_v4i16((lds_s16x4_p)(s + boff[u] + b * 8192 + ks * 4096 + 512));
        bfr[i - 1] = __builtin_shufflevector(__builtin_bit_cast(u32x2, lo), __builtin_bit_cast(u32x2, hi), 0, 1, 2, 3);
      } else {
        s16x4 lo = __builtin_amdgcn_ds_read_tr16_b64_v4i16((lds_s16x4_p)(s + hoff + ks * 2048));
        s16x4 hi = __builtin_amdgcn_ds_read_tr16_b64_v4i16((lds_s16x4_p)(s + hoff + ks * 2048 + 256));
        bfr[NTW - 1] = __builtin_shufflevector(__builtin_bit_cast(u32x2, lo), __builtin_bit_cast(u32x2, hi), 0, 1, 2, 3);
      }
    };
    constexpr int NM = 4 * NTW, NL = NTW + 4;
    auto phase = [&](u32x4(&afc)[4], u32x4(&bfc)[NTW], u32x4(&afn)[4], u32x4(&bfn)[NTW], int nbuf, int nks, auto rd, auto bar) {
      const char* s = smem + nbuf * STAGE;
      constexpr int BAR = decltype(bar)::value, R0 = BAR < 0 ? 0 : BAR + 1;
      constexpr int RSP = (NM - R0 - 2) / NL > 0 ? (NM - R0 - 2) / NL : 1;
      static_assert(R0 + (NL - 1) * RSP < NM, "reads");
#pragma unroll
      for (int mt = 0; mt < 4; ++mt)
#pragma unroll
        for (int j = 0; j < NTW; ++j) {
          const int m = mt * NTW + j;
          if (m == BAR) {
            WAIT_L0();
            __builtin_amdgcn_s_barrier();
            asm volatile("" ::: "memory");
          }
          if (decltype(rd)::value && m >= R0 && (m - R0) % RSP == 0 && (m - R0) / RSP < NL) ldf((m - R0) / RSP, s, nks, afn, bfn);
          if (EPI == EPI_QKV && j >= 4) mfma_acc(acc[mt][j], afc[mt], bfc[j]);
          else mfma_acc(acc[mt][j], bfc[j], afc[mt]);
          __builtin_amdgcn_sched_barrier(0);
        }
    };
    typedef std::integral_constant<int, -1> nobar;
#pragma unroll
    for (int mt = 0; mt < 4; ++mt)
#pragma unroll
      for (int j = 0; j < NTW; ++j) {
        acc[mt][j] = (f32x4){0.f, 0.f, 0.f, 0.f};
        asm volatile("" : "+a"(acc[mt][j]));
      }
    u32x4 af0[4], af1[4], bf0[NTW], bf1[NTW];
    __builtin_amdgcn_s_barrier();
    asm volatile("" ::: "memory");
    STAMP(1);
#pragma unroll
    for (int i = 0; i < NL; ++i) { ldf(i, smem, 0, af0, bf0); __builtin_amdgcn_sched_barrier(0); }
    int buf = 0;
    if constexpr (LAG) {
      phase(af0, bf0, af1, bf1, 0, 1, std::true_type{}, nobar{});
      for (int kt = 0; kt < NT - 1; ++kt) {
        buf = buf + 1 == NST ? 0 : buf + 1;
        phase(af1, bf1, af0, bf0, buf, 0, std::true_type{}, nobar{});
        asm volatile("" ::: "memory");
        __builtin_amdgcn_s_barrier();
        asm volatile("" ::: "memory");
        phase(af0, bf0, af1, bf1, buf, 1, std::true_type{}, nobar{});
      }
      phase(af1, bf1, af0, bf0, buf, 0, std::false_type{}, nobar{});
    } else {
      for (int kt = 0; kt < NT - 1; ++kt) {
        phase(af0, bf0, af1, bf1, buf, 1, std::true_type{}, nobar{});
        buf = buf + 1 == NST ? 0 : buf + 1;
        phase(af1, bf1, af0, bf0, buf, 0, std::true_type{}, std::integral_constant<int, 4>{});
      }
      phase(af0, bf0, af1, bf1, buf, 1, std::true_type{}, nobar{});
      phase(af1, bf1, af0, bf0, buf, 0, std::false_type{}, nobar{});
    }
    asm volatile("s_nop 7\n\ts_nop 7\n\ts_nop 7" ::: "memory");
  }
  WAIT_L0();
  __builtin_amdgcn_s_barrier();
  asm volatile("" ::: "memory");
  STAMP(2);

  if constexpr (EPI == EPI_QKV) {
    const int h = tn * 2 + wn;
    const int q = l15 >> 2, p = lane & 3;
    f32x4 kv[4][2], zz[4];
    float* red = reinterpret_cast<float*>(smem + 4 * 64 * 144) + wn * (64 * 32 + 64);
    if (wid8 < 4) {
      bf16x8 wf[2];
#pragma unroll
      for (int ft = 0; ft < 2; ++ft) {
        const char* src = smem + WP_OFF + wn * 2048 + (4 * g + q) * 64 + ft * 32 + p * 8;
        s16x4 lo = __builtin_amdgcn_ds_read_tr16_b64_v4i16((lds_s16x4_p)(src));
        s16x4 hi = __builtin_amdgcn_ds_read_tr16_b64_v4i16((lds_s16x4_p)(src + 16 * 64));
        wf[ft] = __builtin_shufflevector(lo, hi, 0, 1, 2, 3, 4, 5, 6, 7);
      }
      const float* csl = reinterpret_cast<const float*>(smem + CS_OFF);
      f32x4 cs4[4];
      float csv[2];
#pragma unroll
      for (int t = 0; t < 2; ++t) {
        cs4[t] = *reinterpret_cast<const f32x4*>(csl + wn * 32 + t * 16 + 4 * g);
        cs4[2 + t] = *reinterpret_cast<const f32x4*>(csl + 64 + wn * 32 + t * 16 + 4 * g);
        csv[t] = csl[128 + wn * 32 + t * 16 + l15];
      }
      bf16x8 qf[4], kf[4], vf[2][2];
#pragma unroll
      for (int mt = 0; mt < 4; ++mt) {
        f32x4 st = rs[wm * 64 + mt * 16 + l15];
        unsigned pq_[4], pk_[4];
#pragma unroll
        for (int t = 0; t < 2; ++t) {
          float a[4], b[4];
#pragma unroll
          for (int i = 0; i < 4; ++i) {
            a[i] = st[1] * (acc[mt][t][i] - st[0] * cs4[t][i]);
            b[i] = st[1] * (acc[mt][2 + t][i] - st[0] * cs4[2 + t][i]);
          }
          pq_[t * 2] = pk2(a[0], a[1]); pq_[t * 2 + 1] = pk2(a[2], a[3]);
          pk_[t * 2] = pk2(b[0], b[1]); pk_[t * 2 + 1] = pk2(b[2], b[3]);
        }
        qf[mt] = __builtin_bit_cast(bf16x8, (u32x4){pq_[0], pq_[1], pq_[2], pq_[3]});
        kf[mt] = __builtin_bit_cast(bf16x8, (u32x4){pk_[0], pk_[1], pk_[2], pk_[3]});
      }
#pragma unroll
      for (int s = 0; s < 2; ++s) {
        unsigned pv[2][4];
#pragma unroll
        for (int m2 = 0; m2 < 2; ++m2) {
          int mt = s * 2 + m2;
          f32x4 s0 = rs[wm * 64 + mt * 16 + 4 * g + 0], s1 = rs[wm * 64 + mt * 16 + 4 * g + 1];
          f32x4 s2 = rs[wm * 64 + mt * 16 + 4 * g + 2], s3 = rs[wm * 64 + mt * 16 + 4 * g + 3];
#pragma unroll
          for (int t = 0; t < 2; ++t) {
            float a0 = s0[1] * (acc[mt][4 + t][0] - s0[0] * csv[t]);
            float a1 = s1[1] * (acc[mt][4 + t][1] - s1[0] * csv[t]);
            float a2 = s2[1] * (acc[mt][4 + t][2] - s2[0] * csv[t]);
            float a3 = s3[1] * (acc[mt][4 + t][3] - s3[0] * csv[t]);
            pv[t][m2 * 2] = pk2(a0, a1); pv[t][m2 * 2 + 1] = pk2(a2, a3);
          }
        }
#pragma unroll
        for (int t = 0; t < 2; ++t) vf[s][t] = __builtin_bit_cast(bf16x8, (u32x4){pv[t][0], pv[t][1], pv[t][2], pv[t][3]});
      }
      char* pkl = smem + wid * (64 * 144);
      const float L2E = 1.4426950408889634f;
#pragma unroll
      for (int which = 0; which < 2; ++which)
#pragma unroll
        for (int mt = 0; mt < 4; ++mt) {
          f32x4 z4 = (f32x4){0.f, 0.f, 0.f, 0.f};
          f32x4 p0 = __builtin_amdgcn_mfma_f32_16x16x32_bf16(wf[0], which ? kf[mt] : qf[mt], z4, 0, 0, 0);
          f32x4 p1 = __builtin_amdgcn_mfma_f32_16x16x32_bf16(wf[1], which ? kf[mt] : qf[mt], z4, 0, 0, 0);
          f32x2 v[4];
          {
            const unsigned r0_ = pk2(p0[0], p0[1]), r1_ = pk2(p0[2], p0[3]), r2_ = pk2(p1[0], p1[1]), r3_ = pk2(p1[2], p1[3]);
            v[0] = (f32x2){bflo(r0_), bfhi(r0_)}; v[1] = (f32x2){bflo(r1_), bfhi(r1_)};
            v[2] = (f32x2){bflo(r2_), bfhi(r2_)}; v[3] = (f32x2){bflo(r3_), bfhi(r3_)};
          }
          float mx = max2_(max3_(max3_(v[0][0], v[0][1], v[1][0]), v[1][1], v[2][0]), max3_(v[2][1], v[3][0], v[3][1]));
          float mn = min2_(min3_(min3_(v[0][0], v[0][1], v[1][0]), v[1][1], v[2][0]), min3_(v[2][1], v[3][0], v[3][1]));
          mx = rmax4(mx); mn = rmin4(mn);
          const float mxl = -mx * L2E, mnl = mn * L2E;
          f32x2 ep[4], en[4];
#pragma unroll
          for (int i = 0; i < 4; ++i) {
            const f32x2 ap = v[i] * L2E + mxl, an = v[i] * (-L2E) + mnl;
            ep[i] = (f32x2){__builtin_amdgcn_exp2f(ap[0]), __builtin_amdgcn_exp2f(ap[1])};
            en[i] = (f32x2){__builtin_amdgcn_exp2f(an[0]), __builtin_amdgcn_exp2f(an[1])};
          }
          const f32x2 sp2 = (ep[0] + ep[1]) + (ep[2] + ep[3]), sn2 = (en[0] + en[1]) + (en[2] + en[3]);
          const float ip = __builtin_amdgcn_rcpf(rsum4(sp2[0] + sp2[1])), in_ = __builtin_amdgcn_rcpf(rsum4(sn2[0] + sn2[1]));
          u32x2 o[4];
          o[0] = (u32x2){pk2v(ep[0] * ip), pk2v(ep[1] * ip)};
          o[1] = (u32x2){pk2v(ep[2] * ip), pk2v(ep[3] * ip)};
          o[2] = (u32x2){pk2v(en[0] * in_), pk2v(en[1] * in_)};
          o[3] = (u32x2){pk2v(en[2] * in_), pk2v(en[3] * in_)};
          if (which == 0) {
            u16* dst = out + ((size_t)(row0 + wm * 64 + mt * 16 + l15) * 32 + h) * 64 + 4 * g;
#pragma unroll
            for (int c = 0; c < 4; ++c) *reinterpret_cast<u32x2*>(dst + c * 16) = o[c];
          } else {
            char* dst = pkl + (mt * 16 + l15) * 144 + g * 8;
#pragma unroll
            for (int c = 0; c < 4; ++c) *reinterpret_cast<u32x2*>(dst + c * 32) = o[c];
          }
        }
      WAIT_L0();
      __builtin_amdgcn_sched_barrier(0);
#pragma unroll
      for (int ft = 0; ft < 4; ++ft) { kv[ft][0] = kv[ft][1] = zz[ft] = (f32x4){0.f, 0.f, 0.f, 0.f}; }
      const bf16x8 ones = (bf16x8){0x3F80, 0x3F80, 0x3F80, 0x3F80, 0x3F80, 0x3F80, 0x3F80, 0x3F80};
#pragma unroll
      for (int s = 0; s < 2; ++s)
#pragma unroll
        for (int ft = 0; ft < 4; ++ft) {
          const char* src = pkl + (32 * s + 4 * g + q) * 144 + ft * 32 + p * 8;
          s16x4 lo = __builtin_amdgcn_ds_read_tr16_b64_v4i16((lds_s16x4_p)(src));
          s16x4 hi = __builtin_amdgcn_ds_read_tr16_b64_v4i16((lds_s16x4_p)(src + 16 * 144));
          bf16x8 pf = __builtin_shufflevector(lo, hi, 0, 1, 2, 3, 4, 5, 6, 7);
          kv[ft][0] = __builtin_amdgcn_mfma_f32_16x16x32_bf16(vf[s][0], pf, kv[ft][0], 0, 0, 0);
          kv[ft][1] = __builtin_amdgcn_mfma_f32_16x16x32_bf16(vf[s][1], pf, kv[ft][1], 0, 0, 0);
          zz[ft] = __builtin_amdgcn_mfma_f32_16x16x32_bf16(ones, pf, zz[ft], 0, 0, 0);
        }
      if (wm == 1) {
#pragma unroll
        for (int ft = 0; ft < 4; ++ft) {
#pragma unroll
          for (int dt = 0; dt < 2; ++dt) *reinterpret_cast<f32x4*>(red + (ft * 16 + l15) * 32 + dt * 16 + 4 * g) = kv[ft][dt];
          if (g == 0) red[64 * 32 + ft * 16 + l15] = zz[ft][0];
        }
      }
    }
    __syncthreads();
    if (wid8 < 2) {
      float* kd = kvp + (size_t)(h * 16 + tm) * 2048;
      float* zd = zp + (size_t)(h * 16 + tm) * 64;
#pragma unroll
      for (int ft = 0; ft < 4; ++ft) {
#pragma unroll
        for (int dt = 0; dt < 2; ++dt) {
          f32x4 o = *reinterpret_cast<const f32x4*>(red + (ft * 16 + l15) * 32 + dt * 16 + 4 * g);
          *reinterpret_cast<f32x4*>(kd + (ft * 16 + l15) * 32 + dt * 16 + 4 * g) = o + kv[ft][dt];
        }
        if (g == 0) zd[ft * 16 + l15] = red[64 * 32 + ft * 16 + l15] + zz[ft][0];
      }
    }
  } else {
    if (wid8 < 4) {
      f32x4 stm[4], cs4[NTW];
      if constexpr (EPI == EPI_W1 || EPI == EPI_W3) {
#pragma unroll
        for (int j = 0; j < NTW; ++j) {
          const int colj = (j < 2 * NFULL) ? ((j >> 1) * 64 + (2 * wn + (j & 1)) * 16) : (NFULL * 64 + wn * 16);
          cs4[j] = *reinterpret_cast<const f32x4*>(smem + CS_OFF + (colj + 4 * g) * 4);
        }
#pragma unroll
        for (int mt = 0; mt < 4; ++mt) stm[mt] = rs[wm * 64 + mt * 16 + l15];
      }
#pragma unroll
      for (int j = 0; j < NTW; ++j) {
        const int colj = (j < 2 * NFULL) ? ((j >> 1) * 64 + (2 * wn + (j & 1)) * 16) : (NFULL * 64 + wn * 16);
#pragma unroll
        for (int mt = 0; mt < 4; ++mt) {
          f32x2 v0 = (f32x2){acc[mt][j][0], acc[mt][j][1]}, v1 = (f32x2){acc[mt][j][2], acc[mt][j][3]};
          if constexpr (EPI == EPI_W1 || EPI == EPI_W3) {
            v0 = gelu2((v0 - stm[mt][0] * (f32x2){cs4[j][0], cs4[j][1]}) * stm[mt][1]);
            v1 = gelu2((v1 - stm[mt][0] * (f32x2){cs4[j][2], cs4[j][3]}) * stm[mt][1]);
          }
          *reinterpret_cast<u32x2*>(smem + (wm * 64 + mt * 16 + l15) * PB + (colj + 4 * g) * 2) = (u32x2){pk2(v0[0], v0[1]), pk2(v1[0], v1[1])};
        }
      }
    }
    __syncthreads();
#pragma unroll
    for (int it = 0; it < NIT; ++it) {
      const int c = tid + 512 * it, r = c / CH, ch = c % CH;
      u32x4 v = *reinterpret_cast<const u32x4*>(smem + r * PB + ch * 16);
      const int grow = row0 + r, gcol = tn * BN + ch * 8;
      if constexpr (EPI == EPI_W3) {
        *reinterpret_cast<u32x4*>(out + (size_t)grow * F_ + gcol) = v;
      } else if constexpr (EPI == EPI_W1) {
        const int grp = tn >> 2;
        const int sh = grp == 0 ? 1 : grp == 1 ? -1 : grp == 2 ? 2 : -2;
        const int dr = grow + sh;
        if (dr >= 0 && dr < S_) *reinterpret_cast<u32x4*>(out + (size_t)dr * D_ + gcol) = v;
        const int zr = grow - sh;
        if (zr < 0 || zr >= S_) *reinterpret_cast<u32x4*>(out + (size_t)grow * D_ + gcol) = (u32x4){0u, 0u, 0u, 0u};
      } else {
        float y[8];
        const u32x4 a = ra[it];
        if constexpr (EPI == EPI_WO) {
#pragma unroll
          for (int i = 0; i < 4; ++i) { y[2 * i] = rbf(bflo(a[i]) + bflo(v[i])); y[2 * i + 1] = rbf(bfhi(a[i]) + bfhi(v[i])); }
        } else if constexpr (EPI == EPI_W2) {
          f32x4 st = rs[r];
#pragma unroll
          for (int i = 0; i < 4; ++i) {
            y[2 * i] = rbf(rbf((bflo(a[i]) - st[0]) * st[2]) + bflo(v[i]));
            y[2 * i + 1] = rbf(rbf((bfhi(a[i]) - st[0]) * st[2]) + bfhi(v[i]));
          }
        } else {
          const u32x4 b = rb[it];
#pragma unroll
          for (int i = 0; i < 4; ++i) {
            y[2 * i] = rbf(bflo(a[i]) + rbf(bflo(b[i]) + bflo(v[i])));
            y[2 * i + 1] = rbf(bfhi(a[i]) + rbf(bfhi(b[i]) + bfhi(v[i])));
          }
        }
        *reinterpret_cast<u32x4*>(out + (size_t)grow * D_ + gcol) = (u32x4){pk2(y[0], y[1]), pk2(y[2], y[3]), pk2(y[4], y[5]), pk2(y[6], y[7])};
        float sm = ((y[0] + y[1]) + (y[2] + y[3])) + ((y[4] + y[5]) + (y[6] + y[7]));
        sm = rsum8(sm);
        float mu = sm * (1.0f / 64.f), m2 = 0.f;
#pragma unroll
        for (int i = 0; i < 8; ++i) { float d = y[i] - mu; m2 += d * d; }
        m2 = rsum8(m2);
        if (ch == 0) st_out[(size_t)grow * 16 + tn] = (f32x2){sm, m2};
      }
    }
  }
  STAMP(3);
}

__global__ __launch_bounds__(256, 1) void attn_out_kernel(const u16* __restrict__ phiq, const float* __restrict__ kvp,
                                                          const float* __restrict__ zp, u16* __restrict__ o) {
  __shared__ __attribute__((aligned(16))) u16 kvT[48 * 72];
  const int tid = threadIdx.x, lane = tid & 63, wid = tid >> 6, g = lane >> 4, l15 = lane & 15;
  const int h = blockIdx.x & 31, tg = blockIdx.x >> 5;
  f32x4 kvv[16][2];
  float zv[16];
  bf16x8 pq[4][2];
  {
    const float* src = kvp + (size_t)h * 16 * 2048 + tid * 8;
#pragma unroll
    for (int t = 0; t < 16; ++t) {
      kvv[t][0] = *reinterpret_cast<const f32x4*>(src + t * 2048);
      kvv[t][1] = *reinterpret_cast<const f32x4*>(src + t * 2048 + 4);
    }
#pragma unroll
    for (int t = 0; t < 16; ++t) zv[t] = zp[(size_t)(h * 16 + t) * 64 + lane];
#pragma unroll
    for (int mt = 0; mt < 4; ++mt) {
      const u16* ps = phiq + ((size_t)(tg * 256 + wid * 64 + mt * 16 + l15) * 32 + h) * 64 + g * 8;
      pq[mt][0] = *reinterpret_cast<const bf16x8*>(ps);
      pq[mt][1] = *reinterpret_cast<const bf16x8*>(ps + 32);
    }
  }
  __builtin_amdgcn_sched_barrier(0);
  {
    for (int i = tid; i < 15 * 64; i += 256) kvT[(33 + i / 64) * 72 + (i & 63)] = 0;
    f32x4 a0 = kvv[0][0], a1 = kvv[0][1];
#pragma unroll
    for (int t = 1; t < 16; ++t) { a0 += kvv[t][0]; a1 += kvv[t][1]; }
    const int f = tid >> 2, d0 = (tid & 3) * 8;
#pragma unroll
    for (int i = 0; i < 4; ++i) {
      kvT[(d0 + i) * 72 + f] = (u16)(pk2(a0[i], 0.f) & 0xffff);
      kvT[(d0 + 4 + i) * 72 + f] = (u16)(pk2(a1[i], 0.f) & 0xffff);
    }
    if (tid < 64) {
      float z = 0.f;
#pragma unroll
      for (int t = 0; t < 16; ++t) z += zv[t];
      kvT[32 * 72 + tid] = (u16)(pk2(z, 0.f) & 0xffff);
    }
  }
  __syncthreads();
  bf16x8 kf[3][2];
#pragma unroll
  for (int dt = 0; dt < 3; ++dt)
#pragma unroll
    for (int ks = 0; ks < 2; ++ks) kf[dt][ks] = *reinterpret_cast<const bf16x8*>(&kvT[(dt * 16 + l15) * 72 + ks * 32 + g * 8]);
#pragma unroll
  for (int mt = 0; mt < 4; ++mt) {
    const int tok = tg * 256 + wid * 64 + mt * 16 + l15;
    const bf16x8 p0 = pq[mt][0], p1 = pq[mt][1];
    f32x4 r[3];
#pragma unroll
    for (int dt = 0; dt < 3; ++dt) {
      r[dt] = (f32x4){0.f, 0.f, 0.f, 0.f};
      r[dt] = __builtin_amdgcn_mfma_f32_16x16x32_bf16(kf[dt][0], p0, r[dt], 0, 0, 0);
      r[dt] = __builtin_amdgcn_mfma_f32_16x16x32_bf16(kf[dt][1], p1, r[dt], 0, 0, 0);
    }
    float den = rbf(rbf(__shfl(r[2][0], l15)) + 1e-6f);
    float inv = 1.0f / den;
#pragma unroll
    for (int dt = 0; dt < 2; ++dt) {
      u32x2 w = (u32x2){pk2(rbf(r[dt][0]) * inv, rbf(r[dt][1]) * inv), pk2(rbf(r[dt][2]) * inv, rbf(r[dt][3]) * inv)};
      *reinterpret_cast<u32x2*>(o + (size_t)tok * D_ + h * 32 + dt * 16 + 4 * g) = w;
    }
  }
}

constexpr int WC_QKV = 0, WC_WO = WC_QKV + 2 * 1024 * 384, WC_W1 = WC_WO + 2 * 1024 * 128, WC_W2 = WC_W1 + 2 * 1024 * 128,
              WC_W4 = WC_W2 + 2 * 1024 * 128, WC_END = WC_W4 + 2 * F_ * 128;
__global__ __launch_bounds__(256) void prep_kernel(const u16* __restrict__ x, const u16* __restrict__ wqkv,
                                                   const u16* __restrict__ w1, const u16* __restrict__ w3,
                                                   float* __restrict__ cs_qkv, float* __restrict__ cs_w1,
                                                   float* __restrict__ cs_w3, f32x2* __restrict__ st,
                                                   const u16* __restrict__ wo, const u16* __restrict__ w2,
                                                   const u16* __restrict__ w4, char* __restrict__ wc) {
  __shared__ __attribute__((aligned(16))) float red[4][512];
  const int tid = threadIdx.x, b = blockIdx.x;
  if (b >= 368) {
    const int pb = b - 368;
    const u16* w = pb < 32 ? wo + (size_t)pb * 32768 : pb < 64 ? w2 + (size_t)(pb - 32) * 32768 : w4 + (size_t)(pb - 64) * 32768;
    u32x4 v[16];
#pragma unroll
    for (int r = 0; r < 16; ++r) v[r] = *reinterpret_cast<const u32x4*>(w + r * 2048 + tid * 8);
    if (((tid >> 3) & 7) == 3) {
      const int kd = pb < 64 ? 1024 : F_, row = (pb < 32 ? pb : pb < 64 ? pb - 32 : pb - 64) * 32 + (tid >> 7);
      char* dst = wc + (pb < 32 ? WC_WO : pb < 64 ? WC_W2 : WC_W4) + (size_t)((tid >> 6) & 1) * kd * 128 + (size_t)row * 128 + (tid & 7) * 16;
#pragma unroll
      for (int r = 0; r < 16; ++r) *reinterpret_cast<u32x4*>(dst + r * 256) = v[r];
    }
#pragma unroll
    for (int r = 0; r < 16; ++r) asm volatile("" ::"v"(v[r]));
  } else if (b < 240) {
    const u16* w; float* dst; int ld, ch, rg;
    if (b < 96) { w = wqkv; dst = cs_qkv; ld = 3072; ch = b % 6; rg = b / 6; }
    else if (b < 128) { w = w1; dst = cs_w1; ld = 1024; ch = (b - 96) & 1; rg = (b - 96) >> 1; }
    else { w = w3; dst = cs_w3; ld = F_; ch = (b - 128) % 7; rg = (b - 128) / 7; }
    const int lane = tid & 63, wid = tid >> 6, row = rg * 64 + wid * 16;
    u32x4 v[16];
#pragma unroll
    for (int r = 0; r < 16; ++r) v[r] = *reinterpret_cast<const u32x4*>(w + (size_t)(row + r) * ld + ch * 512 + lane * 8);
    if (b < 128 && (lane >> 3) == 3) {
      const int cpp = b < 96 ? 384 : 128;
      char* cpd = wc + (b < 96 ? WC_QKV + (ch & 1) * (1024 * 384) + (ch >> 1) * 128 : WC_W1 + ch * (1024 * 128)) + row * cpp + (lane & 7) * 16;
#pragma unroll
      for (int r = 0; r < 16; ++r) *reinterpret_cast<u32x4*>(cpd + r * cpp) = v[r];
    }
    f32x4 s0 = (f32x4){0.f, 0.f, 0.f, 0.f}, s1 = s0;
#pragma unroll
    for (int r = 0; r < 16; ++r) {
      s0 += (f32x4){bflo(v[r][0]), bfhi(v[r][0]), bflo(v[r][1]), bfhi(v[r][1])};
      s1 += (f32x4){bflo(v[r][2]), bfhi(v[r][2]), bflo(v[r][3]), bfhi(v[r][3])};
    }
    *reinterpret_cast<f32x4*>(&red[wid][lane * 8]) = s0;
    *reinterpret_cast<f32x4*>(&red[wid][lane * 8 + 4]) = s1;
    __syncthreads();
    f32x2 t = *reinterpret_cast<const f32x2*>(&red[0][tid * 2]);
#pragma unroll
    for (int i = 1; i < 4; ++i) t += *reinterpret_cast<const f32x2*>(&red[i][tid * 2]);
    *reinterpret_cast<f32x2*>(dst + rg * ld + ch * 512 + tid * 2) = t;
  } else {
    const int lane = tid & 63, wid = tid >> 6;
    const int rbase = (b - 240) * 16 + wid * 4;
    u32x4 xv[8];
#pragma unroll
    for (int i = 0; i < 8; ++i) xv[i] = *reinterpret_cast<const u32x4*>(x + (size_t)rbase * D_ + i * 512 + lane * 8);
    __builtin_amdgcn_sched_barrier(0);
#pragma unroll
    for (int rr = 0; rr < 4; ++rr) {
      const int row = rbase + rr;
#pragma unroll
      for (int hf = 0; hf < 2; ++hf) {
        const u32x4 v = xv[rr * 2 + hf];
        float y[8];
#pragma unroll
        for (int i = 0; i < 4; ++i) { y[2 * i] = bflo(v[i]); y[2 * i + 1] = bfhi(v[i]); }
        float sm = ((y[0] + y[1]) + (y[2] + y[3])) + ((y[4] + y[5]) + (y[6] + y[7]));
        sm = rsum8(sm);
        float mu = sm * (1.0f / 64.f), m2 = 0.f;
#pragma unroll
        for (int i = 0; i < 8; ++i) { float d = y[i] - mu; m2 += d * d; }
        m2 = rsum8(m2);
        if ((lane & 7) == 0) st[(size_t)row * 16 + hf * 8 + (lane >> 3)] = (f32x2){sm, m2};
      }
    }
  }
}

extern "C" void kernel_launch(void* const* d_in, const int* in_sizes, int n_in, void* d_out, int out_size, void* d_ws,
                              size_t ws_size, hipStream_t stream) {
  const u16* x_in = (const u16*)d_in[0];
  const u16* wqkv = (const u16*)d_in[1];
  const u16* wphi = (const u16*)d_in[2];
  const u16* wo = (const u16*)d_in[3];
  const u16* w1 = (const u16*)d_in[5];
  const u16* w2 = (const u16*)d_in[6];
  const u16* w3 = (const u16*)d_in[7];
  const u16* w4 = (const u16*)d_in[8];
  char* ws = (char*)d_ws;
  size_t off = 0;
  auto alloc = [&](size_t bytes) { char* p = ws + off; off += (bytes + 4095) & ~(size_t)4095; return p; };
  u16* xb = (u16*)alloc((size_t)S_ * D_ * 2);
  u16* x2 = (u16*)alloc((size_t)S_ * D_ * 2);
  u16* x1 = (u16*)alloc((size_t)S_ * D_ * 2);
  u16* hs = (u16*)alloc((size_t)S_ * D_ * 2);
  u16* ob = (u16*)alloc((size_t)S_ * D_ * 2);
  u16* gb = (u16*)alloc((size_t)S_ * F_ * 2);
  u16* phiq = (u16*)alloc((size_t)S_ * 2048 * 2);
  float* kvp = (float*)alloc((size_t)32 * 16 * 2048 * 4);
  float* zp = (float*)alloc((size_t)32 * 16 * 64 * 4);
  f32x2* st_x = (f32x2*)alloc((size_t)S_ * 16 * 8);
  f32x2* st_x2 = (f32x2*)alloc((size_t)S_ * 16 * 8);
  f32x2* st_x1 = (f32x2*)alloc((size_t)S_ * 16 * 8);
  float* cs_qkv = (float*)alloc(16 * 3072 * 4);
  float* cs_w1 = (float*)alloc(16 * 1024 * 4);
  float* cs_w3 = (float*)alloc(16 * F_ * 4);
  char* wc = alloc(WC_END);

  prep_kernel<<<368 + 176, 256, 0, stream>>>(x_in, wqkv, w1, w3, cs_qkv, cs_w1, cs_w3, st_x, wo, w2, w4, wc);
  for (int layer = 0; layer < 2; ++layer) {
    const u16* x = layer == 0 ? x_in : xb;
    u16* xo = layer == 0 ? xb : (u16*)d_out;
    gemm_kernel<192, 3, 1024, 1024, 3072, EPI_QKV><<<256, 512, 0, stream>>>(x, wqkv, (const u16*)(wc + WC_QKV), cs_qkv, st_x, nullptr, nullptr, phiq, nullptr, wphi, kvp, zp);
    attn_out_kernel<<<256, 256, 0, stream>>>(phiq, kvp, zp, ob);
    gemm_kernel<64, 5, 1024, 1024, 1024, EPI_WO><<<256, 512, 0, stream>>>(ob, wo, (const u16*)(wc + WC_WO), nullptr, nullptr, x, nullptr, x2, st_x2, nullptr, nullptr, nullptr);
    gemm_kernel<64, 5, 1024, 1024, 1024, EPI_W1><<<256, 512, 0, stream>>>(x2, w1, (const u16*)(wc + WC_W1), cs_w1, st_x2, nullptr, nullptr, hs, nullptr, nullptr, nullptr, nullptr);
    gemm_kernel<64, 5, 1024, 1024, 1024, EPI_W2><<<256, 512, 0, stream>>>(hs, w2, (const u16*)(wc + WC_W2), nullptr, st_x2, x2, nullptr, x1, st_x1, nullptr, nullptr, nullptr);
    gemm_kernel<224, 3, 1024, 1024, F_, EPI_W3><<<256, 512, 0, stream>>>(x1, w3, nullptr, cs_w3, st_x1, nullptr, nullptr, gb, nullptr, nullptr, nullptr, nullptr);
    gemm_kernel<64, 5, F_, F_, 1024, EPI_W4><<<256, 512, 0, stream>>>(gb, w4, (const u16*)(wc + WC_W4), nullptr, nullptr, x2, x1, xo, st_x, nullptr, nullptr, nullptr);
  }
}
```

```cpp
#include <hip/hip_runtime.h>
#include <hip/hip_bf16.h>
#include <stdint.h>
#include <type_traits>

typedef short bf16x8 __attribute__((ext_vector_type(8)));
typedef short s16x4 __attribute__((ext_vector_type(4)));
typedef float f32x4 __attribute__((ext_vector_type(4)));
typedef float f32x2 __attribute__((ext_vector_type(2)));
typedef unsigned u32x2 __attribute__((ext_vector_type(2)));
typedef unsigned u32x4 __attribute__((ext_vector_type(4)));
typedef __attribute__((address_space(3))) s16x4* lds_s16x4_p;
typedef unsigned short u16;

constexpr int S_ = 2048, D_ = 1024, H_ = 32, F_ = 3584;

__device__ __forceinline__ float bf2f(u16 v) { return __uint_as_float(((unsigned)v) << 16); }
__device__ __forceinline__ float bflo(unsigned v) { return __uint_as_float(v << 16); }
__device__ __forceinline__ float bfhi(unsigned v) { return __uint_as_float(v & 0xffff0000u); }
typedef __bf16 bf16x2_t __attribute__((ext_vector_type(2)));
__device__ __forceinline__ unsigned pk2(float a, float b) {
  return __builtin_bit_cast(unsigned, __builtin_convertvector((f32x2){a, b}, bf16x2_t));
}
__device__ __forceinline__ unsigned pk2v(f32x2 v) { return __builtin_bit_cast(unsigned, __builtin_convertvector(v, bf16x2_t)); }
__device__ __forceinline__ float max2_(float a, float b) { float r; asm("v_max_f32 %0, %1, %2" : "=v"(r) : "v"(a), "v"(b)); return r; }
__device__ __forceinline__ float min2_(float a, float b) { float r; asm("v_min_f32 %0, %1, %2" : "=v"(r) : "v"(a), "v"(b)); return r; }
__device__ __forceinline__ float max3_(float a, float b, float c) { float r; asm("v_max3_f32 %0, %1, %2, %3" : "=v"(r) : "v"(a), "v"(b), "v"(c)); return r; }
__device__ __forceinline__ float min3_(float a, float b, float c) { float r; asm("v_min3_f32 %0, %1, %2, %3" : "=v"(r) : "v"(a), "v"(b), "v"(c)); return r; }
__device__ __forceinline__ float rbf(float a) { return bflo(pk2(a, 0.f)); }
__device__ __forceinline__ float gelu_t(float x) {
  const float c1 = -2.0f * 0.7978845608028654f * 1.4426950408889634f;
  const float c2 = c1 * 0.044715f;
  float t = x * x;
  float w = x * __builtin_fmaf(t, c2, c1);
  float e = __builtin_amdgcn_exp2f(w);
  return x * __builtin_amdgcn_rcpf(1.0f + e);
}

__device__ __forceinline__ f32x2 gelu2(f32x2 x) {
  const float c1 = -2.0f * 0.7978845608028654f * 1.4426950408889634f;
  const float c2 = c1 * 0.044715f;
  f32x2 t = x * x;
  f32x2 w = x * (t * c2 + c1);
  f32x2 d = (f32x2){__builtin_amdgcn_exp2f(w[0]), __builtin_amdgcn_exp2f(w[1])} + 1.0f;
  return x * (f32x2){__builtin_amdgcn_rcpf(d[0]), __builtin_amdgcn_rcpf(d[1])};
}
__device__ __forceinline__ void x16(float v, float& a, float& b) {
  auto r = __builtin_amdgcn_permlane16_swap(__float_as_uint(v), __float_as_uint(v), false, false);
  a = __uint_as_float(r[0]); b = __uint_as_float(r[1]);
}
__device__ __forceinline__ void x32(float v, float& a, float& b) {
  auto r = __builtin_amdgcn_permlane32_swap(__float_as_uint(v), __float_as_uint(v), false, false);
  a = __uint_as_float(r[0]); b = __uint_as_float(r[1]);
}
__device__ __forceinline__ float rmax4(float v) { float a, b; x16(v, a, b); v = max2_(a, b); x32(v, a, b); return max2_(a, b); }
__device__ __forceinline__ float rmin4(float v) { float a, b; x16(v, a, b); v = min2_(a, b); x32(v, a, b); return min2_(a, b); }
__device__ __forceinline__ float rsum4(float v) { float a, b; x16(v, a, b); v = a + b; x32(v, a, b); return a + b; }

__device__ __forceinline__ float rsum8(float v) {
  v += __uint_as_float(__builtin_amdgcn_mov_dpp(__float_as_uint(v), 0xB1, 0xF, 0xF, true));
  v += __uint_as_float(__builtin_amdgcn_mov_dpp(__float_as_uint(v), 0x4E, 0xF, 0xF, true));
  v += __uint_as_float(__builtin_amdgcn_mov_dpp(__float_as_uint(v), 0x141, 0xF, 0xF, true));
  return v;
}
#define WAIT_V(n) asm volatile("s_waitcnt vmcnt(" #n ")" ::: "memory")
#define WAIT_L0() asm volatile("s_waitcnt lgkmcnt(0)" ::: "memory")
template <int N> __device__ __forceinline__ void wait_vm() {
  asm volatile("s_waitcnt vmcnt(%0)" ::"i"(N) : "memory");
}

__device__ __forceinline__ void glds16(unsigned voff, const void* sbase, unsigned lds) {
  unsigned keep;
  asm volatile("s_mov_b32 %0, m0\n\ts_mov_b32 m0, %3\n\ts_nop 0\n\tglobal_load_lds_dwordx4 %1, %2\n\ts_mov_b32 m0, %0"
               : "=&s"(keep) : "v"(voff), "s"(sbase), "s"(lds) : "memory");
}

__device__ __forceinline__ void mfma_acc(f32x4& c, const u32x4& a, const u32x4& b) {
  asm volatile("v_mfma_f32_16x16x32_bf16 %0, %1, %2, %0" : "+a"(c) : "v"(a), "v"(b));
}

#define STAMP(i)

enum { EPI_QKV = 0, EPI_WO = 1, EPI_W1 = 2, EPI_W2 = 3, EPI_W3 = 4, EPI_W4 = 5 };

template <int BN, int NST, int KD, int LDA, int LDB, int EPI>
__global__ __launch_bounds__(512, 2) void gemm_kernel(
    const u16* __restrict__ A, const u16* __restrict__ W,
    const u16* __restrict__ Wc,
    const float* __restrict__ cs,
    const f32x2* __restrict__ st_in,
    const u16* __restrict__ r0,
    const u16* __restrict__ r1,
    u16* __restrict__ out,
    f32x2* __restrict__ st_out,
    const u16* __restrict__ wphi,
    float* __restrict__ kvp,
    float* __restrict__ zp)
{
  constexpr int NFULL = BN / 64, HALF = (BN % 64) / 32;
  constexpr int A_BYTES = 128 * 64 * 2, B_BYTES = BN * 64 * 2, STAGE = A_BYTES + B_BYTES;
  constexpr int NT = KD / 64, NTW = 2 * NFULL + HALF, LPS = 4 + 2 * NFULL + HALF;
  constexpr int PB = BN * 2 + 16;
  constexpr int EPI_BYTES = (EPI == EPI_QKV) ? (4 * 64 * 144 + 2 * (64 * 32 + 64) * 4) : 128 * PB;
  constexpr int MAINB = NST * STAGE;
  constexpr int RS_OFF = (MAINB > EPI_BYTES ? MAINB : EPI_BYTES);
  constexpr int CS_OFF = RS_OFF + 128 * 16;
  constexpr int WP_OFF = CS_OFF + 1024;
  constexpr int SMEM = WP_OFF + (EPI == EPI_QKV ? 4096 : 0);
  constexpr bool HAS_CS = (EPI == EPI_QKV || EPI == EPI_W1 || EPI == EPI_W3);
  constexpr bool HAS_ST = (EPI == EPI_QKV || EPI == EPI_W1 || EPI == EPI_W3 || EPI == EPI_W2);
  constexpr bool HAS_RES = (EPI == EPI_WO || EPI == EPI_W2 || EPI == EPI_W4);
  constexpr int LAG = (BN == 64) ? 1 : 0;
  constexpr int CH = BN / 8, NIT = CH / 4;
  static_assert(SMEM <= 163840, "LDS");
  __shared__ __attribute__((aligned(16))) char smem[SMEM];

  STAMP(0);
  const int tid = threadIdx.x, lane = tid & 63;
  const int wid8 = __builtin_amdgcn_readfirstlane(tid >> 6), wid = wid8 & 3, wm = wid >> 1, wn = wid & 1;
  const int g = lane >> 4, l15 = lane & 15;
  const int xcd = blockIdx.x & 7, idx = blockIdx.x >> 3;
  const int tm = (xcd >> 1) * 4 + (idx & 3), tn = (xcd & 1) * 8 + (idx >> 2);
  const int row0 = tm * 128;
  f32x4* rs = reinterpret_cast<f32x4*>(smem + RS_OFF);

  f32x4 acc[4][NTW];
  u32x4 ra[HAS_RES ? NIT : 1], rb[(EPI == EPI_W4) ? NIT : 1];

  if constexpr (HAS_RES) {
#pragma unroll
    for (int it = 0; it < NIT; ++it) {
      const int c = tid + 512 * it, r = c / CH, ch = c % CH;
      ra[it] = *reinterpret_cast<const u32x4*>(r0 + (size_t)(row0 + r) * D_ + tn * BN + ch * 8);
      if constexpr (EPI == EPI_W4) rb[it] = *reinterpret_cast<const u32x4*>(r1 + (size_t)(row0 + r) * D_ + tn * BN + ch * 8);
    }
    asm volatile("" ::: "memory");
  }

  if (wid8 >= 4) {
    unsigned voA[4], voB[NFULL > 0 ? NFULL : 1][2], voH = 0;
    const bool pk_ = (HALF == 0) && ((tn & 7) == 3);
    const unsigned pitchB = pk_ ? NFULL * 128 : LDB * 2;
#pragma unroll
    for (int i = 0; i < 4; ++i) {
      int r = (wid * 4 + i) * 8 + (lane >> 3);
      int ch = (lane & 7) ^ ((r >> 1) & 7);
      voA[i] = (unsigned)(row0 + r) * (LDA * 2) + ch * 16;
    }
#pragma unroll
    for (int b = 0; b < NFULL; ++b) {
      int colb = (EPI == EPI_QKV) ? (b * 1024 + tn * 64) : (tn * BN + b * 64);
#pragma unroll
      for (int j = 0; j < 2; ++j) {
        int r = (wid * 2 + j) * 8 + (lane >> 3);
        int f = ((r >> 1) & 1) | (((r >> 3) & 1) << 1);
        int chp = lane & 7;
        int gch = (((chp >> 1) ^ f) << 1) | (chp & 1);
        voB[b][j] = (unsigned)r * pitchB + (pk_ ? b * 128 : colb * 2) + gch * 16;
      }
    }
    if constexpr (HALF) {
      int r = wid * 16 + (lane >> 2);
      int chp = lane & 3;
      int gch = (((chp >> 1) ^ ((r >> 3) & 1)) << 1) | (chp & 1);
      voH = (unsigned)r * (LDB * 2) + (tn * BN + NFULL * 64) * 2 + gch * 16;
    }
    const char* Ab = reinterpret_cast<const char*>(A);
    const char* Wb = pk_ ? reinterpret_cast<const char*>(Wc) + (size_t)(tn >> 3) * (KD * NFULL * 128) : reinterpret_cast<const char*>(W);
    const unsigned lbase = (unsigned)(uintptr_t)(__attribute__((address_space(3))) char*)smem;
    auto stage = [&](int kt, int buf) {
      const char* a = Ab + (size_t)kt * 128;
      const char* w = Wb + (size_t)(kt * (64 * pitchB));
      const unsigned s = lbase + buf * STAGE;
#pragma unroll
      for (int i = 0; i < 4; ++i) glds16(voA[i], a, s + (wid * 4 + i) * 1024);
#pragma unroll
      for (int b = 0; b < NFULL; ++b)
#pragma unroll
        for (int j = 0; j < 2; ++j) glds16(voB[b][j], w, s + A_BYTES + b * 8192 + (wid * 2 + j) * 1024);
      if constexpr (HALF) glds16(voH, w, s + A_BYTES + NFULL * 8192 + wid * 1024);
    };
#pragma unroll
    for (int s = 0; s < NST; ++s) stage(s, s);
    constexpr int XL = 0;
    constexpr int XE = (HAS_CS ? 16 : 0) + (EPI == EPI_QKV ? 1 : 0) + (HAS_ST ? 4 : 0);
    f32x4 stp[4];
    const int lt = tid - 256;
    u32x4 wpx;
    f32x4 csp[HAS_CS ? 16 : 1];
    asm volatile("" ::: "memory");
    if constexpr (HAS_CS) {
      const int c4 = lt < BN / 4 ? lt : 0;
      const float* cp = cs + (EPI == EPI_QKV ? (c4 >> 4) * 1024 + tn * 64 + (c4 & 15) * 4 : tn * BN + c4 * 4);
#pragma unroll
      for (int i = 0; i < 16; ++i) csp[i] = *reinterpret_cast<const f32x4*>(cp + i * LDB);
    }
    if constexpr (EPI == EPI_QKV) wpx = *reinterpret_cast<const u32x4*>(wphi + tn * 2048 + lt * 8);
    if constexpr (HAS_ST) {
      const f32x4* sp = reinterpret_cast<const f32x4*>(st_in + (size_t)(row0 + (lt >> 1)) * 16 + (lt & 1) * 8);
#pragma unroll
      for (int i = 0; i < 4; ++i) stp[i] = sp[i];
    }
    asm volatile("" ::: "memory");
    wait_vm<(NST - 1 - LAG) * LPS + XE>();
    __builtin_amdgcn_s_barrier();
    int buf = 0;
    constexpr int NPEEL = NST - 1 - LAG;
#pragma unroll
    for (int kt = 0; kt < NPEEL; ++kt) {
      wait_vm<(NST - 2 - LAG) * LPS + XE>();
      __builtin_amdgcn_s_barrier();
      stage(kt + NST, kt);
    }
    buf = NPEEL;
    for (int kt = NPEEL; kt < NT - NST; ++kt) {
      wait_vm<(NST - 2 - LAG) * LPS>();
      __builtin_amdgcn_s_barrier();
      stage(kt + NST, buf);
      buf = buf + 1 == NST ? 0 : buf + 1;
    }
    asm volatile("" ::: "memory");
#pragma unroll
    for (int r = 0; r < NST - 1; ++r) {
      constexpr int NSL = NST - LAG;
      if (r == 0) wait_vm<(NSL - 2) * LPS + XL>();
      else if (r == 1) wait_vm<(NSL > 3 ? NSL - 3 : 0) * LPS + XL>();
      else if (r == 2) wait_vm<(NSL > 4 ? NSL - 4 : 0) * LPS + XL>();
      else if (r == 3) wait_vm<(NSL > 5 ? NSL - 5 : 0) * LPS + XL>();
      else wait_vm<XL>();
      __builtin_amdgcn_s_barrier();
    }
    if constexpr (HAS_ST) {
#pragma unroll
      for (int i = 0; i < 4; ++i) asm volatile("" : "+v"(stp[i]));
      float sm = 0.f;
#pragma unroll
      for (int i = 0; i < 4; ++i) sm += stp[i][0] + stp[i][2];
      sm += __shfl_xor(sm, 1);
      float mean = sm * (1.0f / 1024.f);
      float m2 = 0.f;
#pragma unroll
      for (int i = 0; i < 4; ++i) {
        float d0 = stp[i][0] * (1.0f / 64.f) - mean, d1 = stp[i][2] * (1.0f / 64.f) - mean;
        m2 += stp[i][1] + stp[i][3] + 64.f * (d0 * d0 + d1 * d1);
      }
      m2 += __shfl_xor(m2, 1);
      float var = m2 * (1.0f / 1024.f);
      float r1_ = 1.0f / sqrtf(var + 1e-5f);
      float sc = r1_;
      if constexpr (EPI == EPI_W1) sc = r1_ / sqrtf(var * r1_ * r1_ + 1e-5f);
      if ((lt & 1) == 0) rs[lt >> 1] = (f32x4){mean, sc, r1_, 0.f};
    }
    if constexpr (HAS_CS) {
#pragma unroll
      for (int i = 0; i < 16; ++i) asm volatile("" : "+v"(csp[i]));
#pragma unroll
      for (int i = 8; i > 0; i >>= 1)
#pragma unroll
        for (int j = 0; j < i; ++j) csp[j] += csp[j + i];
      if (lt < BN / 4) *reinterpret_cast<f32x4*>(smem + CS_OFF + lt * 16) = csp[0];
    }
    if constexpr (EPI == EPI_QKV) *reinterpret_cast<u32x4*>(smem + WP_OFF + lt * 16) = wpx;
  } else {
    const int q = l15 >> 2, p = lane & 3;
    const int fB = (q >> 1) | ((g & 1) << 1);
    unsigned aoff[2], boff[2], hoff = 0;
#pragma unroll
    for (int ks = 0; ks < 2; ++ks) aoff[ks] = (wm * 64 + l15) * 128 + (((4 * ks + g) ^ (l15 >> 1)) * 16);
#pragma unroll
    for (int u = 0; u < 2; ++u) boff[u] = A_BYTES + (8 * g + q) * 128 + (((2 * wn + u) ^ fB) * 32) + p * 8;
    if constexpr (HALF) hoff = A_BYTES + NFULL * 8192 + (8 * g + q) * 64 + ((wn ^ (g & 1)) * 32) + p * 8;

    auto ldf = [&](int i, const char* s, int ks, u32x4(&af)[4], u32x4(&bfr)[NTW]) {
      if (i == 0 || i > NTW) {
        const int mt = i == 0 ? 0 : i - NTW;
        af[mt] = *reinterpret_cast<const u32x4*>(s + aoff[ks] + mt * 2048);
      } else if (i - 1 < 2 * NFULL) {
        const int b = (i - 1) >> 1, u = (i - 1) & 1;
        s16x4 lo = __builtin_amdgcn_ds_read_tr16_b64_v4i16((lds_s16x4_p)(s + boff[u] + b * 8192 + ks * 4096));
        s16x4 hi = __builtin_amdgcn_ds_read_tr16_b64_v4i16((lds_s16x4_p)(s + boff[u] + b * 8192 + ks * 4096 + 512));
        bfr[i - 1] = __builtin_shufflevector(__builtin_bit_cast(u32x2, lo), __builtin_bit_cast(u32x2, hi), 0, 1, 2, 3);
      } else {
        s16x4 lo = __builtin_amdgcn_ds_read_tr16_b64_v4i16((lds_s16x4_p)(s + hoff + ks * 2048));
        s16x4 hi = __builtin_amdgcn_ds_read_tr16_b64_v4i16((lds_s16x4_p)(s + hoff + ks * 2048 + 256));
        bfr[NTW - 1] = __builtin_shufflevector(__builtin_bit_cast(u32x2, lo), __builtin_bit_cast(u32x2, hi), 0, 1, 2, 3);
      }
    };
    constexpr int NM = 4 * NTW, NL = NTW + 4;
    auto phase = [&](u32x4(&afc)[4], u32x4(&bfc)[NTW], u32x4(&afn)[4], u32x4(&bfn)[NTW], int nbuf, int nks, auto rd, auto bar) {
      const char* s = smem + nbuf * STAGE;
      constexpr int BAR = decltype(bar)::value, R0 = BAR < 0 ? 0 : BAR + 1;
      constexpr int RSP = (NM - R0 - 2) / NL > 0 ? (NM - R0 - 2) / NL : 1;
      static_assert(R0 + (NL - 1) * RSP < NM, "reads");
#pragma unroll
      for (int mt = 0; mt < 4; ++mt)
#pragma unroll
        for (int j = 0; j < NTW; ++j) {
          const int m = mt * NTW + j;
          if (m == BAR) {
            WAIT_L0();
            __builtin_amdgcn_s_barrier();
            asm volatile("" ::: "memory");
          }
          if (decltype(rd)::value && m >= R0 && (m - R0) % RSP == 0 && (m - R0) / RSP < NL) ldf((m - R0) / RSP, s, nks, afn, bfn);
          if (EPI == EPI_QKV && j >= 4) mfma_acc(acc[mt][j], afc[mt], bfc[j]);
          else mfma_acc(acc[mt][j], bfc[j], afc[mt]);
          __builtin_amdgcn_sched_barrier(0);
        }
    };
    typedef std::integral_constant<int, -1> nobar;
#pragma unroll
    for (int mt = 0; mt < 4; ++mt)
#pragma unroll
      for (int j = 0; j < NTW; ++j) {
        acc[mt][j] = (f32x4){0.f, 0.f, 0.f, 0.f};
        asm volatile("" : "+a"(acc[mt][j]));
      }
    u32x4 af0[4], af1[4], bf0[NTW], bf1[NTW];
    __builtin_amdgcn_s_barrier();
    asm volatile("" ::: "memory");
    STAMP(1);
#pragma unroll
    for (int i = 0; i < NL; ++i) { ldf(i, smem, 0, af0, bf0); __builtin_amdgcn_sched_barrier(0); }
    int buf = 0;
    if constexpr (LAG) {
      phase(af0, bf0, af1, bf1, 0, 1, std::true_type{}, nobar{});
      for (int kt = 0; kt < NT - 1; ++kt) {
        buf = buf + 1 == NST ? 0 : buf + 1;
        phase(af1, bf1, af0, bf0, buf, 0, std::true_type{}, nobar{});
        asm volatile("" ::: "memory");
        __builtin_amdgcn_s_barrier();
        asm volatile("" ::: "memory");
        phase(af0, bf0, af1, bf1, buf, 1, std::true_type{}, nobar{});
      }
      phase(af1, bf1, af0, bf0, buf, 0, std::false_type{}, nobar{});
    } else {
      for (int kt = 0; kt < NT - 1; ++kt) {
        phase(af0, bf0, af1, bf1, buf, 1, std::true_type{}, nobar{});
        buf = buf + 1 == NST ? 0 : buf + 1;
        phase(af1, bf1, af0, bf0, buf, 0, std::true_type{}, std::integral_constant<int, 4>{});
      }
      phase(af0, bf0, af1, bf1, buf, 1, std::true_type{}, nobar{});
      phase(af1, bf1, af0, bf0, buf, 0, std::false_type{}, nobar{});
    }
    asm volatile("s_nop 7\n\ts_nop 7\n\ts_nop 7" ::: "memory");
  }
  WAIT_L0();
  __builtin_amdgcn_s_barrier();
  asm volatile("" ::: "memory");
  STAMP(2);

  if constexpr (EPI == EPI_QKV) {
    const int h = tn * 2 + wn;
    const int q = l15 >> 2, p = lane & 3;
    f32x4 kv[4][2], zz[4];
    float* red = reinterpret_cast<float*>(smem + 4 * 64 * 144) + wn * (64 * 32 + 64);
    if (wid8 < 4) {
      bf16x8 wf[2];
#pragma unroll
      for (int ft = 0; ft < 2; ++ft) {
        const char* src = smem + WP_OFF + wn * 2048 + (4 * g + q) * 64 + ft * 32 + p * 8;
        s16x4 lo = __builtin_amdgcn_ds_read_tr16_b64_v4i16((lds_s16x4_p)(src));
        s16x4 hi = __builtin_amdgcn_ds_read_tr16_b64_v4i16((lds_s16x4_p)(src + 16 * 64));
        wf[ft] = __builtin_shufflevector(lo, hi, 0, 1, 2, 3, 4, 5, 6, 7);
      }
      const float* csl = reinterpret_cast<const float*>(smem + CS_OFF);
      f32x4 cs4[4];
      float csv[2];
#pragma unroll
      for (int t = 0; t < 2; ++t) {
        cs4[t] = *reinterpret_cast<const f32x4*>(csl + wn * 32 + t * 16 + 4 * g);
        cs4[2 + t] = *reinterpret_cast<const f32x4*>(csl + 64 + wn * 32 + t * 16 + 4 * g);
        csv[t] = csl[128 + wn * 32 + t * 16 + l15];
      }
      bf16x8 qf[4], kf[4], vf[2][2];
#pragma unroll
      for (int mt = 0; mt < 4; ++mt) {
        f32x4 st = rs[wm * 64 + mt * 16 + l15];
        unsigned pq_[4], pk_[4];
#pragma unroll
        for (int t = 0; t < 2; ++t) {
          float a[4], b[4];
#pragma unroll
          for (int i = 0; i < 4; ++i) {
            a[i] = st[1] * (acc[mt][t][i] - st[0] * cs4[t][i]);
            b[i] = st[1] * (acc[mt][2 + t][i] - st[0] * cs4[2 + t][i]);
          }
          pq_[t * 2] = pk2(a[0], a[1]); pq_[t * 2 + 1] = pk2(a[2], a[3]);
          pk_[t * 2] = pk2(b[0], b[1]); pk_[t * 2 + 1] = pk2(b[2], b[3]);
        }
        qf[mt] = __builtin_bit_cast(bf16x8, (u32x4){pq_[0], pq_[1], pq_[2], pq_[3]});
        kf[mt] = __builtin_bit_cast(bf16x8, (u32x4){pk_[0], pk_[1], pk_[2], pk_[3]});
      }
#pragma unroll
      for (int s = 0; s < 2; ++s) {
        unsigned pv[2][4];
#pragma unroll
        for (int m2 = 0; m2 < 2; ++m2) {
          int mt = s * 2 + m2;
          f32x4 s0 = rs[wm * 64 + mt * 16 + 4 * g + 0], s1 = rs[wm * 64 + mt * 16 + 4 * g + 1];
          f32x4 s2 = rs[wm * 64 + mt * 16 + 4 * g + 2], s3 = rs[wm * 64 + mt * 16 + 4 * g + 3];
#pragma unroll
          for (int t = 0; t < 2; ++t) {
            float a0 = s0[1] * (acc[mt][4 + t][0] - s0[0] * csv[t]);
            float a1 = s1[1] * (acc[mt][4 + t][1] - s1[0] * csv[t]);
            float a2 = s2[1] * (acc[mt][4 + t][2] - s2[0] * csv[t]);
            float a3 = s3[1] * (acc[mt][4 + t][3] - s3[0] * csv[t]);
            pv[t][m2 * 2] = pk2(a0, a1); pv[t][m2 * 2 + 1] = pk2(a2, a3);
          }
        }
#pragma unroll
        for (int t = 0; t < 2; ++t) vf[s][t] = __builtin_bit_cast(bf16x8, (u32x4){pv[t][0], pv[t][1], pv[t][2], pv[t][3]});
      }
      char* pkl = smem + wid * (64 * 144);
      const float L2E = 1.4426950408889634f;
#pragma unroll
      for (int which = 0; which < 2; ++which)
#pragma unroll
        for (int mt = 0; mt < 4; ++mt) {
          f32x4 z4 = (f32x4){0.f, 0.f, 0.f, 0.f};
          f32x4 p0 = __builtin_amdgcn_mfma_f32_16x16x32_bf16(wf[0], which ? kf[mt] : qf[mt], z4, 0, 0, 0);
          f32x4 p1 = __builtin_amdgcn_mfma_f32_16x16x32_bf16(wf[1], which ? kf[mt] : qf[mt], z4, 0, 0, 0);
          f32x2 v[4];
          {
            const unsigned r0_ = pk2(p0[0], p0[1]), r1_ = pk2(p0[2], p0[3]), r2_ = pk2(p1[0], p1[1]), r3_ = pk2(p1[2], p1[3]);
            v[0] = (f32x2){bflo(r0_), bfhi(r0_)}; v[1] = (f32x2){bflo(r1_), bfhi(r1_)};
            v[2] = (f32x2){bflo(r2_), bfhi(r2_)}; v[3] = (f32x2){bflo(r3_), bfhi(r3_)};
          }
          float mx = max2_(max3_(max3_(v[0][0], v[0][1], v[1][0]), v[1][1], v[2][0]), max3_(v[2][1], v[3][0], v[3][1]));
          float mn = min2_(min3_(min3_(v[0][0], v[0][1], v[1][0]), v[1][1], v[2][0]), min3_(v[2][1], v[3][0], v[3][1]));
          mx = rmax4(mx); mn = rmin4(mn);
          const float mxl = -mx * L2E, mnl = mn * L2E;
          f32x2 ep[4], en[4];
#pragma unroll
          for (int i = 0; i < 4; ++i) {
            const f32x2 ap = v[i] * L2E + mxl, an = v[i] * (-L2E) + mnl;
            ep[i] = (f32x2){__builtin_amdgcn_exp2f(ap[0]), __builtin_amdgcn_exp2f(ap[1])};
            en[i] = (f32x2){__builtin_amdgcn_exp2f(an[0]), __builtin_amdgcn_exp2f(an[1])};
          }
          const f32x2 sp2 = (ep[0] + ep[1]) + (ep[2] + ep[3]), sn2 = (en[0] + en[1]) + (en[2] + en[3]);
          const float ip = __builtin_amdgcn_rcpf(rsum4(sp2[0] + sp2[1])), in_ = __builtin_amdgcn_rcpf(rsum4(sn2[0] + sn2[1]));
          u32x2 o[4];
          o[0] = (u32x2){pk2v(ep[0] * ip), pk2v(ep[1] * ip)};
          o[1] = (u32x2){pk2v(ep[2] * ip), pk2v(ep[3] * ip)};
          o[2] = (u32x2){pk2v(en[0] * in_), pk2v(en[1] * in_)};
          o[3] = (u32x2){pk2v(en[2] * in_), pk2v(en[3] * in_)};
          if (which == 0) {
            u16* dst = out + ((size_t)(row0 + wm * 64 + mt * 16 + l15) * 32 + h) * 64 + 4 * g;
#pragma unroll
            for (int c = 0; c < 4; ++c) *reinterpret_cast<u32x2*>(dst + c * 16) = o[c];
          } else {
            char* dst = pkl + (mt * 16 + l15) * 144 + g * 8;
#pragma unroll
            for (int c = 0; c < 4; ++c) *reinterpret_cast<u32x2*>(dst + c * 32) = o[c];
          }
        }
      WAIT_L0();
      __builtin_amdgcn_sched_barrier(0);
#pragma unroll
      for (int ft = 0; ft < 4; ++ft) { kv[ft][0] = kv[ft][1] = zz[ft] = (f32x4){0.f, 0.f, 0.f, 0.f}; }
      const bf16x8 ones = (bf16x8){0x3F80, 0x3F80, 0x3F80, 0x3F80, 0x3F80, 0x3F80, 0x3F80, 0x3F80};
#pragma unroll
      for (int s = 0; s < 2; ++s)
#pragma unroll
        for (int ft = 0; ft < 4; ++ft) {
          const char* src = pkl + (32 * s + 4 * g + q) * 144 + ft * 32 + p * 8;
          s16x4 lo = __builtin_amdgcn_ds_read_tr16_b64_v4i16((lds_s16x4_p)(src));
          s16x4 hi = __builtin_amdgcn_ds_read_tr16_b64_v4i16((lds_s16x4_p)(src + 16 * 144));
          bf16x8 pf = __builtin_shufflevector(lo, hi, 0, 1, 2, 3, 4, 5, 6, 7);
          kv[ft][0] = __builtin_amdgcn_mfma_f32_16x16x32_bf16(vf[s][0], pf, kv[ft][0], 0, 0, 0);
          kv[ft][1] = __builtin_amdgcn_mfma_f32_16x16x32_bf16(vf[s][1], pf, kv[ft][1], 0, 0, 0);
          zz[ft] = __builtin_amdgcn_mfma_f32_16x16x32_bf16(ones, pf, zz[ft], 0, 0, 0);
        }
      if (wm == 1) {
#pragma unroll
        for (int ft = 0; ft < 4; ++ft) {
#pragma unroll
          for (int dt = 0; dt < 2; ++dt) *reinterpret_cast<f32x4*>(red + (ft * 16 + l15) * 32 + dt * 16 + 4 * g) = kv[ft][dt];
          if (g == 0) red[64 * 32 + ft * 16 + l15] = zz[ft][0];
        }
      }
    }
    __syncthreads();
    if (wid8 < 2) {
      float* kd = kvp + (size_t)(h * 16 + tm) * 2048;
      float* zd = zp + (size_t)(h * 16 + tm) * 64;
#pragma unroll
      for (int ft = 0; ft < 4; ++ft) {
#pragma unroll
        for (int dt = 0; dt < 2; ++dt) {
          f32x4 o = *reinterpret_cast<const f32x4*>(red + (ft * 16 + l15) * 32 + dt * 16 + 4 * g);
          *reinterpret_cast<f32x4*>(kd + (ft * 16 + l15) * 32 + dt * 16 + 4 * g) = o + kv[ft][dt];
        }
        if (g == 0) zd[ft * 16 + l15] = red[64 * 32 + ft * 16 + l15] + zz[ft][0];
      }
    }
  } else {
    if (wid8 < 4) {
      f32x4 stm[4], cs4[NTW];
      if constexpr (EPI == EPI_W1 || EPI == EPI_W3) {
#pragma unroll
        for (int j = 0; j < NTW; ++j) {
          const int colj = (j < 2 * NFULL) ? ((j >> 1) * 64 + (2 * wn + (j & 1)) * 16) : (NFULL * 64 + wn * 16);
          cs4[j] = *reinterpret_cast<const f32x4*>(smem + CS_OFF + (colj + 4 * g) * 4);
        }
#pragma unroll
        for (int mt = 0; mt < 4; ++mt) stm[mt] = rs[wm * 64 + mt * 16 + l15];
      }
#pragma unroll
      for (int j = 0; j < NTW; ++j) {
        const int colj = (j < 2 * NFULL) ? ((j >> 1) * 64 + (2 * wn + (j & 1)) * 16) : (NFULL * 64 + wn * 16);
#pragma unroll
        for (int mt = 0; mt < 4; ++mt) {
          f32x2 v0 = (f32x2){acc[mt][j][0], acc[mt][j][1]}, v1 = (f32x2){acc[mt][j][2], acc[mt][j][3]};
          if constexpr (EPI == EPI_W1 || EPI == EPI_W3) {
            v0 = gelu2((v0 - stm[mt][0] * (f32x2){cs4[j][0], cs4[j][1]}) * stm[mt][1]);
            v1 = gelu2((v1 - stm[mt][0] * (f32x2){cs4[j][2], cs4[j][3]}) * stm[mt][1]);
          }
          *reinterpret_cast<u32x2*>(smem + (wm * 64 + mt * 16 + l15) * PB + (colj + 4 * g) * 2) = (u32x2){pk2(v0[0], v0[1]), pk2(v1[0], v1[1])};
        }
      }
    }
    __syncthreads();
#pragma unroll
    for (int it = 0; it < NIT; ++it) {
      const int c = tid + 512 * it, r = c / CH, ch = c % CH;
      u32x4 v = *reinterpret_cast<const u32x4*>(smem + r * PB + ch * 16);
      const int grow = row0 + r, gcol = tn * BN + ch * 8;
      if constexpr (EPI == EPI_W3) {
        *reinterpret_cast<u32x4*>(out + (size_t)grow * F_ + gcol) = v;
      } else if constexpr (EPI == EPI_W1) {
        const int grp = tn >> 2;
        const int sh = grp == 0 ? 1 : grp == 1 ? -1 : grp == 2 ? 2 : -2;
        const int dr = grow + sh;
        if (dr >= 0 && dr < S_) *reinterpret_cast<u32x4*>(out + (size_t)dr * D_ + gcol) = v;
        const int zr = grow - sh;
        if (zr < 0 || zr >= S_) *reinterpret_cast<u32x4*>(out + (size_t)grow * D_ + gcol) = (u32x4){0u, 0u, 0u, 0u};
      } else {
        float y[8];
        const u32x4 a = ra[it];
        if constexpr (EPI == EPI_WO) {
#pragma unroll
          for (int i = 0; i < 4; ++i) { y[2 * i] = rbf(bflo(a[i]) + bflo(v[i])); y[2 * i + 1] = rbf(bfhi(a[i]) + bfhi(v[i])); }
        } else if constexpr (EPI == EPI_W2) {
          f32x4 st = rs[r];
#pragma unroll
          for (int i = 0; i < 4; ++i) {
            y[2 * i] = rbf(rbf((bflo(a[i]) - st[0]) * st[2]) + bflo(v[i]));
            y[2 * i + 1] = rbf(rbf((bfhi(a[i]) - st[0]) * st[2]) + bfhi(v[i]));
          }
        } else {
          const u32x4 b = rb[it];
#pragma unroll
          for (int i = 0; i < 4; ++i) {
            y[2 * i] = rbf(bflo(a[i]) + rbf(bflo(b[i]) + bflo(v[i])));
            y[2 * i + 1] = rbf(bfhi(a[i]) + rbf(bfhi(b[i]) + bfhi(v[i])));
          }
        }
        *reinterpret_cast<u32x4*>(out + (size_t)grow * D_ + gcol) = (u32x4){pk2(y[0], y[1]), pk2(y[2], y[3]), pk2(y[4], y[5]), pk2(y[6], y[7])};
        float sm = ((y[0] + y[1]) + (y[2] + y[3])) + ((y[4] + y[5]) + (y[6] + y[7]));
        sm = rsum8(sm);
        float mu = sm * (1.0f / 64.f), m2 = 0.f;
#pragma unroll
        for (int i = 0; i < 8; ++i) { float d = y[i] - mu; m2 += d * d; }
        m2 = rsum8(m2);
        if (ch == 0) st_out[(size_t)grow * 16 + tn] = (f32x2){sm, m2};
      }
    }
  }
  STAMP(3);
}

__global__ __launch_bounds__(256, 1) void attn_out_kernel(const u16* __restrict__ phiq, const float* __restrict__ kvp,
                                                          const float* __restrict__ zp, u16* __restrict__ o) {
  __shared__ __attribute__((aligned(16))) u16 kvT[48 * 72];
  const int tid = threadIdx.x, lane = tid & 63, wid = tid >> 6, g = lane >> 4, l15 = lane & 15;
  const int h = blockIdx.x & 31, tg = blockIdx.x >> 5;
  f32x4 kvv[16][2];
  float zv[16];
  bf16x8 pq[4][2];
  {
    const float* src = kvp + (size_t)h * 16 * 2048 + tid * 8;
#pragma unroll
    for (int t = 0; t < 16; ++t) {
      kvv[t][0] = *reinterpret_cast<const f32x4*>(src + t * 2048);
      kvv[t][1] = *reinterpret_cast<const f32x4*>(src + t * 2048 + 4);
    }
#pragma unroll
    for (int t = 0; t < 16; ++t) zv[t] = zp[(size_t)(h * 16 + t) * 64 + lane];
#pragma unroll
    for (int mt = 0; mt < 4; ++mt) {
      const u16* ps = phiq + ((size_t)(tg * 256 + wid * 64 + mt * 16 + l15) * 32 + h) * 64 + g * 8;
      pq[mt][0] = *reinterpret_cast<const bf16x8*>(ps);
      pq[mt][1] = *reinterpret_cast<const bf16x8*>(ps + 32);
    }
  }
  __builtin_amdgcn_sched_barrier(0);
  {
    for (int i = tid; i < 15 * 64; i += 256) kvT[(33 + i / 64) * 72 + (i & 63)] = 0;
    f32x4 a0 = kvv[0][0], a1 = kvv[0][1];
#pragma unroll
    for (int t = 1; t < 16; ++t) { a0 += kvv[t][0]; a1 += kvv[t][1]; }
    const int f = tid >> 2, d0 = (tid & 3) * 8;
#pragma unroll
    for (int i = 0; i < 4; ++i) {
      kvT[(d0 + i) * 72 + f] = (u16)(pk2(a0[i], 0.f) & 0xffff);
      kvT[(d0 + 4 + i) * 72 + f] = (u16)(pk2(a1[i], 0.f) & 0xffff);
    }
    if (tid < 64) {
      float z = 0.f;
#pragma unroll
      for (int t = 0; t < 16; ++t) z += zv[t];
      kvT[32 * 72 + tid] = (u16)(pk2(z, 0.f) & 0xffff);
    }
  }
  __syncthreads();
  bf16x8 kf[3][2];
#pragma unroll
  for (int dt = 0; dt < 3; ++dt)
#pragma unroll
    for (int ks = 0; ks < 2; ++ks) kf[dt][ks] = *reinterpret_cast<const bf16x8*>(&kvT[(dt * 16 + l15) * 72 + ks * 32 + g * 8]);
#pragma unroll
  for (int mt = 0; mt < 4; ++mt) {
    const int tok = tg * 256 + wid * 64 + mt * 16 + l15;
    const bf16x8 p0 = pq[mt][0], p1 = pq[mt][1];
    f32x4 r[3];
#pragma unroll
    for (int dt = 0; dt < 3; ++dt) {
      r[dt] = (f32x4){0.f, 0.f, 0.f, 0.f};
      r[dt] = __builtin_amdgcn_mfma_f32_16x16x32_bf16(kf[dt][0], p0, r[dt], 0, 0, 0);
      r[dt] = __builtin_amdgcn_mfma_f32_16x16x32_bf16(kf[dt][1], p1, r[dt], 0, 0, 0);
    }
    float den = rbf(rbf(__shfl(r[2][0], l15)) + 1e-6f);
    float inv = 1.0f / den;
#pragma unroll
    for (int dt = 0; dt < 2; ++dt) {
      u32x2 w = (u32x2){pk2(rbf(r[dt][0]) * inv, rbf(r[dt][1]) * inv), pk2(rbf(r[dt][2]) * inv, rbf(r[dt][3]) * inv)};
      *reinterpret_cast<u32x2*>(o + (size_t)tok * D_ + h * 32 + dt * 16 + 4 * g) = w;
    }
  }
}

constexpr int WC_QKV = 0, WC_WO = WC_QKV + 2 * 1024 * 384, WC_W1 = WC_WO + 2 * 1024 * 128, WC_W2 = WC_W1 + 2 * 1024 * 128,
              WC_W4 = WC_W2 + 2 * 1024 * 128, WC_END = WC_W4 + 2 * F_ * 128;
__global__ __launch_bounds__(256) void prep_kernel(const u16* __restrict__ x, const u16* __restrict__ wqkv,
                                                   const u16* __restrict__ w1, const u16* __restrict__ w3,
                                                   float* __restrict__ cs_qkv, float* __restrict__ cs_w1,
                                                   float* __restrict__ cs_w3, f32x2* __restrict__ st,
                                                   const u16* __restrict__ wo, const u16* __restrict__ w2,
                                                   const u16* __restrict__ w4, char* __restrict__ wc) {
  __shared__ __attribute__((aligned(16))) float red[4][512];
  const int tid = threadIdx.x, b = blockIdx.x;
  if (b >= 368) {
    const int pb = b - 368;
    const u16* w = pb < 32 ? wo + (size_t)pb * 32768 : pb < 64 ? w2 + (size_t)(pb - 32) * 32768 : w4 + (size_t)(pb - 64) * 32768;
    u32x4 v[16];
#pragma unroll
    for (int r = 0; r < 16; ++r) v[r] = *reinterpret_cast<const u32x4*>(w + r * 2048 + tid * 8);
    if (((tid >> 3) & 7) == 3) {
      const int kd = pb < 64 ? 1024 : F_, row = (pb < 32 ? pb : pb < 64 ? pb - 32 : pb - 64) * 32 + (tid >> 7);
      char* dst = wc + (pb < 32 ? WC_WO : pb < 64 ? WC_W2 : WC_W4) + (size_t)((tid >> 6) & 1) * kd * 128 + (size_t)row * 128 + (tid & 7) * 16;
#pragma unroll
      for (int r = 0; r < 16; ++r) *reinterpret_cast<u32x4*>(dst + r * 256) = v[r];
    }
#pragma unroll
    for (int r = 0; r < 16; ++r) asm volatile("" ::"v"(v[r]));
  } else if (b < 240) {
    const u16* w; float* dst; int ld, ch, rg;
    if (b < 96) { w = wqkv; dst = cs_qkv; ld = 3072; ch = b % 6; rg = b / 6; }
    else if (b < 128) { w = w1; dst = cs_w1; ld = 1024; ch = (b - 96) & 1; rg = (b - 96) >> 1; }
    else { w = w3; dst = cs_w3; ld = F_; ch = (b - 128) % 7; rg = (b - 128) / 7; }
    const int lane = tid & 63, wid = tid >> 6, row = rg * 64 + wid * 16;
    u32x4 v[16];
#pragma unroll
    for (int r = 0; r < 16; ++r) v[r] = *reinterpret_cast<const u32x4*>(w + (size_t)(row + r) * ld + ch * 512 + lane * 8);
    if (b < 128 && (lane >> 3) == 3) {
      const int cpp = b < 96 ? 384 : 128;
      char* cpd = wc + (b < 96 ? WC_QKV + (ch & 1) * (1024 * 384) + (ch >> 1) * 128 : WC_W1 + ch * (1024 * 128)) + row * cpp + (lane & 7) * 16;
#pragma unroll
      for (int r = 0; r < 16; ++r) *reinterpret_cast<u32x4*>(cpd + r * cpp) = v[r];
    }
    f32x4 s0 = (f32x4){0.f, 0.f, 0.f, 0.f}, s1 = s0;
#pragma unroll
    for (int r = 0; r < 16; ++r) {
      s0 += (f32x4){bflo(v[r][0]), bfhi(v[r][0]), bflo(v[r][1]), bfhi(v[r][1])};
      s1 += (f32x4){bflo(v[r][2]), bfhi(v[r][2]), bflo(v[r][3]), bfhi(v[r][3])};
    }
    *reinterpret_cast<f32x4*>(&red[wid][lane * 8]) = s0;
    *reinterpret_cast<f32x4*>(&red[wid][lane * 8 + 4]) = s1;
    __syncthreads();
    f32x2 t = *reinterpret_cast<const f32x2*>(&red[0][tid * 2]);
#pragma unroll
    for (int i = 1; i < 4; ++i) t += *reinterpret_cast<const f32x2*>(&red[i][tid * 2]);
    *reinterpret_cast<f32x2*>(dst + rg * ld + ch * 512 + tid * 2) = t;
  } else {
    const int lane = tid & 63, wid = tid >> 6;
    const int rbase = (b - 240) * 16 + wid * 4;
    u32x4 xv[8];
#pragma unroll
    for (int i = 0; i < 8; ++i) xv[i] = *reinterpret_cast<const u32x4*>(x + (size_t)rbase * D_ + i * 512 + lane * 8);
    __builtin_amdgcn_sched_barrier(0);
#pragma unroll
    for (int rr = 0; rr < 4; ++rr) {
      const int row = rbase + rr;
#pragma unroll
      for (int hf = 0; hf < 2; ++hf) {
        const u32x4 v = xv[rr * 2 + hf];
        float y[8];
#pragma unroll
        for (int i = 0; i < 4; ++i) { y[2 * i] = bflo(v[i]); y[2 * i + 1] = bfhi(v[i]); }
        float sm = ((y[0] + y[1]) + (y[2] + y[3])) + ((y[4] + y[5]) + (y[6] + y[7]));
        sm = rsum8(sm);
        float mu = sm * (1.0f / 64.f), m2 = 0.f;
#pragma unroll
        for (int i = 0; i < 8; ++i) { float d = y[i] - mu; m2 += d * d; }
        m2 = rsum8(m2);
        if ((lane & 7) == 0) st[(size_t)row * 16 + hf * 8 + (lane >> 3)] = (f32x2){sm, m2};
      }
    }
  }
}

extern "C" void kernel_launch(void* const* d_in, const int* in_sizes, int n_in, void* d_out, int out_size, void* d_ws,
                              size_t ws_size, hipStream_t stream) {
  const u16* x_in = (const u16*)d_in[0];
  const u16* wqkv = (const u16*)d_in[1];
  const u16* wphi = (const u16*)d_in[2];
  const u16* wo = (const u16*)d_in[3];
  const u16* w1 = (const u16*)d_in[5];
  const u16* w2 = (const u16*)d_in[6];
  const u16* w3 = (const u16*)d_in[7];
  const u16* w4 = (const u16*)d_in[8];
  char* ws = (char*)d_ws;
  size_t off = 0;
  auto alloc = [&](size_t bytes) { char* p = ws + off; off += (bytes + 4095) & ~(size_t)4095; return p; };
  u16* xb = (u16*)alloc((size_t)S_ * D_ * 2);
  u16* x2 = (u16*)alloc((size_t)S_ * D_ * 2);
  u16* x1 = (u16*)alloc((size_t)S_ * D_ * 2);
  u16* hs = (u16*)alloc((size_t)S_ * D_ * 2);
  u16* ob = (u16*)alloc((size_t)S_ * D_ * 2);
  u16* gb = (u16*)alloc((size_t)S_ * F_ * 2);
  u16* phiq = (u16*)alloc((size_t)S_ * 2048 * 2);
  float* kvp = (float*)alloc((size_t)32 * 16 * 2048 * 4);
  float* zp = (float*)alloc((size_t)32 * 16 * 64 * 4);
  f32x2* st_x = (f32x2*)alloc((size_t)S_ * 16 * 8);
  f32x2* st_x2 = (f32x2*)alloc((size_t)S_ * 16 * 8);
  f32x2* st_x1 = (f32x2*)alloc((size_t)S_ * 16 * 8);
  float* cs_qkv = (float*)alloc(16 * 3072 * 4);
  float* cs_w1 = (float*)alloc(16 * 1024 * 4);
  float* cs_w3 = (float*)alloc(16 * F_ * 4);
  char* wc = alloc(WC_END);

  prep_kernel<<<368 + 176, 256, 0, stream>>>(x_in, wqkv, w1, w3, cs_qkv, cs_w1, cs_w3, st_x, wo, w2, w4, wc);
  for (int layer = 0; layer < 2; ++layer) {
    const u16* x = layer == 0 ? x_in : xb;
    u16* xo = layer == 0 ? xb : (u16*)d_out;
    gemm_kernel<192, 3, 1024, 1024, 3072, EPI_QKV><<<256, 512, 0, stream>>>(x, wqkv, (const u16*)(wc + WC_QKV), cs_qkv, st_x, nullptr, nullptr, phiq, nullptr, wphi, kvp, zp);
    attn_out_kernel<<<256, 256, 0, stream>>>(phiq, kvp, zp, ob);
    gemm_kernel<64, 5, 1024, 1024, 1024, EPI_WO><<<256, 512, 0, stream>>>(ob, wo, (const u16*)(wc + WC_WO), nullptr, nullptr, x, nullptr, x2, st_x2, nullptr, nullptr, nullptr);
    gemm_kernel<64, 5, 1024, 1024, 1024, EPI_W1><<<256, 512, 0, stream>>>(x2, w1, (const u16*)(wc + WC_W1), cs_w1, st_x2, nullptr, nullptr, hs, nullptr, nullptr, nullptr, nullptr);
    gemm_kernel<64, 5, 1024, 1024, 1024, EPI_W2><<<256, 512, 0, stream>>>(hs, w2, (const u16*)(wc + WC_W2), nullptr, st_x2, x2, nullptr, x1, st_x1, nullptr, nullptr, nullptr);
    gemm_kernel<224, 3, 1024, 1024, F_, EPI_W3><<<256, 512, 0, stream>>>(x1, w3, nullptr, cs_w3, st_x1, nullptr, nullptr, gb, nullptr, nullptr, nullptr, nullptr);
    gemm_kernel<64, 5, F_, F_, 1024, EPI_W4><<<256, 512, 0, stream>>>(gb, w4, (const u16*)(wc + WC_W4), nullptr, nullptr, x2, x1, xo, st_x, nullptr, nullptr, nullptr);
  }
}
```

```cpp
#include <hip/hip_runtime.h>
#include <hip/hip_bf16.h>
#include <stdint.h>
#include <type_traits>

typedef short bf16x8 __attribute__((ext_vector_type(8)));
typedef short s16x4 __attribute__((ext_vector_type(4)));
typedef float f32x4 __attribute__((ext_vector_type(4)));
typedef float f32x2 __attribute__((ext_vector_type(2)));
typedef unsigned u32x2 __attribute__((ext_vector_type(2)));
typedef unsigned u32x4 __attribute__((ext_vector_type(4)));
typedef __attribute__((address_space(3))) s16x4* lds_s16x4_p;
typedef unsigned short u16;

constexpr int S_ = 2048, D_ = 1024, H_ = 32, F_ = 3584;

__device__ __forceinline__ float bf2f(u16 v) { return __uint_as_float(((unsigned)v) << 16); }
__device__ __forceinline__ float bflo(unsigned v) { return __uint_as_float(v << 16); }
__device__ __forceinline__ float bfhi(unsigned v) { return __uint_as_float(v & 0xffff0000u); }
typedef __bf16 bf16x2_t __attribute__((ext_vector_type(2)));
__device__ __forceinline__ unsigned pk2(float a, float b) {
  return __builtin_bit_cast(unsigned, __builtin_convertvector((f32x2){a, b}, bf16x2_t));
}
__device__ __forceinline__ unsigned pk2v(f32x2 v) { return __builtin_bit_cast(unsigned, __builtin_convertvector(v, bf16x2_t)); }
__device__ __forceinline__ float max2_(float a, float b) { float r; asm("v_max_f32 %0, %1, %2" : "=v"(r) : "v"(a), "v"(b)); return r; }
__device__ __forceinline__ float min2_(float a, float b) { float r; asm("v_min_f32 %0, %1, %2" : "=v"(r) : "v"(a), "v"(b)); return r; }
__device__ __forceinline__ float max3_(float a, float b, float c) { float r; asm("v_max3_f32 %0, %1, %2, %3" : "=v"(r) : "v"(a), "v"(b), "v"(c)); return r; }
__device__ __forceinline__ float min3_(float a, float b, float c) { float r; asm("v_min3_f32 %0, %1, %2, %3" : "=v"(r) : "v"(a), "v"(b), "v"(c)); return r; }
__device__ __forceinline__ float rbf(float a) { return bflo(pk2(a, 0.f)); }
__device__ __forceinline__ float gelu_t(float x) {
  const float c1 = -2.0f * 0.7978845608028654f * 1.4426950408889634f;
  const float c2 = c1 * 0.044715f;
  float t = x * x;
  float w = x * __builtin_fmaf(t, c2, c1);
  float e = __builtin_amdgcn_exp2f(w);
  return x * __builtin_amdgcn_rcpf(1.0f + e);
}

__device__ __forceinline__ f32x2 gelu2(f32x2 x) {
  const float c1 = -2.0f * 0.7978845608028654f * 1.4426950408889634f;
  const float c2 = c1 * 0.044715f;
  f32x2 t = x * x;
  f32x2 w = x * (t * c2 + c1);
  f32x2 d = (f32x2){__builtin_amdgcn_exp2f(w[0]), __builtin_amdgcn_exp2f(w[1])} + 1.0f;
  return x * (f32x2){__builtin_amdgcn_rcpf(d[0]), __builtin_amdgcn_rcpf(d[1])};
}
__device__ __forceinline__ void x16(float v, float& a, float& b) {
  auto r = __builtin_amdgcn_permlane16_swap(__float_as_uint(v), __float_as_uint(v), false, false);
  a = __uint_as_float(r[0]); b = __uint_as_float(r[1]);
}
__device__ __forceinline__ void x32(float v, float& a, float& b) {
  auto r = __builtin_amdgcn_permlane32_swap(__float_as_uint(v), __float_as_uint(v), false, false);
  a = __uint_as_float(r[0]); b = __uint_as_float(r[1]);
}
__device__ __forceinline__ float rmax4(float v) { float a, b; x16(v, a, b); v = max2_(a, b); x32(v, a, b); return max2_(a, b); }
__device__ __forceinline__ float rmin4(float v) { float a, b; x16(v, a, b); v = min2_(a, b); x32(v, a, b); return min2_(a, b); }
__device__ __forceinline__ float rsum4(float v) { float a, b; x16(v, a, b); v = a + b; x32(v, a, b); return a + b; }

__device__ __forceinline__ float rsum8(float v) {
  v += __uint_as_float(__builtin_amdgcn_mov_dpp(__float_as_uint(v), 0xB1, 0xF, 0xF, true));
  v += __uint_as_float(__builtin_amdgcn_mov_dpp(__float_as_uint(v), 0x4E, 0xF, 0xF, true));
  v += __uint_as_float(__builtin_amdgcn_mov_dpp(__float_as_uint(v), 0x141, 0xF, 0xF, true));
  return v;
}
#define WAIT_V(n) asm volatile("s_waitcnt vmcnt(" #n ")" ::: "memory")
#define WAIT_L0() asm volatile("s_waitcnt lgkmcnt(0)" ::: "memory")
template <int N> __device__ __forceinline__ void wait_vm() {
  asm volatile("s_waitcnt vmcnt(%0)" ::"i"(N) : "memory");
}

__device__ __forceinline__ void glds16(unsigned voff, const void* sbase, unsigned lds) {
  unsigned keep;
  asm volatile("s_mov_b32 %0, m0\n\ts_mov_b32 m0, %3\n\ts_nop 0\n\tglobal_load_lds_dwordx4 %1, %2\n\ts_mov_b32 m0, %0"
               : "=&s"(keep) : "v"(voff), "s"(sbase), "s"(lds) : "memory");
}

__device__ __forceinline__ void mfma_acc(f32x4& c, const u32x4& a, const u32x4& b) {
  asm volatile("v_mfma_f32_16x16x32_bf16 %0, %1, %2, %0" : "+a"(c) : "v"(a), "v"(b));
}

#define STAMP(i)

enum { EPI_QKV = 0, EPI_WO = 1, EPI_W1 = 2, EPI_W2 = 3, EPI_W3 = 4, EPI_W4 = 5 };

template <int BN, int NST, int KD, int LDA, int LDB, int EPI>
__global__ __launch_bounds__(512, 2) void gemm_kernel(
    const u16* __restrict__ A, const u16* __restrict__ W,
    const u16* __restrict__ Wc,
    const float* __restrict__ cs,
    const f32x2* __restrict__ st_in,
    const u16* __restrict__ r0,
    const u16* __restrict__ r1,
    u16* __restrict__ out,
    f32x2* __restrict__ st_out,
    const u16* __restrict__ wphi,
    float* __restrict__ kvp,
    float* __restrict__ zp)
{
  constexpr int NFULL = BN / 64, HALF = (BN % 64) / 32;
  constexpr int A_BYTES = 128 * 64 * 2, B_BYTES = BN * 64 * 2, STAGE = A_BYTES + B_BYTES;
  constexpr int NT = KD / 64, NTW = 2 * NFULL + HALF, LPS = 4 + 2 * NFULL + HALF;
  constexpr int PB = BN * 2 + 16;
  constexpr int EPI_BYTES = (EPI == EPI_QKV) ? (4 * 64 * 144 + 2 * (64 * 32 + 64) * 4) : 128 * PB;
  constexpr int MAINB = NST * STAGE;
  constexpr int RS_OFF = (MAINB > EPI_BYTES ? MAINB : EPI_BYTES);
  constexpr int CS_OFF = RS_OFF + 128 * 16;
  constexpr int WP_OFF = CS_OFF + 1024;
  constexpr int SMEM = WP_OFF + (EPI == EPI_QKV ? 4096 : 0);
  constexpr bool HAS_CS = (EPI == EPI_QKV || EPI == EPI_W1 || EPI == EPI_W3);
  constexpr bool HAS_ST = (EPI == EPI_QKV || EPI == EPI_W1 || EPI == EPI_W3 || EPI == EPI_W2);
  constexpr bool HAS_RES = (EPI == EPI_WO || EPI == EPI_W2 || EPI == EPI_W4);
  constexpr int LAG = (BN == 64) ? 1 : 0;
  constexpr int CH = BN / 8, NIT = CH / 4;
  static_assert(SMEM <= 163840, "LDS");
  __shared__ __attribute__((aligned(16))) char smem[SMEM];

  STAMP(0);
  const int tid = threadIdx.x, lane = tid & 63;
  const int wid8 = __builtin_amdgcn_readfirstlane(tid >> 6), wid = wid8 & 3, wm = wid >> 1, wn = wid & 1;
  const int g = lane >> 4, l15 = lane & 15;
  const int xcd = blockIdx.x & 7, idx = blockIdx.x >> 3;
  const int tm = (xcd >> 1) * 4 + (idx & 3), tn = (xcd & 1) * 8 + (idx >> 2);
  const int row0 = tm * 128;
  f32x4* rs = reinterpret_cast<f32x4*>(smem + RS_OFF);

  f32x4 acc[4][NTW];
  u32x4 ra[HAS_RES ? NIT : 1], rb[(EPI == EPI_W4) ? NIT : 1];

  unsigned rof[HAS_RES ? NIT : 1];
  if constexpr (HAS_RES) {
#pragma unroll
    for (int it = 0; it < NIT; ++it) {
      const int c = tid + 512 * it, r = c / CH, ch = c % CH;
      rof[it] = (unsigned)((row0 + r) * D_ + tn * BN + ch * 8) * 2u;
      asm volatile("" : "+v"(rof[it]));
    }
  }
  auto ldres = [&]() {
    if constexpr (HAS_RES) {
#pragma unroll
      for (int it = 0; it < NIT; ++it) {
        ra[it] = *reinterpret_cast<const u32x4*>(reinterpret_cast<const char*>(r0) + rof[it]);
        if constexpr (EPI == EPI_W4) rb[it] = *reinterpret_cast<const u32x4*>(reinterpret_cast<const char*>(r1) + rof[it]);
      }
    }
  };

  if (wid8 >= 4) {
    unsigned voA[4], voB[NFULL > 0 ? NFULL : 1][2], voH = 0;
    const bool pk_ = (HALF == 0) && ((tn & 7) == 3);
    const unsigned pitchB = pk_ ? NFULL * 128 : LDB * 2;
#pragma unroll
    for (int i = 0; i < 4; ++i) {
      int r = (wid * 4 + i) * 8 + (lane >> 3);
      int ch = (lane & 7) ^ ((r >> 1) & 7);
      voA[i] = (unsigned)(row0 + r) * (LDA * 2) + ch * 16;
    }
#pragma unroll
    for (int b = 0; b < NFULL; ++b) {
      int colb = (EPI == EPI_QKV) ? (b * 1024 + tn * 64) : (tn * BN + b * 64);
#pragma unroll
      for (int j = 0; j < 2; ++j) {
        int r = (wid * 2 + j) * 8 + (lane >> 3);
        int f = ((r >> 1) & 1) | (((r >> 3) & 1) << 1);
        int chp = lane & 7;
        int gch = (((chp >> 1) ^ f) << 1) | (chp & 1);
        voB[b][j] = (unsigned)r * pitchB + (pk_ ? b * 128 : colb * 2) + gch * 16;
      }
    }
    if constexpr (HALF) {
      int r = wid * 16 + (lane >> 2);
      int chp = lane & 3;
      int gch = (((chp >> 1) ^ ((r >> 3) & 1)) << 1) | (chp & 1);
      voH = (unsigned)r * (LDB * 2) + (tn * BN + NFULL * 64) * 2 + gch * 16;
    }
    const char* Ab = reinterpret_cast<const char*>(A);
    const char* Wb = pk_ ? reinterpret_cast<const char*>(Wc) + (size_t)(tn >> 3) * (KD * NFULL * 128) : reinterpret_cast<const char*>(W);
    const unsigned lbase = (unsigned)(uintptr_t)(__attribute__((address_space(3))) char*)smem;
    auto stage = [&](int kt, int buf) {
      const char* a = Ab + (size_t)kt * 128;
      const char* w = Wb + (size_t)(kt * (64 * pitchB));
      const unsigned s = lbase + buf * STAGE;
#pragma unroll
      for (int i = 0; i < 4; ++i) glds16(voA[i], a, s + (wid * 4 + i) * 1024);
#pragma unroll
      for (int b = 0; b < NFULL; ++b)
#pragma unroll
        for (int j = 0; j < 2; ++j) glds16(voB[b][j], w, s + A_BYTES + b * 8192 + (wid * 2 + j) * 1024);
      if constexpr (HALF) glds16(voH, w, s + A_BYTES + NFULL * 8192 + wid * 1024);
    };
#pragma unroll
    for (int s = 0; s < NST; ++s) stage(s, s);
    constexpr int XL = 0;
    constexpr int XE = (HAS_CS ? 16 : 0) + (EPI == EPI_QKV ? 1 : 0) + (HAS_ST ? 4 : 0) + (HAS_RES ? NIT * (EPI == EPI_W4 ? 2 : 1) : 0);
    f32x4 stp[4];
    const int lt = tid - 256;
    u32x4 wpx;
    f32x4 csp[HAS_CS ? 16 : 1];
    asm volatile("" ::: "memory");
    if constexpr (HAS_CS) {
      if (lt < BN / 4) {
        const float* cp = cs + (EPI == EPI_QKV ? (lt >> 4) * 1024 + tn * 64 + (lt & 15) * 4 : tn * BN + lt * 4);
#pragma unroll
        for (int i = 0; i < 16; ++i) csp[i] = *reinterpret_cast<const f32x4*>(cp + i * LDB);
      }
    }
    if constexpr (EPI == EPI_QKV) wpx = *reinterpret_cast<const u32x4*>(wphi + tn * 2048 + lt * 8);
    if constexpr (HAS_ST) {
      const f32x4* sp = reinterpret_cast<const f32x4*>(st_in + (size_t)(row0 + (lt >> 1)) * 16 + (lt & 1) * 8);
#pragma unroll
      for (int i = 0; i < 4; ++i) stp[i] = sp[i];
    }
    ldres();
    asm volatile("" ::: "memory");
    constexpr int NPEEL = NST - 1 - LAG;
    auto head = [&](auto xe) {
      constexpr int X = decltype(xe)::value;
      wait_vm<(NST - 1 - LAG) * LPS + X>();
      __builtin_amdgcn_s_barrier();
#pragma unroll
      for (int kt = 0; kt < NPEEL; ++kt) {
        wait_vm<(NST - 2 - LAG) * LPS + X>();
        __builtin_amdgcn_s_barrier();
        stage(kt + NST, kt);
      }
    };
    if (HAS_CS && wid == 0) head(std::integral_constant<int, XE>{});
    else head(std::integral_constant<int, XE - (HAS_CS ? 16 : 0)>{});
    int buf = NPEEL;
    for (int kt = NPEEL; kt < NT - NST; ++kt) {
      wait_vm<(NST - 2 - LAG) * LPS>();
      __builtin_amdgcn_s_barrier();
      stage(kt + NST, buf);
      buf = buf + 1 == NST ? 0 : buf + 1;
    }
    asm volatile("" ::: "memory");
#pragma unroll
    for (int r = 0; r < NST - 1; ++r) {
      constexpr int NSL = NST - LAG;
      if (r == 0) wait_vm<(NSL - 2) * LPS + XL>();
      else if (r == 1) wait_vm<(NSL > 3 ? NSL - 3 : 0) * LPS + XL>();
      else if (r == 2) wait_vm<(NSL > 4 ? NSL - 4 : 0) * LPS + XL>();
      else if (r == 3) wait_vm<(NSL > 5 ? NSL - 5 : 0) * LPS + XL>();
      else wait_vm<XL>();
      __builtin_amdgcn_s_barrier();
    }
    if constexpr (HAS_ST) {
#pragma unroll
      for (int i = 0; i < 4; ++i) asm volatile("" : "+v"(stp[i]));
      float sm = 0.f;
#pragma unroll
      for (int i = 0; i < 4; ++i) sm += stp[i][0] + stp[i][2];
      sm += __shfl_xor(sm, 1);
      float mean = sm * (1.0f / 1024.f);
      float m2 = 0.f;
#pragma unroll
      for (int i = 0; i < 4; ++i) {
        float d0 = stp[i][0] * (1.0f / 64.f) - mean, d1 = stp[i][2] * (1.0f / 64.f) - mean;
        m2 += stp[i][1] + stp[i][3] + 64.f * (d0 * d0 + d1 * d1);
      }
      m2 += __shfl_xor(m2, 1);
      float var = m2 * (1.0f / 1024.f);
      float r1_ = 1.0f / sqrtf(var + 1e-5f);
      float sc = r1_;
      if constexpr (EPI == EPI_W1) sc = r1_ / sqrtf(var * r1_ * r1_ + 1e-5f);
      if ((lt & 1) == 0) rs[lt >> 1] = (f32x4){mean, sc, r1_, 0.f};
    }
    if constexpr (HAS_CS) {
      if (lt < BN / 4) {
#pragma unroll
        for (int i = 0; i < 16; ++i) asm volatile("" : "+v"(csp[i]));
#pragma unroll
        for (int i = 8; i > 0; i >>= 1)
#pragma unroll
          for (int j = 0; j < i; ++j) csp[j] += csp[j + i];
        *reinterpret_cast<f32x4*>(smem + CS_OFF + lt * 16) = csp[0];
      }
    }
    if constexpr (EPI == EPI_QKV) *reinterpret_cast<u32x4*>(smem + WP_OFF + lt * 16) = wpx;
  } else {
    ldres();
    const int q = l15 >> 2, p = lane & 3;
    const int fB = (q >> 1) | ((g & 1) << 1);
    unsigned aoff[2], boff[2], hoff = 0;
#pragma unroll
    for (int ks = 0; ks < 2; ++ks) aoff[ks] = (wm * 64 + l15) * 128 + (((4 * ks + g) ^ (l15 >> 1)) * 16);
#pragma unroll
    for (int u = 0; u < 2; ++u) boff[u] = A_BYTES + (8 * g + q) * 128 + (((2 * wn + u) ^ fB) * 32) + p * 8;
    if constexpr (HALF) hoff = A_BYTES + NFULL * 8192 + (8 * g + q) * 64 + ((wn ^ (g & 1)) * 32) + p * 8;

    auto ldf = [&](int i, const char* s, int ks, u32x4(&af)[4], u32x4(&bfr)[NTW]) {
      if (i == 0 || i > NTW) {
        const int mt = i == 0 ? 0 : i - NTW;
        af[mt] = *reinterpret_cast<const u32x4*>(s + aoff[ks] + mt * 2048);
      } else if (i - 1 < 2 * NFULL) {
        const int b = (i - 1) >> 1, u = (i - 1) & 1;
        s16x4 lo = __builtin_amdgcn_ds_read_tr16_b64_v4i16((lds_s16x4_p)(s + boff[u] + b * 8192 + ks * 4096));
        s16x4 hi = __builtin_amdgcn_ds_read_tr16_b64_v4i16((lds_s16x4_p)(s + boff[u] + b * 8192 + ks * 4096 + 512));
        bfr[i - 1] = __builtin_shufflevector(__builtin_bit_cast(u32x2, lo), __builtin_bit_cast(u32x2, hi), 0, 1, 2, 3);
      } else {
        s16x4 lo = __builtin_amdgcn_ds_read_tr16_b64_v4i16((lds_s16x4_p)(s + hoff + ks * 2048));
        s16x4 hi = __builtin_amdgcn_ds_read_tr16_b64_v4i16((lds_s16x4_p)(s + hoff + ks * 2048 + 256));
        bfr[NTW - 1] = __builtin_shufflevector(__builtin_bit_cast(u32x2, lo), __builtin_bit_cast(u32x2, hi), 0, 1, 2, 3);
      }
    };
    constexpr int NM = 4 * NTW, NL = NTW + 4;
    auto phase = [&](u32x4(&afc)[4], u32x4(&bfc)[NTW], u32x4(&afn)[4], u32x4(&bfn)[NTW], int nbuf, int nks, auto rd, auto bar) {
      const char* s = smem + nbuf * STAGE;
      constexpr int BAR = decltype(bar)::value, R0 = BAR < 0 ? 0 : BAR + 1;
      constexpr int RSP = (NM - R0 - 2) / NL > 0 ? (NM - R0 - 2) / NL : 1;
      static_assert(R0 + (NL - 1) * RSP < NM, "reads");
#pragma unroll
      for (int mt = 0; mt < 4; ++mt)
#pragma unroll
        for (int j = 0; j < NTW; ++j) {
          const int m = mt * NTW + j;
          if (m == BAR) {
            WAIT_L0();
            __builtin_amdgcn_s_barrier();
            asm volatile("" ::: "memory");
          }
          if (decltype(rd)::value && m >= R0 && (m - R0) % RSP == 0 && (m - R0) / RSP < NL) ldf((m - R0) / RSP, s, nks, afn, bfn);
          if (EPI == EPI_QKV && j >= 4) mfma_acc(acc[mt][j], afc[mt], bfc[j]);
          else mfma_acc(acc[mt][j], bfc[j], afc[mt]);
          __builtin_amdgcn_sched_barrier(0);
        }
    };
    typedef std::integral_constant<int, -1> nobar;
#pragma unroll
    for (int mt = 0; mt < 4; ++mt)
#pragma unroll
      for (int j = 0; j < NTW; ++j) {
        acc[mt][j] = (f32x4){0.f, 0.f, 0.f, 0.f};
        asm volatile("" : "+a"(acc[mt][j]));
      }
    u32x4 af0[4], af1[4], bf0[NTW], bf1[NTW];
    __builtin_amdgcn_s_barrier();
    asm volatile("" ::: "memory");
    STAMP(1);
#pragma unroll
    for (int i = 0; i < NL; ++i) { ldf(i, smem, 0, af0, bf0); __builtin_amdgcn_sched_barrier(0); }
    int buf = 0;
    if constexpr (LAG) {
      phase(af0, bf0, af1, bf1, 0, 1, std::true_type{}, nobar{});
      for (int kt = 0; kt < NT - 1; ++kt) {
        buf = buf + 1 == NST ? 0 : buf + 1;
        phase(af1, bf1, af0, bf0, buf, 0, std::true_type{}, nobar{});
        asm volatile("" ::: "memory");
        __builtin_amdgcn_s_barrier();
        asm volatile("" ::: "memory");
        phase(af0, bf0, af1, bf1, buf, 1, std::true_type{}, nobar{});
      }
      phase(af1, bf1, af0, bf0, buf, 0, std::false_type{}, nobar{});
    } else {
      for (int kt = 0; kt < NT - 1; ++kt) {
        phase(af0, bf0, af1, bf1, buf, 1, std::true_type{}, nobar{});
        buf = buf + 1 == NST ? 0 : buf + 1;
        phase(af1, bf1, af0, bf0, buf, 0, std::true_type{}, std::integral_constant<int, 4>{});
      }
      phase(af0, bf0, af1, bf1, buf, 1, std::true_type{}, nobar{});
      phase(af1, bf1, af0, bf0, buf, 0, std::false_type{}, nobar{});
    }
    asm volatile("s_nop 7\n\ts_nop 7\n\ts_nop 7" ::: "memory");
  }
  WAIT_L0();
  __builtin_amdgcn_s_barrier();
  asm volatile("" ::: "memory");
  STAMP(2);

  if constexpr (EPI == EPI_QKV) {
    const int h = tn * 2 + wn;
    const int q = l15 >> 2, p = lane & 3;
    f32x4 kv[4][2], zz[4];
    float* red = reinterpret_cast<float*>(smem + 4 * 64 * 144) + wn * (64 * 32 + 64);
    if (wid8 < 4) {
      bf16x8 wf[2];
#pragma unroll
      for (int ft = 0; ft < 2; ++ft) {
        const char* src = smem + WP_OFF + wn * 2048 + (4 * g + q) * 64 + ft * 32 + p * 8;
        s16x4 lo = __builtin_amdgcn_ds_read_tr16_b64_v4i16((lds_s16x4_p)(src));
        s16x4 hi = __builtin_amdgcn_ds_read_tr16_b64_v4i16((lds_s16x4_p)(src + 16 * 64));
        wf[ft] = __builtin_shufflevector(lo, hi, 0, 1, 2, 3, 4, 5, 6, 7);
      }
      const float* csl = reinterpret_cast<const float*>(smem + CS_OFF);
      f32x4 cs4[4];
      float csv[2];
#pragma unroll
      for (int t = 0; t < 2; ++t) {
        cs4[t] = *reinterpret_cast<const f32x4*>(csl + wn * 32 + t * 16 + 4 * g);
        cs4[2 + t] = *reinterpret_cast<const f32x4*>(csl + 64 + wn * 32 + t * 16 + 4 * g);
        csv[t] = csl[128 + wn * 32 + t * 16 + l15];
      }
      bf16x8 qf[4], kf[4], vf[2][2];
#pragma unroll
      for (int mt = 0; mt < 4; ++mt) {
        f32x4 st = rs[wm * 64 + mt * 16 + l15];
        unsigned pq_[4], pk_[4];
#pragma unroll
        for (int t = 0; t < 2; ++t) {
          float a[4], b[4];
#pragma unroll
          for (int i = 0; i < 4; ++i) {
            a[i] = st[1] * (acc[mt][t][i] - st[0] * cs4[t][i]);
            b[i] = st[1] * (acc[mt][2 + t][i] - st[0] * cs4[2 + t][i]);
          }
          pq_[t * 2] = pk2(a[0], a[1]); pq_[t * 2 + 1] = pk2(a[2], a[3]);
          pk_[t * 2] = pk2(b[0], b[1]); pk_[t * 2 + 1] = pk2(b[2], b[3]);
        }
        qf[mt] = __builtin_bit_cast(bf16x8, (u32x4){pq_[0], pq_[1], pq_[2], pq_[3]});
        kf[mt] = __builtin_bit_cast(bf16x8, (u32x4){pk_[0], pk_[1], pk_[2], pk_[3]});
      }
#pragma unroll
      for (int s = 0; s < 2; ++s) {
        unsigned pv[2][4];
#pragma unroll
        for (int m2 = 0; m2 < 2; ++m2) {
          int mt = s * 2 + m2;
          f32x4 s0 = rs[wm * 64 + mt * 16 + 4 * g + 0], s1 = rs[wm * 64 + mt * 16 + 4 * g + 1];
          f32x4 s2 = rs[wm * 64 + mt * 16 + 4 * g + 2], s3 = rs[wm * 64 + mt * 16 + 4 * g + 3];
#pragma unroll
          for (int t = 0; t < 2; ++t) {
            float a0 = s0[1] * (acc[mt][4 + t][0] - s0[0] * csv[t]);
            float a1 = s1[1] * (acc[mt][4 + t][1] - s1[0] * csv[t]);
            float a2 = s2[1] * (acc[mt][4 + t][2] - s2[0] * csv[t]);
            float a3 = s3[1] * (acc[mt][4 + t][3] - s3[0] * csv[t]);
            pv[t][m2 * 2] = pk2(a0, a1); pv[t][m2 * 2 + 1] = pk2(a2, a3);
          }
        }
#pragma unroll
        for (int t = 0; t < 2; ++t) vf[s][t] = __builtin_bit_cast(bf16x8, (u32x4){pv[t][0], pv[t][1], pv[t][2], pv[t][3]});
      }
      char* pkl = smem + wid * (64 * 144);
      const float L2E = 1.4426950408889634f;
#pragma unroll
      for (int which = 0; which < 2; ++which)
#pragma unroll
        for (int mt = 0; mt < 4; ++mt) {
          f32x4 z4 = (f32x4){0.f, 0.f, 0.f, 0.f};
          f32x4 p0 = __builtin_amdgcn_mfma_f32_16x16x32_bf16(wf[0], which ? kf[mt] : qf[mt], z4, 0, 0, 0);
          f32x4 p1 = __builtin_amdgcn_mfma_f32_16x16x32_bf16(wf[1], which ? kf[mt] : qf[mt], z4, 0, 0, 0);
          f32x2 v[4];
          {
            const unsigned r0_ = pk2(p0[0], p0[1]), r1_ = pk2(p0[2], p0[3]), r2_ = pk2(p1[0], p1[1]), r3_ = pk2(p1[2], p1[3]);
            v[0] = (f32x2){bflo(r0_), bfhi(r0_)}; v[1] = (f32x2){bflo(r1_), bfhi(r1_)};
            v[2] = (f32x2){bflo(r2_), bfhi(r2_)}; v[3] = (f32x2){bflo(r3_), bfhi(r3_)};
          }
          float mx = max2_(max3_(max3_(v[0][0], v[0][1], v[1][0]), v[1][1], v[2][0]), max3_(v[2][1], v[3][0], v[3][1]));
          float mn = min2_(min3_(min3_(v[0][0], v[0][1], v[1][0]), v[1][1], v[2][0]), min3_(v[2][1], v[3][0], v[3][1]));
          mx = rmax4(mx); mn = rmin4(mn);
          const float mxl = -mx * L2E, mnl = mn * L2E;
          f32x2 ep[4], en[4];
#pragma unroll
          for (int i = 0; i < 4; ++i) {
            const f32x2 ap = v[i] * L2E + mxl, an = v[i] * (-L2E) + mnl;
            ep[i] = (f32x2){__builtin_amdgcn_exp2f(ap[0]), __builtin_amdgcn_exp2f(ap[1])};
            en[i] = (f32x2){__builtin_amdgcn_exp2f(an[0]), __builtin_amdgcn_exp2f(an[1])};
          }
          const f32x2 sp2 = (ep[0] + ep[1]) + (ep[2] + ep[3]), sn2 = (en[0] + en[1]) + (en[2] + en[3]);
          const float ip = __builtin_amdgcn_rcpf(rsum4(sp2[0] + sp2[1])), in_ = __builtin_amdgcn_rcpf(rsum4(sn2[0] + sn2[1]));
          u32x2 o[4];
          o[0] = (u32x2){pk2v(ep[0] * ip), pk2v(ep[1] * ip)};
          o[1] = (u32x2){pk2v(ep[2] * ip), pk2v(ep[3] * ip)};
          o[2] = (u32x2){pk2v(en[0] * in_), pk2v(en[1] * in_)};
          o[3] = (u32x2){pk2v(en[2] * in_), pk2v(en[3] * in_)};
          if (which == 0) {
            u16* dst = out + ((size_t)(row0 + wm * 64 + mt * 16 + l15) * 32 + h) * 64 + 4 * g;
#pragma unroll
            for (int c = 0; c < 4; ++c) *reinterpret_cast<u32x2*>(dst + c * 16) = o[c];
          } else {
            char* dst = pkl + (mt * 16 + l15) * 144 + g * 8;
#pragma unroll
            for (int c = 0; c < 4; ++c) *reinterpret_cast<u32x2*>(dst + c * 32) = o[c];
          }
        }
      WAIT_L0();
      __builtin_amdgcn_sched_barrier(0);
#pragma unroll
      for (int ft = 0; ft < 4; ++ft) { kv[ft][0] = kv[ft][1] = zz[ft] = (f32x4){0.f, 0.f, 0.f, 0.f}; }
      const bf16x8 ones = (bf16x8){0x3F80, 0x3F80, 0x3F80, 0x3F80, 0x3F80, 0x3F80, 0x3F80, 0x3F80};
#pragma unroll
      for (int s = 0; s < 2; ++s)
#pragma unroll
        for (int ft = 0; ft < 4; ++ft) {
          const char* src = pkl + (32 * s + 4 * g + q) * 144 + ft * 32 + p * 8;
          s16x4 lo = __builtin_amdgcn_ds_read_tr16_b64_v4i16((lds_s16x4_p)(src));
          s16x4 hi = __builtin_amdgcn_ds_read_tr16_b64_v4i16((lds_s16x4_p)(src + 16 * 144));
          bf16x8 pf = __builtin_shufflevector(lo, hi, 0, 1, 2, 3, 4, 5, 6, 7);
          kv[ft][0] = __builtin_amdgcn_mfma_f32_16x16x32_bf16(vf[s][0], pf, kv[ft][0], 0, 0, 0);
          kv[ft][1] = __builtin_amdgcn_mfma_f32_16x16x32_bf16(vf[s][1], pf, kv[ft][1], 0, 0, 0);
          zz[ft] = __builtin_amdgcn_mfma_f32_16x16x32_bf16(ones, pf, zz[ft], 0, 0, 0);
        }
      if (wm == 1) {
#pragma unroll
        for (int ft = 0; ft < 4; ++ft) {
#pragma unroll
          for (int dt = 0; dt < 2; ++dt) *reinterpret_cast<f32x4*>(red + (ft * 16 + l15) * 32 + dt * 16 + 4 * g) = kv[ft][dt];
          if (g == 0) red[64 * 32 + ft * 16 + l15] = zz[ft][0];
        }
      }
    }
    __syncthreads();
    if (wid8 < 2) {
      float* kd = kvp + (size_t)(h * 16 + tm) * 2048;
      float* zd = zp + (size_t)(h * 16 + tm) * 64;
#pragma unroll
      for (int ft = 0; ft < 4; ++ft) {
#pragma unroll
        for (int dt = 0; dt < 2; ++dt) {
          f32x4 o = *reinterpret_cast<const f32x4*>(red + (ft * 16 + l15) * 32 + dt * 16 + 4 * g);
          *reinterpret_cast<f32x4*>(kd + (ft * 16 + l15) * 32 + dt * 16 + 4 * g) = o + kv[ft][dt];
        }
        if (g == 0) zd[ft * 16 + l15] = red[64 * 32 + ft * 16 + l15] + zz[ft][0];
      }
    }
  } else {
    if (wid8 < 4) {
      f32x4 stm[4], cs4[NTW];
      if constexpr (EPI == EPI_W1 || EPI == EPI_W3) {
#pragma unroll
        for (int j = 0; j < NTW; ++j) {
          const int colj = (j < 2 * NFULL) ? ((j >> 1) * 64 + (2 * wn + (j & 1)) * 16) : (NFULL * 64 + wn * 16);
          cs4[j] = *reinterpret_cast<const f32x4*>(smem + CS_OFF + (colj + 4 * g) * 4);
        }
#pragma unroll
        for (int mt = 0; mt < 4; ++mt) stm[mt] = rs[wm * 64 + mt * 16 + l15];
      }
#pragma unroll
      for (int j = 0; j < NTW; ++j) {
        const int colj = (j < 2 * NFULL) ? ((j >> 1) * 64 + (2 * wn + (j & 1)) * 16) : (NFULL * 64 + wn * 16);
#pragma unroll
        for (int mt = 0; mt < 4; ++mt) {
          f32x2 v0 = (f32x2){acc[mt][j][0], acc[mt][j][1]}, v1 = (f32x2){acc[mt][j][2], acc[mt][j][3]};
          if constexpr (EPI == EPI_W1 || EPI == EPI_W3) {
            v0 = gelu2((v0 - stm[mt][0] * (f32x2){cs4[j][0], cs4[j][1]}) * stm[mt][1]);
            v1 = gelu2((v1 - stm[mt][0] * (f32x2){cs4[j][2], cs4[j][3]}) * stm[mt][1]);
          }
          *reinterpret_cast<u32x2*>(smem + (wm * 64 + mt * 16 + l15) * PB + (colj + 4 * g) * 2) = (u32x2){pk2(v0[0], v0[1]), pk2(v1[0], v1[1])};
        }
      }
    }
    __syncthreads();
#pragma unroll
    for (int it = 0; it < NIT; ++it) {
      const int c = tid + 512 * it, r = c / CH, ch = c % CH;
      u32x4 v = *reinterpret_cast<const u32x4*>(smem + r * PB + ch * 16);
      const int grow = row0 + r, gcol = tn * BN + ch * 8;
      if constexpr (EPI == EPI_W3) {
        *reinterpret_cast<u32x4*>(out + (size_t)grow * F_ + gcol) = v;
      } else if constexpr (EPI == EPI_W1) {
        const int grp = tn >> 2;
        const int sh = grp == 0 ? 1 : grp == 1 ? -1 : grp == 2 ? 2 : -2;
        const int dr = grow + sh;
        if (dr >= 0 && dr < S_) *reinterpret_cast<u32x4*>(out + (size_t)dr * D_ + gcol) = v;
        const int zr = grow - sh;
        if (zr < 0 || zr >= S_) *reinterpret_cast<u32x4*>(out + (size_t)grow * D_ + gcol) = (u32x4){0u, 0u, 0u, 0u};
      } else {
        float y[8];
        const u32x4 a = ra[it];
        if constexpr (EPI == EPI_WO) {
#pragma unroll
          for (int i = 0; i < 4; ++i) { y[2 * i] = rbf(bflo(a[i]) + bflo(v[i])); y[2 * i + 1] = rbf(bfhi(a[i]) + bfhi(v[i])); }
        } else if constexpr (EPI == EPI_W2) {
          f32x4 st = rs[r];
#pragma unroll
          for (int i = 0; i < 4; ++i) {
            y[2 * i] = rbf(rbf((bflo(a[i]) - st[0]) * st[2]) + bflo(v[i]));
            y[2 * i + 1] = rbf(rbf((bfhi(a[i]) - st[0]) * st[2]) + bfhi(v[i]));
          }
        } else {
          const u32x4 b = rb[it];
#pragma unroll
          for (int i = 0; i < 4; ++i) {
            y[2 * i] = rbf(bflo(a[i]) + rbf(bflo(b[i]) + bflo(v[i])));
            y[2 * i + 1] = rbf(bfhi(a[i]) + rbf(bfhi(b[i]) + bfhi(v[i])));
          }
        }
        *reinterpret_cast<u32x4*>(out + (size_t)grow * D_ + gcol) = (u32x4){pk2(y[0], y[1]), pk2(y[2], y[3]), pk2(y[4], y[5]), pk2(y[6], y[7])};
        float sm = ((y[0] + y[1]) + (y[2] + y[3])) + ((y[4] + y[5]) + (y[6] + y[7]));
        sm = rsum8(sm);
        float mu = sm * (1.0f / 64.f), m2 = 0.f;
#pragma unroll
        for (int i = 0; i < 8; ++i) { float d = y[i] - mu; m2 += d * d; }
        m2 = rsum8(m2);
        if (ch == 0) st_out[(size_t)grow * 16 + tn] = (f32x2){sm, m2};
      }
    }
  }
  STAMP(3);
}

__global__ __launch_bounds__(256, 1) void attn_out_kernel(const u16* __restrict__ phiq, const float* __restrict__ kvp,
                                                          const float* __restrict__ zp, u16* __restrict__ o) {
  __shared__ __attribute__((aligned(16))) u16 kvT[48 * 72];
  const int tid = threadIdx.x, lane = tid & 63, wid = tid >> 6, g = lane >> 4, l15 = lane & 15;
  const int h = blockIdx.x & 31, tg = blockIdx.x >> 5;
  f32x4 kvv[16][2];
  float zv[16];
  bf16x8 pq[4][2];
  {
    const float* src = kvp + (size_t)h * 16 * 2048 + tid * 8;
#pragma unroll
    for (int t = 0; t < 16; ++t) {
      kvv[t][0] = *reinterpret_cast<const f32x4*>(src + t * 2048);
      kvv[t][1] = *reinterpret_cast<const f32x4*>(src + t * 2048 + 4);
    }
#pragma unroll
    for (int t = 0; t < 16; ++t) zv[t] = zp[(size_t)(h * 16 + t) * 64 + lane];
#pragma unroll
    for (int mt = 0; mt < 4; ++mt) {
      const u16* ps = phiq + ((size_t)(tg * 256 + wid * 64 + mt * 16 + l15) * 32 + h) * 64 + g * 8;
      pq[mt][0] = *reinterpret_cast<const bf16x8*>(ps);
      pq[mt][1] = *reinterpret_cast<const bf16x8*>(ps + 32);
    }
  }
  __builtin_amdgcn_sched_barrier(0);
  {
    for (int i = tid; i < 15 * 64; i += 256) kvT[(33 + i / 64) * 72 + (i & 63)] = 0;
    f32x4 a0 = kvv[0][0], a1 = kvv[0][1];
#pragma unroll
    for (int t = 1; t < 16; ++t) { a0 += kvv[t][0]; a1 += kvv[t][1]; }
    const int f = tid >> 2, d0 = (tid & 3) * 8;
#pragma unroll
    for (int i = 0; i < 4; ++i) {
      kvT[(d0 + i) * 72 + f] = (u16)(pk2(a0[i], 0.f) & 0xffff);
      kvT[(d0 + 4 + i) * 72 + f] = (u16)(pk2(a1[i], 0.f) & 0xffff);
    }
    if (tid < 64) {
      float z = 0.f;
#pragma unroll
      for (int t = 0; t < 16; ++t) z += zv[t];
      kvT[32 * 72 + tid] = (u16)(pk2(z, 0.f) & 0xffff);
    }
  }
  __syncthreads();
  bf16x8 kf[3][2];
#pragma unroll
  for (int dt = 0; dt < 3; ++dt)
#pragma unroll
    for (int ks = 0; ks < 2; ++ks) kf[dt][ks] = *reinterpret_cast<const bf16x8*>(&kvT[(dt * 16 + l15) * 72 + ks * 32 + g * 8]);
#pragma unroll
  for (int mt = 0; mt < 4; ++mt) {
    const int tok = tg * 256 + wid * 64 + mt * 16 + l15;
    const bf16x8 p0 = pq[mt][0], p1 = pq[mt][1];
    f32x4 r[3];
#pragma unroll
    for (int dt = 0; dt < 3; ++dt) {
      r[dt] = (f32x4){0.f, 0.f, 0.f, 0.f};
      r[dt] = __builtin_amdgcn_mfma_f32_16x16x32_bf16(kf[dt][0], p0, r[dt], 0, 0, 0);
      r[dt] = __builtin_amdgcn_mfma_f32_16x16x32_bf16(kf[dt][1], p1, r[dt], 0, 0, 0);
    }
    float den = rbf(rbf(__shfl(r[2][0], l15)) + 1e-6f);
    float inv = 1.0f / den;
#pragma unroll
    for (int dt = 0; dt < 2; ++dt) {
      u32x2 w = (u32x2){pk2(rbf(r[dt][0]) * inv, rbf(r[dt][1]) * inv), pk2(rbf(r[dt][2]) * inv, rbf(r[dt][3]) * inv)};
      *reinterpret_cast<u32x2*>(o + (size_t)tok * D_ + h * 32 + dt * 16 + 4 * g) = w;
    }
  }
}

constexpr int WC_QKV = 0, WC_WO = WC_QKV + 2 * 1024 * 384, WC_W1 = WC_WO + 2 * 1024 * 128, WC_W2 = WC_W1 + 2 * 1024 * 128,
              WC_W4 = WC_W2 + 2 * 1024 * 128, WC_END = WC_W4 + 2 * F_ * 128;
__global__ __launch_bounds__(256) void prep_kernel(const u16* __restrict__ x, const u16* __restrict__ wqkv,
                                                   const u16* __restrict__ w1, const u16* __restrict__ w3,
                                                   float* __restrict__ cs_qkv, float* __restrict__ cs_w1,
                                                   float* __restrict__ cs_w3, f32x2* __restrict__ st,
                                                   const u16* __restrict__ wo, const u16* __restrict__ w2,
                                                   const u16* __restrict__ w4, char* __restrict__ wc) {
  __shared__ __attribute__((aligned(16))) float red[4][512];
  const int tid = threadIdx.x, b = blockIdx.x;
  if (b >= 368) {
    const int pb = b - 368;
    const u16* w = pb < 32 ? wo + (size_t)pb * 32768 : pb < 64 ? w2 + (size_t)(pb - 32) * 32768 : w4 + (size_t)(pb - 64) * 32768;
    u32x4 v[16];
#pragma unroll
    for (int r = 0; r < 16; ++r) v[r] = *reinterpret_cast<const u32x4*>(w + r * 2048 + tid * 8);
    if (((tid >> 3) & 7) == 3) {
      const int kd = pb < 64 ? 1024 : F_, row = (pb < 32 ? pb : pb < 64 ? pb - 32 : pb - 64) * 32 + (tid >> 7);
      char* dst = wc + (pb < 32 ? WC_WO : pb < 64 ? WC_W2 : WC_W4) + (size_t)((tid >> 6) & 1) * kd * 128 + (size_t)row * 128 + (tid & 7) * 16;
#pragma unroll
      for (int r = 0; r < 16; ++r) *reinterpret_cast<u32x4*>(dst + r * 256) = v[r];
    }
#pragma unroll
    for (int r = 0; r < 16; ++r) asm volatile("" ::"v"(v[r]));
  } else if (b < 240) {
    const u16* w; float* dst; int ld, ch, rg;
    if (b < 96) { w = wqkv; dst = cs_qkv; ld = 3072; ch = b % 6; rg = b / 6; }
    else if (b < 128) { w = w1; dst = cs_w1; ld = 1024; ch = (b - 96) & 1; rg = (b - 96) >> 1; }
    else { w = w3; dst = cs_w3; ld = F_; ch = (b - 128) % 7; rg = (b - 128) / 7; }
    const int lane = tid & 63, wid = tid >> 6, row = rg * 64 + wid * 16;
    u32x4 v[16];
#pragma unroll
    for (int r = 0; r < 16; ++r) v[r] = *reinterpret_cast<const u32x4*>(w + (size_t)(row + r) * ld + ch * 512 + lane * 8);
    if (b < 128 && (lane >> 3) == 3) {
      const int cpp = b < 96 ? 384 : 128;
      char* cpd = wc + (b < 96 ? WC_QKV + (ch & 1) * (1024 * 384) + (ch >> 1) * 128 : WC_W1 + ch * (1024 * 128)) + row * cpp + (lane & 7) * 16;
#pragma unroll
      for (int r = 0; r < 16; ++r) *reinterpret_cast<u32x4*>(cpd + r * cpp) = v[r];
    }
    f32x4 s0 = (f32x4){0.f, 0.f, 0.f, 0.f}, s1 = s0;
#pragma unroll
    for (int r = 0; r < 16; ++r) {
      s0 += (f32x4){bflo(v[r][0]), bfhi(v[r][0]), bflo(v[r][1]), bfhi(v[r][1])};
      s1 += (f32x4){bflo(v[r][2]), bfhi(v[r][2]), bflo(v[r][3]), bfhi(v[r][3])};
    }
    *reinterpret_cast<f32x4*>(&red[wid][lane * 8]) = s0;
    *reinterpret_cast<f32x4*>(&red[wid][lane * 8 + 4]) = s1;
    __syncthreads();
    f32x2 t = *reinterpret_cast<const f32x2*>(&red[0][tid * 2]);
#pragma unroll
    for (int i = 1; i < 4; ++i) t += *reinterpret_cast<const f32x2*>(&red[i][tid * 2]);
    *reinterpret_cast<f32x2*>(dst + rg * ld + ch * 512 + tid * 2) = t;
  } else {
    const int lane = tid & 63, wid = tid >> 6;
    const int rbase = (b - 240) * 16 + wid * 4;
    u32x4 xv[8];
#pragma unroll
    for (int i = 0; i < 8; ++i) xv[i] = *reinterpret_cast<const u32x4*>(x + (size_t)rbase * D_ + i * 512 + lane * 8);
    __builtin_amdgcn_sched_barrier(0);
#pragma unroll
    for (int rr = 0; rr < 4; ++rr) {
      const int row = rbase + rr;
#pragma unroll
      for (int hf = 0; hf < 2; ++hf) {
        const u32x4 v = xv[rr * 2 + hf];
        float y[8];
#pragma unroll
        for (int i = 0; i < 4; ++i) { y[2 * i] = bflo(v[i]); y[2 * i + 1] = bfhi(v[i]); }
        float sm = ((y[0] + y[1]) + (y[2] + y[3])) + ((y[4] + y[5]) + (y[6] + y[7]));
        sm = rsum8(sm);
        float mu = sm * (1.0f / 64.f), m2 = 0.f;
#pragma unroll
        for (int i = 0; i < 8; ++i) { float d = y[i] - mu; m2 += d * d; }
        m2 = rsum8(m2);
        if ((lane & 7) == 0) st[(size_t)row * 16 + hf * 8 + (lane >> 3)] = (f32x2){sm, m2};
      }
    }
  }
}

extern "C" void kernel_launch(void* const* d_in, const int* in_sizes, int n_in, void* d_out, int out_size, void* d_ws,
                              size_t ws_size, hipStream_t stream) {
  const u16* x_in = (const u16*)d_in[0];
  const u16* wqkv = (const u16*)d_in[1];
  const u16* wphi = (const u16*)d_in[2];
  const u16* wo = (const u16*)d_in[3];
  const u16* w1 = (const u16*)d_in[5];
  const u16* w2 = (const u16*)d_in[6];
  const u16* w3 = (const u16*)d_in[7];
  const u16* w4 = (const u16*)d_in[8];
  char* ws = (char*)d_ws;
  size_t off = 0;
  auto alloc = [&](size_t bytes) { char* p = ws + off; off += (bytes + 4095) & ~(size_t)4095; return p; };
  u16* xb = (u16*)alloc((size_t)S_ * D_ * 2);
  u16* x2 = (u16*)alloc((size_t)S_ * D_ * 2);
  u16* x1 = (u16*)alloc((size_t)S_ * D_ * 2);
  u16* hs = (u16*)alloc((size_t)S_ * D_ * 2);
  u16* ob = (u16*)alloc((size_t)S_ * D_ * 2);
  u16* gb = (u16*)alloc((size_t)S_ * F_ * 2);
  u16* phiq = (u16*)alloc((size_t)S_ * 2048 * 2);
  float* kvp = (float*)alloc((size_t)32 * 16 * 2048 * 4);
  float* zp = (float*)alloc((size_t)32 * 16 * 64 * 4);
  f32x2* st_x = (f32x2*)alloc((size_t)S_ * 16 * 8);
  f32x2* st_x2 = (f32x2*)alloc((size_t)S_ * 16 * 8);
  f32x2* st_x1 = (f32x2*)alloc((size_t)S_ * 16 * 8);
  float* cs_qkv = (float*)alloc(16 * 3072 * 4);
  float* cs_w1 = (float*)alloc(16 * 1024 * 4);
  float* cs_w3 = (float*)alloc(16 * F_ * 4);
  char* wc = alloc(WC_END);

  prep_kernel<<<368 + 176, 256, 0, stream>>>(x_in, wqkv, w1, w3, cs_qkv, cs_w1, cs_w3, st_x, wo, w2, w4, wc);
  for (int layer = 0; layer < 2; ++layer) {
    const u16* x = layer == 0 ? x_in : xb;
    u16* xo = layer == 0 ? xb : (u16*)d_out;
    gemm_kernel<192, 3, 1024, 1024, 3072, EPI_QKV><<<256, 512, 0, stream>>>(x, wqkv, (const u16*)(wc + WC_QKV), cs_qkv, st_x, nullptr, nullptr, phiq, nullptr, wphi, kvp, zp);
    attn_out_kernel<<<256, 256, 0, stream>>>(phiq, kvp, zp, ob);
    gemm_kernel<64, 5, 1024, 1024, 1024, EPI_WO><<<256, 512, 0, stream>>>(ob, wo, (const u16*)(wc + WC_WO), nullptr, nullptr, x, nullptr, x2, st_x2, nullptr, nullptr, nullptr);
    gemm_kernel<64, 5, 1024, 1024, 1024, EPI_W1><<<256, 512, 0, stream>>>(x2, w1, (const u16*)(wc + WC_W1), cs_w1, st_x2, nullptr, nullptr, hs, nullptr, nullptr, nullptr, nullptr);
    gemm_kernel<64, 5, 1024, 1024, 1024, EPI_W2><<<256, 512, 0, stream>>>(hs, w2, (const u16*)(wc + WC_W2), nullptr, st_x2, x2, nullptr, x1, st_x1, nullptr, nullptr, nullptr);
    gemm_kernel<224, 3, 1024, 1024, F_, EPI_W3><<<256, 512, 0, stream>>>(x1, w3, nullptr, cs_w3, st_x1, nullptr, nullptr, gb, nullptr, nullptr, nullptr, nullptr);
    gemm_kernel<64, 5, F_, F_, 1024, EPI_W4><<<256, 512, 0, stream>>>(gb, w4, (const u16*)(wc + WC_W4), nullptr, nullptr, x2, x1, xo, st_x, nullptr, nullptr, nullptr);
  }
}
```

```cpp
#include <hip/hip_runtime.h>
#include <hip/hip_bf16.h>
#include <stdint.h>
#include <type_traits>

typedef short bf16x8 __attribute__((ext_vector_type(8)));
typedef short s16x4 __attribute__((ext_vector_type(4)));
typedef float f32x4 __attribute__((ext_vector_type(4)));
typedef float f32x2 __attribute__((ext_vector_type(2)));
typedef unsigned u32x2 __attribute__((ext_vector_type(2)));
typedef unsigned u32x4 __attribute__((ext_vector_type(4)));
typedef __attribute__((address_space(3))) s16x4* lds_s16x4_p;
typedef unsigned short u16;

constexpr int S_ = 2048, D_ = 1024, H_ = 32, F_ = 3584;

__device__ __forceinline__ float bf2f(u16 v) { return __uint_as_float(((unsigned)v) << 16); }
__device__ __forceinline__ float bflo(unsigned v) { return __uint_as_float(v << 16); }
__device__ __forceinline__ float bfhi(unsigned v) { return __uint_as_float(v & 0xffff0000u); }
typedef __bf16 bf16x2_t __attribute__((ext_vector_type(2)));
__device__ __forceinline__ unsigned pk2(float a, float b) {
  return __builtin_bit_cast(unsigned, __builtin_convertvector((f32x2){a, b}, bf16x2_t));
}
__device__ __forceinline__ unsigned pk2v(f32x2 v) { return __builtin_bit_cast(unsigned, __builtin_convertvector(v, bf16x2_t)); }
__device__ __forceinline__ float max2_(float a, float b) { float r; asm("v_max_f32 %0, %1, %2" : "=v"(r) : "v"(a), "v"(b)); return r; }
__device__ __forceinline__ float min2_(float a, float b) { float r; asm("v_min_f32 %0, %1, %2" : "=v"(r) : "v"(a), "v"(b)); return r; }
__device__ __forceinline__ float max3_(float a, float b, float c) { float r; asm("v_max3_f32 %0, %1, %2, %3" : "=v"(r) : "v"(a), "v"(b), "v"(c)); return r; }
__device__ __forceinline__ float min3_(float a, float b, float c) { float r; asm("v_min3_f32 %0, %1, %2, %3" : "=v"(r) : "v"(a), "v"(b), "v"(c)); return r; }
__device__ __forceinline__ float rbf(float a) { return bflo(pk2(a, 0.f)); }
__device__ __forceinline__ float gelu_t(float x) {
  const float c1 = -2.0f * 0.7978845608028654f * 1.4426950408889634f;
  const float c2 = c1 * 0.044715f;
  float t = x * x;
  float w = x * __builtin_fmaf(t, c2, c1);
  float e = __builtin_amdgcn_exp2f(w);
  return x * __builtin_amdgcn_rcpf(1.0f + e);
}

__device__ __forceinline__ f32x2 gelu2(f32x2 x) {
  const float c1 = -2.0f * 0.7978845608028654f * 1.4426950408889634f;
  const float c2 = c1 * 0.044715f;
  f32x2 t = x * x;
  f32x2 w = x * (t * c2 + c1);
  f32x2 d = (f32x2){__builtin_amdgcn_exp2f(w[0]), __builtin_amdgcn_exp2f(w[1])} + 1.0f;
  return x * (f32x2){__builtin_amdgcn_rcpf(d[0]), __builtin_amdgcn_rcpf(d[1])};
}
__device__ __forceinline__ void x16(float v, float& a, float& b) {
  auto r = __builtin_amdgcn_permlane16_swap(__float_as_uint(v), __float_as_uint(v), false, false);
  a = __uint_as_float(r[0]); b = __uint_as_float(r[1]);
}
__device__ __forceinline__ void x32(float v, float& a, float& b) {
  auto r = __builtin_amdgcn_permlane32_swap(__float_as_uint(v), __float_as_uint(v), false, false);
  a = __uint_as_float(r[0]); b = __uint_as_float(r[1]);
}
__device__ __forceinline__ float rmax4(float v) { float a, b; x16(v, a, b); v = max2_(a, b); x32(v, a, b); return max2_(a, b); }
__device__ __forceinline__ float rmin4(float v) { float a, b; x16(v, a, b); v = min2_(a, b); x32(v, a, b); return min2_(a, b); }
__device__ __forceinline__ float rsum4(float v) { float a, b; x16(v, a, b); v = a + b; x32(v, a, b); return a + b; }

__device__ __forceinline__ float rsum8(float v) {
  v += __uint_as_float(__builtin_amdgcn_mov_dpp(__float_as_uint(v), 0xB1, 0xF, 0xF, true));
  v += __uint_as_float(__builtin_amdgcn_mov_dpp(__float_as_uint(v), 0x4E, 0xF, 0xF, true));
  v += __uint_as_float(__builtin_amdgcn_mov_dpp(__float_as_uint(v), 0x141, 0xF, 0xF, true));
  return v;
}
#define WAIT_V(n) asm volatile("s_waitcnt vmcnt(" #n ")" ::: "memory")
#define WAIT_L0() asm volatile("s_waitcnt lgkmcnt(0)" ::: "memory")
template <int N> __device__ __forceinline__ void wait_vm() {
  asm volatile("s_waitcnt vmcnt(%0)" ::"i"(N) : "memory");
}

__device__ __forceinline__ void glds16(unsigned voff, const void* sbase, unsigned lds) {
  unsigned keep;
  asm volatile("s_mov_b32 %0, m0\n\ts_mov_b32 m0, %3\n\ts_nop 0\n\tglobal_load_lds_dwordx4 %1, %2\n\ts_mov_b32 m0, %0"
               : "=&s"(keep) : "v"(voff), "s"(sbase), "s"(lds) : "memory");
}

__device__ __forceinline__ void mfma_acc(f32x4& c, const u32x4& a, const u32x4& b) {
  asm volatile("v_mfma_f32_16x16x32_bf16 %0, %1, %2, %0" : "+a"(c) : "v"(a), "v"(b));
}

#define STAMP(i)

#define XPOS 2
enum { EPI_QKV = 0, EPI_WO = 1, EPI_W1 = 2, EPI_W2 = 3, EPI_W3 = 4, EPI_W4 = 5 };

template <int BN, int NST, int KD, int LDA, int LDB, int EPI>
__global__ __launch_bounds__(512, 2) void gemm_kernel(
    const u16* __restrict__ A, const u16* __restrict__ W,
    const u16* __restrict__ Wc,
    const float* __restrict__ cs,
    const f32x2* __restrict__ st_in,
    const u16* __restrict__ r0,
    const u16* __restrict__ r1,
    u16* __restrict__ out,
    f32x2* __restrict__ st_out,
    const u16* __restrict__ wphi,
    float* __restrict__ kvp,
    float* __restrict__ zp)
{
  constexpr int NFULL = BN / 64, HALF = (BN % 64) / 32;
  constexpr int A_BYTES = 128 * 64 * 2, B_BYTES = BN * 64 * 2, STAGE = A_BYTES + B_BYTES;
  constexpr int NT = KD / 64, NTW = 2 * NFULL + HALF, LPS = 4 + 2 * NFULL + HALF;
  constexpr int PB = BN * 2 + 16;
  constexpr int EPI_BYTES = (EPI == EPI_QKV) ? (4 * 64 * 144 + 2 * (64 * 32 + 64) * 4) : 128 * PB;
  constexpr int MAINB = NST * STAGE;
  constexpr int RS_OFF = (MAINB > EPI_BYTES ? MAINB : EPI_BYTES);
  constexpr int CS_OFF = RS_OFF + 128 * 16;
  constexpr int WP_OFF = CS_OFF + 1024;
  constexpr int SMEM = WP_OFF + (EPI == EPI_QKV ? 4096 : 0);
  constexpr bool HAS_CS = (EPI == EPI_QKV || EPI == EPI_W1 || EPI == EPI_W3);
  constexpr bool HAS_ST = (EPI == EPI_QKV || EPI == EPI_W1 || EPI == EPI_W3 || EPI == EPI_W2);
  constexpr bool HAS_RES = (EPI == EPI_WO || EPI == EPI_W2 || EPI == EPI_W4);
  constexpr int LAG = (BN == 64) ? 1 : 0;
  constexpr int CH = BN / 8, NIT = CH / 4;
  static_assert(SMEM <= 163840, "LDS");
  __shared__ __attribute__((aligned(16))) char smem[SMEM];

  STAMP(0);
  const int tid = threadIdx.x, lane = tid & 63;
  const int wid8 = __builtin_amdgcn_readfirstlane(tid >> 6), wid = wid8 & 3, wm = wid >> 1, wn = wid & 1;
  const int g = lane >> 4, l15 = lane & 15;
  const int xcd = blockIdx.x & 7, idx = blockIdx.x >> 3;
  const int tm = (xcd >> 1) * 4 + (idx & 3), tn = (xcd & 1) * 8 + (idx >> 2);
  const int row0 = tm * 128;
  f32x4* rs = reinterpret_cast<f32x4*>(smem + RS_OFF);

  f32x4 acc[4][NTW];
  u32x4 ra[HAS_RES ? NIT : 1], rb[(EPI == EPI_W4) ? NIT : 1];

  unsigned rof[HAS_RES ? NIT : 1];
  if constexpr (HAS_RES) {
#pragma unroll
    for (int it = 0; it < NIT; ++it) {
      const int c = tid + 512 * it, r = c / CH, ch = c % CH;
      rof[it] = (unsigned)((row0 + r) * D_ + tn * BN + ch * 8) * 2u;
      asm volatile("" : "+v"(rof[it]));
    }
  }
  auto ldres = [&]() {
    if constexpr (HAS_RES) {
#pragma unroll
      for (int it = 0; it < NIT; ++it) {
        ra[it] = *reinterpret_cast<const u32x4*>(reinterpret_cast<const char*>(r0) + rof[it]);
        if constexpr (EPI == EPI_W4) rb[it] = *reinterpret_cast<const u32x4*>(reinterpret_cast<const char*>(r1) + rof[it]);
      }
    }
  };

  if (wid8 >= 4) {
    unsigned voA[4], voB[NFULL > 0 ? NFULL : 1][2], voH = 0;
    const bool pk_ = (HALF == 0) && ((tn & 7) == 3);
    const unsigned pitchB = pk_ ? NFULL * 128 : LDB * 2;
#pragma unroll
    for (int i = 0; i < 4; ++i) {
      int r = (wid * 4 + i) * 8 + (lane >> 3);
      int ch = (lane & 7) ^ ((r >> 1) & 7);
      voA[i] = (unsigned)(row0 + r) * (LDA * 2) + ch * 16;
    }
#pragma unroll
    for (int b = 0; b < NFULL; ++b) {
      int colb = (EPI == EPI_QKV) ? (b * 1024 + tn * 64) : (tn * BN + b * 64);
#pragma unroll
      for (int j = 0; j < 2; ++j) {
        int r = (wid * 2 + j) * 8 + (lane >> 3);
        int f = ((r >> 1) & 1) | (((r >> 3) & 1) << 1);
        int chp = lane & 7;
        int gch = (((chp >> 1) ^ f) << 1) | (chp & 1);
        voB[b][j] = (unsigned)r * pitchB + (pk_ ? b * 128 : colb * 2) + gch * 16;
      }
    }
    if constexpr (HALF) {
      int r = wid * 16 + (lane >> 2);
      int chp = lane & 3;
      int gch = (((chp >> 1) ^ ((r >> 3) & 1)) << 1) | (chp & 1);
      voH = (unsigned)r * (LDB * 2) + (tn * BN + NFULL * 64) * 2 + gch * 16;
    }
    const char* Ab = reinterpret_cast<const char*>(A);
    const char* Wb = pk_ ? reinterpret_cast<const char*>(Wc) + (size_t)(tn >> 3) * (KD * NFULL * 128) : reinterpret_cast<const char*>(W);
    const unsigned lbase = (unsigned)(uintptr_t)(__attribute__((address_space(3))) char*)smem;
    auto stage = [&](int kt, int buf) {
      const char* a = Ab + (size_t)kt * 128;
      const char* w = Wb + (size_t)(kt * (64 * pitchB));
      const unsigned s = lbase + buf * STAGE;
#pragma unroll
      for (int i = 0; i < 4; ++i) glds16(voA[i], a, s + (wid * 4 + i) * 1024);
#pragma unroll
      for (int b = 0; b < NFULL; ++b)
#pragma unroll
        for (int j = 0; j < 2; ++j) glds16(voB[b][j], w, s + A_BYTES + b * 8192 + (wid * 2 + j) * 1024);
      if constexpr (HALF) glds16(voH, w, s + A_BYTES + NFULL * 8192 + wid * 1024);
    };
#pragma unroll
    for (int s = 0; s < NST; ++s) stage(s, s);
    constexpr int XL = 0;
    constexpr int NCS = HAS_CS ? (EPI == EPI_QKV ? 16 : 1) : 0;
    constexpr int XE = NCS + (EPI == EPI_QKV ? 1 : 0) + (HAS_ST ? 4 : 0) + (HAS_RES ? NIT * (EPI == EPI_W4 ? 2 : 1) : 0);
    f32x4 stp[4];
    const int lt = tid - 256;
    u32x4 wpx;
    f32x4 csp[HAS_CS ? NCS : 1];
    constexpr int XP = XPOS;
    wait_vm<(NST - 1 - LAG) * LPS>();
    __builtin_amdgcn_s_barrier();
#pragma unroll
    for (int kt = 0; kt < XP; ++kt) {
      wait_vm<(NST - 2 - LAG) * LPS>();
      __builtin_amdgcn_s_barrier();
      stage(kt + NST, kt % NST);
    }
    asm volatile("" ::: "memory");
    if constexpr (HAS_CS) {
      if (lt < BN / 4) {
        const float* cp = cs + (EPI == EPI_QKV ? (lt >> 4) * 1024 + tn * 64 + (lt & 15) * 4 : tn * BN + lt * 4);
#pragma unroll
        for (int i = 0; i < NCS; ++i) csp[i] = *reinterpret_cast<const f32x4*>(cp + i * LDB);
      }
    }
    if constexpr (EPI == EPI_QKV) wpx = *reinterpret_cast<const u32x4*>(wphi + tn * 2048 + lt * 8);
    if constexpr (HAS_ST) {
      const f32x4* sp = reinterpret_cast<const f32x4*>(st_in + (size_t)(row0 + (lt >> 1)) * 16 + (lt & 1) * 8);
#pragma unroll
      for (int i = 0; i < 4; ++i) stp[i] = sp[i];
    }
    ldres();
    asm volatile("" ::: "memory");
    constexpr int NPEEL = XP + NST - 1 - LAG;
    auto head = [&](auto xe) {
      constexpr int X = decltype(xe)::value;
#pragma unroll
      for (int kt = XP; kt < NPEEL; ++kt) {
        wait_vm<(NST - 2 - LAG) * LPS + X>();
        __builtin_amdgcn_s_barrier();
        stage(kt + NST, kt % NST);
      }
    };
    if (HAS_CS && wid == 0) head(std::integral_constant<int, XE>{});
    else head(std::integral_constant<int, XE - NCS>{});
    int buf = NPEEL % NST;
    for (int kt = NPEEL; kt < NT - NST; ++kt) {
      wait_vm<(NST - 2 - LAG) * LPS>();
      __builtin_amdgcn_s_barrier();
      stage(kt + NST, buf);
      buf = buf + 1 == NST ? 0 : buf + 1;
    }
    asm volatile("" ::: "memory");
#pragma unroll
    for (int r = 0; r < NST - 1; ++r) {
      constexpr int NSL = NST - LAG;
      if (r == 0) wait_vm<(NSL - 2) * LPS + XL>();
      else if (r == 1) wait_vm<(NSL > 3 ? NSL - 3 : 0) * LPS + XL>();
      else if (r == 2) wait_vm<(NSL > 4 ? NSL - 4 : 0) * LPS + XL>();
      else if (r == 3) wait_vm<(NSL > 5 ? NSL - 5 : 0) * LPS + XL>();
      else wait_vm<XL>();
      __builtin_amdgcn_s_barrier();
    }
    if constexpr (HAS_ST) {
#pragma unroll
      for (int i = 0; i < 4; ++i) asm volatile("" : "+v"(stp[i]));
      float sm = 0.f;
#pragma unroll
      for (int i = 0; i < 4; ++i) sm += stp[i][0] + stp[i][2];
      sm += __shfl_xor(sm, 1);
      float mean = sm * (1.0f / 1024.f);
      float m2 = 0.f;
#pragma unroll
      for (int i = 0; i < 4; ++i) {
        float d0 = stp[i][0] * (1.0f / 64.f) - mean, d1 = stp[i][2] * (1.0f / 64.f) - mean;
        m2 += stp[i][1] + stp[i][3] + 64.f * (d0 * d0 + d1 * d1);
      }
      m2 += __shfl_xor(m2, 1);
      float var = m2 * (1.0f / 1024.f);
      float r1_ = 1.0f / sqrtf(var + 1e-5f);
      float sc = r1_;
      if constexpr (EPI == EPI_W1) sc = r1_ / sqrtf(var * r1_ * r1_ + 1e-5f);
      if ((lt & 1) == 0) rs[lt >> 1] = (f32x4){mean, sc, r1_, 0.f};
    }
    if constexpr (HAS_CS) {
      if (lt < BN / 4) {
#pragma unroll
        for (int i = 0; i < NCS; ++i) asm volatile("" : "+v"(csp[i]));
#pragma unroll
        for (int i = NCS / 2; i > 0; i >>= 1)
#pragma unroll
          for (int j = 0; j < i; ++j) csp[j] += csp[j + i];
        *reinterpret_cast<f32x4*>(smem + CS_OFF + lt * 16) = csp[0];
      }
    }
    if constexpr (EPI == EPI_QKV) *reinterpret_cast<u32x4*>(smem + WP_OFF + lt * 16) = wpx;
  } else {
    ldres();
    const int q = l15 >> 2, p = lane & 3;
    const int fB = (q >> 1) | ((g & 1) << 1);
    unsigned aoff[2], boff[2], hoff = 0;
#pragma unroll
    for (int ks = 0; ks < 2; ++ks) aoff[ks] = (wm * 64 + l15) * 128 + (((4 * ks + g) ^ (l15 >> 1)) * 16);
#pragma unroll
    for (int u = 0; u < 2; ++u) boff[u] = A_BYTES + (8 * g + q) * 128 + (((2 * wn + u) ^ fB) * 32) + p * 8;
    if constexpr (HALF) hoff = A_BYTES + NFULL * 8192 + (8 * g + q) * 64 + ((wn ^ (g & 1)) * 32) + p * 8;

    auto ldf = [&](int i, const char* s, int ks, u32x4(&af)[4], u32x4(&bfr)[NTW]) {
      if (i == 0 || i > NTW) {
        const int mt = i == 0 ? 0 : i - NTW;
        af[mt] = *reinterpret_cast<const u32x4*>(s + aoff[ks] + mt * 2048);
      } else if (i - 1 < 2 * NFULL) {
        const int b = (i - 1) >> 1, u = (i - 1) & 1;
        s16x4 lo = __builtin_amdgcn_ds_read_tr16_b64_v4i16((lds_s16x4_p)(s + boff[u] + b * 8192 + ks * 4096));
        s16x4 hi = __builtin_amdgcn_ds_read_tr16_b64_v4i16((lds_s16x4_p)(s + boff[u] + b * 8192 + ks * 4096 + 512));
        bfr[i - 1] = __builtin_shufflevector(__builtin_bit_cast(u32x2, lo), __builtin_bit_cast(u32x2, hi), 0, 1, 2, 3);
      } else {
        s16x4 lo = __builtin_amdgcn_ds_read_tr16_b64_v4i16((lds_s16x4_p)(s + hoff + ks * 2048));
        s16x4 hi = __builtin_amdgcn_ds_read_tr16_b64_v4i16((lds_s16x4_p)(s + hoff + ks * 2048 + 256));
        bfr[NTW - 1] = __builtin_shufflevector(__builtin_bit_cast(u32x2, lo), __builtin_bit_cast(u32x2, hi), 0, 1, 2, 3);
      }
    };
    constexpr int NM = 4 * NTW, NL = NTW + 4;
    auto phase = [&](u32x4(&afc)[4], u32x4(&bfc)[NTW], u32x4(&afn)[4], u32x4(&bfn)[NTW], int nbuf, int nks, auto rd, auto bar) {
      const char* s = smem + nbuf * STAGE;
      constexpr int BAR = decltype(bar)::value, R0 = BAR < 0 ? 0 : BAR + 1;
      constexpr int RSP = (NM - R0 - 2) / NL > 0 ? (NM - R0 - 2) / NL : 1;
      static_assert(R0 + (NL - 1) * RSP < NM, "reads");
#pragma unroll
      for (int mt = 0; mt < 4; ++mt)
#pragma unroll
        for (int j = 0; j < NTW; ++j) {
          const int m = mt * NTW + j;
          if (m == BAR) {
            WAIT_L0();
            __builtin_amdgcn_s_barrier();
            asm volatile("" ::: "memory");
          }
          if (decltype(rd)::value && m >= R0 && (m - R0) % RSP == 0 && (m - R0) / RSP < NL) ldf((m - R0) / RSP, s, nks, afn, bfn);
          if (EPI == EPI_QKV && j >= 4) mfma_acc(acc[mt][j], afc[mt], bfc[j]);
          else mfma_acc(acc[mt][j], bfc[j], afc[mt]);
          __builtin_amdgcn_sched_barrier(0);
        }
    };
    typedef std::integral_constant<int, -1> nobar;
#pragma unroll
    for (int mt = 0; mt < 4; ++mt)
#pragma unroll
      for (int j = 0; j < NTW; ++j) {
        acc[mt][j] = (f32x4){0.f, 0.f, 0.f, 0.f};
        asm volatile("" : "+a"(acc[mt][j]));
      }
    u32x4 af0[4], af1[4], bf0[NTW], bf1[NTW];
    __builtin_amdgcn_s_barrier();
    asm volatile("" ::: "memory");
    STAMP(1);
#pragma unroll
    for (int i = 0; i < NL; ++i) { ldf(i, smem, 0, af0, bf0); __builtin_amdgcn_sched_barrier(0); }
    int buf = 0;
    if constexpr (LAG) {
      phase(af0, bf0, af1, bf1, 0, 1, std::true_type{}, nobar{});
      for (int kt = 0; kt < NT - 1; ++kt) {
        buf = buf + 1 == NST ? 0 : buf + 1;
        phase(af1, bf1, af0, bf0, buf, 0, std::true_type{}, nobar{});
        asm volatile("" ::: "memory");
        __builtin_amdgcn_s_barrier();
        asm volatile("" ::: "memory");
        phase(af0, bf0, af1, bf1, buf, 1, std::true_type{}, nobar{});
      }
      phase(af1, bf1, af0, bf0, buf, 0, std::false_type{}, nobar{});
    } else {
      for (int kt = 0; kt < NT - 1; ++kt) {
        phase(af0, bf0, af1, bf1, buf, 1, std::true_type{}, nobar{});
        buf = buf + 1 == NST ? 0 : buf + 1;
        phase(af1, bf1, af0, bf0, buf, 0, std::true_type{}, std::integral_constant<int, 4>{});
      }
      phase(af0, bf0, af1, bf1, buf, 1, std::true_type{}, nobar{});
      phase(af1, bf1, af0, bf0, buf, 0, std::false_type{}, nobar{});
    }
    asm volatile("s_nop 7\n\ts_nop 7\n\ts_nop 7" ::: "memory");
  }
  WAIT_L0();
  __builtin_amdgcn_s_barrier();
  asm volatile("" ::: "memory");
  STAMP(2);

  if constexpr (EPI == EPI_QKV) {
    const int h = tn * 2 + wn;
    const int q = l15 >> 2, p = lane & 3;
    f32x4 kv[4][2], zz[4];
    float* red = reinterpret_cast<float*>(smem + 4 * 64 * 144) + wn * (64 * 32 + 64);
    if (wid8 < 4) {
      bf16x8 wf[2];
#pragma unroll
      for (int ft = 0; ft < 2; ++ft) {
        const char* src = smem + WP_OFF + wn * 2048 + (4 * g + q) * 64 + ft * 32 + p * 8;
        s16x4 lo = __builtin_amdgcn_ds_read_tr16_b64_v4i16((lds_s16x4_p)(src));
        s16x4 hi = __builtin_amdgcn_ds_read_tr16_b64_v4i16((lds_s16x4_p)(src + 16 * 64));
        wf[ft] = __builtin_shufflevector(lo, hi, 0, 1, 2, 3, 4, 5, 6, 7);
      }
      const float* csl = reinterpret_cast<const float*>(smem + CS_OFF);
      f32x4 cs4[4];
      float csv[2];
#pragma unroll
      for (int t = 0; t < 2; ++t) {
        cs4[t] = *reinterpret_cast<const f32x4*>(csl + wn * 32 + t * 16 + 4 * g);
        cs4[2 + t] = *reinterpret_cast<const f32x4*>(csl + 64 + wn * 32 + t * 16 + 4 * g);
        csv[t] = csl[128 + wn * 32 + t * 16 + l15];
      }
      bf16x8 qf[4], kf[4], vf[2][2];
#pragma unroll
      for (int mt = 0; mt < 4; ++mt) {
        f32x4 st = rs[wm * 64 + mt * 16 + l15];
        unsigned pq_[4], pk_[4];
#pragma unroll
        for (int t = 0; t < 2; ++t) {
          float a[4], b[4];
#pragma unroll
          for (int i = 0; i < 4; ++i) {
            a[i] = st[1] * (acc[mt][t][i] - st[0] * cs4[t][i]);
            b[i] = st[1] * (acc[mt][2 + t][i] - st[0] * cs4[2 + t][i]);
          }
          pq_[t * 2] = pk2(a[0], a[1]); pq_[t * 2 + 1] = pk2(a[2], a[3]);
          pk_[t * 2] = pk2(b[0], b[1]); pk_[t * 2 + 1] = pk2(b[2], b[3]);
        }
        qf[mt] = __builtin_bit_cast(bf16x8, (u32x4){pq_[0], pq_[1], pq_[2], pq_[3]});
        kf[mt] = __builtin_bit_cast(bf16x8, (u32x4){pk_[0], pk_[1], pk_[2], pk_[3]});
      }
#pragma unroll
      for (int s = 0; s < 2; ++s) {
        unsigned pv[2][4];
#pragma unroll
        for (int m2 = 0; m2 < 2; ++m2) {
          int mt = s * 2 + m2;
          f32x4 s0 = rs[wm * 64 + mt * 16 + 4 * g + 0], s1 = rs[wm * 64 + mt * 16 + 4 * g + 1];
          f32x4 s2 = rs[wm * 64 + mt * 16 + 4 * g + 2], s3 = rs[wm * 64 + mt * 16 + 4 * g + 3];
#pragma unroll
          for (int t = 0; t < 2; ++t) {
            float a0 = s0[1] * (acc[mt][4 + t][0] - s0[0] * csv[t]);
            float a1 = s1[1] * (acc[mt][4 + t][1] - s1[0] * csv[t]);
            float a2 = s2[1] * (acc[mt][4 + t][2] - s2[0] * csv[t]);
            float a3 = s3[1] * (acc[mt][4 + t][3] - s3[0] * csv[t]);
            pv[t][m2 * 2] = pk2(a0, a1); pv[t][m2 * 2 + 1] = pk2(a2, a3);
          }
        }
#pragma unroll
        for (int t = 0; t < 2; ++t) vf[s][t] = __builtin_bit_cast(bf16x8, (u32x4){pv[t][0], pv[t][1], pv[t][2], pv[t][3]});
      }
      char* pkl = smem + wid * (64 * 144);
      const float L2E = 1.4426950408889634f;
#pragma unroll
      for (int which = 0; which < 2; ++which)
#pragma unroll
        for (int mt = 0; mt < 4; ++mt) {
          f32x4 z4 = (f32x4){0.f, 0.f, 0.f, 0.f};
          f32x4 p0 = __builtin_amdgcn_mfma_f32_16x16x32_bf16(wf[0], which ? kf[mt] : qf[mt], z4, 0, 0, 0);
          f32x4 p1 = __builtin_amdgcn_mfma_f32_16x16x32_bf16(wf[1], which ? kf[mt] : qf[mt], z4, 0, 0, 0);
          f32x2 v[4];
          {
            const unsigned r0_ = pk2(p0[0], p0[1]), r1_ = pk2(p0[2], p0[3]), r2_ = pk2(p1[0], p1[1]), r3_ = pk2(p1[2], p1[3]);
            v[0] = (f32x2){bflo(r0_), bfhi(r0_)}; v[1] = (f32x2){bflo(r1_), bfhi(r1_)};
            v[2] = (f32x2){bflo(r2_), bfhi(r2_)}; v[3] = (f32x2){bflo(r3_), bfhi(r3_)};
          }
          float mx = max2_(max3_(max3_(v[0][0], v[0][1], v[1][0]), v[1][1], v[2][0]), max3_(v[2][1], v[3][0], v[3][1]));
          float mn = min2_(min3_(min3_(v[0][0], v[0][1], v[1][0]), v[1][1], v[2][0]), min3_(v[2][1], v[3][0], v[3][1]));
          mx = rmax4(mx); mn = rmin4(mn);
          const float mxl = -mx * L2E, mnl = mn * L2E;
          f32x2 ep[4], en[4];
#pragma unroll
          for (int i = 0; i < 4; ++i) {
            const f32x2 ap = v[i] * L2E + mxl, an = v[i] * (-L2E) + mnl;
            ep[i] = (f32x2){__builtin_amdgcn_exp2f(ap[0]), __builtin_amdgcn_exp2f(ap[1])};
            en[i] = (f32x2){__builtin_amdgcn_exp2f(an[0]), __builtin_amdgcn_exp2f(an[1])};
          }
          const f32x2 sp2 = (ep[0] + ep[1]) + (ep[2] + ep[3]), sn2 = (en[0] + en[1]) + (en[2] + en[3]);
          const float ip = __builtin_amdgcn_rcpf(rsum4(sp2[0] + sp2[1])), in_ = __builtin_amdgcn_rcpf(rsum4(sn2[0] + sn2[1]));
          u32x2 o[4];
          o[0] = (u32x2){pk2v(ep[0] * ip), pk2v(ep[1] * ip)};
          o[1] = (u32x2){pk2v(ep[2] * ip), pk2v(ep[3] * ip)};
          o[2] = (u32x2){pk2v(en[0] * in_), pk2v(en[1] * in_)};
          o[3] = (u32x2){pk2v(en[2] * in_), pk2v(en[3] * in_)};
          if (which == 0) {
            u16* dst = out + ((size_t)(row0 + wm * 64 + mt * 16 + l15) * 32 + h) * 64 + 4 * g;
#pragma unroll
            for (int c = 0; c < 4; ++c) *reinterpret_cast<u32x2*>(dst + c * 16) = o[c];
          } else {
            char* dst = pkl + (mt * 16 + l15) * 144 + g * 8;
#pragma unroll
            for (int c = 0; c < 4; ++c) *reinterpret_cast<u32x2*>(dst + c * 32) = o[c];
          }
        }
      WAIT_L0();
      __builtin_amdgcn_sched_barrier(0);
#pragma unroll
      for (int ft = 0; ft < 4; ++ft) { kv[ft][0] = kv[ft][1] = zz[ft] = (f32x4){0.f, 0.f, 0.f, 0.f}; }
      const bf16x8 ones = (bf16x8){0x3F80, 0x3F80, 0x3F80, 0x3F80, 0x3F80, 0x3F80, 0x3F80, 0x3F80};
#pragma unroll
      for (int s = 0; s < 2; ++s)
#pragma unroll
        for (int ft = 0; ft < 4; ++ft) {
          const char* src = pkl + (32 * s + 4 * g + q) * 144 + ft * 32 + p * 8;
          s16x4 lo = __builtin_amdgcn_ds_read_tr16_b64_v4i16((lds_s16x4_p)(src));
          s16x4 hi = __builtin_amdgcn_ds_read_tr16_b64_v4i16((lds_s16x4_p)(src + 16 * 144));
          bf16x8 pf = __builtin_shufflevector(lo, hi, 0, 1, 2, 3, 4, 5, 6, 7);
          kv[ft][0] = __builtin_amdgcn_mfma_f32_16x16x32_bf16(vf[s][0], pf, kv[ft][0], 0, 0, 0);
          kv[ft][1] = __builtin_amdgcn_mfma_f32_16x16x32_bf16(vf[s][1], pf, kv[ft][1], 0, 0, 0);
          zz[ft] = __builtin_amdgcn_mfma_f32_16x16x32_bf16(ones, pf, zz[ft], 0, 0, 0);
        }
      if (wm == 1) {
#pragma unroll
        for (int ft = 0; ft < 4; ++ft) {
#pragma unroll
          for (int dt = 0; dt < 2; ++dt) *reinterpret_cast<f32x4*>(red + (ft * 16 + l15) * 32 + dt * 16 + 4 * g) = kv[ft][dt];
          if (g == 0) red[64 * 32 + ft * 16 + l15] = zz[ft][0];
        }
      }
    }
    __syncthreads();
    if (wid8 < 2) {
      float* kd = kvp + (size_t)(h * 16 + tm) * 2048;
      float* zd = zp + (size_t)(h * 16 + tm) * 64;
#pragma unroll
      for (int ft = 0; ft < 4; ++ft) {
#pragma unroll
        for (int dt = 0; dt < 2; ++dt) {
          f32x4 o = *reinterpret_cast<const f32x4*>(red + (ft * 16 + l15) * 32 + dt * 16 + 4 * g);
          *reinterpret_cast<f32x4*>(kd + (ft * 16 + l15) * 32 + dt * 16 + 4 * g) = o + kv[ft][dt];
        }
        if (g == 0) zd[ft * 16 + l15] = red[64 * 32 + ft * 16 + l15] + zz[ft][0];
      }
    }
  } else {
    if (wid8 < 4) {
      f32x4 stm[4], cs4[NTW];
      if constexpr (EPI == EPI_W1 || EPI == EPI_W3) {
#pragma unroll
        for (int j = 0; j < NTW; ++j) {
          const int colj = (j < 2 * NFULL) ? ((j >> 1) * 64 + (2 * wn + (j & 1)) * 16) : (NFULL * 64 + wn * 16);
          cs4[j] = *reinterpret_cast<const f32x4*>(smem + CS_OFF + (colj + 4 * g) * 4);
        }
#pragma unroll
        for (int mt = 0; mt < 4; ++mt) stm[mt] = rs[wm * 64 + mt * 16 + l15];
      }
#pragma unroll
      for (int j = 0; j < NTW; ++j) {
        const int colj = (j < 2 * NFULL) ? ((j >> 1) * 64 + (2 * wn + (j & 1)) * 16) : (NFULL * 64 + wn * 16);
#pragma unroll
        for (int mt = 0; mt < 4; ++mt) {
          f32x2 v0 = (f32x2){acc[mt][j][0], acc[mt][j][1]}, v1 = (f32x2){acc[mt][j][2], acc[mt][j][3]};
          if constexpr (EPI == EPI_W1 || EPI == EPI_W3) {
            v0 = gelu2((v0 - stm[mt][0] * (f32x2){cs4[j][0], cs4[j][1]}) * stm[mt][1]);
            v1 = gelu2((v1 - stm[mt][0] * (f32x2){cs4[j][2], cs4[j][3]}) * stm[mt][1]);
          }
          *reinterpret_cast<u32x2*>(smem + (wm * 64 + mt * 16 + l15) * PB + (colj + 4 * g) * 2) = (u32x2){pk2(v0[0], v0[1]), pk2(v1[0], v1[1])};
        }
      }
    }
    __syncthreads();
#pragma unroll
    for (int it = 0; it < NIT; ++it) {
      const int c = tid + 512 * it, r = c / CH, ch = c % CH;
      u32x4 v = *reinterpret_cast<const u32x4*>(smem + r * PB + ch * 16);
      const int grow = row0 + r, gcol = tn * BN + ch * 8;
      if constexpr (EPI == EPI_W3) {
        *reinterpret_cast<u32x4*>(out + (size_t)grow * F_ + gcol) = v;
      } else if constexpr (EPI == EPI_W1) {
        const int grp = tn >> 2;
        const int sh = grp == 0 ? 1 : grp == 1 ? -1 : grp == 2 ? 2 : -2;
        const int dr = grow + sh;
        if (dr >= 0 && dr < S_) *reinterpret_cast<u32x4*>(out + (size_t)dr * D_ + gcol) = v;
        const int zr = grow - sh;
        if (zr < 0 || zr >= S_) *reinterpret_cast<u32x4*>(out + (size_t)grow * D_ + gcol) = (u32x4){0u, 0u, 0u, 0u};
      } else {
        float y[8];
        const u32x4 a = ra[it];
        if constexpr (EPI == EPI_WO) {
#pragma unroll
          for (int i = 0; i < 4; ++i) { y[2 * i] = rbf(bflo(a[i]) + bflo(v[i])); y[2 * i + 1] = rbf(bfhi(a[i]) + bfhi(v[i])); }
        } else if constexpr (EPI == EPI_W2) {
          f32x4 st = rs[r];
#pragma unroll
          for (int i = 0; i < 4; ++i) {
            y[2 * i] = rbf(rbf((bflo(a[i]) - st[0]) * st[2]) + bflo(v[i]));
            y[2 * i + 1] = rbf(rbf((bfhi(a[i]) - st[0]) * st[2]) + bfhi(v[i]));
          }
        } else {
          const u32x4 b = rb[it];
#pragma unroll
          for (int i = 0; i < 4; ++i) {
            y[2 * i] = rbf(bflo(a[i]) + rbf(bflo(b[i]) + bflo(v[i])));
            y[2 * i + 1] = rbf(bfhi(a[i]) + rbf(bfhi(b[i]) + bfhi(v[i])));
          }
        }
        *reinterpret_cast<u32x4*>(out + (size_t)grow * D_ + gcol) = (u32x4){pk2(y[0], y[1]), pk2(y[2], y[3]), pk2(y[4], y[5]), pk2(y[6], y[7])};
        float sm = ((y[0] + y[1]) + (y[2] + y[3])) + ((y[4] + y[5]) + (y[6] + y[7]));
        sm = rsum8(sm);
        float mu = sm * (1.0f / 64.f), m2 = 0.f;
#pragma unroll
        for (int i = 0; i < 8; ++i) { float d = y[i] - mu; m2 += d * d; }
        m2 = rsum8(m2);
        if (ch == 0) st_out[(size_t)grow * 16 + tn] = (f32x2){sm, m2};
      }
    }
  }
  STAMP(3);
}

__global__ __launch_bounds__(256, 1) void attn_out_kernel(const u16* __restrict__ phiq, const float* __restrict__ kvp,
                                                          const float* __restrict__ zp, u16* __restrict__ o,
                                                          const float* __restrict__ cp1, const float* __restrict__ cp3,
                                                          float* __restrict__ cs1, float* __restrict__ cs3) {
  __shared__ __attribute__((aligned(16))) u16 kvT[48 * 72];
  const int tid = threadIdx.x, lane = tid & 63, wid = tid >> 6, g = lane >> 4, l15 = lane & 15;
  const int h = blockIdx.x & 31, tg = blockIdx.x >> 5;
  f32x4 kvv[16][2];
  float zv[16];
  bf16x8 pq[4][2];
  {
    const float* src = kvp + (size_t)h * 16 * 2048 + tid * 8;
#pragma unroll
    for (int t = 0; t < 16; ++t) {
      kvv[t][0] = *reinterpret_cast<const f32x4*>(src + t * 2048);
      kvv[t][1] = *reinterpret_cast<const f32x4*>(src + t * 2048 + 4);
    }
#pragma unroll
    for (int t = 0; t < 16; ++t) zv[t] = zp[(size_t)(h * 16 + t) * 64 + lane];
#pragma unroll
    for (int mt = 0; mt < 4; ++mt) {
      const u16* ps = phiq + ((size_t)(tg * 256 + wid * 64 + mt * 16 + l15) * 32 + h) * 64 + g * 8;
      pq[mt][0] = *reinterpret_cast<const bf16x8*>(ps);
      pq[mt][1] = *reinterpret_cast<const bf16x8*>(ps + 32);
    }
  }
  if (blockIdx.x < 18) {
    const int i = blockIdx.x * 256 + tid;
    const float* src = i < 1024 ? cp1 + i : cp3 + (i - 1024);
    const int ld = i < 1024 ? 1024 : F_;
    float c[16];
#pragma unroll
    for (int t = 0; t < 16; ++t) c[t] = src[t * ld];
#pragma unroll
    for (int t = 8; t > 0; t >>= 1)
#pragma unroll
      for (int j = 0; j < t; ++j) c[j] += c[j + t];
    *(i < 1024 ? cs1 + i : cs3 + (i - 1024)) = c[0];
  }
  __builtin_amdgcn_sched_barrier(0);
  {
    for (int i = tid; i < 15 * 64; i += 256) kvT[(33 + i / 64) * 72 + (i & 63)] = 0;
    f32x4 a0 = kvv[0][0], a1 = kvv[0][1];
#pragma unroll
    for (int t = 1; t < 16; ++t) { a0 += kvv[t][0]; a1 += kvv[t][1]; }
    const int f = tid >> 2, d0 = (tid & 3) * 8;
#pragma unroll
    for (int i = 0; i < 4; ++i) {
      kvT[(d0 + i) * 72 + f] = (u16)(pk2(a0[i], 0.f) & 0xffff);
      kvT[(d0 + 4 + i) * 72 + f] = (u16)(pk2(a1[i], 0.f) & 0xffff);
    }
    if (tid < 64) {
      float z = 0.f;
#pragma unroll
      for (int t = 0; t < 16; ++t) z += zv[t];
      kvT[32 * 72 + tid] = (u16)(pk2(z, 0.f) & 0xffff);
    }
  }
  __syncthreads();
  bf16x8 kf[3][2];
#pragma unroll
  for (int dt = 0; dt < 3; ++dt)
#pragma unroll
    for (int ks = 0; ks < 2; ++ks) kf[dt][ks] = *reinterpret_cast<const bf16x8*>(&kvT[(dt * 16 + l15) * 72 + ks * 32 + g * 8]);
#pragma unroll
  for (int mt = 0; mt < 4; ++mt) {
    const int tok = tg * 256 + wid * 64 + mt * 16 + l15;
    const bf16x8 p0 = pq[mt][0], p1 = pq[mt][1];
    f32x4 r[3];
#pragma unroll
    for (int dt = 0; dt < 3; ++dt) {
      r[dt] = (f32x4){0.f, 0.f, 0.f, 0.f};
      r[dt] = __builtin_amdgcn_mfma_f32_16x16x32_bf16(kf[dt][0], p0, r[dt], 0, 0, 0);
      r[dt] = __builtin_amdgcn_mfma_f32_16x16x32_bf16(kf[dt][1], p1, r[dt], 0, 0, 0);
    }
    float den = rbf(rbf(__shfl(r[2][0], l15)) + 1e-6f);
    float inv = 1.0f / den;
#pragma unroll
    for (int dt = 0; dt < 2; ++dt) {
      u32x2 w = (u32x2){pk2(rbf(r[dt][0]) * inv, rbf(r[dt][1]) * inv), pk2(rbf(r[dt][2]) * inv, rbf(r[dt][3]) * inv)};
      *reinterpret_cast<u32x2*>(o + (size_t)tok * D_ + h * 32 + dt * 16 + 4 * g) = w;
    }
  }
}

constexpr int WC_QKV = 0, WC_WO = WC_QKV + 2 * 1024 * 384, WC_W1 = WC_WO + 2 * 1024 * 128, WC_W2 = WC_W1 + 2 * 1024 * 128,
              WC_W4 = WC_W2 + 2 * 1024 * 128, WC_END = WC_W4 + 2 * F_ * 128;
__global__ __launch_bounds__(256) void prep_kernel(const u16* __restrict__ x, const u16* __restrict__ wqkv,
                                                   const u16* __restrict__ w1, const u16* __restrict__ w3,
                                                   float* __restrict__ cs_qkv, float* __restrict__ cs_w1,
                                                   float* __restrict__ cs_w3, f32x2* __restrict__ st,
                                                   const u16* __restrict__ wo, const u16* __restrict__ w2,
                                                   const u16* __restrict__ w4, char* __restrict__ wc) {
  __shared__ __attribute__((aligned(16))) float red[4][512];
  const int tid = threadIdx.x, b = blockIdx.x;
  if (b >= 368) {
    const int pb = b - 368;
    const u16* w = pb < 32 ? wo + (size_t)pb * 32768 : pb < 64 ? w2 + (size_t)(pb - 32) * 32768 : w4 + (size_t)(pb - 64) * 32768;
    u32x4 v[16];
#pragma unroll
    for (int r = 0; r < 16; ++r) v[r] = *reinterpret_cast<const u32x4*>(w + r * 2048 + tid * 8);
    if (((tid >> 3) & 7) == 3) {
      const int kd = pb < 64 ? 1024 : F_, row = (pb < 32 ? pb : pb < 64 ? pb - 32 : pb - 64) * 32 + (tid >> 7);
      char* dst = wc + (pb < 32 ? WC_WO : pb < 64 ? WC_W2 : WC_W4) + (size_t)((tid >> 6) & 1) * kd * 128 + (size_t)row * 128 + (tid & 7) * 16;
#pragma unroll
      for (int r = 0; r < 16; ++r) *reinterpret_cast<u32x4*>(dst + r * 256) = v[r];
    }
#pragma unroll
    for (int r = 0; r < 16; ++r) asm volatile("" ::"v"(v[r]));
  } else if (b < 240) {
    const u16* w; float* dst; int ld, ch, rg;
    if (b < 96) { w = wqkv; dst = cs_qkv; ld = 3072; ch = b % 6; rg = b / 6; }
    else if (b < 128) { w = w1; dst = cs_w1; ld = 1024; ch = (b - 96) & 1; rg = (b - 96) >> 1; }
    else { w = w3; dst = cs_w3; ld = F_; ch = (b - 128) % 7; rg = (b - 128) / 7; }
    const int lane = tid & 63, wid = tid >> 6, row = rg * 64 + wid * 16;
    u32x4 v[16];
#pragma unroll
    for (int r = 0; r < 16; ++r) v[r] = *reinterpret_cast<const u32x4*>(w + (size_t)(row + r) * ld + ch * 512 + lane * 8);
    if (b < 128 && (lane >> 3) == 3) {
      const int cpp = b < 96 ? 384 : 128;
      char* cpd = wc + (b < 96 ? WC_QKV + (ch & 1) * (1024 * 384) + (ch >> 1) * 128 : WC_W1 + ch * (1024 * 128)) + row * cpp + (lane & 7) * 16;
#pragma unroll
      for (int r = 0; r < 16; ++r) *reinterpret_cast<u32x4*>(cpd + r * cpp) = v[r];
    }
    f32x4 s0 = (f32x4){0.f, 0.f, 0.f, 0.f}, s1 = s0;
#pragma unroll
    for (int r = 0; r < 16; ++r) {
      s0 += (f32x4){bflo(v[r][0]), bfhi(v[r][0]), bflo(v[r][1]), bfhi(v[r][1])};
      s1 += (f32x4){bflo(v[r][2]), bfhi(v[r][2]), bflo(v[r][3]), bfhi(v[r][3])};
    }
    *reinterpret_cast<f32x4*>(&red[wid][lane * 8]) = s0;
    *reinterpret_cast<f32x4*>(&red[wid][lane * 8 + 4]) = s1;
    __syncthreads();
    f32x2 t = *reinterpret_cast<const f32x2*>(&red[0][tid * 2]);
#pragma unroll
    for (int i = 1; i < 4; ++i) t += *reinterpret_cast<const f32x2*>(&red[i][tid * 2]);
    *reinterpret_cast<f32x2*>(dst + rg * ld + ch * 512 + tid * 2) = t;
  } else {
    const int lane = tid & 63, wid = tid >> 6;
    const int rbase = (b - 240) * 16 + wid * 4;
    u32x4 xv[8];
#pragma unroll
    for (int i = 0; i < 8; ++i) xv[i] = *reinterpret_cast<const u32x4*>(x + (size_t)rbase * D_ + i * 512 + lane * 8);
    __builtin_amdgcn_sched_barrier(0);
#pragma unroll
    for (int rr = 0; rr < 4; ++rr) {
      const int row = rbase + rr;
#pragma unroll
      for (int hf = 0; hf < 2; ++hf) {
        const u32x4 v = xv[rr * 2 + hf];
        float y[8];
#pragma unroll
        for (int i = 0; i < 4; ++i) { y[2 * i] = bflo(v[i]); y[2 * i + 1] = bfhi(v[i]); }
        float sm = ((y[0] + y[1]) + (y[2] + y[3])) + ((y[4] + y[5]) + (y[6] + y[7]));
        sm = rsum8(sm);
        float mu = sm * (1.0f / 64.f), m2 = 0.f;
#pragma unroll
        for (int i = 0; i < 8; ++i) { float d = y[i] - mu; m2 += d * d; }
        m2 = rsum8(m2);
        if ((lane & 7) == 0) st[(size_t)row * 16 + hf * 8 + (lane >> 3)] = (f32x2){sm, m2};
      }
    }
  }
}

extern "C" void kernel_launch(void* const* d_in, const int* in_sizes, int n_in, void* d_out, int out_size, void* d_ws,
                              size_t ws_size, hipStream_t stream) {
  const u16* x_in = (const u16*)d_in[0];
  const u16* wqkv = (const u16*)d_in[1];
  const u16* wphi = (const u16*)d_in[2];
  const u16* wo = (const u16*)d_in[3];
  const u16* w1 = (const u16*)d_in[5];
  const u16* w2 = (const u16*)d_in[6];
  const u16* w3 = (const u16*)d_in[7];
  const u16* w4 = (const u16*)d_in[8];
  char* ws = (char*)d_ws;
  size_t off = 0;
  auto alloc = [&](size_t bytes) { char* p = ws + off; off += (bytes + 4095) & ~(size_t)4095; return p; };
  u16* xb = (u16*)alloc((size_t)S_ * D_ * 2);
  u16* x2 = (u16*)alloc((size_t)S_ * D_ * 2);
  u16* x1 = (u16*)alloc((size_t)S_ * D_ * 2);
  u16* hs = (u16*)alloc((size_t)S_ * D_ * 2);
  u16* ob = (u16*)alloc((size_t)S_ * D_ * 2);
  u16* gb = (u16*)alloc((size_t)S_ * F_ * 2);
  u16* phiq = (u16*)alloc((size_t)S_ * 2048 * 2);
  float* kvp = (float*)alloc((size_t)32 * 16 * 2048 * 4);
  float* zp = (float*)alloc((size_t)32 * 16 * 64 * 4);
  f32x2* st_x = (f32x2*)alloc((size_t)S_ * 16 * 8);
  f32x2* st_x2 = (f32x2*)alloc((size_t)S_ * 16 * 8);
  f32x2* st_x1 = (f32x2*)alloc((size_t)S_ * 16 * 8);
  float* cs_qkv = (float*)alloc(16 * 3072 * 4);
  float* cs_w1 = (float*)alloc(16 * 1024 * 4);
  float* cs_w3 = (float*)alloc(16 * F_ * 4);
  float* cf_w1 = (float*)alloc(1024 * 4);
  float* cf_w3 = (float*)alloc(F_ * 4);
  char* wc = alloc(WC_END);

  prep_kernel<<<368 + 176, 256, 0, stream>>>(x_in, wqkv, w1, w3, cs_qkv, cs_w1, cs_w3, st_x, wo, w2, w4, wc);
  for (int layer = 0; layer < 2; ++layer) {
    const u16* x = layer == 0 ? x_in : xb;
    u16* xo = layer == 0 ? xb : (u16*)d_out;
    gemm_kernel<192, 3, 1024, 1024, 3072, EPI_QKV><<<256, 512, 0, stream>>>(x, wqkv, (const u16*)(wc + WC_QKV), cs_qkv, st_x, nullptr, nullptr, phiq, nullptr, wphi, kvp, zp);
    attn_out_kernel<<<256, 256, 0, stream>>>(phiq, kvp, zp, ob, cs_w1, cs_w3, cf_w1, cf_w3);
    gemm_kernel<64, 5, 1024, 1024, 1024, EPI_WO><<<256, 512, 0, stream>>>(ob, wo, (const u16*)(wc + WC_WO), nullptr, nullptr, x, nullptr, x2, st_x2, nullptr, nullptr, nullptr);
    gemm_kernel<64, 5, 1024, 1024, 1024, EPI_W1><<<256, 512, 0, stream>>>(x2, w1, (const u16*)(wc + WC_W1), cf_w1, st_x2, nullptr, nullptr, hs, nullptr, nullptr, nullptr, nullptr);
    gemm_kernel<64, 5, 1024, 1024, 1024, EPI_W2><<<256, 512, 0, stream>>>(hs, w2, (const u16*)(wc + WC_W2), nullptr, st_x2, x2, nullptr, x1, st_x1, nullptr, nullptr, nullptr);
    gemm_kernel<224, 3, 1024, 1024, F_, EPI_W3><<<256, 512, 0, stream>>>(x1, w3, nullptr, cf_w3, st_x1, nullptr, nullptr, gb, nullptr, nullptr, nullptr, nullptr);
    gemm_kernel<64, 5, F_, F_, 1024, EPI_W4><<<256, 512, 0, stream>>>(gb, w4, (const u16*)(wc + WC_W4), nullptr, nullptr, x2, x1, xo, st_x, nullptr, nullptr, nullptr);
  }
}
```

```cpp
#include <hip/hip_runtime.h>
#include <hip/hip_bf16.h>
#include <stdint.h>
#include <type_traits>

typedef short bf16x8 __attribute__((ext_vector_type(8)));
typedef short s16x4 __attribute__((ext_vector_type(4)));
typedef float f32x4 __attribute__((ext_vector_type(4)));
typedef float f32x2 __attribute__((ext_vector_type(2)));
typedef unsigned u32x2 __attribute__((ext_vector_type(2)));
typedef unsigned u32x4 __attribute__((ext_vector_type(4)));
typedef __attribute__((address_space(3))) s16x4* lds_s16x4_p;
typedef unsigned short u16;

constexpr int S_ = 2048, D_ = 1024, H_ = 32, F_ = 3584;

__device__ __forceinline__ float bf2f(u16 v) { return __uint_as_float(((unsigned)v) << 16); }
__device__ __forceinline__ float bflo(unsigned v) { return __uint_as_float(v << 16); }
__device__ __forceinline__ float bfhi(unsigned v) { return __uint_as_float(v & 0xffff0000u); }
typedef __bf16 bf16x2_t __attribute__((ext_vector_type(2)));
__device__ __forceinline__ unsigned pk2(float a, float b) {
  return __builtin_bit_cast(unsigned, __builtin_convertvector((f32x2){a, b}, bf16x2_t));
}
__device__ __forceinline__ unsigned pk2v(f32x2 v) { return __builtin_bit_cast(unsigned, __builtin_convertvector(v, bf16x2_t)); }
__device__ __forceinline__ float max2_(float a, float b) { float r; asm("v_max_f32 %0, %1, %2" : "=v"(r) : "v"(a), "v"(b)); return r; }
__device__ __forceinline__ float min2_(float a, float b) { float r; asm("v_min_f32 %0, %1, %2" : "=v"(r) : "v"(a), "v"(b)); return r; }
__device__ __forceinline__ float max3_(float a, float b, float c) { float r; asm("v_max3_f32 %0, %1, %2, %3" : "=v"(r) : "v"(a), "v"(b), "v"(c)); return r; }
__device__ __forceinline__ float min3_(float a, float b, float c) { float r; asm("v_min3_f32 %0, %1, %2, %3" : "=v"(r) : "v"(a), "v"(b), "v"(c)); return r; }
__device__ __forceinline__ float rbf(float a) { return bflo(pk2(a, 0.f)); }
__device__ __forceinline__ float gelu_t(float x) {
  const float c1 = -2.0f * 0.7978845608028654f * 1.4426950408889634f;
  const float c2 = c1 * 0.044715f;
  float t = x * x;
  float w = x * __builtin_fmaf(t, c2, c1);
  float e = __builtin_amdgcn_exp2f(w);
  return x * __builtin_amdgcn_rcpf(1.0f + e);
}

__device__ __forceinline__ f32x2 gelu2(f32x2 x) {
  const float c1 = -2.0f * 0.7978845608028654f * 1.4426950408889634f;
  const float c2 = c1 * 0.044715f;
  f32x2 t = x * x;
  f32x2 w = x * (t * c2 + c1);
  f32x2 d = (f32x2){__builtin_amdgcn_exp2f(w[0]), __builtin_amdgcn_exp2f(w[1])} + 1.0f;
  return x * (f32x2){__builtin_amdgcn_rcpf(d[0]), __builtin_amdgcn_rcpf(d[1])};
}
__device__ __forceinline__ void x16(float v, float& a, float& b) {
  auto r = __builtin_amdgcn_permlane16_swap(__float_as_uint(v), __float_as_uint(v), false, false);
  a = __uint_as_float(r[0]); b = __uint_as_float(r[1]);
}
__device__ __forceinline__ void x32(float v, float& a, float& b) {
  auto r = __builtin_amdgcn_permlane32_swap(__float_as_uint(v), __float_as_uint(v), false, false);
  a = __uint_as_float(r[0]); b = __uint_as_float(r[1]);
}
__device__ __forceinline__ float rmax4(float v) { float a, b; x16(v, a, b); v = max2_(a, b); x32(v, a, b); return max2_(a, b); }
__device__ __forceinline__ float rmin4(float v) { float a, b; x16(v, a, b); v = min2_(a, b); x32(v, a, b); return min2_(a, b); }
__device__ __forceinline__ float rsum4(float v) { float a, b; x16(v, a, b); v = a + b; x32(v, a, b); return a + b; }

__device__ __forceinline__ float rsum8(float v) {
  v += __uint_as_float(__builtin_amdgcn_mov_dpp(__float_as_uint(v), 0xB1, 0xF, 0xF, true));
  v += __uint_as_float(__builtin_amdgcn_mov_dpp(__float_as_uint(v), 0x4E, 0xF, 0xF, true));
  v += __uint_as_float(__builtin_amdgcn_mov_dpp(__float_as_uint(v), 0x141, 0xF, 0xF, true));
  return v;
}
#define WAIT_V(n) asm volatile("s_waitcnt vmcnt(" #n ")" ::: "memory")
#define WAIT_L0() asm volatile("s_waitcnt lgkmcnt(0)" ::: "memory")
template <int N> __device__ __forceinline__ void wait_vm() {
  asm volatile("s_waitcnt vmcnt(%0)" ::"i"(N) : "memory");
}

__device__ __forceinline__ void glds16(unsigned voff, const void* sbase, unsigned lds) {
  unsigned keep;
  asm volatile("s_mov_b32 %0, m0\n\ts_mov_b32 m0, %3\n\ts_nop 0\n\tglobal_load_lds_dwordx4 %1, %2\n\ts_mov_b32 m0, %0"
               : "=&s"(keep) : "v"(voff), "s"(sbase), "s"(lds) : "memory");
}

__device__ __forceinline__ void mfma_acc(f32x4& c, const u32x4& a, const u32x4& b) {
  asm volatile("v_mfma_f32_16x16x32_bf16 %0, %1, %2, %0" : "+a"(c) : "v"(a), "v"(b));
}

#define STAMP(i)

#define XPOS 2
enum { EPI_QKV = 0, EPI_WO = 1, EPI_W1 = 2, EPI_W2 = 3, EPI_W3 = 4, EPI_W4 = 5 };

template <int BN, int NST, int KD, int LDA, int LDB, int EPI>
__global__ __launch_bounds__(512, 2) void gemm_kernel(
    const u16* __restrict__ A, const u16* __restrict__ W,
    const u16* __restrict__ Wc,
    const float* __restrict__ cs,
    const f32x2* __restrict__ st_in,
    const u16* __restrict__ r0,
    const u16* __restrict__ r1,
    u16* __restrict__ out,
    f32x2* __restrict__ st_out,
    const u16* __restrict__ wphi,
    float* __restrict__ kvp,
    float* __restrict__ zp)
{
  constexpr int NFULL = BN / 64, HALF = (BN % 64) / 32;
  constexpr int A_BYTES = 128 * 64 * 2, B_BYTES = BN * 64 * 2, STAGE = A_BYTES + B_BYTES;
  constexpr int NT = KD / 64, NTW = 2 * NFULL + HALF, LPS = 4 + 2 * NFULL + HALF;
  constexpr int PB = BN * 2 + 16;
  constexpr int EPI_BYTES = (EPI == EPI_QKV) ? (4 * 64 * 144 + 2 * (64 * 32 + 64) * 4) : 128 * PB;
  constexpr int MAINB = NST * STAGE;
  constexpr int RS_OFF = (MAINB > EPI_BYTES ? MAINB : EPI_BYTES);
  constexpr int CS_OFF = RS_OFF + 128 * 16;
  constexpr int WP_OFF = CS_OFF + 1024;
  constexpr int SMEM = WP_OFF + (EPI == EPI_QKV ? 4096 : 0);
  constexpr bool HAS_CS = (EPI == EPI_QKV || EPI == EPI_W1 || EPI == EPI_W3);
  constexpr bool HAS_ST = (EPI == EPI_QKV || EPI == EPI_W1 || EPI == EPI_W3 || EPI == EPI_W2);
  constexpr bool HAS_RES = (EPI == EPI_WO || EPI == EPI_W2 || EPI == EPI_W4);
  constexpr int LAG = (BN == 64) ? 1 : 0;
  constexpr int CH = BN / 8, NIT = CH / 4;
  static_assert(SMEM <= 163840, "LDS");
  __shared__ __attribute__((aligned(16))) char smem[SMEM];

  STAMP(0);
  const int tid = threadIdx.x, lane = tid & 63;
  const int wid8 = __builtin_amdgcn_readfirstlane(tid >> 6), wid = wid8 & 3, wm = wid >> 1, wn = wid & 1;
  const int g = lane >> 4, l15 = lane & 15;
  const int xcd = blockIdx.x & 7, idx = blockIdx.x >> 3;
  const int tm = (xcd >> 1) * 4 + (idx & 3), tn = (xcd & 1) * 8 + (idx >> 2);
  const int row0 = tm * 128;
  f32x4* rs = reinterpret_cast<f32x4*>(smem + RS_OFF);

  f32x4 acc[4][NTW];
  u32x4 ra[HAS_RES ? NIT : 1], rb[(EPI == EPI_W4) ? NIT : 1];

  unsigned rof[HAS_RES ? NIT : 1];
  if constexpr (HAS_RES) {
#pragma unroll
    for (int it = 0; it < NIT; ++it) {
      const int c = tid + 512 * it, r = c / CH, ch = c % CH;
      rof[it] = (unsigned)((row0 + r) * D_ + tn * BN + ch * 8) * 2u;
      asm volatile("" : "+v"(rof[it]));
    }
  }
  auto ldres = [&]() {
    if constexpr (HAS_RES) {
#pragma unroll
      for (int it = 0; it < NIT; ++it) {
        ra[it] = *reinterpret_cast<const u32x4*>(reinterpret_cast<const char*>(r0) + rof[it]);
        if constexpr (EPI == EPI_W4) rb[it] = *reinterpret_cast<const u32x4*>(reinterpret_cast<const char*>(r1) + rof[it]);
      }
    }
  };

  if (wid8 >= 4) {
    unsigned voA[4], voB[NFULL > 0 ? NFULL : 1][2], voH = 0;
    const bool pk_ = (HALF == 0) && ((tn & 7) == 3);
    const unsigned pitchB = pk_ ? NFULL * 128 : LDB * 2;
#pragma unroll
    for (int i = 0; i < 4; ++i) {
      int r = (wid * 4 + i) * 8 + (lane >> 3);
      int ch = (lane & 7) ^ ((r >> 1) & 7);
      voA[i] = (unsigned)(row0 + r) * (LDA * 2) + ch * 16;
    }
#pragma unroll
    for (int b = 0; b < NFULL; ++b) {
      int colb = (EPI == EPI_QKV) ? (b * 1024 + tn * 64) : (tn * BN + b * 64);
#pragma unroll
      for (int j = 0; j < 2; ++j) {
        int r = (wid * 2 + j) * 8 + (lane >> 3);
        int f = ((r >> 1) & 1) | (((r >> 3) & 1) << 1);
        int chp = lane & 7;
        int gch = (((chp >> 1) ^ f) << 1) | (chp & 1);
        voB[b][j] = (unsigned)r * pitchB + (pk_ ? b * 128 : colb * 2) + gch * 16;
      }
    }
    if constexpr (HALF) {
      int r = wid * 16 + (lane >> 2);
      int chp = lane & 3;
      int gch = (((chp >> 1) ^ ((r >> 3) & 1)) << 1) | (chp & 1);
      voH = (unsigned)r * (LDB * 2) + (tn * BN + NFULL * 64) * 2 + gch * 16;
    }
    const char* Ab = reinterpret_cast<const char*>(A);
    const char* Wb = pk_ ? reinterpret_cast<const char*>(Wc) + (size_t)(tn >> 3) * (KD * NFULL * 128) : reinterpret_cast<const char*>(W);
    const unsigned lbase = (unsigned)(uintptr_t)(__attribute__((address_space(3))) char*)smem;
    auto stage = [&](int kt, int buf) {
      const char* a = Ab + (size_t)kt * 128;
      const char* w = Wb + (size_t)(kt * (64 * pitchB));
      const unsigned s = lbase + buf * STAGE;
#pragma unroll
      for (int i = 0; i < 4; ++i) glds16(voA[i], a, s + (wid * 4 + i) * 1024);
#pragma unroll
      for (int b = 0; b < NFULL; ++b)
#pragma unroll
        for (int j = 0; j < 2; ++j) glds16(voB[b][j], w, s + A_BYTES + b * 8192 + (wid * 2 + j) * 1024);
      if constexpr (HALF) glds16(voH, w, s + A_BYTES + NFULL * 8192 + wid * 1024);
    };
#pragma unroll
    for (int s = 0; s < NST; ++s) stage(s, s);
    constexpr int XL = 0;
    constexpr int NCS = HAS_CS ? (EPI == EPI_QKV ? 16 : 1) : 0;
    constexpr int XE = NCS + (EPI == EPI_QKV ? 1 : 0) + (HAS_ST ? 4 : 0) + (HAS_RES ? NIT * (EPI == EPI_W4 ? 2 : 1) : 0);
    f32x4 stp[4];
    const int lt = tid - 256;
    u32x4 wpx;
    f32x4 csp[HAS_CS ? NCS : 1];
    constexpr int XP = XPOS;
    wait_vm<(NST - 1 - LAG) * LPS>();
    __builtin_amdgcn_s_barrier();
#pragma unroll
    for (int kt = 0; kt < XP; ++kt) {
      wait_vm<(NST - 2 - LAG) * LPS>();
      __builtin_amdgcn_s_barrier();
      stage(kt + NST, kt % NST);
    }
    asm volatile("" ::: "memory");
    if constexpr (HAS_CS) {
      if (lt < BN / 4) {
        const float* cp = cs + (EPI == EPI_QKV ? (lt >> 4) * 1024 + tn * 64 + (lt & 15) * 4 : tn * BN + lt * 4);
#pragma unroll
        for (int i = 0; i < NCS; ++i) csp[i] = *reinterpret_cast<const f32x4*>(cp + i * LDB);
      }
    }
    if constexpr (EPI == EPI_QKV) wpx = *reinterpret_cast<const u32x4*>(wphi + tn * 2048 + lt * 8);
    if constexpr (HAS_ST) {
      const f32x4* sp = reinterpret_cast<const f32x4*>(st_in + (size_t)(row0 + (lt >> 1)) * 16 + (lt & 1) * 8);
#pragma unroll
      for (int i = 0; i < 4; ++i) stp[i] = sp[i];
    }
    ldres();
    asm volatile("" ::: "memory");
    constexpr int NPEEL = XP + NST - 1 - LAG;
    auto head = [&](auto xe) {
      constexpr int X = decltype(xe)::value;
#pragma unroll
      for (int kt = XP; kt < NPEEL; ++kt) {
        wait_vm<(NST - 2 - LAG) * LPS + X>();
        __builtin_amdgcn_s_barrier();
        stage(kt + NST, kt % NST);
      }
    };
    if (HAS_CS && wid == 0) head(std::integral_constant<int, XE>{});
    else head(std::integral_constant<int, XE - NCS>{});
    int buf = NPEEL % NST;
    for (int kt = NPEEL; kt < NT - NST; ++kt) {
      wait_vm<(NST - 2 - LAG) * LPS>();
      __builtin_amdgcn_s_barrier();
      stage(kt + NST, buf);
      buf = buf + 1 == NST ? 0 : buf + 1;
    }
    asm volatile("" ::: "memory");
#pragma unroll
    for (int r = 0; r < NST - 1; ++r) {
      constexpr int NSL = NST - LAG;
      if (r == 0) wait_vm<(NSL - 2) * LPS + XL>();
      else if (r == 1) wait_vm<(NSL > 3 ? NSL - 3 : 0) * LPS + XL>();
      else if (r == 2) wait_vm<(NSL > 4 ? NSL - 4 : 0) * LPS + XL>();
      else if (r == 3) wait_vm<(NSL > 5 ? NSL - 5 : 0) * LPS + XL>();
      else wait_vm<XL>();
      __builtin_amdgcn_s_barrier();
    }
    if constexpr (HAS_ST) {
#pragma unroll
      for (int i = 0; i < 4; ++i) asm volatile("" : "+v"(stp[i]));
      float sm = 0.f;
#pragma unroll
      for (int i = 0; i < 4; ++i) sm += stp[i][0] + stp[i][2];
      sm += __shfl_xor(sm, 1);
      float mean = sm * (1.0f / 1024.f);
      float m2 = 0.f;
#pragma unroll
      for (int i = 0; i < 4; ++i) {
        float d0 = stp[i][0] * (1.0f / 64.f) - mean, d1 = stp[i][2] * (1.0f / 64.f) - mean;
        m2 += stp[i][1] + stp[i][3] + 64.f * (d0 * d0 + d1 * d1);
      }
      m2 += __shfl_xor(m2, 1);
      float var = m2 * (1.0f / 1024.f);
      float r1_ = 1.0f / sqrtf(var + 1e-5f);
      float sc = r1_;
      if constexpr (EPI == EPI_W1) sc = r1_ / sqrtf(var * r1_ * r1_ + 1e-5f);
      if ((lt & 1) == 0) rs[lt >> 1] = (f32x4){mean, sc, r1_, 0.f};
    }
    if constexpr (HAS_CS) {
      if (lt < BN / 4) {
#pragma unroll
        for (int i = 0; i < NCS; ++i) asm volatile("" : "+v"(csp[i]));
#pragma unroll
        for (int i = NCS / 2; i > 0; i >>= 1)
#pragma unroll
          for (int j = 0; j < i; ++j) csp[j] += csp[j + i];
        *reinterpret_cast<f32x4*>(smem + CS_OFF + lt * 16) = csp[0];
      }
    }
    if constexpr (EPI == EPI_QKV) *reinterpret_cast<u32x4*>(smem + WP_OFF + lt * 16) = wpx;
  } else {
    ldres();
    const int q = l15 >> 2, p = lane & 3;
    const int fB = (q >> 1) | ((g & 1) << 1);
    unsigned aoff[2], boff[2], hoff = 0;
#pragma unroll
    for (int ks = 0; ks < 2; ++ks) aoff[ks] = (wm * 64 + l15) * 128 + (((4 * ks + g) ^ (l15 >> 1)) * 16);
#pragma unroll
    for (int u = 0; u < 2; ++u) boff[u] = A_BYTES + (8 * g + q) * 128 + (((2 * wn + u) ^ fB) * 32) + p * 8;
    if constexpr (HALF) hoff = A_BYTES + NFULL * 8192 + (8 * g + q) * 64 + ((wn ^ (g & 1)) * 32) + p * 8;

    auto ldf = [&](int i, const char* s, int ks, u32x4(&af)[4], u32x4(&bfr)[NTW]) {
      if (i == 0 || i > NTW) {
        const int mt = i == 0 ? 0 : i - NTW;
        af[mt] = *reinterpret_cast<const u32x4*>(s + aoff[ks] + mt * 2048);
      } else if (i - 1 < 2 * NFULL) {
        const int b = (i - 1) >> 1, u = (i - 1) & 1;
        s16x4 lo = __builtin_amdgcn_ds_read_tr16_b64_v4i16((lds_s16x4_p)(s + boff[u] + b * 8192 + ks * 4096));
        s16x4 hi = __builtin_amdgcn_ds_read_tr16_b64_v4i16((lds_s16x4_p)(s + boff[u] + b * 8192 + ks * 4096 + 512));
        bfr[i - 1] = __builtin_shufflevector(__builtin_bit_cast(u32x2, lo), __builtin_bit_cast(u32x2, hi), 0, 1, 2, 3);
      } else {
        s16x4 lo = __builtin_amdgcn_ds_read_tr16_b64_v4i16((lds_s16x4_p)(s + hoff + ks * 2048));
        s16x4 hi = __builtin_amdgcn_ds_read_tr16_b64_v4i16((lds_s16x4_p)(s + hoff + ks * 2048 + 256));
        bfr[NTW - 1] = __builtin_shufflevector(__builtin_bit_cast(u32x2, lo), __builtin_bit_cast(u32x2, hi), 0, 1, 2, 3);
      }
    };
    constexpr int NM = 4 * NTW, NL = NTW + 4;
    auto phase = [&](u32x4(&afc)[4], u32x4(&bfc)[NTW], u32x4(&afn)[4], u32x4(&bfn)[NTW], int nbuf, int nks, auto rd, auto bar) {
      const char* s = smem + nbuf * STAGE;
      constexpr int BAR = decltype(bar)::value, R0 = BAR < 0 ? 0 : BAR + 1;
      constexpr int RSP = (NM - R0 - 2) / NL > 0 ? (NM - R0 - 2) / NL : 1;
      static_assert(R0 + (NL - 1) * RSP < NM, "reads");
#pragma unroll
      for (int mt = 0; mt < 4; ++mt)
#pragma unroll
        for (int j = 0; j < NTW; ++j) {
          const int m = mt * NTW + j;
          if (m == BAR) {
            WAIT_L0();
            __builtin_amdgcn_s_barrier();
            asm volatile("" ::: "memory");
          }
          if (decltype(rd)::value && m >= R0 && (m - R0) % RSP == 0 && (m - R0) / RSP < NL) ldf((m - R0) / RSP, s, nks, afn, bfn);
          if (EPI == EPI_QKV && j >= 4) mfma_acc(acc[mt][j], afc[mt], bfc[j]);
          else mfma_acc(acc[mt][j], bfc[j], afc[mt]);
          __builtin_amdgcn_sched_barrier(0);
        }
    };
    typedef std::integral_constant<int, -1> nobar;
#pragma unroll
    for (int mt = 0; mt < 4; ++mt)
#pragma unroll
      for (int j = 0; j < NTW; ++j) {
        acc[mt][j] = (f32x4){0.f, 0.f, 0.f, 0.f};
        asm volatile("" : "+a"(acc[mt][j]));
      }
    u32x4 af0[4], af1[4], bf0[NTW], bf1[NTW];
    __builtin_amdgcn_s_barrier();
    asm volatile("" ::: "memory");
    STAMP(1);
#pragma unroll
    for (int i = 0; i < NL; ++i) { ldf(i, smem, 0, af0, bf0); __builtin_amdgcn_sched_barrier(0); }
    int buf = 0;
    if constexpr (LAG) {
      phase(af0, bf0, af1, bf1, 0, 1, std::true_type{}, nobar{});
      for (int kt = 0; kt < NT - 1; ++kt) {
        buf = buf + 1 == NST ? 0 : buf + 1;
        phase(af1, bf1, af0, bf0, buf, 0, std::true_type{}, nobar{});
        asm volatile("" ::: "memory");
        __builtin_amdgcn_s_barrier();
        asm volatile("" ::: "memory");
        phase(af0, bf0, af1, bf1, buf, 1, std::true_type{}, nobar{});
      }
      phase(af1, bf1, af0, bf0, buf, 0, std::false_type{}, nobar{});
    } else {
      for (int kt = 0; kt < NT - 1; ++kt) {
        phase(af0, bf0, af1, bf1, buf, 1, std::true_type{}, nobar{});
        buf = buf + 1 == NST ? 0 : buf + 1;
        phase(af1, bf1, af0, bf0, buf, 0, std::true_type{}, std::integral_constant<int, 4>{});
      }
      phase(af0, bf0, af1, bf1, buf, 1, std::true_type{}, nobar{});
      phase(af1, bf1, af0, bf0, buf, 0, std::false_type{}, nobar{});
    }
    asm volatile("s_nop 7\n\ts_nop 7\n\ts_nop 7" ::: "memory");
  }
  WAIT_L0();
  __builtin_amdgcn_s_barrier();
  asm volatile("" ::: "memory");
  STAMP(2);

  if constexpr (EPI == EPI_QKV) {
    const int h = tn * 2 + wn;
    const int q = l15 >> 2, p = lane & 3;
    f32x4 kv[4][2], zz[4];
    float* red = reinterpret_cast<float*>(smem + 4 * 64 * 144) + wn * (64 * 32 + 64);
    if (wid8 < 4) {
      bf16x8 wf[2];
#pragma unroll
      for (int ft = 0; ft < 2; ++ft) {
        const char* src = smem + WP_OFF + wn * 2048 + (4 * g + q) * 64 + ft * 32 + p * 8;
        s16x4 lo = __builtin_amdgcn_ds_read_tr16_b64_v4i16((lds_s16x4_p)(src));
        s16x4 hi = __builtin_amdgcn_ds_read_tr16_b64_v4i16((lds_s16x4_p)(src + 16 * 64));
        wf[ft] = __builtin_shufflevector(lo, hi, 0, 1, 2, 3, 4, 5, 6, 7);
      }
      const float* csl = reinterpret_cast<const float*>(smem + CS_OFF);
      f32x4 cs4[4];
      float csv[2];
#pragma unroll
      for (int t = 0; t < 2; ++t) {
        cs4[t] = *reinterpret_cast<const f32x4*>(csl + wn * 32 + t * 16 + 4 * g);
        cs4[2 + t] = *reinterpret_cast<const f32x4*>(csl + 64 + wn * 32 + t * 16 + 4 * g);
        csv[t] = csl[128 + wn * 32 + t * 16 + l15];
      }
      bf16x8 qf[4], kf[4], vf[2][2];
#pragma unroll
      for (int mt = 0; mt < 4; ++mt) {
        f32x4 st = rs[wm * 64 + mt * 16 + l15];
        unsigned pq_[4], pk_[4];
#pragma unroll
        for (int t = 0; t < 2; ++t) {
          float a[4], b[4];
#pragma unroll
          for (int i = 0; i < 4; ++i) {
            a[i] = st[1] * (acc[mt][t][i] - st[0] * cs4[t][i]);
            b[i] = st[1] * (acc[mt][2 + t][i] - st[0] * cs4[2 + t][i]);
          }
          pq_[t * 2] = pk2(a[0], a[1]); pq_[t * 2 + 1] = pk2(a[2], a[3]);
          pk_[t * 2] = pk2(b[0], b[1]); pk_[t * 2 + 1] = pk2(b[2], b[3]);
        }
        qf[mt] = __builtin_bit_cast(bf16x8, (u32x4){pq_[0], pq_[1], pq_[2], pq_[3]});
        kf[mt] = __builtin_bit_cast(bf16x8, (u32x4){pk_[0], pk_[1], pk_[2], pk_[3]});
      }
#pragma unroll
      for (int s = 0; s < 2; ++s) {
        unsigned pv[2][4];
#pragma unroll
        for (int m2 = 0; m2 < 2; ++m2) {
          int mt = s * 2 + m2;
          f32x4 s0 = rs[wm * 64 + mt * 16 + 4 * g + 0], s1 = rs[wm * 64 + mt * 16 + 4 * g + 1];
          f32x4 s2 = rs[wm * 64 + mt * 16 + 4 * g + 2], s3 = rs[wm * 64 + mt * 16 + 4 * g + 3];
#pragma unroll
          for (int t = 0; t < 2; ++t) {
            float a0 = s0[1] * (acc[mt][4 + t][0] - s0[0] * csv[t]);
            float a1 = s1[1] * (acc[mt][4 + t][1] - s1[0] * csv[t]);
            float a2 = s2[1] * (acc[mt][4 + t][2] - s2[0] * csv[t]);
            float a3 = s3[1] * (acc[mt][4 + t][3] - s3[0] * csv[t]);
            pv[t][m2 * 2] = pk2(a0, a1); pv[t][m2 * 2 + 1] = pk2(a2, a3);
          }
        }
#pragma unroll
        for (int t = 0; t < 2; ++t) vf[s][t] = __builtin_bit_cast(bf16x8, (u32x4){pv[t][0], pv[t][1], pv[t][2], pv[t][3]});
      }
      char* pkl = smem + wid * (64 * 144);
      const float L2E = 1.4426950408889634f;
#pragma unroll
      for (int which = 0; which < 2; ++which)
#pragma unroll
        for (int mt = 0; mt < 4; ++mt) {
          f32x4 z4 = (f32x4){0.f, 0.f, 0.f, 0.f};
          f32x4 p0 = __builtin_amdgcn_mfma_f32_16x16x32_bf16(wf[0], which ? kf[mt] : qf[mt], z4, 0, 0, 0);
          f32x4 p1 = __builtin_amdgcn_mfma_f32_16x16x32_bf16(wf[1], which ? kf[mt] : qf[mt], z4, 0, 0, 0);
          f32x2 v[4];
          {
            const unsigned r0_ = pk2(p0[0], p0[1]), r1_ = pk2(p0[2], p0[3]), r2_ = pk2(p1[0], p1[1]), r3_ = pk2(p1[2], p1[3]);
            v[0] = (f32x2){bflo(r0_), bfhi(r0_)}; v[1] = (f32x2){bflo(r1_), bfhi(r1_)};
            v[2] = (f32x2){bflo(r2_), bfhi(r2_)}; v[3] = (f32x2){bflo(r3_), bfhi(r3_)};
          }
          float mx = max2_(max3_(max3_(v[0][0], v[0][1], v[1][0]), v[1][1], v[2][0]), max3_(v[2][1], v[3][0], v[3][1]));
          float mn = min2_(min3_(min3_(v[0][0], v[0][1], v[1][0]), v[1][1], v[2][0]), min3_(v[2][1], v[3][0], v[3][1]));
          mx = rmax4(mx); mn = rmin4(mn);
          const float mxl = -mx * L2E, mnl = mn * L2E;
          f32x2 ep[4], en[4];
#pragma unroll
          for (int i = 0; i < 4; ++i) {
            const f32x2 ap = v[i] * L2E + mxl, an = v[i] * (-L2E) + mnl;
            ep[i] = (f32x2){__builtin_amdgcn_exp2f(ap[0]), __builtin_amdgcn_exp2f(ap[1])};
            en[i] = (f32x2){__builtin_amdgcn_exp2f(an[0]), __builtin_amdgcn_exp2f(an[1])};
          }
          const f32x2 sp2 = (ep[0] + ep[1]) + (ep[2] + ep[3]), sn2 = (en[0] + en[1]) + (en[2] + en[3]);
          const float ip = __builtin_amdgcn_rcpf(rsum4(sp2[0] + sp2[1])), in_ = __builtin_amdgcn_rcpf(rsum4(sn2[0] + sn2[1]));
          u32x2 o[4];
          o[0] = (u32x2){pk2v(ep[0] * ip), pk2v(ep[1] * ip)};
          o[1] = (u32x2){pk2v(ep[2] * ip), pk2v(ep[3] * ip)};
          o[2] = (u32x2){pk2v(en[0] * in_), pk2v(en[1] * in_)};
          o[3] = (u32x2){pk2v(en[2] * in_), pk2v(en[3] * in_)};
          if (which == 0) {
            u16* dst = out + ((size_t)(row0 + wm * 64 + mt * 16 + l15) * 32 + h) * 64 + 4 * g;
#pragma unroll
            for (int c = 0; c < 4; ++c) *reinterpret_cast<u32x2*>(dst + c * 16) = o[c];
          } else {
            char* dst = pkl + (mt * 16 + l15) * 144 + g * 8;
#pragma unroll
            for (int c = 0; c < 4; ++c) *reinterpret_cast<u32x2*>(dst + c * 32) = o[c];
          }
        }
      WAIT_L0();
      __builtin_amdgcn_sched_barrier(0);
#pragma unroll
      for (int ft = 0; ft < 4; ++ft) { kv[ft][0] = kv[ft][1] = zz[ft] = (f32x4){0.f, 0.f, 0.f, 0.f}; }
      const bf16x8 ones = (bf16x8){0x3F80, 0x3F80, 0x3F80, 0x3F80, 0x3F80, 0x3F80, 0x3F80, 0x3F80};
#pragma unroll
      for (int s = 0; s < 2; ++s)
#pragma unroll
        for (int ft = 0; ft < 4; ++ft) {
          const char* src = pkl + (32 * s + 4 * g + q) * 144 + ft * 32 + p * 8;
          s16x4 lo = __builtin_amdgcn_ds_read_tr16_b64_v4i16((lds_s16x4_p)(src));
          s16x4 hi = __builtin_amdgcn_ds_read_tr16_b64_v4i16((lds_s16x4_p)(src + 16 * 144));
          bf16x8 pf = __builtin_shufflevector(lo, hi, 0, 1, 2, 3, 4, 5, 6, 7);
          kv[ft][0] = __builtin_amdgcn_mfma_f32_16x16x32_bf16(vf[s][0], pf, kv[ft][0], 0, 0, 0);
          kv[ft][1] = __builtin_amdgcn_mfma_f32_16x16x32_bf16(vf[s][1], pf, kv[ft][1], 0, 0, 0);
          zz[ft] = __builtin_amdgcn_mfma_f32_16x16x32_bf16(ones, pf, zz[ft], 0, 0, 0);
        }
      if (wm == 1) {
#pragma unroll
        for (int ft = 0; ft < 4; ++ft) {
#pragma unroll
          for (int dt = 0; dt < 2; ++dt) *reinterpret_cast<f32x4*>(red + (ft * 16 + l15) * 32 + dt * 16 + 4 * g) = kv[ft][dt];
          if (g == 0) red[64 * 32 + ft * 16 + l15] = zz[ft][0];
        }
      }
    }
    __syncthreads();
    if (wid8 < 2) {
      float* kd = kvp + (size_t)(h * 16 + tm) * 2048;
      float* zd = zp + (size_t)(h * 16 + tm) * 64;
#pragma unroll
      for (int ft = 0; ft < 4; ++ft) {
#pragma unroll
        for (int dt = 0; dt < 2; ++dt) {
          f32x4 o = *reinterpret_cast<const f32x4*>(red + (ft * 16 + l15) * 32 + dt * 16 + 4 * g);
          *reinterpret_cast<f32x4*>(kd + (ft * 16 + l15) * 32 + dt * 16 + 4 * g) = o + kv[ft][dt];
        }
        if (g == 0) zd[ft * 16 + l15] = red[64 * 32 + ft * 16 + l15] + zz[ft][0];
      }
    }
  } else {
    if (wid8 < 4) {
      f32x4 stm[4], cs4[NTW];
      if constexpr (EPI == EPI_W1 || EPI == EPI_W3) {
#pragma unroll
        for (int j = 0; j < NTW; ++j) {
          const int colj = (j < 2 * NFULL) ? ((j >> 1) * 64 + (2 * wn + (j & 1)) * 16) : (NFULL * 64 + wn * 16);
          cs4[j] = *reinterpret_cast<const f32x4*>(smem + CS_OFF + (colj + 4 * g) * 4);
        }
#pragma unroll
        for (int mt = 0; mt < 4; ++mt) stm[mt] = rs[wm * 64 + mt * 16 + l15];
      }
#pragma unroll
      for (int j = 0; j < NTW; ++j) {
        const int colj = (j < 2 * NFULL) ? ((j >> 1) * 64 + (2 * wn + (j & 1)) * 16) : (NFULL * 64 + wn * 16);
#pragma unroll
        for (int mt = 0; mt < 4; ++mt) {
          f32x2 v0 = (f32x2){acc[mt][j][0], acc[mt][j][1]}, v1 = (f32x2){acc[mt][j][2], acc[mt][j][3]};
          if constexpr (EPI == EPI_W1 || EPI == EPI_W3) {
            v0 = gelu2((v0 - stm[mt][0] * (f32x2){cs4[j][0], cs4[j][1]}) * stm[mt][1]);
            v1 = gelu2((v1 - stm[mt][0] * (f32x2){cs4[j][2], cs4[j][3]}) * stm[mt][1]);
          }
          *reinterpret_cast<u32x2*>(smem + (wm * 64 + mt * 16 + l15) * PB + (colj + 4 * g) * 2) = (u32x2){pk2(v0[0], v0[1]), pk2(v1[0], v1[1])};
        }
      }
    }
    __syncthreads();
#pragma unroll
    for (int it = 0; it < NIT; ++it) {
      const int c = tid + 512 * it, r = c / CH, ch = c % CH;
      u32x4 v = *reinterpret_cast<const u32x4*>(smem + r * PB + ch * 16);
      const int grow = row0 + r, gcol = tn * BN + ch * 8;
      if constexpr (EPI == EPI_W3) {
        *reinterpret_cast<u32x4*>(out + (size_t)grow * F_ + gcol) = v;
      } else if constexpr (EPI == EPI_W1) {
        const int grp = tn >> 2;
        const int sh = grp == 0 ? 1 : grp == 1 ? -1 : grp == 2 ? 2 : -2;
        const int dr = grow + sh;
        if (dr >= 0 && dr < S_) *reinterpret_cast<u32x4*>(out + (size_t)dr * D_ + gcol) = v;
        const int zr = grow - sh;
        if (zr < 0 || zr >= S_) *reinterpret_cast<u32x4*>(out + (size_t)grow * D_ + gcol) = (u32x4){0u, 0u, 0u, 0u};
      } else {
        float y[8];
        const u32x4 a = ra[it];
        if constexpr (EPI == EPI_WO) {
#pragma unroll
          for (int i = 0; i < 4; ++i) { y[2 * i] = rbf(bflo(a[i]) + bflo(v[i])); y[2 * i + 1] = rbf(bfhi(a[i]) + bfhi(v[i])); }
        } else if constexpr (EPI == EPI_W2) {
          f32x4 st = rs[r];
#pragma unroll
          for (int i = 0; i < 4; ++i) {
            y[2 * i] = rbf(rbf((bflo(a[i]) - st[0]) * st[2]) + bflo(v[i]));
            y[2 * i + 1] = rbf(rbf((bfhi(a[i]) - st[0]) * st[2]) + bfhi(v[i]));
          }
        } else {
          const u32x4 b = rb[it];
#pragma unroll
          for (int i = 0; i < 4; ++i) {
            y[2 * i] = rbf(bflo(a[i]) + rbf(bflo(b[i]) + bflo(v[i])));
            y[2 * i + 1] = rbf(bfhi(a[i]) + rbf(bfhi(b[i]) + bfhi(v[i])));
          }
        }
        *reinterpret_cast<u32x4*>(out + (size_t)grow * D_ + gcol) = (u32x4){pk2(y[0], y[1]), pk2(y[2], y[3]), pk2(y[4], y[5]), pk2(y[6], y[7])};
        float sm = ((y[0] + y[1]) + (y[2] + y[3])) + ((y[4] + y[5]) + (y[6] + y[7]));
        sm = rsum8(sm);
        float mu = sm * (1.0f / 64.f), m2 = 0.f;
#pragma unroll
        for (int i = 0; i < 8; ++i) { float d = y[i] - mu; m2 += d * d; }
        m2 = rsum8(m2);
        if (ch == 0) st_out[(size_t)grow * 16 + tn] = (f32x2){sm, m2};
      }
    }
  }
  STAMP(3);
}

__global__ __launch_bounds__(256, 1) void attn_out_kernel(const u16* __restrict__ phiq, const float* __restrict__ kvp,
                                                          const float* __restrict__ zp, u16* __restrict__ o,
                                                          const float* __restrict__ cp1, const float* __restrict__ cp3,
                                                          float* __restrict__ cs1, float* __restrict__ cs3) {
  __shared__ __attribute__((aligned(16))) u16 kvT[48 * 72];
  const int tid = threadIdx.x, lane = tid & 63, wid = tid >> 6, g = lane >> 4, l15 = lane & 15;
  if (blockIdx.x >= 256) {
    const int i = (blockIdx.x - 256) * 256 + tid;
    const float* src = i < 1024 ? cp1 + i : cp3 + (i - 1024);
    const int ld = i < 1024 ? 1024 : F_;
    float c[16];
#pragma unroll
    for (int t = 0; t < 16; ++t) c[t] = src[t * ld];
#pragma unroll
    for (int t = 8; t > 0; t >>= 1)
#pragma unroll
      for (int j = 0; j < t; ++j) c[j] += c[j + t];
    *(i < 1024 ? cs1 + i : cs3 + (i - 1024)) = c[0];
    return;
  }
  const int h = blockIdx.x & 31, tg = blockIdx.x >> 5;
  f32x4 kvv[16][2];
  float zv[16];
  bf16x8 pq[4][2];
  {
    const float* src = kvp + (size_t)h * 16 * 2048 + tid * 8;
#pragma unroll
    for (int t = 0; t < 16; ++t) {
      kvv[t][0] = *reinterpret_cast<const f32x4*>(src + t * 2048);
      kvv[t][1] = *reinterpret_cast<const f32x4*>(src + t * 2048 + 4);
    }
#pragma unroll
    for (int t = 0; t < 16; ++t) zv[t] = zp[(size_t)(h * 16 + t) * 64 + lane];
#pragma unroll
    for (int mt = 0; mt < 4; ++mt) {
      const u16* ps = phiq + ((size_t)(tg * 256 + wid * 64 + mt * 16 + l15) * 32 + h) * 64 + g * 8;
      pq[mt][0] = *reinterpret_cast<const bf16x8*>(ps);
      pq[mt][1] = *reinterpret_cast<const bf16x8*>(ps + 32);
    }
  }
  __builtin_amdgcn_sched_barrier(0);
  {
    for (int i = tid; i < 15 * 64; i += 256) kvT[(33 + i / 64) * 72 + (i & 63)] = 0;
    f32x4 a0 = kvv[0][0], a1 = kvv[0][1];
#pragma unroll
    for (int t = 1; t < 16; ++t) { a0 += kvv[t][0]; a1 += kvv[t][1]; }
    const int f = tid >> 2, d0 = (tid & 3) * 8;
#pragma unroll
    for (int i = 0; i < 4; ++i) {
      kvT[(d0 + i) * 72 + f] = (u16)(pk2(a0[i], 0.f) & 0xffff);
      kvT[(d0 + 4 + i) * 72 + f] = (u16)(pk2(a1[i], 0.f) & 0xffff);
    }
    if (tid < 64) {
      float z = 0.f;
#pragma unroll
      for (int t = 0; t < 16; ++t) z += zv[t];
      kvT[32 * 72 + tid] = (u16)(pk2(z, 0.f) & 0xffff);
    }
  }
  __syncthreads();
  bf16x8 kf[3][2];
#pragma unroll
  for (int dt = 0; dt < 3; ++dt)
#pragma unroll
    for (int ks = 0; ks < 2; ++ks) kf[dt][ks] = *reinterpret_cast<const bf16x8*>(&kvT[(dt * 16 + l15) * 72 + ks * 32 + g * 8]);
#pragma unroll
  for (int mt = 0; mt < 4; ++mt) {
    const int tok = tg * 256 + wid * 64 + mt * 16 + l15;
    const bf16x8 p0 = pq[mt][0], p1 = pq[mt][1];
    f32x4 r[3];
#pragma unroll
    for (int dt = 0; dt < 3; ++dt) {
      r[dt] = (f32x4){0.f, 0.f, 0.f, 0.f};
      r[dt] = __builtin_amdgcn_mfma_f32_16x16x32_bf16(kf[dt][0], p0, r[dt], 0, 0, 0);
      r[dt] = __builtin_amdgcn_mfma_f32_16x16x32_bf16(kf[dt][1], p1, r[dt], 0, 0, 0);
    }
    float den = rbf(rbf(__shfl(r[2][0], l15)) + 1e-6f);
    float inv = 1.0f / den;
#pragma unroll
    for (int dt = 0; dt < 2; ++dt) {
      u32x2 w = (u32x2){pk2(rbf(r[dt][0]) * inv, rbf(r[dt][1]) * inv), pk2(rbf(r[dt][2]) * inv, rbf(r[dt][3]) * inv)};
      *reinterpret_cast<u32x2*>(o + (size_t)tok * D_ + h * 32 + dt * 16 + 4 * g) = w;
    }
  }
}

constexpr int WC_QKV = 0, WC_WO = WC_QKV + 2 * 1024 * 384, WC_W1 = WC_WO + 2 * 1024 * 128, WC_W2 = WC_W1 + 2 * 1024 * 128,
              WC_W4 = WC_W2 + 2 * 1024 * 128, WC_END = WC_W4 + 2 * F_ * 128;
__global__ __launch_bounds__(256) void prep_kernel(const u16* __restrict__ x, const u16* __restrict__ wqkv,
                                                   const u16* __restrict__ w1, const u16* __restrict__ w3,
                                                   float* __restrict__ cs_qkv, float* __restrict__ cs_w1,
                                                   float* __restrict__ cs_w3, f32x2* __restrict__ st,
                                                   const u16* __restrict__ wo, const u16* __restrict__ w2,
                                                   const u16* __restrict__ w4, char* __restrict__ wc) {
  __shared__ __attribute__((aligned(16))) float red[4][512];
  const int tid = threadIdx.x, b = blockIdx.x;
  if (b >= 368) {
    const int pb = b - 368;
    const u16* w = pb < 32 ? wo + (size_t)pb * 32768 : pb < 64 ? w2 + (size_t)(pb - 32) * 32768 : w4 + (size_t)(pb - 64) * 32768;
    u32x4 v[16];
#pragma unroll
    for (int r = 0; r < 16; ++r) v[r] = *reinterpret_cast<const u32x4*>(w + r * 2048 + tid * 8);
    if (((tid >> 3) & 7) == 3) {
      const int kd = pb < 64 ? 1024 : F_, row = (pb < 32 ? pb : pb < 64 ? pb - 32 : pb - 64) * 32 + (tid >> 7);
      char* dst = wc + (pb < 32 ? WC_WO : pb < 64 ? WC_W2 : WC_W4) + (size_t)((tid >> 6) & 1) * kd * 128 + (size_t)row * 128 + (tid & 7) * 16;
#pragma unroll
      for (int r = 0; r < 16; ++r) *reinterpret_cast<u32x4*>(dst + r * 256) = v[r];
    }
#pragma unroll
    for (int r = 0; r < 16; ++r) asm volatile("" ::"v"(v[r]));
  } else if (b < 240) {
    const u16* w; float* dst; int ld, ch, rg;
    if (b < 96) { w = wqkv; dst = cs_qkv; ld = 3072; ch = b % 6; rg = b / 6; }
    else if (b < 128) { w = w1; dst = cs_w1; ld = 1024; ch = (b - 96) & 1; rg = (b - 96) >> 1; }
    else { w = w3; dst = cs_w3; ld = F_; ch = (b - 128) % 7; rg = (b - 128) / 7; }
    const int lane = tid & 63, wid = tid >> 6, row = rg * 64 + wid * 16;
    u32x4 v[16];
#pragma unroll
    for (int r = 0; r < 16; ++r) v[r] = *reinterpret_cast<const u32x4*>(w + (size_t)(row + r) * ld + ch * 512 + lane * 8);
    if (b < 128 && (lane >> 3) == 3) {
      const int cpp = b < 96 ? 384 : 128;
      char* cpd = wc + (b < 96 ? WC_QKV + (ch & 1) * (1024 * 384) + (ch >> 1) * 128 : WC_W1 + ch * (1024 * 128)) + row * cpp + (lane & 7) * 16;
#pragma unroll
      for (int r = 0; r < 16; ++r) *reinterpret_cast<u32x4*>(cpd + r * cpp) = v[r];
    }
    f32x4 s0 = (f32x4){0.f, 0.f, 0.f, 0.f}, s1 = s0;
#pragma unroll
    for (int r = 0; r < 16; ++r) {
      s0 += (f32x4){bflo(v[r][0]), bfhi(v[r][0]), bflo(v[r][1]), bfhi(v[r][1])};
      s1 += (f32x4){bflo(v[r][2]), bfhi(v[r][2]), bflo(v[r][3]), bfhi(v[r][3])};
    }
    *reinterpret_cast<f32x4*>(&red[wid][lane * 8]) = s0;
    *reinterpret_cast<f32x4*>(&red[wid][lane * 8 + 4]) = s1;
    __syncthreads();
    f32x2 t = *reinterpret_cast<const f32x2*>(&red[0][tid * 2]);
#pragma unroll
    for (int i = 1; i < 4; ++i) t += *reinterpret_cast<const f32x2*>(&red[i][tid * 2]);
    *reinterpret_cast<f32x2*>(dst + rg * ld + ch * 512 + tid * 2) = t;
  } else {
    const int lane = tid & 63, wid = tid >> 6;
    const int rbase = (b - 240) * 16 + wid * 4;
    u32x4 xv[8];
#pragma unroll
    for (int i = 0; i < 8; ++i) xv[i] = *reinterpret_cast<const u32x4*>(x + (size_t)rbase * D_ + i * 512 + lane * 8);
    __builtin_amdgcn_sched_barrier(0);
#pragma unroll
    for (int rr = 0; rr < 4; ++rr) {
      const int row = rbase + rr;
#pragma unroll
      for (int hf = 0; hf < 2; ++hf) {
        const u32x4 v = xv[rr * 2 + hf];
        float y[8];
#pragma unroll
        for (int i = 0; i < 4; ++i) { y[2 * i] = bflo(v[i]); y[2 * i + 1] = bfhi(v[i]); }
        float sm = ((y[0] + y[1]) + (y[2] + y[3])) + ((y[4] + y[5]) + (y[6] + y[7]));
        sm = rsum8(sm);
        float mu = sm * (1.0f / 64.f), m2 = 0.f;
#pragma unroll
        for (int i = 0; i < 8; ++i) { float d = y[i] - mu; m2 += d * d; }
        m2 = rsum8(m2);
        if ((lane & 7) == 0) st[(size_t)row * 16 + hf * 8 + (lane >> 3)] = (f32x2){sm, m2};
      }
    }
  }
}

extern "C" void kernel_launch(void* const* d_in, const int* in_sizes, int n_in, void* d_out, int out_size, void* d_ws,
                              size_t ws_size, hipStream_t stream) {
  const u16* x_in = (const u16*)d_in[0];
  const u16* wqkv = (const u16*)d_in[1];
  const u16* wphi = (const u16*)d_in[2];
  const u16* wo = (const u16*)d_in[3];
  const u16* w1 = (const u16*)d_in[5];
  const u16* w2 = (const u16*)d_in[6];
  const u16* w3 = (const u16*)d_in[7];
  const u16* w4 = (const u16*)d_in[8];
  char* ws = (char*)d_ws;
  size_t off = 0;
  auto alloc = [&](size_t bytes) { char* p = ws + off; off += (bytes + 4095) & ~(size_t)4095; return p; };
  u16* xb = (u16*)alloc((size_t)S_ * D_ * 2);
  u16* x2 = (u16*)alloc((size_t)S_ * D_ * 2);
  u16* x1 = (u16*)alloc((size_t)S_ * D_ * 2);
  u16* hs = (u16*)alloc((size_t)S_ * D_ * 2);
  u16* ob = (u16*)alloc((size_t)S_ * D_ * 2);
  u16* gb = (u16*)alloc((size_t)S_ * F_ * 2);
  u16* phiq = (u16*)alloc((size_t)S_ * 2048 * 2);
  float* kvp = (float*)alloc((size_t)32 * 16 * 2048 * 4);
  float* zp = (float*)alloc((size_t)32 * 16 * 64 * 4);
  f32x2* st_x = (f32x2*)alloc((size_t)S_ * 16 * 8);
  f32x2* st_x2 = (f32x2*)alloc((size_t)S_ * 16 * 8);
  f32x2* st_x1 = (f32x2*)alloc((size_t)S_ * 16 * 8);
  float* cs_qkv = (float*)alloc(16 * 3072 * 4);
  float* cs_w1 = (float*)alloc(16 * 1024 * 4);
  float* cs_w3 = (float*)alloc(16 * F_ * 4);
  float* cf_w1 = (float*)alloc(1024 * 4);
  float* cf_w3 = (float*)alloc(F_ * 4);
  char* wc = alloc(WC_END);

  prep_kernel<<<368 + 176, 256, 0, stream>>>(x_in, wqkv, w1, w3, cs_qkv, cs_w1, cs_w3, st_x, wo, w2, w4, wc);
  for (int layer = 0; layer < 2; ++layer) {
    const u16* x = layer == 0 ? x_in : xb;
    u16* xo = layer == 0 ? xb : (u16*)d_out;
    gemm_kernel<192, 3, 1024, 1024, 3072, EPI_QKV><<<256, 512, 0, stream>>>(x, wqkv, (const u16*)(wc + WC_QKV), cs_qkv, st_x, nullptr, nullptr, phiq, nullptr, wphi, kvp, zp);
    attn_out_kernel<<<layer == 0 ? 256 + 18 : 256, 256, 0, stream>>>(phiq, kvp, zp, ob, cs_w1, cs_w3, cf_w1, cf_w3);
    gemm_kernel<64, 5, 1024, 1024, 1024, EPI_WO><<<256, 512, 0, stream>>>(ob, wo, (const u16*)(wc + WC_WO), nullptr, nullptr, x, nullptr, x2, st_x2, nullptr, nullptr, nullptr);
    gemm_kernel<64, 5, 1024, 1024, 1024, EPI_W1><<<256, 512, 0, stream>>>(x2, w1, (const u16*)(wc + WC_W1), cf_w1, st_x2, nullptr, nullptr, hs, nullptr, nullptr, nullptr, nullptr);
    gemm_kernel<64, 5, 1024, 1024, 1024, EPI_W2><<<256, 512, 0, stream>>>(hs, w2, (const u16*)(wc + WC_W2), nullptr, st_x2, x2, nullptr, x1, st_x1, nullptr, nullptr, nullptr);
    gemm_kernel<224, 3, 1024, 1024, F_, EPI_W3><<<256, 512, 0, stream>>>(x1, w3, nullptr, cf_w3, st_x1, nullptr, nullptr, gb, nullptr, nullptr, nullptr, nullptr);
    gemm_kernel<64, 5, F_, F_, 1024, EPI_W4><<<256, 512, 0, stream>>>(gb, w4, (const u16*)(wc + WC_W4), nullptr, nullptr, x2, x1, xo, st_x, nullptr, nullptr, nullptr);
  }
}
```

```cpp
#include <hip/hip_runtime.h>
#include <hip/hip_bf16.h>
#include <stdint.h>
#include <type_traits>

typedef short bf16x8 __attribute__((ext_vector_type(8)));
typedef short s16x4 __attribute__((ext_vector_type(4)));
typedef float f32x4 __attribute__((ext_vector_type(4)));
typedef float f32x2 __attribute__((ext_vector_type(2)));
typedef unsigned u32x2 __attribute__((ext_vector_type(2)));
typedef unsigned u32x4 __attribute__((ext_vector_type(4)));
typedef __attribute__((address_space(3))) s16x4* lds_s16x4_p;
typedef unsigned short u16;

constexpr int S_ = 2048, D_ = 1024, H_ = 32, F_ = 3584;

__device__ __forceinline__ float bf2f(u16 v) { return __uint_as_float(((unsigned)v) << 16); }
__device__ __forceinline__ float bflo(unsigned v) { return __uint_as_float(v << 16); }
__device__ __forceinline__ float bfhi(unsigned v) { return __uint_as_float(v & 0xffff0000u); }
typedef __bf16 bf16x2_t __attribute__((ext_vector_type(2)));
__device__ __forceinline__ unsigned pk2(float a, float b) {
  return __builtin_bit_cast(unsigned, __builtin_convertvector((f32x2){a, b}, bf16x2_t));
}
__device__ __forceinline__ unsigned pk2v(f32x2 v) { return __builtin_bit_cast(unsigned, __builtin_convertvector(v, bf16x2_t)); }
__device__ __forceinline__ float max2_(float a, float b) { float r; asm("v_max_f32 %0, %1, %2" : "=v"(r) : "v"(a), "v"(b)); return r; }
__device__ __forceinline__ float min2_(float a, float b) { float r; asm("v_min_f32 %0, %1, %2" : "=v"(r) : "v"(a), "v"(b)); return r; }
__device__ __forceinline__ float max3_(float a, float b, float c) { float r; asm("v_max3_f32 %0, %1, %2, %3" : "=v"(r) : "v"(a), "v"(b), "v"(c)); return r; }
__device__ __forceinline__ float min3_(float a, float b, float c) { float r; asm("v_min3_f32 %0, %1, %2, %3" : "=v"(r) : "v"(a), "v"(b), "v"(c)); return r; }
__device__ __forceinline__ float rbf(float a) { return bflo(pk2(a, 0.f)); }
__device__ __forceinline__ float gelu_t(float x) {
  const float c1 = -2.0f * 0.7978845608028654f * 1.4426950408889634f;
  const float c2 = c1 * 0.044715f;
  float t = x * x;
  float w = x * __builtin_fmaf(t, c2, c1);
  float e = __builtin_amdgcn_exp2f(w);
  return x * __builtin_amdgcn_rcpf(1.0f + e);
}

__device__ __forceinline__ f32x2 gelu2(f32x2 x) {
  const float c1 = -2.0f * 0.7978845608028654f * 1.4426950408889634f;
  const float c2 = c1 * 0.044715f;
  f32x2 t = x * x;
  f32x2 w = x * (t * c2 + c1);
  f32x2 d = (f32x2){__builtin_amdgcn_exp2f(w[0]), __builtin_amdgcn_exp2f(w[1])} + 1.0f;
  return x * (f32x2){__builtin_amdgcn_rcpf(d[0]), __builtin_amdgcn_rcpf(d[1])};
}
__device__ __forceinline__ void x16(float v, float& a, float& b) {
  auto r = __builtin_amdgcn_permlane16_swap(__float_as_uint(v), __float_as_uint(v), false, false);
  a = __uint_as_float(r[0]); b = __uint_as_float(r[1]);
}
__device__ __forceinline__ void x32(float v, float& a, float& b) {
  auto r = __builtin_amdgcn_permlane32_swap(__float_as_uint(v), __float_as_uint(v), false, false);
  a = __uint_as_float(r[0]); b = __uint_as_float(r[1]);
}
__device__ __forceinline__ float rmax4(float v) { float a, b; x16(v, a, b); v = max2_(a, b); x32(v, a, b); return max2_(a, b); }
__device__ __forceinline__ float rmin4(float v) { float a, b; x16(v, a, b); v = min2_(a, b); x32(v, a, b); return min2_(a, b); }
__device__ __forceinline__ float rsum4(float v) { float a, b; x16(v, a, b); v = a + b; x32(v, a, b); return a + b; }

__device__ __forceinline__ float rsum8(float v) {
  v += __uint_as_float(__builtin_amdgcn_mov_dpp(__float_as_uint(v), 0xB1, 0xF, 0xF, true));
  v += __uint_as_float(__builtin_amdgcn_mov_dpp(__float_as_uint(v), 0x4E, 0xF, 0xF, true));
  v += __uint_as_float(__builtin_amdgcn_mov_dpp(__float_as_uint(v), 0x141, 0xF, 0xF, true));
  return v;
}

__device__ __forceinline__ void wt_store(u32x4 v, u32x4* p) { asm volatile("global_store_dwordx4 %0, %1, off sc1\n\ts_nop 1" :: "v"(p), "v"(v) : "memory"); }
__device__ __forceinline__ void wt_store(f32x4 v, f32x4* p) { asm volatile("global_store_dwordx4 %0, %1, off sc1\n\ts_nop 1" :: "v"(p), "v"(v) : "memory"); }
__device__ __forceinline__ void wt_store(u32x2 v, u32x2* p) { asm volatile("global_store_dwordx2 %0, %1, off sc1\n\ts_nop 1" :: "v"(p), "v"(v) : "memory"); }
#define WAIT_V(n) asm volatile("s_waitcnt vmcnt(" #n ")" ::: "memory")
#define WAIT_L0() asm volatile("s_waitcnt lgkmcnt(0)" ::: "memory")
template <int N> __device__ __forceinline__ void wait_vm() {
  asm volatile("s_waitcnt vmcnt(%0)" ::"i"(N) : "memory");
}

__device__ __forceinline__ void glds16(unsigned voff, const void* sbase, unsigned lds) {
  unsigned keep;
  asm volatile("s_mov_b32 %0, m0\n\ts_mov_b32 m0, %3\n\ts_nop 0\n\tglobal_load_lds_dwordx4 %1, %2\n\ts_mov_b32 m0, %0"
               : "=&s"(keep) : "v"(voff), "s"(sbase), "s"(lds) : "memory");
}

__device__ __forceinline__ void mfma_acc(f32x4& c, const u32x4& a, const u32x4& b) {
  asm volatile("v_mfma_f32_16x16x32_bf16 %0, %1, %2, %0" : "+a"(c) : "v"(a), "v"(b));
}

#define STAMP(i)

#define XPOS 2
enum { EPI_QKV = 0, EPI_WO = 1, EPI_W1 = 2, EPI_W2 = 3, EPI_W3 = 4, EPI_W4 = 5 };

template <int BN, int NST, int KD, int LDA, int LDB, int EPI>
__global__ __launch_bounds__(512, 2) void gemm_kernel(
    const u16* __restrict__ A, const u16* __restrict__ W,
    const u16* __restrict__ Wc,
    const float* __restrict__ cs,
    const f32x2* __restrict__ st_in,
    const u16* __restrict__ r0,
    const u16* __restrict__ r1,
    u16* __restrict__ out,
    f32x2* __restrict__ st_out,
    const u16* __restrict__ wphi,
    float* __restrict__ kvp,
    float* __restrict__ zp)
{
  constexpr int NFULL = BN / 64, HALF = (BN % 64) / 32;
  constexpr int A_BYTES = 128 * 64 * 2, B_BYTES = BN * 64 * 2, STAGE = A_BYTES + B_BYTES;
  constexpr int NT = KD / 64, NTW = 2 * NFULL + HALF, LPS = 4 + 2 * NFULL + HALF;
  constexpr int PB = BN * 2 + 16;
  constexpr int EPI_BYTES = (EPI == EPI_QKV) ? (4 * 64 * 144 + 2 * (64 * 32 + 64) * 4) : 128 * PB;
  constexpr int MAINB = NST * STAGE;
  constexpr int RS_OFF = (MAINB > EPI_BYTES ? MAINB : EPI_BYTES);
  constexpr int CS_OFF = RS_OFF + 128 * 16;
  constexpr int WP_OFF = CS_OFF + 1024;
  constexpr int SMEM = WP_OFF + (EPI == EPI_QKV ? 4096 : 0);
  constexpr bool HAS_CS = (EPI == EPI_QKV || EPI == EPI_W1 || EPI == EPI_W3);
  constexpr bool HAS_ST = (EPI == EPI_QKV || EPI == EPI_W1 || EPI == EPI_W3 || EPI == EPI_W2);
  constexpr bool HAS_RES = (EPI == EPI_WO || EPI == EPI_W2 || EPI == EPI_W4);
  constexpr int LAG = (BN == 64) ? 1 : 0;
  constexpr int CH = BN / 8, NIT = CH / 4;
  static_assert(SMEM <= 163840, "LDS");
  __shared__ __attribute__((aligned(16))) char smem[SMEM];

  STAMP(0);
  const int tid = threadIdx.x, lane = tid & 63;
  const int wid8 = __builtin_amdgcn_readfirstlane(tid >> 6), wid = wid8 & 3, wm = wid >> 1, wn = wid & 1;
  const int g = lane >> 4, l15 = lane & 15;
  const int xcd = blockIdx.x & 7, idx = blockIdx.x >> 3;
  const int tm = (xcd >> 1) * 4 + (idx & 3), tn = (xcd & 1) * 8 + (idx >> 2);
  const int row0 = tm * 128;
  f32x4* rs = reinterpret_cast<f32x4*>(smem + RS_OFF);

  f32x4 acc[4][NTW];
  u32x4 ra[HAS_RES ? NIT : 1], rb[(EPI == EPI_W4) ? NIT : 1];

  unsigned rof[HAS_RES ? NIT : 1];
  if constexpr (HAS_RES) {
#pragma unroll
    for (int it = 0; it < NIT; ++it) {
      const int c = tid + 512 * it, r = c / CH, ch = c % CH;
      rof[it] = (unsigned)((row0 + r) * D_ + tn * BN + ch * 8) * 2u;
      asm volatile("" : "+v"(rof[it]));
    }
  }
  auto ldres = [&]() {
    if constexpr (HAS_RES) {
#pragma unroll
      for (int it = 0; it < NIT; ++it) {
        ra[it] = *reinterpret_cast<const u32x4*>(reinterpret_cast<const char*>(r0) + rof[it]);
        if constexpr (EPI == EPI_W4) rb[it] = *reinterpret_cast<const u32x4*>(reinterpret_cast<const char*>(r1) + rof[it]);
      }
    }
  };

  if (wid8 >= 4) {
    unsigned voA[4], voB[NFULL > 0 ? NFULL : 1][2], voH = 0;
    const bool pk_ = (HALF == 0) && ((tn & 7) == 3);
    const unsigned pitchB = pk_ ? NFULL * 128 : LDB * 2;
#pragma unroll
    for (int i = 0; i < 4; ++i) {
      int r = (wid * 4 + i) * 8 + (lane >> 3);
      int ch = (lane & 7) ^ ((r >> 1) & 7);
      voA[i] = (unsigned)(row0 + r) * (LDA * 2) + ch * 16;
    }
#pragma unroll
    for (int b = 0; b < NFULL; ++b) {
      int colb = (EPI == EPI_QKV) ? (b * 1024 + tn * 64) : (tn * BN + b * 64);
#pragma unroll
      for (int j = 0; j < 2; ++j) {
        int r = (wid * 2 + j) * 8 + (lane >> 3);
        int f = ((r >> 1) & 1) | (((r >> 3) & 1) << 1);
        int chp = lane & 7;
        int gch = (((chp >> 1) ^ f) << 1) | (chp & 1);
        voB[b][j] = (unsigned)r * pitchB + (pk_ ? b * 128 : colb * 2) + gch * 16;
      }
    }
    if constexpr (HALF) {
      int r = wid * 16 + (lane >> 2);
      int chp = lane & 3;
      int gch = (((chp >> 1) ^ ((r >> 3) & 1)) << 1) | (chp & 1);
      voH = (unsigned)r * (LDB * 2) + (tn * BN + NFULL * 64) * 2 + gch * 16;
    }
    const char* Ab = reinterpret_cast<const char*>(A);
    const char* Wb = pk_ ? reinterpret_cast<const char*>(Wc) + (size_t)(tn >> 3) * (KD * NFULL * 128) : reinterpret_cast<const char*>(W);
    const unsigned lbase = (unsigned)(uintptr_t)(__attribute__((address_space(3))) char*)smem;
    auto stage = [&](int kt, int buf) {
      const char* a = Ab + (size_t)kt * 128;
      const char* w = Wb + (size_t)(kt * (64 * pitchB));
      const unsigned s = lbase + buf * STAGE;
#pragma unroll
      for (int i = 0; i < 4; ++i) glds16(voA[i], a, s + (wid * 4 + i) * 1024);
#pragma unroll
      for (int b = 0; b < NFULL; ++b)
#pragma unroll
        for (int j = 0; j < 2; ++j) glds16(voB[b][j], w, s + A_BYTES + b * 8192 + (wid * 2 + j) * 1024);
      if constexpr (HALF) glds16(voH, w, s + A_BYTES + NFULL * 8192 + wid * 1024);
    };
#pragma unroll
    for (int s = 0; s < NST; ++s) stage(s, s);
    constexpr int XL = 0;
    constexpr int NCS = HAS_CS ? (EPI == EPI_QKV ? 16 : 1) : 0;
    constexpr int XE = NCS + (EPI == EPI_QKV ? 1 : 0) + (HAS_ST ? 4 : 0) + (HAS_RES ? NIT * (EPI == EPI_W4 ? 2 : 1) : 0);
    f32x4 stp[4];
    const int lt = tid - 256;
    u32x4 wpx;
    f32x4 csp[HAS_CS ? NCS : 1];
    constexpr int XP = XPOS;
    wait_vm<(NST - 1 - LAG) * LPS>();
    __builtin_amdgcn_s_barrier();
#pragma unroll
    for (int kt = 0; kt < XP; ++kt) {
      wait_vm<(NST - 2 - LAG) * LPS>();
      __builtin_amdgcn_s_barrier();
      stage(kt + NST, kt % NST);
    }
    asm volatile("" ::: "memory");
    if constexpr (HAS_CS) {
      if (lt < BN / 4) {
        const float* cp = cs + (EPI == EPI_QKV ? (lt >> 4) * 1024 + tn * 64 + (lt & 15) * 4 : tn * BN + lt * 4);
#pragma unroll
        for (int i = 0; i < NCS; ++i) csp[i] = *reinterpret_cast<const f32x4*>(cp + i * LDB);
      }
    }
    if constexpr (EPI == EPI_QKV) wpx = *reinterpret_cast<const u32x4*>(wphi + tn * 2048 + lt * 8);
    if constexpr (HAS_ST) {
      const f32x4* sp = reinterpret_cast<const f32x4*>(st_in + (size_t)(row0 + (lt >> 1)) * 16 + (lt & 1) * 8);
#pragma unroll
      for (int i = 0; i < 4; ++i) stp[i] = sp[i];
    }
    ldres();
    asm volatile("" ::: "memory");
    constexpr int NPEEL = XP + NST - 1 - LAG;
    auto head = [&](auto xe) {
      constexpr int X = decltype(xe)::value;
#pragma unroll
      for (int kt = XP; kt < NPEEL; ++kt) {
        wait_vm<(NST - 2 - LAG) * LPS + X>();
        __builtin_amdgcn_s_barrier();
        stage(kt + NST, kt % NST);
      }
    };
    if (HAS_CS && wid == 0) head(std::integral_constant<int, XE>{});
    else head(std::integral_constant<int, XE - NCS>{});
    int buf = NPEEL % NST;
    for (int kt = NPEEL; kt < NT - NST; ++kt) {
      wait_vm<(NST - 2 - LAG) * LPS>();
      __builtin_amdgcn_s_barrier();
      stage(kt + NST, buf);
      buf = buf + 1 == NST ? 0 : buf + 1;
    }
    asm volatile("" ::: "memory");
#pragma unroll
    for (int r = 0; r < NST - 1; ++r) {
      constexpr int NSL = NST - LAG;
      if (r == 0) wait_vm<(NSL - 2) * LPS + XL>();
      else if (r == 1) wait_vm<(NSL > 3 ? NSL - 3 : 0) * LPS + XL>();
      else if (r == 2) wait_vm<(NSL > 4 ? NSL - 4 : 0) * LPS + XL>();
      else if (r == 3) wait_vm<(NSL > 5 ? NSL - 5 : 0) * LPS + XL>();
      else wait_vm<XL>();
      __builtin_amdgcn_s_barrier();
    }
    if constexpr (HAS_ST) {
#pragma unroll
      for (int i = 0; i < 4; ++i) asm volatile("" : "+v"(stp[i]));
      float sm = 0.f;
#pragma unroll
      for (int i = 0; i < 4; ++i) sm += stp[i][0] + stp[i][2];
      sm += __shfl_xor(sm, 1);
      float mean = sm * (1.0f / 1024.f);
      float m2 = 0.f;
#pragma unroll
      for (int i = 0; i < 4; ++i) {
        float d0 = stp[i][0] * (1.0f / 64.f) - mean, d1 = stp[i][2] * (1.0f / 64.f) - mean;
        m2 += stp[i][1] + stp[i][3] + 64.f * (d0 * d0 + d1 * d1);
      }
      m2 += __shfl_xor(m2, 1);
      float var = m2 * (1.0f / 1024.f);
      float r1_ = 1.0f / sqrtf(var + 1e-5f);
      float sc = r1_;
      if constexpr (EPI == EPI_W1) sc = r1_ / sqrtf(var * r1_ * r1_ + 1e-5f);
      if ((lt & 1) == 0) rs[lt >> 1] = (f32x4){mean, sc, r1_, 0.f};
    }
    if constexpr (HAS_CS) {
      if (lt < BN / 4) {
#pragma unroll
        for (int i = 0; i < NCS; ++i) asm volatile("" : "+v"(csp[i]));
#pragma unroll
        for (int i = NCS / 2; i > 0; i >>= 1)
#pragma unroll
          for (int j = 0; j < i; ++j) csp[j] += csp[j + i];
        *reinterpret_cast<f32x4*>(smem + CS_OFF + lt * 16) = csp[0];
      }
    }
    if constexpr (EPI == EPI_QKV) *reinterpret_cast<u32x4*>(smem + WP_OFF + lt * 16) = wpx;
  } else {
    ldres();
    const int q = l15 >> 2, p = lane & 3;
    const int fB = (q >> 1) | ((g & 1) << 1);
    unsigned aoff[2], boff[2], hoff = 0;
#pragma unroll
    for (int ks = 0; ks < 2; ++ks) aoff[ks] = (wm * 64 + l15) * 128 + (((4 * ks + g) ^ (l15 >> 1)) * 16);
#pragma unroll
    for (int u = 0; u < 2; ++u) boff[u] = A_BYTES + (8 * g + q) * 128 + (((2 * wn + u) ^ fB) * 32) + p * 8;
    if constexpr (HALF) hoff = A_BYTES + NFULL * 8192 + (8 * g + q) * 64 + ((wn ^ (g & 1)) * 32) + p * 8;

    auto ldf = [&](int i, const char* s, int ks, u32x4(&af)[4], u32x4(&bfr)[NTW]) {
      if (i == 0 || i > NTW) {
        const int mt = i == 0 ? 0 : i - NTW;
        af[mt] = *reinterpret_cast<const u32x4*>(s + aoff[ks] + mt * 2048);
      } else if (i - 1 < 2 * NFULL) {
        const int b = (i - 1) >> 1, u = (i - 1) & 1;
        s16x4 lo = __builtin_amdgcn_ds_read_tr16_b64_v4i16((lds_s16x4_p)(s + boff[u] + b * 8192 + ks * 4096));
        s16x4 hi = __builtin_amdgcn_ds_read_tr16_b64_v4i16((lds_s16x4_p)(s + boff[u] + b * 8192 + ks * 4096 + 512));
        bfr[i - 1] = __builtin_shufflevector(__builtin_bit_cast(u32x2, lo), __builtin_bit_cast(u32x2, hi), 0, 1, 2, 3);
      } else {
        s16x4 lo = __builtin_amdgcn_ds_read_tr16_b64_v4i16((lds_s16x4_p)(s + hoff + ks * 2048));
        s16x4 hi = __builtin_amdgcn_ds_read_tr16_b64_v4i16((lds_s16x4_p)(s + hoff + ks * 2048 + 256));
        bfr[NTW - 1] = __builtin_shufflevector(__builtin_bit_cast(u32x2, lo), __builtin_bit_cast(u32x2, hi), 0, 1, 2, 3);
      }
    };
    constexpr int NM = 4 * NTW, NL = NTW + 4;
    auto phase = [&](u32x4(&afc)[4], u32x4(&bfc)[NTW], u32x4(&afn)[4], u32x4(&bfn)[NTW], int nbuf, int nks, auto rd, auto bar) {
      const char* s = smem + nbuf * STAGE;
      constexpr int BAR = decltype(bar)::value, R0 = BAR < 0 ? 0 : BAR + 1;
      constexpr int RSP = (NM - R0 - 2) / NL > 0 ? (NM - R0 - 2) / NL : 1;
      static_assert(R0 + (NL - 1) * RSP < NM, "reads");
#pragma unroll
      for (int mt = 0; mt < 4; ++mt)
#pragma unroll
        for (int j = 0; j < NTW; ++j) {
          const int m = mt * NTW + j;
          if (m == BAR) {
            WAIT_L0();
            __builtin_amdgcn_s_barrier();
            asm volatile("" ::: "memory");
          }
          if (decltype(rd)::value && m >= R0 && (m - R0) % RSP == 0 && (m - R0) / RSP < NL) ldf((m - R0) / RSP, s, nks, afn, bfn);
          if (EPI == EPI_QKV && j >= 4) mfma_acc(acc[mt][j], afc[mt], bfc[j]);
          else mfma_acc(acc[mt][j], bfc[j], afc[mt]);
          __builtin_amdgcn_sched_barrier(0);
        }
    };
    typedef std::integral_constant<int, -1> nobar;
#pragma unroll
    for (int mt = 0; mt < 4; ++mt)
#pragma unroll
      for (int j = 0; j < NTW; ++j) {
        acc[mt][j] = (f32x4){0.f, 0.f, 0.f, 0.f};
        asm volatile("" : "+a"(acc[mt][j]));
      }
    u32x4 af0[4], af1[4], bf0[NTW], bf1[NTW];
    __builtin_amdgcn_s_barrier();
    asm volatile("" ::: "memory");
    STAMP(1);
#pragma unroll
    for (int i = 0; i < NL; ++i) { ldf(i, smem, 0, af0, bf0); __builtin_amdgcn_sched_barrier(0); }
    int buf = 0;
    if constexpr (LAG) {
      phase(af0, bf0, af1, bf1, 0, 1, std::true_type{}, nobar{});
      for (int kt = 0; kt < NT - 1; ++kt) {
        buf = buf + 1 == NST ? 0 : buf + 1;
        phase(af1, bf1, af0, bf0, buf, 0, std::true_type{}, nobar{});
        asm volatile("" ::: "memory");
        __builtin_amdgcn_s_barrier();
        asm volatile("" ::: "memory");
        phase(af0, bf0, af1, bf1, buf, 1, std::true_type{}, nobar{});
      }
      phase(af1, bf1, af0, bf0, buf, 0, std::false_type{}, nobar{});
    } else {
      for (int kt = 0; kt < NT - 1; ++kt) {
        phase(af0, bf0, af1, bf1, buf, 1, std::true_type{}, nobar{});
        buf = buf + 1 == NST ? 0 : buf + 1;
        phase(af1, bf1, af0, bf0, buf, 0, std::true_type{}, std::integral_constant<int, 4>{});
      }
      phase(af0, bf0, af1, bf1, buf, 1, std::true_type{}, nobar{});
      phase(af1, bf1, af0, bf0, buf, 0, std::false_type{}, nobar{});
    }
    asm volatile("s_nop 7\n\ts_nop 7\n\ts_nop 7" ::: "memory");
  }
  WAIT_L0();
  __builtin_amdgcn_s_barrier();
  asm volatile("" ::: "memory");
  STAMP(2);

  if constexpr (EPI == EPI_QKV) {
    const int h = tn * 2 + wn;
    const int q = l15 >> 2, p = lane & 3;
    f32x4 kv[4][2], zz[4];
    float* red = reinterpret_cast<float*>(smem + 4 * 64 * 144) + wn * (64 * 32 + 64);
    if (wid8 < 4) {
      bf16x8 wf[2];
#pragma unroll
      for (int ft = 0; ft < 2; ++ft) {
        const char* src = smem + WP_OFF + wn * 2048 + (4 * g + q) * 64 + ft * 32 + p * 8;
        s16x4 lo = __builtin_amdgcn_ds_read_tr16_b64_v4i16((lds_s16x4_p)(src));
        s16x4 hi = __builtin_amdgcn_ds_read_tr16_b64_v4i16((lds_s16x4_p)(src + 16 * 64));
        wf[ft] = __builtin_shufflevector(lo, hi, 0, 1, 2, 3, 4, 5, 6, 7);
      }
      const float* csl = reinterpret_cast<const float*>(smem + CS_OFF);
      f32x4 cs4[4];
      float csv[2];
#pragma unroll
      for (int t = 0; t < 2; ++t) {
        cs4[t] = *reinterpret_cast<const f32x4*>(csl + wn * 32 + t * 16 + 4 * g);
        cs4[2 + t] = *reinterpret_cast<const f32x4*>(csl + 64 + wn * 32 + t * 16 + 4 * g);
        csv[t] = csl[128 + wn * 32 + t * 16 + l15];
      }
      bf16x8 qf[4], kf[4], vf[2][2];
#pragma unroll
      for (int mt = 0; mt < 4; ++mt) {
        f32x4 st = rs[wm * 64 + mt * 16 + l15];
        unsigned pq_[4], pk_[4];
#pragma unroll
        for (int t = 0; t < 2; ++t) {
          float a[4], b[4];
#pragma unroll
          for (int i = 0; i < 4; ++i) {
            a[i] = st[1] * (acc[mt][t][i] - st[0] * cs4[t][i]);
            b[i] = st[1] * (acc[mt][2 + t][i] - st[0] * cs4[2 + t][i]);
          }
          pq_[t * 2] = pk2(a[0], a[1]); pq_[t * 2 + 1] = pk2(a[2], a[3]);
          pk_[t * 2] = pk2(b[0], b[1]); pk_[t * 2 + 1] = pk2(b[2], b[3]);
        }
        qf[mt] = __builtin_bit_cast(bf16x8, (u32x4){pq_[0], pq_[1], pq_[2], pq_[3]});
        kf[mt] = __builtin_bit_cast(bf16x8, (u32x4){pk_[0], pk_[1], pk_[2], pk_[3]});
      }
#pragma unroll
      for (int s = 0; s < 2; ++s) {
        unsigned pv[2][4];
#pragma unroll
        for (int m2 = 0; m2 < 2; ++m2) {
          int mt = s * 2 + m2;
          f32x4 s0 = rs[wm * 64 + mt * 16 + 4 * g + 0], s1 = rs[wm * 64 + mt * 16 + 4 * g + 1];
          f32x4 s2 = rs[wm * 64 + mt * 16 + 4 * g + 2], s3 = rs[wm * 64 + mt * 16 + 4 * g + 3];
#pragma unroll
          for (int t = 0; t < 2; ++t) {
            float a0 = s0[1] * (acc[mt][4 + t][0] - s0[0] * csv[t]);
            float a1 = s1[1] * (acc[mt][4 + t][1] - s1[0] * csv[t]);
            float a2 = s2[1] * (acc[mt][4 + t][2] - s2[0] * csv[t]);
            float a3 = s3[1] * (acc[mt][4 + t][3] - s3[0] * csv[t]);
            pv[t][m2 * 2] = pk2(a0, a1); pv[t][m2 * 2 + 1] = pk2(a2, a3);
          }
        }
#pragma unroll
        for (int t = 0; t < 2; ++t) vf[s][t] = __builtin_bit_cast(bf16x8, (u32x4){pv[t][0], pv[t][1], pv[t][2], pv[t][3]});
      }
      char* pkl = smem + wid * (64 * 144);
      const float L2E = 1.4426950408889634f;
#pragma unroll
      for (int which = 0; which < 2; ++which)
#pragma unroll
        for (int mt = 0; mt < 4; ++mt) {
          f32x4 z4 = (f32x4){0.f, 0.f, 0.f, 0.f};
          f32x4 p0 = __builtin_amdgcn_mfma_f32_16x16x32_bf16(wf[0], which ? kf[mt] : qf[mt], z4, 0, 0, 0);
          f32x4 p1 = __builtin_amdgcn_mfma_f32_16x16x32_bf16(wf[1], which ? kf[mt] : qf[mt], z4, 0, 0, 0);
          f32x2 v[4];
          {
            const unsigned r0_ = pk2(p0[0], p0[1]), r1_ = pk2(p0[2], p0[3]), r2_ = pk2(p1[0], p1[1]), r3_ = pk2(p1[2], p1[3]);
            v[0] = (f32x2){bflo(r0_), bfhi(r0_)}; v[1] = (f32x2){bflo(r1_), bfhi(r1_)};
            v[2] = (f32x2){bflo(r2_), bfhi(r2_)}; v[3] = (f32x2){bflo(r3_), bfhi(r3_)};
          }
          float mx = max2_(max3_(max3_(v[0][0], v[0][1], v[1][0]), v[1][1], v[2][0]), max3_(v[2][1], v[3][0], v[3][1]));
          float mn = min2_(min3_(min3_(v[0][0], v[0][1], v[1][0]), v[1][1], v[2][0]), min3_(v[2][1], v[3][0], v[3][1]));
          mx = rmax4(mx); mn = rmin4(mn);
          const float mxl = -mx * L2E, mnl = mn * L2E;
          f32x2 ep[4], en[4];
#pragma unroll
          for (int i = 0; i < 4; ++i) {
            const f32x2 ap = v[i] * L2E + mxl, an = v[i] * (-L2E) + mnl;
            ep[i] = (f32x2){__builtin_amdgcn_exp2f(ap[0]), __builtin_amdgcn_exp2f(ap[1])};
            en[i] = (f32x2){__builtin_amdgcn_exp2f(an[0]), __builtin_amdgcn_exp2f(an[1])};
          }
          const f32x2 sp2 = (ep[0] + ep[1]) + (ep[2] + ep[3]), sn2 = (en[0] + en[1]) + (en[2] + en[3]);
          const float ip = __builtin_amdgcn_rcpf(rsum4(sp2[0] + sp2[1])), in_ = __builtin_amdgcn_rcpf(rsum4(sn2[0] + sn2[1]));
          u32x2 o[4];
          o[0] = (u32x2){pk2v(ep[0] * ip), pk2v(ep[1] * ip)};
          o[1] = (u32x2){pk2v(ep[2] * ip), pk2v(ep[3] * ip)};
          o[2] = (u32x2){pk2v(en[0] * in_), pk2v(en[1] * in_)};
          o[3] = (u32x2){pk2v(en[2] * in_), pk2v(en[3] * in_)};
          if (which == 0) {
            u16* dst = out + ((size_t)(row0 + wm * 64 + mt * 16 + l15) * 32 + h) * 64 + 4 * g;
#pragma unroll
            for (int c = 0; c < 4; ++c) wt_store(o[c], reinterpret_cast<u32x2*>(dst + c * 16));
          } else {
            char* dst = pkl + (mt * 16 + l15) * 144 + g * 8;
#pragma unroll
            for (int c = 0; c < 4; ++c) *reinterpret_cast<u32x2*>(dst + c * 32) = o[c];
          }
        }
      WAIT_L0();
      __builtin_amdgcn_sched_barrier(0);
#pragma unroll
      for (int ft = 0; ft < 4; ++ft) { kv[ft][0] = kv[ft][1] = zz[ft] = (f32x4){0.f, 0.f, 0.f, 0.f}; }
      const bf16x8 ones = (bf16x8){0x3F80, 0x3F80, 0x3F80, 0x3F80, 0x3F80, 0x3F80, 0x3F80, 0x3F80};
#pragma unroll
      for (int s = 0; s < 2; ++s)
#pragma unroll
        for (int ft = 0; ft < 4; ++ft) {
          const char* src = pkl + (32 * s + 4 * g + q) * 144 + ft * 32 + p * 8;
          s16x4 lo = __builtin_amdgcn_ds_read_tr16_b64_v4i16((lds_s16x4_p)(src));
          s16x4 hi = __builtin_amdgcn_ds_read_tr16_b64_v4i16((lds_s16x4_p)(src + 16 * 144));
          bf16x8 pf = __builtin_shufflevector(lo, hi, 0, 1, 2, 3, 4, 5, 6, 7);
          kv[ft][0] = __builtin_amdgcn_mfma_f32_16x16x32_bf16(vf[s][0], pf, kv[ft][0], 0, 0, 0);
          kv[ft][1] = __builtin_amdgcn_mfma_f32_16x16x32_bf16(vf[s][1], pf, kv[ft][1], 0, 0, 0);
          zz[ft] = __builtin_amdgcn_mfma_f32_16x16x32_bf16(ones, pf, zz[ft], 0, 0, 0);
        }
      if (wm == 1) {
#pragma unroll
        for (int ft = 0; ft < 4; ++ft) {
#pragma unroll
          for (int dt = 0; dt < 2; ++dt) *reinterpret_cast<f32x4*>(red + (ft * 16 + l15) * 32 + dt * 16 + 4 * g) = kv[ft][dt];
          if (g == 0) red[64 * 32 + ft * 16 + l15] = zz[ft][0];
        }
      }
    }
    __syncthreads();
    if (wid8 < 2) {
      float* kd = kvp + (size_t)(h * 16 + tm) * 2048;
      float* zd = zp + (size_t)(h * 16 + tm) * 64;
#pragma unroll
      for (int ft = 0; ft < 4; ++ft) {
#pragma unroll
        for (int dt = 0; dt < 2; ++dt) {
          f32x4 o = *reinterpret_cast<const f32x4*>(red + (ft * 16 + l15) * 32 + dt * 16 + 4 * g);
          wt_store(o + kv[ft][dt], reinterpret_cast<f32x4*>(kd + (ft * 16 + l15) * 32 + dt * 16 + 4 * g));
        }
        if (g == 0) zd[ft * 16 + l15] = red[64 * 32 + ft * 16 + l15] + zz[ft][0];
      }
    }
  } else {
    if (wid8 < 4) {
      f32x4 stm[4], cs4[NTW];
      if constexpr (EPI == EPI_W1 || EPI == EPI_W3) {
#pragma unroll
        for (int j = 0; j < NTW; ++j) {
          const int colj = (j < 2 * NFULL) ? ((j >> 1) * 64 + (2 * wn + (j & 1)) * 16) : (NFULL * 64 + wn * 16);
          cs4[j] = *reinterpret_cast<const f32x4*>(smem + CS_OFF + (colj + 4 * g) * 4);
        }
#pragma unroll
        for (int mt = 0; mt < 4; ++mt) stm[mt] = rs[wm * 64 + mt * 16 + l15];
      }
#pragma unroll
      for (int j = 0; j < NTW; ++j) {
        const int colj = (j < 2 * NFULL) ? ((j >> 1) * 64 + (2 * wn + (j & 1)) * 16) : (NFULL * 64 + wn * 16);
#pragma unroll
        for (int mt = 0; mt < 4; ++mt) {
          f32x2 v0 = (f32x2){acc[mt][j][0], acc[mt][j][1]}, v1 = (f32x2){acc[mt][j][2], acc[mt][j][3]};
          if constexpr (EPI == EPI_W1 || EPI == EPI_W3) {
            v0 = gelu2((v0 - stm[mt][0] * (f32x2){cs4[j][0], cs4[j][1]}) * stm[mt][1]);
            v1 = gelu2((v1 - stm[mt][0] * (f32x2){cs4[j][2], cs4[j][3]}) * stm[mt][1]);
          }
          *reinterpret_cast<u32x2*>(smem + (wm * 64 + mt * 16 + l15) * PB + (colj + 4 * g) * 2) = (u32x2){pk2(v0[0], v0[1]), pk2(v1[0], v1[1])};
        }
      }
    }
    __syncthreads();
#pragma unroll
    for (int it = 0; it < NIT; ++it) {
      const int c = tid + 512 * it, r = c / CH, ch = c % CH;
      u32x4 v = *reinterpret_cast<const u32x4*>(smem + r * PB + ch * 16);
      const int grow = row0 + r, gcol = tn * BN + ch * 8;
      if constexpr (EPI == EPI_W3) {
        wt_store(v, reinterpret_cast<u32x4*>(out + (size_t)grow * F_ + gcol));
      } else if constexpr (EPI == EPI_W1) {
        const int grp = tn >> 2;
        const int sh = grp == 0 ? 1 : grp == 1 ? -1 : grp == 2 ? 2 : -2;
        const int dr = grow + sh;
        if (dr >= 0 && dr < S_) wt_store(v, reinterpret_cast<u32x4*>(out + (size_t)dr * D_ + gcol));
        const int zr = grow - sh;
        if (zr < 0 || zr >= S_) *reinterpret_cast<u32x4*>(out + (size_t)grow * D_ + gcol) = (u32x4){0u, 0u, 0u, 0u};
      } else {
        float y[8];
        const u32x4 a = ra[it];
        if constexpr (EPI == EPI_WO) {
#pragma unroll
          for (int i = 0; i < 4; ++i) { y[2 * i] = rbf(bflo(a[i]) + bflo(v[i])); y[2 * i + 1] = rbf(bfhi(a[i]) + bfhi(v[i])); }
        } else if constexpr (EPI == EPI_W2) {
          f32x4 st = rs[r];
#pragma unroll
          for (int i = 0; i < 4; ++i) {
            y[2 * i] = rbf(rbf((bflo(a[i]) - st[0]) * st[2]) + bflo(v[i]));
            y[2 * i + 1] = rbf(rbf((bfhi(a[i]) - st[0]) * st[2]) + bfhi(v[i]));
          }
        } else {
          const u32x4 b = rb[it];
#pragma unroll
          for (int i = 0; i < 4; ++i) {
            y[2 * i] = rbf(bflo(a[i]) + rbf(bflo(b[i]) + bflo(v[i])));
            y[2 * i + 1] = rbf(bfhi(a[i]) + rbf(bfhi(b[i]) + bfhi(v[i])));
          }
        }
        wt_store((u32x4){pk2(y[0], y[1]), pk2(y[2], y[3]), pk2(y[4], y[5]), pk2(y[6], y[7])}, reinterpret_cast<u32x4*>(out + (size_t)grow * D_ + gcol));
        float sm = ((y[0] + y[1]) + (y[2] + y[3])) + ((y[4] + y[5]) + (y[6] + y[7]));
        sm = rsum8(sm);
        float mu = sm * (1.0f / 64.f), m2 = 0.f;
#pragma unroll
        for (int i = 0; i < 8; ++i) { float d = y[i] - mu; m2 += d * d; }
        m2 = rsum8(m2);
        if (ch == 0) st_out[(size_t)grow * 16 + tn] = (f32x2){sm, m2};
      }
    }
  }
  STAMP(3);
}

__global__ __launch_bounds__(256, 1) void attn_out_kernel(const u16* __restrict__ phiq, const float* __restrict__ kvp,
                                                          const float* __restrict__ zp, u16* __restrict__ o,
                                                          const float* __restrict__ cp1, const float* __restrict__ cp3,
                                                          float* __restrict__ cs1, float* __restrict__ cs3) {
  __shared__ __attribute__((aligned(16))) u16 kvT[48 * 72];
  const int tid = threadIdx.x, lane = tid & 63, wid = tid >> 6, g = lane >> 4, l15 = lane & 15;
  if (blockIdx.x >= 256) {
    const int i = (blockIdx.x - 256) * 256 + tid;
    const float* src = i < 1024 ? cp1 + i : cp3 + (i - 1024);
    const int ld = i < 1024 ? 1024 : F_;
    float c[16];
#pragma unroll
    for (int t = 0; t < 16; ++t) c[t] = src[t * ld];
#pragma unroll
    for (int t = 8; t > 0; t >>= 1)
#pragma unroll
      for (int j = 0; j < t; ++j) c[j] += c[j + t];
    *(i < 1024 ? cs1 + i : cs3 + (i - 1024)) = c[0];
    return;
  }
  const int h = blockIdx.x & 31, tg = blockIdx.x >> 5;
  f32x4 kvv[16][2];
  float zv[16];
  bf16x8 pq[4][2];
  {
    const float* src = kvp + (size_t)h * 16 * 2048 + tid * 8;
#pragma unroll
    for (int t = 0; t < 16; ++t) {
      kvv[t][0] = *reinterpret_cast<const f32x4*>(src + t * 2048);
      kvv[t][1] = *reinterpret_cast<const f32x4*>(src + t * 2048 + 4);
    }
#pragma unroll
    for (int t = 0; t < 16; ++t) zv[t] = zp[(size_t)(h * 16 + t) * 64 + lane];
#pragma unroll
    for (int mt = 0; mt < 4; ++mt) {
      const u16* ps = phiq + ((size_t)(tg * 256 + wid * 64 + mt * 16 + l15) * 32 + h) * 64 + g * 8;
      pq[mt][0] = *reinterpret_cast<const bf16x8*>(ps);
      pq[mt][1] = *reinterpret_cast<const bf16x8*>(ps + 32);
    }
  }
  __builtin_amdgcn_sched_barrier(0);
  {
    for (int i = tid; i < 15 * 64; i += 256) kvT[(33 + i / 64) * 72 + (i & 63)] = 0;
    f32x4 a0 = kvv[0][0], a1 = kvv[0][1];
#pragma unroll
    for (int t = 1; t < 16; ++t) { a0 += kvv[t][0]; a1 += kvv[t][1]; }
    const int f = tid >> 2, d0 = (tid & 3) * 8;
#pragma unroll
    for (int i = 0; i < 4; ++i) {
      kvT[(d0 + i) * 72 + f] = (u16)(pk2(a0[i], 0.f) & 0xffff);
      kvT[(d0 + 4 + i) * 72 + f] = (u16)(pk2(a1[i], 0.f) & 0xffff);
    }
    if (tid < 64) {
      float z = 0.f;
#pragma unroll
      for (int t = 0; t < 16; ++t) z += zv[t];
      kvT[32 * 72 + tid] = (u16)(pk2(z, 0.f) & 0xffff);
    }
  }
  __syncthreads();
  bf16x8 kf[3][2];
#pragma unroll
  for (int dt = 0; dt < 3; ++dt)
#pragma unroll
    for (int ks = 0; ks < 2; ++ks) kf[dt][ks] = *reinterpret_cast<const bf16x8*>(&kvT[(dt * 16 + l15) * 72 + ks * 32 + g * 8]);
#pragma unroll
  for (int mt = 0; mt < 4; ++mt) {
    const int tok = tg * 256 + wid * 64 + mt * 16 + l15;
    const bf16x8 p0 = pq[mt][0], p1 = pq[mt][1];
    f32x4 r[3];
#pragma unroll
    for (int dt = 0; dt < 3; ++dt) {
      r[dt] = (f32x4){0.f, 0.f, 0.f, 0.f};
      r[dt] = __builtin_amdgcn_mfma_f32_16x16x32_bf16(kf[dt][0], p0, r[dt], 0, 0, 0);
      r[dt] = __builtin_amdgcn_mfma_f32_16x16x32_bf16(kf[dt][1], p1, r[dt], 0, 0, 0);
    }
    float den = rbf(rbf(__shfl(r[2][0], l15)) + 1e-6f);
    float inv = 1.0f / den;
#pragma unroll
    for (int dt = 0; dt < 2; ++dt) {
      u32x2 w = (u32x2){pk2(rbf(r[dt][0]) * inv, rbf(r[dt][1]) * inv), pk2(rbf(r[dt][2]) * inv, rbf(r[dt][3]) * inv)};
      *reinterpret_cast<u32x2*>(o + (size_t)tok * D_ + h * 32 + dt * 16 + 4 * g) = w;
    }
  }
}

constexpr int WC_QKV = 0, WC_WO = WC_QKV + 2 * 1024 * 384, WC_W1 = WC_WO + 2 * 1024 * 128, WC_W2 = WC_W1 + 2 * 1024 * 128,
              WC_W4 = WC_W2 + 2 * 1024 * 128, WC_END = WC_W4 + 2 * F_ * 128;
__global__ __launch_bounds__(256) void prep_kernel(const u16* __restrict__ x, const u16* __restrict__ wqkv,
                                                   const u16* __restrict__ w1, const u16* __restrict__ w3,
                                                   float* __restrict__ cs_qkv, float* __restrict__ cs_w1,
                                                   float* __restrict__ cs_w3, f32x2* __restrict__ st,
                                                   const u16* __restrict__ wo, const u16* __restrict__ w2,
                                                   const u16* __restrict__ w4, char* __restrict__ wc) {
  __shared__ __attribute__((aligned(16))) float red[4][512];
  const int tid = threadIdx.x, b = blockIdx.x;
  if (b >= 368) {
    const int pb = b - 368;
    const u16* w = pb < 32 ? wo + (size_t)pb * 32768 : pb < 64 ? w2 + (size_t)(pb - 32) * 32768 : w4 + (size_t)(pb - 64) * 32768;
    u32x4 v[16];
#pragma unroll
    for (int r = 0; r < 16; ++r) v[r] = *reinterpret_cast<const u32x4*>(w + r * 2048 + tid * 8);
    if (((tid >> 3) & 7) == 3) {
      const int kd = pb < 64 ? 1024 : F_, row = (pb < 32 ? pb : pb < 64 ? pb - 32 : pb - 64) * 32 + (tid >> 7);
      char* dst = wc + (pb < 32 ? WC_WO : pb < 64 ? WC_W2 : WC_W4) + (size_t)((tid >> 6) & 1) * kd * 128 + (size_t)row * 128 + (tid & 7) * 16;
#pragma unroll
      for (int r = 0; r < 16; ++r) *reinterpret_cast<u32x4*>(dst + r * 256) = v[r];
    }
#pragma unroll
    for (int r = 0; r < 16; ++r) asm volatile("" ::"v"(v[r]));
  } else if (b < 240) {
    const u16* w; float* dst; int ld, ch, rg;
    if (b < 96) { w = wqkv; dst = cs_qkv; ld = 3072; ch = b % 6; rg = b / 6; }
    else if (b < 128) { w = w1; dst = cs_w1; ld = 1024; ch = (b - 96) & 1; rg = (b - 96) >> 1; }
    else { w = w3; dst = cs_w3; ld = F_; ch = (b - 128) % 7; rg = (b - 128) / 7; }
    const int lane = tid & 63, wid = tid >> 6, row = rg * 64 + wid * 16;
    u32x4 v[16];
#pragma unroll
    for (int r = 0; r < 16; ++r) v[r] = *reinterpret_cast<const u32x4*>(w + (size_t)(row + r) * ld + ch * 512 + lane * 8);
    if (b < 128 && (lane >> 3) == 3) {
      const int cpp = b < 96 ? 384 : 128;
      char* cpd = wc + (b < 96 ? WC_QKV + (ch & 1) * (1024 * 384) + (ch >> 1) * 128 : WC_W1 + ch * (1024 * 128)) + row * cpp + (lane & 7) * 16;
#pragma unroll
      for (int r = 0; r < 16; ++r) *reinterpret_cast<u32x4*>(cpd + r * cpp) = v[r];
    }
    f32x4 s0 = (f32x4){0.f, 0.f, 0.f, 0.f}, s1 = s0;
#pragma unroll
    for (int r = 0; r < 16; ++r) {
      s0 += (f32x4){bflo(v[r][0]), bfhi(v[r][0]), bflo(v[r][1]), bfhi(v[r][1])};
      s1 += (f32x4){bflo(v[r][2]), bfhi(v[r][2]), bflo(v[r][3]), bfhi(v[r][3])};
    }
    *reinterpret_cast<f32x4*>(&red[wid][lane * 8]) = s0;
    *reinterpret_cast<f32x4*>(&red[wid][lane * 8 + 4]) = s1;
    __syncthreads();
    f32x2 t = *reinterpret_cast<const f32x2*>(&red[0][tid * 2]);
#pragma unroll
    for (int i = 1; i < 4; ++i) t += *reinterpret_cast<const f32x2*>(&red[i][tid * 2]);
    *reinterpret_cast<f32x2*>(dst + rg * ld + ch * 512 + tid * 2) = t;
  } else {
    const int lane = tid & 63, wid = tid >> 6;
    const int rbase = (b - 240) * 16 + wid * 4;
    u32x4 xv[8];
#pragma unroll
    for (int i = 0; i < 8; ++i) xv[i] = *reinterpret_cast<const u32x4*>(x + (size_t)rbase * D_ + i * 512 + lane * 8);
    __builtin_amdgcn_sched_barrier(0);
#pragma unroll
    for (int rr = 0; rr < 4; ++rr) {
      const int row = rbase + rr;
#pragma unroll
      for (int hf = 0; hf < 2; ++hf) {
        const u32x4 v = xv[rr * 2 + hf];
        float y[8];
#pragma unroll
        for (int i = 0; i < 4; ++i) { y[2 * i] = bflo(v[i]); y[2 * i + 1] = bfhi(v[i]); }
        float sm = ((y[0] + y[1]) + (y[2] + y[3])) + ((y[4] + y[5]) + (y[6] + y[7]));
        sm = rsum8(sm);
        float mu = sm * (1.0f / 64.f), m2 = 0.f;
#pragma unroll
        for (int i = 0; i < 8; ++i) { float d = y[i] - mu; m2 += d * d; }
        m2 = rsum8(m2);
        if ((lane & 7) == 0) st[(size_t)row * 16 + hf * 8 + (lane >> 3)] = (f32x2){sm, m2};
      }
    }
  }
}

extern "C" void kernel_launch(void* const* d_in, const int* in_sizes, int n_in, void* d_out, int out_size, void* d_ws,
                              size_t ws_size, hipStream_t stream) {
  const u16* x_in = (const u16*)d_in[0];
  const u16* wqkv = (const u16*)d_in[1];
  const u16* wphi = (const u16*)d_in[2];
  const u16* wo = (const u16*)d_in[3];
  const u16* w1 = (const u16*)d_in[5];
  const u16* w2 = (const u16*)d_in[6];
  const u16* w3 = (const u16*)d_in[7];
  const u16* w4 = (const u16*)d_in[8];
  char* ws = (char*)d_ws;
  size_t off = 0;
  auto alloc = [&](size_t bytes) { char* p = ws + off; off += (bytes + 4095) & ~(size_t)4095; return p; };
  u16* xb = (u16*)alloc((size_t)S_ * D_ * 2);
  u16* x2 = (u16*)alloc((size_t)S_ * D_ * 2);
  u16* x1 = (u16*)alloc((size_t)S_ * D_ * 2);
  u16* hs = (u16*)alloc((size_t)S_ * D_ * 2);
  u16* ob = (u16*)alloc((size_t)S_ * D_ * 2);
  u16* gb = (u16*)alloc((size_t)S_ * F_ * 2);
  u16* phiq = (u16*)alloc((size_t)S_ * 2048 * 2);
  float* kvp = (float*)alloc((size_t)32 * 16 * 2048 * 4);
  float* zp = (float*)alloc((size_t)32 * 16 * 64 * 4);
  f32x2* st_x = (f32x2*)alloc((size_t)S_ * 16 * 8);
  f32x2* st_x2 = (f32x2*)alloc((size_t)S_ * 16 * 8);
  f32x2* st_x1 = (f32x2*)alloc((size_t)S_ * 16 * 8);
  float* cs_qkv = (float*)alloc(16 * 3072 * 4);
  float* cs_w1 = (float*)alloc(16 * 1024 * 4);
  float* cs_w3 = (float*)alloc(16 * F_ * 4);
  float* cf_w1 = (float*)alloc(1024 * 4);
  float* cf_w3 = (float*)alloc(F_ * 4);
  char* wc = alloc(WC_END);

  prep_kernel<<<368 + 176, 256, 0, stream>>>(x_in, wqkv, w1, w3, cs_qkv, cs_w1, cs_w3, st_x, wo, w2, w4, wc);
  for (int layer = 0; layer < 2; ++layer) {
    const u16* x = layer == 0 ? x_in : xb;
    u16* xo = layer == 0 ? xb : (u16*)d_out;
    gemm_kernel<192, 3, 1024, 1024, 3072, EPI_QKV><<<256, 512, 0, stream>>>(x, wqkv, (const u16*)(wc + WC_QKV), cs_qkv, st_x, nullptr, nullptr, phiq, nullptr, wphi, kvp, zp);
    attn_out_kernel<<<layer == 0 ? 256 + 18 : 256, 256, 0, stream>>>(phiq, kvp, zp, ob, cs_w1, cs_w3, cf_w1, cf_w3);
    gemm_kernel<64, 5, 1024, 1024, 1024, EPI_WO><<<256, 512, 0, stream>>>(ob, wo, (const u16*)(wc + WC_WO), nullptr, nullptr, x, nullptr, x2, st_x2, nullptr, nullptr, nullptr);
    gemm_kernel<64, 5, 1024, 1024, 1024, EPI_W1><<<256, 512, 0, stream>>>(x2, w1, (const u16*)(wc + WC_W1), cf_w1, st_x2, nullptr, nullptr, hs, nullptr, nullptr, nullptr, nullptr);
    gemm_kernel<64, 5, 1024, 1024, 1024, EPI_W2><<<256, 512, 0, stream>>>(hs, w2, (const u16*)(wc + WC_W2), nullptr, st_x2, x2, nullptr, x1, st_x1, nullptr, nullptr, nullptr);
    gemm_kernel<224, 3, 1024, 1024, F_, EPI_W3><<<256, 512, 0, stream>>>(x1, w3, nullptr, cf_w3, st_x1, nullptr, nullptr, gb, nullptr, nullptr, nullptr, nullptr);
    gemm_kernel<64, 5, F_, F_, 1024, EPI_W4><<<256, 512, 0, stream>>>(gb, w4, (const u16*)(wc + WC_W4), nullptr, nullptr, x2, x1, xo, st_x, nullptr, nullptr, nullptr);
  }
}
```

```cpp
#include <hip/hip_runtime.h>
#include <hip/hip_bf16.h>
#include <stdint.h>
#include <type_traits>

typedef short bf16x8 __attribute__((ext_vector_type(8)));
typedef short s16x4 __attribute__((ext_vector_type(4)));
typedef float f32x4 __attribute__((ext_vector_type(4)));
typedef float f32x2 __attribute__((ext_vector_type(2)));
typedef unsigned u32x2 __attribute__((ext_vector_type(2)));
typedef unsigned u32x4 __attribute__((ext_vector_type(4)));
typedef __attribute__((address_space(3))) s16x4* lds_s16x4_p;
typedef unsigned short u16;

constexpr int S_ = 2048, D_ = 1024, H_ = 32, F_ = 3584;

__device__ __forceinline__ float bf2f(u16 v) { return __uint_as_float(((unsigned)v) << 16); }
__device__ __forceinline__ float bflo(unsigned v) { return __uint_as_float(v << 16); }
__device__ __forceinline__ float bfhi(unsigned v) { return __uint_as_float(v & 0xffff0000u); }
typedef __bf16 bf16x2_t __attribute__((ext_vector_type(2)));
__device__ __forceinline__ unsigned pk2(float a, float b) {
  return __builtin_bit_cast(unsigned, __builtin_convertvector((f32x2){a, b}, bf16x2_t));
}
__device__ __forceinline__ unsigned pk2v(f32x2 v) { return __builtin_bit_cast(unsigned, __builtin_convertvector(v, bf16x2_t)); }
__device__ __forceinline__ float max2_(float a, float b) { float r; asm("v_max_f32 %0, %1, %2" : "=v"(r) : "v"(a), "v"(b)); return r; }
__device__ __forceinline__ float min2_(float a, float b) { float r; asm("v_min_f32 %0, %1, %2" : "=v"(r) : "v"(a), "v"(b)); return r; }
__device__ __forceinline__ float max3_(float a, float b, float c) { float r; asm("v_max3_f32 %0, %1, %2, %3" : "=v"(r) : "v"(a), "v"(b), "v"(c)); return r; }
__device__ __forceinline__ float min3_(float a, float b, float c) { float r; asm("v_min3_f32 %0, %1, %2, %3" : "=v"(r) : "v"(a), "v"(b), "v"(c)); return r; }
__device__ __forceinline__ float rbf(float a) { return bflo(pk2(a, 0.f)); }
__device__ __forceinline__ float gelu_t(float x) {
  const float c1 = -2.0f * 0.7978845608028654f * 1.4426950408889634f;
  const float c2 = c1 * 0.044715f;
  float t = x * x;
  float w = x * __builtin_fmaf(t, c2, c1);
  float e = __builtin_amdgcn_exp2f(w);
  return x * __builtin_amdgcn_rcpf(1.0f + e);
}

__device__ __forceinline__ f32x2 gelu2(f32x2 x) {
  const float c1 = -2.0f * 0.7978845608028654f * 1.4426950408889634f;
  const float c2 = c1 * 0.044715f;
  f32x2 t = x * x;
  f32x2 w = x * (t * c2 + c1);
  f32x2 d = (f32x2){__builtin_amdgcn_exp2f(w[0]), __builtin_amdgcn_exp2f(w[1])} + 1.0f;
  return x * (f32x2){__builtin_amdgcn_rcpf(d[0]), __builtin_amdgcn_rcpf(d[1])};
}
__device__ __forceinline__ void x16(float v, float& a, float& b) {
  auto r = __builtin_amdgcn_permlane16_swap(__float_as_uint(v), __float_as_uint(v), false, false);
  a = __uint_as_float(r[0]); b = __uint_as_float(r[1]);
}
__device__ __forceinline__ void x32(float v, float& a, float& b) {
  auto r = __builtin_amdgcn_permlane32_swap(__float_as_uint(v), __float_as_uint(v), false, false);
  a = __uint_as_float(r[0]); b = __uint_as_float(r[1]);
}
__device__ __forceinline__ float rmax4(float v) { float a, b; x16(v, a, b); v = max2_(a, b); x32(v, a, b); return max2_(a, b); }
__device__ __forceinline__ float rmin4(float v) { float a, b; x16(v, a, b); v = min2_(a, b); x32(v, a, b); return min2_(a, b); }
__device__ __forceinline__ float rsum4(float v) { float a, b; x16(v, a, b); v = a + b; x32(v, a, b); return a + b; }

__device__ __forceinline__ float rsum8(float v) {
  v += __uint_as_float(__builtin_amdgcn_mov_dpp(__float_as_uint(v), 0xB1, 0xF, 0xF, true));
  v += __uint_as_float(__builtin_amdgcn_mov_dpp(__float_as_uint(v), 0x4E, 0xF, 0xF, true));
  v += __uint_as_float(__builtin_amdgcn_mov_dpp(__float_as_uint(v), 0x141, 0xF, 0xF, true));
  return v;
}

__device__ __forceinline__ void wt_store(u32x4 v, u32x4* p) { asm volatile("global_store_dwordx4 %0, %1, off sc1\n\ts_nop 1" :: "v"(p), "v"(v) : "memory"); }
__device__ __forceinline__ void wt_store(f32x4 v, f32x4* p) { asm volatile("global_store_dwordx4 %0, %1, off sc1\n\ts_nop 1" :: "v"(p), "v"(v) : "memory"); }
__device__ __forceinline__ void wt_store(u32x2 v, u32x2* p) { asm volatile("global_store_dwordx2 %0, %1, off sc1\n\ts_nop 1" :: "v"(p), "v"(v) : "memory"); }
#define WAIT_V(n) asm volatile("s_waitcnt vmcnt(" #n ")" ::: "memory")
#define WAIT_L0() asm volatile("s_waitcnt lgkmcnt(0)" ::: "memory")
template <int N> __device__ __forceinline__ void wait_vm() {
  asm volatile("s_waitcnt vmcnt(%0)" ::"i"(N) : "memory");
}

__device__ __forceinline__ void glds16(unsigned voff, const void* sbase, unsigned lds) {
  unsigned keep;
  asm volatile("s_mov_b32 %0, m0\n\ts_mov_b32 m0, %3\n\ts_nop 0\n\tglobal_load_lds_dwordx4 %1, %2\n\ts_mov_b32 m0, %0"
               : "=&s"(keep) : "v"(voff), "s"(sbase), "s"(lds) : "memory");
}

__device__ __forceinline__ void mfma_acc(f32x4& c, const u32x4& a, const u32x4& b) {
  asm volatile("v_mfma_f32_16x16x32_bf16 %0, %1, %2, %0" : "+a"(c) : "v"(a), "v"(b));
}

#define STAMP(i)

#define XPOS 2
enum { EPI_QKV = 0, EPI_WO = 1, EPI_W1 = 2, EPI_W2 = 3, EPI_W3 = 4, EPI_W4 = 5 };

template <int BN, int NST, int KD, int LDA, int LDB, int EPI>
__global__ __launch_bounds__(512, 2) void gemm_kernel(
    const u16* __restrict__ A, const u16* __restrict__ W,
    const u16* __restrict__ Wc,
    const float* __restrict__ cs,
    const f32x2* __restrict__ st_in,
    const u16* __restrict__ r0,
    const u16* __restrict__ r1,
    u16* __restrict__ out,
    f32x2* __restrict__ st_out,
    const u16* __restrict__ wphi,
    float* __restrict__ kvp,
    float* __restrict__ zp)
{
  constexpr int NFULL = BN / 64, HALF = (BN % 64) / 32;
  constexpr int A_BYTES = 128 * 64 * 2, B_BYTES = BN * 64 * 2, STAGE = A_BYTES + B_BYTES;
  constexpr int NT = KD / 64, NTW = 2 * NFULL + HALF, LPS = 4 + 2 * NFULL + HALF;
  constexpr int PB = BN * 2 + 16;
  constexpr int EPI_BYTES = (EPI == EPI_QKV) ? (4 * 64 * 144 + 2 * (64 * 32 + 64) * 4) : 128 * PB;
  constexpr int MAINB = NST * STAGE;
  constexpr int RS_OFF = (MAINB > EPI_BYTES ? MAINB : EPI_BYTES);
  constexpr int CS_OFF = RS_OFF + 128 * 16;
  constexpr int WP_OFF = CS_OFF + 1024;
  constexpr int QX_OFF = WP_OFF + 4096;
  constexpr int SMEM = WP_OFF + (EPI == EPI_QKV ? 4096 + 32768 : 0);
  constexpr bool HAS_CS = (EPI == EPI_QKV || EPI == EPI_W1 || EPI == EPI_W3);
  constexpr bool HAS_ST = (EPI == EPI_QKV || EPI == EPI_W1 || EPI == EPI_W3 || EPI == EPI_W2);
  constexpr bool HAS_RES = (EPI == EPI_WO || EPI == EPI_W2 || EPI == EPI_W4);
  constexpr int LAG = (BN == 64) ? 1 : 0;
  constexpr int CH = BN / 8, NIT = CH / 4;
  static_assert(SMEM <= 163840, "LDS");
  __shared__ __attribute__((aligned(16))) char smem[SMEM];

  STAMP(0);
  const int tid = threadIdx.x, lane = tid & 63;
  const int wid8 = __builtin_amdgcn_readfirstlane(tid >> 6), wid = wid8 & 3, wm = wid >> 1, wn = wid & 1;
  const int g = lane >> 4, l15 = lane & 15;
  const int xcd = blockIdx.x & 7, idx = blockIdx.x >> 3;
  const int tm = (xcd >> 1) * 4 + (idx & 3), tn = (xcd & 1) * 8 + (idx >> 2);
  const int row0 = tm * 128;
  f32x4* rs = reinterpret_cast<f32x4*>(smem + RS_OFF);

  f32x4 acc[4][NTW];
  u32x4 ra[HAS_RES ? NIT : 1], rb[(EPI == EPI_W4) ? NIT : 1];

  unsigned rof[HAS_RES ? NIT : 1];
  if constexpr (HAS_RES) {
#pragma unroll
    for (int it = 0; it < NIT; ++it) {
      const int c = tid + 512 * it, r = c / CH, ch = c % CH;
      rof[it] = (unsigned)((row0 + r) * D_ + tn * BN + ch * 8) * 2u;
      asm volatile("" : "+v"(rof[it]));
    }
  }
  auto ldres = [&]() {
    if constexpr (HAS_RES) {
#pragma unroll
      for (int it = 0; it < NIT; ++it) {
        ra[it] = *reinterpret_cast<const u32x4*>(reinterpret_cast<const char*>(r0) + rof[it]);
        if constexpr (EPI == EPI_W4) rb[it] = *reinterpret_cast<const u32x4*>(reinterpret_cast<const char*>(r1) + rof[it]);
      }
    }
  };

  if (wid8 >= 4) {
    unsigned voA[4], voB[NFULL > 0 ? NFULL : 1][2], voH = 0;
    const bool pk_ = (HALF == 0) && ((tn & 7) == 3);
    const unsigned pitchB = pk_ ? NFULL * 128 : LDB * 2;
#pragma unroll
    for (int i = 0; i < 4; ++i) {
      int r = (wid * 4 + i) * 8 + (lane >> 3);
      int ch = (lane & 7) ^ ((r >> 1) & 7);
      voA[i] = (unsigned)(row0 + r) * (LDA * 2) + ch * 16;
    }
#pragma unroll
    for (int b = 0; b < NFULL; ++b) {
      int colb = (EPI == EPI_QKV) ? (b * 1024 + tn * 64) : (tn * BN + b * 64);
#pragma unroll
      for (int j = 0; j < 2; ++j) {
        int r = (wid * 2 + j) * 8 + (lane >> 3);
        int f = ((r >> 1) & 1) | (((r >> 3) & 1) << 1);
        int chp = lane & 7;
        int gch = (((chp >> 1) ^ f) << 1) | (chp & 1);
        voB[b][j] = (unsigned)r * pitchB + (pk_ ? b * 128 : colb * 2) + gch * 16;
      }
    }
    if constexpr (HALF) {
      int r = wid * 16 + (lane >> 2);
      int chp = lane & 3;
      int gch = (((chp >> 1) ^ ((r >> 3) & 1)) << 1) | (chp & 1);
      voH = (unsigned)r * (LDB * 2) + (tn * BN + NFULL * 64) * 2 + gch * 16;
    }
    const char* Ab = reinterpret_cast<const char*>(A);
    const char* Wb = pk_ ? reinterpret_cast<const char*>(Wc) + (size_t)(tn >> 3) * (KD * NFULL * 128) : reinterpret_cast<const char*>(W);
    const unsigned lbase = (unsigned)(uintptr_t)(__attribute__((address_space(3))) char*)smem;
    auto stage = [&](int kt, int buf) {
      const char* a = Ab + (size_t)kt * 128;
      const char* w = Wb + (size_t)(kt * (64 * pitchB));
      const unsigned s = lbase + buf * STAGE;
#pragma unroll
      for (int i = 0; i < 4; ++i) glds16(voA[i], a, s + (wid * 4 + i) * 1024);
#pragma unroll
      for (int b = 0; b < NFULL; ++b)
#pragma unroll
        for (int j = 0; j < 2; ++j) glds16(voB[b][j], w, s + A_BYTES + b * 8192 + (wid * 2 + j) * 1024);
      if constexpr (HALF) glds16(voH, w, s + A_BYTES + NFULL * 8192 + wid * 1024);
    };
#pragma unroll
    for (int s = 0; s < NST; ++s) stage(s, s);
    constexpr int XL = 0;
    constexpr int NCS = HAS_CS ? (EPI == EPI_QKV ? 16 : 1) : 0;
    constexpr int XE = NCS + (EPI == EPI_QKV ? 1 : 0) + (HAS_ST ? 4 : 0) + (HAS_RES ? NIT * (EPI == EPI_W4 ? 2 : 1) : 0);
    f32x4 stp[4];
    const int lt = tid - 256;
    u32x4 wpx;
    f32x4 csp[HAS_CS ? NCS : 1];
    constexpr int XP = XPOS;
    wait_vm<(NST - 1 - LAG) * LPS>();
    __builtin_amdgcn_s_barrier();
#pragma unroll
    for (int kt = 0; kt < XP; ++kt) {
      wait_vm<(NST - 2 - LAG) * LPS>();
      __builtin_amdgcn_s_barrier();
      stage(kt + NST, kt % NST);
    }
    asm volatile("" ::: "memory");
    if constexpr (HAS_CS) {
      if (lt < BN / 4) {
        const float* cp = cs + (EPI == EPI_QKV ? (lt >> 4) * 1024 + tn * 64 + (lt & 15) * 4 : tn * BN + lt * 4);
#pragma unroll
        for (int i = 0; i < NCS; ++i) csp[i] = *reinterpret_cast<const f32x4*>(cp + i * LDB);
      }
    }
    if constexpr (EPI == EPI_QKV) wpx = *reinterpret_cast<const u32x4*>(wphi + tn * 2048 + lt * 8);
    if constexpr (HAS_ST) {
      const f32x4* sp = reinterpret_cast<const f32x4*>(st_in + (size_t)(row0 + (lt >> 1)) * 16 + (lt & 1) * 8);
#pragma unroll
      for (int i = 0; i < 4; ++i) stp[i] = sp[i];
    }
    ldres();
    asm volatile("" ::: "memory");
    constexpr int NPEEL = XP + NST - 1 - LAG;
    auto head = [&](auto xe) {
      constexpr int X = decltype(xe)::value;
#pragma unroll
      for (int kt = XP; kt < NPEEL; ++kt) {
        wait_vm<(NST - 2 - LAG) * LPS + X>();
        __builtin_amdgcn_s_barrier();
        stage(kt + NST, kt % NST);
      }
    };
    if (HAS_CS && wid == 0) head(std::integral_constant<int, XE>{});
    else head(std::integral_constant<int, XE - NCS>{});
    int buf = NPEEL % NST;
    for (int kt = NPEEL; kt < NT - NST; ++kt) {
      wait_vm<(NST - 2 - LAG) * LPS>();
      __builtin_amdgcn_s_barrier();
      stage(kt + NST, buf);
      buf = buf + 1 == NST ? 0 : buf + 1;
    }
    asm volatile("" ::: "memory");
#pragma unroll
    for (int r = 0; r < NST - 1; ++r) {
      constexpr int NSL = NST - LAG;
      if (r == 0) wait_vm<(NSL - 2) * LPS + XL>();
      else if (r == 1) wait_vm<(NSL > 3 ? NSL - 3 : 0) * LPS + XL>();
      else if (r == 2) wait_vm<(NSL > 4 ? NSL - 4 : 0) * LPS + XL>();
      else if (r == 3) wait_vm<(NSL > 5 ? NSL - 5 : 0) * LPS + XL>();
      else wait_vm<XL>();
      __builtin_amdgcn_s_barrier();
    }
    if constexpr (HAS_ST) {
#pragma unroll
      for (int i = 0; i < 4; ++i) asm volatile("" : "+v"(stp[i]));
      float sm = 0.f;
#pragma unroll
      for (int i = 0; i < 4; ++i) sm += stp[i][0] + stp[i][2];
      sm += __shfl_xor(sm, 1);
      float mean = sm * (1.0f / 1024.f);
      float m2 = 0.f;
#pragma unroll
      for (int i = 0; i < 4; ++i) {
        float d0 = stp[i][0] * (1.0f / 64.f) - mean, d1 = stp[i][2] * (1.0f / 64.f) - mean;
        m2 += stp[i][1] + stp[i][3] + 64.f * (d0 * d0 + d1 * d1);
      }
      m2 += __shfl_xor(m2, 1);
      float var = m2 * (1.0f / 1024.f);
      float r1_ = 1.0f / sqrtf(var + 1e-5f);
      float sc = r1_;
      if constexpr (EPI == EPI_W1) sc = r1_ / sqrtf(var * r1_ * r1_ + 1e-5f);
      if ((lt & 1) == 0) rs[lt >> 1] = (f32x4){mean, sc, r1_, 0.f};
    }
    if constexpr (HAS_CS) {
      if (lt < BN / 4) {
#pragma unroll
        for (int i = 0; i < NCS; ++i) asm volatile("" : "+v"(csp[i]));
#pragma unroll
        for (int i = NCS / 2; i > 0; i >>= 1)
#pragma unroll
          for (int j = 0; j < i; ++j) csp[j] += csp[j + i];
        *reinterpret_cast<f32x4*>(smem + CS_OFF + lt * 16) = csp[0];
      }
    }
    if constexpr (EPI == EPI_QKV) *reinterpret_cast<u32x4*>(smem + WP_OFF + lt * 16) = wpx;
  } else {
    ldres();
    const int q = l15 >> 2, p = lane & 3;
    const int fB = (q >> 1) | ((g & 1) << 1);
    unsigned aoff[2], boff[2], hoff = 0;
#pragma unroll
    for (int ks = 0; ks < 2; ++ks) aoff[ks] = (wm * 64 + l15) * 128 + (((4 * ks + g) ^ (l15 >> 1)) * 16);
#pragma unroll
    for (int u = 0; u < 2; ++u) boff[u] = A_BYTES + (8 * g + q) * 128 + (((2 * wn + u) ^ fB) * 32) + p * 8;
    if constexpr (HALF) hoff = A_BYTES + NFULL * 8192 + (8 * g + q) * 64 + ((wn ^ (g & 1)) * 32) + p * 8;

    auto ldf = [&](int i, const char* s, int ks, u32x4(&af)[4], u32x4(&bfr)[NTW]) {
      if (i == 0 || i > NTW) {
        const int mt = i == 0 ? 0 : i - NTW;
        af[mt] = *reinterpret_cast<const u32x4*>(s + aoff[ks] + mt * 2048);
      } else if (i - 1 < 2 * NFULL) {
        const int b = (i - 1) >> 1, u = (i - 1) & 1;
        s16x4 lo = __builtin_amdgcn_ds_read_tr16_b64_v4i16((lds_s16x4_p)(s + boff[u] + b * 8192 + ks * 4096));
        s16x4 hi = __builtin_amdgcn_ds_read_tr16_b64_v4i16((lds_s16x4_p)(s + boff[u] + b * 8192 + ks * 4096 + 512));
        bfr[i - 1] = __builtin_shufflevector(__builtin_bit_cast(u32x2, lo), __builtin_bit_cast(u32x2, hi), 0, 1, 2, 3);
      } else {
        s16x4 lo = __builtin_amdgcn_ds_read_tr16_b64_v4i16((lds_s16x4_p)(s + hoff + ks * 2048));
        s16x4 hi = __builtin_amdgcn_ds_read_tr16_b64_v4i16((lds_s16x4_p)(s + hoff + ks * 2048 + 256));
        bfr[NTW - 1] = __builtin_shufflevector(__builtin_bit_cast(u32x2, lo), __builtin_bit_cast(u32x2, hi), 0, 1, 2, 3);
      }
    };
    constexpr int NM = 4 * NTW, NL = NTW + 4;
    auto phase = [&](u32x4(&afc)[4], u32x4(&bfc)[NTW], u32x4(&afn)[4], u32x4(&bfn)[NTW], int nbuf, int nks, auto rd, auto bar) {
      const char* s = smem + nbuf * STAGE;
      constexpr int BAR = decltype(bar)::value, R0 = BAR < 0 ? 0 : BAR + 1;
      constexpr int RSP = (NM - R0 - 2) / NL > 0 ? (NM - R0 - 2) / NL : 1;
      static_assert(R0 + (NL - 1) * RSP < NM, "reads");
#pragma unroll
      for (int mt = 0; mt < 4; ++mt)
#pragma unroll
        for (int j = 0; j < NTW; ++j) {
          const int m = mt * NTW + j;
          if (m == BAR) {
            WAIT_L0();
            __builtin_amdgcn_s_barrier();
            asm volatile("" ::: "memory");
          }
          if (decltype(rd)::value && m >= R0 && (m - R0) % RSP == 0 && (m - R0) / RSP < NL) ldf((m - R0) / RSP, s, nks, afn, bfn);
          if (EPI == EPI_QKV && j >= 4) mfma_acc(acc[mt][j], afc[mt], bfc[j]);
          else mfma_acc(acc[mt][j], bfc[j], afc[mt]);
          __builtin_amdgcn_sched_barrier(0);
        }
    };
    typedef std::integral_constant<int, -1> nobar;
#pragma unroll
    for (int mt = 0; mt < 4; ++mt)
#pragma unroll
      for (int j = 0; j < NTW; ++j) {
        acc[mt][j] = (f32x4){0.f, 0.f, 0.f, 0.f};
        asm volatile("" : "+a"(acc[mt][j]));
      }
    u32x4 af0[4], af1[4], bf0[NTW], bf1[NTW];
    __builtin_amdgcn_s_barrier();
    asm volatile("" ::: "memory");
    STAMP(1);
#pragma unroll
    for (int i = 0; i < NL; ++i) { ldf(i, smem, 0, af0, bf0); __builtin_amdgcn_sched_barrier(0); }
    int buf = 0;
    if constexpr (LAG) {
      phase(af0, bf0, af1, bf1, 0, 1, std::true_type{}, nobar{});
      for (int kt = 0; kt < NT - 1; ++kt) {
        buf = buf + 1 == NST ? 0 : buf + 1;
        phase(af1, bf1, af0, bf0, buf, 0, std::true_type{}, nobar{});
        asm volatile("" ::: "memory");
        __builtin_amdgcn_s_barrier();
        asm volatile("" ::: "memory");
        phase(af0, bf0, af1, bf1, buf, 1, std::true_type{}, nobar{});
      }
      phase(af1, bf1, af0, bf0, buf, 0, std::false_type{}, nobar{});
    } else {
      for (int kt = 0; kt < NT - 1; ++kt) {
        phase(af0, bf0, af1, bf1, buf, 1, std::true_type{}, nobar{});
        buf = buf + 1 == NST ? 0 : buf + 1;
        phase(af1, bf1, af0, bf0, buf, 0, std::true_type{}, std::integral_constant<int, 4>{});
      }
      phase(af0, bf0, af1, bf1, buf, 1, std::true_type{}, nobar{});
      phase(af1, bf1, af0, bf0, buf, 0, std::false_type{}, nobar{});
    }
    asm volatile("s_nop 7\n\ts_nop 7\n\ts_nop 7" ::: "memory");
    if constexpr (EPI == EPI_QKV) {
#pragma unroll
      for (int mt = 0; mt < 4; ++mt)
#pragma unroll
        for (int t = 0; t < 2; ++t) *reinterpret_cast<f32x4*>(smem + QX_OFF + wid * 8192 + (mt * 2 + t) * 1024 + lane * 16) = acc[mt][t];
    }
  }
  WAIT_L0();
  __builtin_amdgcn_s_barrier();
  asm volatile("" ::: "memory");
  STAMP(2);

  if constexpr (EPI == EPI_QKV) {
    const int h = tn * 2 + wn;
    const int q = l15 >> 2, p = lane & 3;
    f32x4 kv[4][2], zz[4];
    float* red = reinterpret_cast<float*>(smem + 4 * 64 * 144) + wn * (64 * 32 + 64);
    bf16x8 wf[2];
#pragma unroll
    for (int ft = 0; ft < 2; ++ft) {
      const char* src = smem + WP_OFF + wn * 2048 + (4 * g + q) * 64 + ft * 32 + p * 8;
      s16x4 lo = __builtin_amdgcn_ds_read_tr16_b64_v4i16((lds_s16x4_p)(src));
      s16x4 hi = __builtin_amdgcn_ds_read_tr16_b64_v4i16((lds_s16x4_p)(src + 16 * 64));
      wf[ft] = __builtin_shufflevector(lo, hi, 0, 1, 2, 3, 4, 5, 6, 7);
    }
    const float* csl = reinterpret_cast<const float*>(smem + CS_OFF);
    auto feat = [&](const bf16x8& xf, u32x2 (&o)[4]) {
      const float L2E = 1.4426950408889634f;
      f32x4 z4 = (f32x4){0.f, 0.f, 0.f, 0.f};
      f32x4 p0 = __builtin_amdgcn_mfma_f32_16x16x32_bf16(wf[0], xf, z4, 0, 0, 0);
      f32x4 p1 = __builtin_amdgcn_mfma_f32_16x16x32_bf16(wf[1], xf, z4, 0, 0, 0);
      f32x2 v[4];
      {
        const unsigned r0_ = pk2(p0[0], p0[1]), r1_ = pk2(p0[2], p0[3]), r2_ = pk2(p1[0], p1[1]), r3_ = pk2(p1[2], p1[3]);
        v[0] = (f32x2){bflo(r0_), bfhi(r0_)}; v[1] = (f32x2){bflo(r1_), bfhi(r1_)};
        v[2] = (f32x2){bflo(r2_), bfhi(r2_)}; v[3] = (f32x2){bflo(r3_), bfhi(r3_)};
      }
      float mx = max2_(max3_(max3_(v[0][0], v[0][1], v[1][0]), v[1][1], v[2][0]), max3_(v[2][1], v[3][0], v[3][1]));
      float mn = min2_(min3_(min3_(v[0][0], v[0][1], v[1][0]), v[1][1], v[2][0]), min3_(v[2][1], v[3][0], v[3][1]));
      mx = rmax4(mx); mn = rmin4(mn);
      const float mxl = -mx * L2E, mnl = mn * L2E;
      f32x2 ep[4], en[4];
#pragma unroll
      for (int i = 0; i < 4; ++i) {
        const f32x2 ap = v[i] * L2E + mxl, an = v[i] * (-L2E) + mnl;
        ep[i] = (f32x2){__builtin_amdgcn_exp2f(ap[0]), __builtin_amdgcn_exp2f(ap[1])};
        en[i] = (f32x2){__builtin_amdgcn_exp2f(an[0]), __builtin_amdgcn_exp2f(an[1])};
      }
      const f32x2 sp2 = (ep[0] + ep[1]) + (ep[2] + ep[3]), sn2 = (en[0] + en[1]) + (en[2] + en[3]);
      const float ip = __builtin_amdgcn_rcpf(rsum4(sp2[0] + sp2[1])), in_ = __builtin_amdgcn_rcpf(rsum4(sn2[0] + sn2[1]));
      o[0] = (u32x2){pk2v(ep[0] * ip), pk2v(ep[1] * ip)};
      o[1] = (u32x2){pk2v(ep[2] * ip), pk2v(ep[3] * ip)};
      o[2] = (u32x2){pk2v(en[0] * in_), pk2v(en[1] * in_)};
      o[3] = (u32x2){pk2v(en[2] * in_), pk2v(en[3] * in_)};
    };
    if (wid8 >= 4) {
      f32x4 cs4[2];
#pragma unroll
      for (int t = 0; t < 2; ++t) cs4[t] = *reinterpret_cast<const f32x4*>(csl + wn * 32 + t * 16 + 4 * g);
      bf16x8 qf[4];
#pragma unroll
      for (int mt = 0; mt < 4; ++mt) {
        f32x4 st = rs[wm * 64 + mt * 16 + l15];
        unsigned pq_[4];
#pragma unroll
        for (int t = 0; t < 2; ++t) {
          const f32x4 ac = *reinterpret_cast<const f32x4*>(smem + QX_OFF + wid * 8192 + (mt * 2 + t) * 1024 + lane * 16);
          float a[4];
#pragma unroll
          for (int i = 0; i < 4; ++i) a[i] = st[1] * (ac[i] - st[0] * cs4[t][i]);
          pq_[t * 2] = pk2(a[0], a[1]); pq_[t * 2 + 1] = pk2(a[2], a[3]);
        }
        qf[mt] = __builtin_bit_cast(bf16x8, (u32x4){pq_[0], pq_[1], pq_[2], pq_[3]});
      }
#pragma unroll
      for (int mt = 0; mt < 4; ++mt) {
        u32x2 o[4];
        feat(qf[mt], o);
        u16* dst = out + ((size_t)(row0 + wm * 64 + mt * 16 + l15) * 32 + h) * 64 + 4 * g;
#pragma unroll
        for (int c = 0; c < 4; ++c) wt_store(o[c], reinterpret_cast<u32x2*>(dst + c * 16));
      }
    } else {
      f32x4 cs4[2];
      float csv[2];
#pragma unroll
      for (int t = 0; t < 2; ++t) {
        cs4[t] = *reinterpret_cast<const f32x4*>(csl + 64 + wn * 32 + t * 16 + 4 * g);
        csv[t] = csl[128 + wn * 32 + t * 16 + l15];
      }
      bf16x8 kf[4], vf[2][2];
#pragma unroll
      for (int mt = 0; mt < 4; ++mt) {
        f32x4 st = rs[wm * 64 + mt * 16 + l15];
        unsigned pk_[4];
#pragma unroll
        for (int t = 0; t < 2; ++t) {
          float b[4];
#pragma unroll
          for (int i = 0; i < 4; ++i) b[i] = st[1] * (acc[mt][2 + t][i] - st[0] * cs4[t][i]);
          pk_[t * 2] = pk2(b[0], b[1]); pk_[t * 2 + 1] = pk2(b[2], b[3]);
        }
        kf[mt] = __builtin_bit_cast(bf16x8, (u32x4){pk_[0], pk_[1], pk_[2], pk_[3]});
      }
      char* pkl = smem + wid * (64 * 144);
#pragma unroll
      for (int mt = 0; mt < 4; ++mt) {
        u32x2 o[4];
        feat(kf[mt], o);
        char* dst = pkl + (mt * 16 + l15) * 144 + g * 8;
#pragma unroll
        for (int c = 0; c < 4; ++c) *reinterpret_cast<u32x2*>(dst + c * 32) = o[c];
      }
#pragma unroll
      for (int s = 0; s < 2; ++s) {
        unsigned pv[2][4];
#pragma unroll
        for (int m2 = 0; m2 < 2; ++m2) {
          int mt = s * 2 + m2;
          f32x4 s0 = rs[wm * 64 + mt * 16 + 4 * g + 0], s1 = rs[wm * 64 + mt * 16 + 4 * g + 1];
          f32x4 s2 = rs[wm * 64 + mt * 16 + 4 * g + 2], s3 = rs[wm * 64 + mt * 16 + 4 * g + 3];
#pragma unroll
          for (int t = 0; t < 2; ++t) {
            float a0 = s0[1] * (acc[mt][4 + t][0] - s0[0] * csv[t]);
            float a1 = s1[1] * (acc[mt][4 + t][1] - s1[0] * csv[t]);
            float a2 = s2[1] * (acc[mt][4 + t][2] - s2[0] * csv[t]);
            float a3 = s3[1] * (acc[mt][4 + t][3] - s3[0] * csv[t]);
            pv[t][m2 * 2] = pk2(a0, a1); pv[t][m2 * 2 + 1] = pk2(a2, a3);
          }
        }
#pragma unroll
        for (int t = 0; t < 2; ++t) vf[s][t] = __builtin_bit_cast(bf16x8, (u32x4){pv[t][0], pv[t][1], pv[t][2], pv[t][3]});
      }
      WAIT_L0();
      __builtin_amdgcn_sched_barrier(0);
#pragma unroll
      for (int ft = 0; ft < 4; ++ft) { kv[ft][0] = kv[ft][1] = zz[ft] = (f32x4){0.f, 0.f, 0.f, 0.f}; }
      const bf16x8 ones = (bf16x8){0x3F80, 0x3F80, 0x3F80, 0x3F80, 0x3F80, 0x3F80, 0x3F80, 0x3F80};
#pragma unroll
      for (int s = 0; s < 2; ++s)
#pragma unroll
        for (int ft = 0; ft < 4; ++ft) {
          const char* src = pkl + (32 * s + 4 * g + q) * 144 + ft * 32 + p * 8;
          s16x4 lo = __builtin_amdgcn_ds_read_tr16_b64_v4i16((lds_s16x4_p)(src));
          s16x4 hi = __builtin_amdgcn_ds_read_tr16_b64_v4i16((lds_s16x4_p)(src + 16 * 144));
          bf16x8 pf = __builtin_shufflevector(lo, hi, 0, 1, 2, 3, 4, 5, 6, 7);
          kv[ft][0] = __builtin_amdgcn_mfma_f32_16x16x32_bf16(vf[s][0], pf, kv[ft][0], 0, 0, 0);
          kv[ft][1] = __builtin_amdgcn_mfma_f32_16x16x32_bf16(vf[s][1], pf, kv[ft][1], 0, 0, 0);
          zz[ft] = __builtin_amdgcn_mfma_f32_16x16x32_bf16(ones, pf, zz[ft], 0, 0, 0);
        }
      if (wm == 1) {
#pragma unroll
        for (int ft = 0; ft < 4; ++ft) {
#pragma unroll
          for (int dt = 0; dt < 2; ++dt) *reinterpret_cast<f32x4*>(red + (ft * 16 + l15) * 32 + dt * 16 + 4 * g) = kv[ft][dt];
          if (g == 0) red[64 * 32 + ft * 16 + l15] = zz[ft][0];
        }
      }
    }
    __syncthreads();
    if (wid8 < 2) {
      float* kd = kvp + (size_t)(h * 16 + tm) * 2048;
      float* zd = zp + (size_t)(h * 16 + tm) * 64;
#pragma unroll
      for (int ft = 0; ft < 4; ++ft) {
#pragma unroll
        for (int dt = 0; dt < 2; ++dt) {
          f32x4 o = *reinterpret_cast<const f32x4*>(red + (ft * 16 + l15) * 32 + dt * 16 + 4 * g);
          wt_store(o + kv[ft][dt], reinterpret_cast<f32x4*>(kd + (ft * 16 + l15) * 32 + dt * 16 + 4 * g));
        }
        if (g == 0) zd[ft * 16 + l15] = red[64 * 32 + ft * 16 + l15] + zz[ft][0];
      }
    }
  } else {
    if (wid8 < 4) {
      f32x4 stm[4], cs4[NTW];
      if constexpr (EPI == EPI_W1 || EPI == EPI_W3) {
#pragma unroll
        for (int j = 0; j < NTW; ++j) {
          const int colj = (j < 2 * NFULL) ? ((j >> 1) * 64 + (2 * wn + (j & 1)) * 16) : (NFULL * 64 + wn * 16);
          cs4[j] = *reinterpret_cast<const f32x4*>(smem + CS_OFF + (colj + 4 * g) * 4);
        }
#pragma unroll
        for (int mt = 0; mt < 4; ++mt) stm[mt] = rs[wm * 64 + mt * 16 + l15];
      }
#pragma unroll
      for (int j = 0; j < NTW; ++j) {
        const int colj = (j < 2 * NFULL) ? ((j >> 1) * 64 + (2 * wn + (j & 1)) * 16) : (NFULL * 64 + wn * 16);
#pragma unroll
        for (int mt = 0; mt < 4; ++mt) {
          f32x2 v0 = (f32x2){acc[mt][j][0], acc[mt][j][1]}, v1 = (f32x2){acc[mt][j][2], acc[mt][j][3]};
          if constexpr (EPI == EPI_W1 || EPI == EPI_W3) {
            v0 = gelu2((v0 - stm[mt][0] * (f32x2){cs4[j][0], cs4[j][1]}) * stm[mt][1]);
            v1 = gelu2((v1 - stm[mt][0] * (f32x2){cs4[j][2], cs4[j][3]}) * stm[mt][1]);
          }
          *reinterpret_cast<u32x2*>(smem + (wm * 64 + mt * 16 + l15) * PB + (colj + 4 * g) * 2) = (u32x2){pk2(v0[0], v0[1]), pk2(v1[0], v1[1])};
        }
      }
    }
    __syncthreads();
#pragma unroll
    for (int it = 0; it < NIT; ++it) {
      const int c = tid + 512 * it, r = c / CH, ch = c % CH;
      u32x4 v = *reinterpret_cast<const u32x4*>(smem + r * PB + ch * 16);
      const int grow = row0 + r, gcol = tn * BN + ch * 8;
      if constexpr (EPI == EPI_W3) {
        wt_store(v, reinterpret_cast<u32x4*>(out + (size_t)grow * F_ + gcol));
      } else if constexpr (EPI == EPI_W1) {
        const int grp = tn >> 2;
        const int sh = grp == 0 ? 1 : grp == 1 ? -1 : grp == 2 ? 2 : -2;
        const int dr = grow + sh;
        if (dr >= 0 && dr < S_) wt_store(v, reinterpret_cast<u32x4*>(out + (size_t)dr * D_ + gcol));
        const int zr = grow - sh;
        if (zr < 0 || zr >= S_) *reinterpret_cast<u32x4*>(out + (size_t)grow * D_ + gcol) = (u32x4){0u, 0u, 0u, 0u};
      } else {
        float y[8];
        const u32x4 a = ra[it];
        if constexpr (EPI == EPI_WO) {
#pragma unroll
          for (int i = 0; i < 4; ++i) { y[2 * i] = rbf(bflo(a[i]) + bflo(v[i])); y[2 * i + 1] = rbf(bfhi(a[i]) + bfhi(v[i])); }
        } else if constexpr (EPI == EPI_W2) {
          f32x4 st = rs[r];
#pragma unroll
          for (int i = 0; i < 4; ++i) {
            y[2 * i] = rbf(rbf((bflo(a[i]) - st[0]) * st[2]) + bflo(v[i]));
            y[2 * i + 1] = rbf(rbf((bfhi(a[i]) - st[0]) * st[2]) + bfhi(v[i]));
          }
        } else {
          const u32x4 b = rb[it];
#pragma unroll
          for (int i = 0; i < 4; ++i) {
            y[2 * i] = rbf(bflo(a[i]) + rbf(bflo(b[i]) + bflo(v[i])));
            y[2 * i + 1] = rbf(bfhi(a[i]) + rbf(bfhi(b[i]) + bfhi(v[i])));
          }
        }
        wt_store((u32x4){pk2(y[0], y[1]), pk2(y[2], y[3]), pk2(y[4], y[5]), pk2(y[6], y[7])}, reinterpret_cast<u32x4*>(out + (size_t)grow * D_ + gcol));
        float sm = ((y[0] + y[1]) + (y[2] + y[3])) + ((y[4] + y[5]) + (y[6] + y[7]));
        sm = rsum8(sm);
        float mu = sm * (1.0f / 64.f), m2 = 0.f;
#pragma unroll
        for (int i = 0; i < 8; ++i) { float d = y[i] - mu; m2 += d * d; }
        m2 = rsum8(m2);
        if (ch == 0) st_out[(size_t)grow * 16 + tn] = (f32x2){sm, m2};
      }
    }
  }
  STAMP(3);
}

__global__ __launch_bounds__(256, 1) void attn_out_kernel(const u16* __restrict__ phiq, const float* __restrict__ kvp,
                                                          const float* __restrict__ zp, u16* __restrict__ o,
                                                          const float* __restrict__ cp1, const float* __restrict__ cp3,
                                                          float* __restrict__ cs1, float* __restrict__ cs3) {
  __shared__ __attribute__((aligned(16))) u16 kvT[48 * 72];
  const int tid = threadIdx.x, lane = tid & 63, wid = tid >> 6, g = lane >> 4, l15 = lane & 15;
  if (blockIdx.x >= 256) {
    const int i = (blockIdx.x - 256) * 256 + tid;
    const float* src = i < 1024 ? cp1 + i : cp3 + (i - 1024);
    const int ld = i < 1024 ? 1024 : F_;
    float c[16];
#pragma unroll
    for (int t = 0; t < 16; ++t) c[t] = src[t * ld];
#pragma unroll
    for (int t = 8; t > 0; t >>= 1)
#pragma unroll
      for (int j = 0; j < t; ++j) c[j] += c[j + t];
    *(i < 1024 ? cs1 + i : cs3 + (i - 1024)) = c[0];
    return;
  }
  const int h = blockIdx.x & 31, tg = blockIdx.x >> 5;
  f32x4 kvv[16][2];
  float zv[16];
  bf16x8 pq[4][2];
  {
    const float* src = kvp + (size_t)h * 16 * 2048 + tid * 8;
#pragma unroll
    for (int t = 0; t < 16; ++t) {
      kvv[t][0] = *reinterpret_cast<const f32x4*>(src + t * 2048);
      kvv[t][1] = *reinterpret_cast<const f32x4*>(src + t * 2048 + 4);
    }
#pragma unroll
    for (int t = 0; t < 16; ++t) zv[t] = zp[(size_t)(h * 16 + t) * 64 + lane];
#pragma unroll
    for (int mt = 0; mt < 4; ++mt) {
      const u16* ps = phiq + ((size_t)(tg * 256 + wid * 64 + mt * 16 + l15) * 32 + h) * 64 + g * 8;
      pq[mt][0] = *reinterpret_cast<const bf16x8*>(ps);
      pq[mt][1] = *reinterpret_cast<const bf16x8*>(ps + 32);
    }
  }
  __builtin_amdgcn_sched_barrier(0);
  {
    for (int i = tid; i < 15 * 64; i += 256) kvT[(33 + i / 64) * 72 + (i & 63)] = 0;
    f32x4 a0 = kvv[0][0], a1 = kvv[0][1];
#pragma unroll
    for (int t = 1; t < 16; ++t) { a0 += kvv[t][0]; a1 += kvv[t][1]; }
    const int f = tid >> 2, d0 = (tid & 3) * 8;
#pragma unroll
    for (int i = 0; i < 4; ++i) {
      kvT[(d0 + i) * 72 + f] = (u16)(pk2(a0[i], 0.f) & 0xffff);
      kvT[(d0 + 4 + i) * 72 + f] = (u16)(pk2(a1[i], 0.f) & 0xffff);
    }
    if (tid < 64) {
      float z = 0.f;
#pragma unroll
      for (int t = 0; t < 16; ++t) z += zv[t];
      kvT[32 * 72 + tid] = (u16)(pk2(z, 0.f) & 0xffff);
    }
  }
  __syncthreads();
  bf16x8 kf[3][2];
#pragma unroll
  for (int dt = 0; dt < 3; ++dt)
#pragma unroll
    for (int ks = 0; ks < 2; ++ks) kf[dt][ks] = *reinterpret_cast<const bf16x8*>(&kvT[(dt * 16 + l15) * 72 + ks * 32 + g * 8]);
#pragma unroll
  for (int mt = 0; mt < 4; ++mt) {
    const int tok = tg * 256 + wid * 64 + mt * 16 + l15;
    const bf16x8 p0 = pq[mt][0], p1 = pq[mt][1];
    f32x4 r[3];
#pragma unroll
    for (int dt = 0; dt < 3; ++dt) {
      r[dt] = (f32x4){0.f, 0.f, 0.f, 0.f};
      r[dt] = __builtin_amdgcn_mfma_f32_16x16x32_bf16(kf[dt][0], p0, r[dt], 0, 0, 0);
      r[dt] = __builtin_amdgcn_mfma_f32_16x16x32_bf16(kf[dt][1], p1, r[dt], 0, 0, 0);
    }
    float den = rbf(rbf(__shfl(r[2][0], l15)) + 1e-6f);
    float inv = 1.0f / den;
#pragma unroll
    for (int dt = 0; dt < 2; ++dt) {
      u32x2 w = (u32x2){pk2(rbf(r[dt][0]) * inv, rbf(r[dt][1]) * inv), pk2(rbf(r[dt][2]) * inv, rbf(r[dt][3]) * inv)};
      *reinterpret_cast<u32x2*>(o + (size_t)tok * D_ + h * 32 + dt * 16 + 4 * g) = w;
    }
  }
}

constexpr int WC_QKV = 0, WC_WO = WC_QKV + 2 * 1024 * 384, WC_W1 = WC_WO + 2 * 1024 * 128, WC_W2 = WC_W1 + 2 * 1024 * 128,
              WC_W4 = WC_W2 + 2 * 1024 * 128, WC_END = WC_W4 + 2 * F_ * 128;
__global__ __launch_bounds__(256) void prep_kernel(const u16* __restrict__ x, const u16* __restrict__ wqkv,
                                                   const u16* __restrict__ w1, const u16* __restrict__ w3,
                                                   float* __restrict__ cs_qkv, float* __restrict__ cs_w1,
                                                   float* __restrict__ cs_w3, f32x2* __restrict__ st,
                                                   const u16* __restrict__ wo, const u16* __restrict__ w2,
                                                   const u16* __restrict__ w4, char* __restrict__ wc) {
  __shared__ __attribute__((aligned(16))) float red[4][512];
  const int tid = threadIdx.x, b = blockIdx.x;
  if (b >= 368) {
    const int pb = b - 368;
    const u16* w = pb < 32 ? wo + (size_t)pb * 32768 : pb < 64 ? w2 + (size_t)(pb - 32) * 32768 : w4 + (size_t)(pb - 64) * 32768;
    u32x4 v[16];
#pragma unroll
    for (int r = 0; r < 16; ++r) v[r] = *reinterpret_cast<const u32x4*>(w + r * 2048 + tid * 8);
    if (((tid >> 3) & 7) == 3) {
      const int kd = pb < 64 ? 1024 : F_, row = (pb < 32 ? pb : pb < 64 ? pb - 32 : pb - 64) * 32 + (tid >> 7);
      char* dst = wc + (pb < 32 ? WC_WO : pb < 64 ? WC_W2 : WC_W4) + (size_t)((tid >> 6) & 1) * kd * 128 + (size_t)row * 128 + (tid & 7) * 16;
#pragma unroll
      for (int r = 0; r < 16; ++r) *reinterpret_cast<u32x4*>(dst + r * 256) = v[r];
    }
#pragma unroll
    for (int r = 0; r < 16; ++r) asm volatile("" ::"v"(v[r]));
  } else if (b < 240) {
    const u16* w; float* dst; int ld, ch, rg;
    if (b < 96) { w = wqkv; dst = cs_qkv; ld = 3072; ch = b % 6; rg = b / 6; }
    else if (b < 128) { w = w1; dst = cs_w1; ld = 1024; ch = (b - 96) & 1; rg = (b - 96) >> 1; }
    else { w = w3; dst = cs_w3; ld = F_; ch = (b - 128) % 7; rg = (b - 128) / 7; }
    const int lane = tid & 63, wid = tid >> 6, row = rg * 64 + wid * 16;
    u32x4 v[16];
#pragma unroll
    for (int r = 0; r < 16; ++r) v[r] = *reinterpret_cast<const u32x4*>(w + (size_t)(row + r) * ld + ch * 512 + lane * 8);
    if (b < 128 && (lane >> 3) == 3) {
      const int cpp = b < 96 ? 384 : 128;
      char* cpd = wc + (b < 96 ? WC_QKV + (ch & 1) * (1024 * 384) + (ch >> 1) * 128 : WC_W1 + ch * (1024 * 128)) + row * cpp + (lane & 7) * 16;
#pragma unroll
      for (int r = 0; r < 16; ++r) *reinterpret_cast<u32x4*>(cpd + r * cpp) = v[r];
    }
    f32x4 s0 = (f32x4){0.f, 0.f, 0.f, 0.f}, s1 = s0;
#pragma unroll
    for (int r = 0; r < 16; ++r) {
      s0 += (f32x4){bflo(v[r][0]), bfhi(v[r][0]), bflo(v[r][1]), bfhi(v[r][1])};
      s1 += (f32x4){bflo(v[r][2]), bfhi(v[r][2]), bflo(v[r][3]), bfhi(v[r][3])};
    }
    *reinterpret_cast<f32x4*>(&red[wid][lane * 8]) = s0;
    *reinterpret_cast<f32x4*>(&red[wid][lane * 8 + 4]) = s1;
    __syncthreads();
    f32x2 t = *reinterpret_cast<const f32x2*>(&red[0][tid * 2]);
#pragma unroll
    for (int i = 1; i < 4; ++i) t += *reinterpret_cast<const f32x2*>(&red[i][tid * 2]);
    *reinterpret_cast<f32x2*>(dst + rg * ld + ch * 512 + tid * 2) = t;
  } else {
    const int lane = tid & 63, wid = tid >> 6;
    const int rbase = (b - 240) * 16 + wid * 4;
    u32x4 xv[8];
#pragma unroll
    for (int i = 0; i < 8; ++i) xv[i] = *reinterpret_cast<const u32x4*>(x + (size_t)rbase * D_ + i * 512 + lane * 8);
    __builtin_amdgcn_sched_barrier(0);
#pragma unroll
    for (int rr = 0; rr < 4; ++rr) {
      const int row = rbase + rr;
#pragma unroll
      for (int hf = 0; hf < 2; ++hf) {
        const u32x4 v = xv[rr * 2 + hf];
        float y[8];
#pragma unroll
        for (int i = 0; i < 4; ++i) { y[2 * i] = bflo(v[i]); y[2 * i + 1] = bfhi(v[i]); }
        float sm = ((y[0] + y[1]) + (y[2] + y[3])) + ((y[4] + y[5]) + (y[6] + y[7]));
        sm = rsum8(sm);
        float mu = sm * (1.0f / 64.f), m2 = 0.f;
#pragma unroll
        for (int i = 0; i < 8; ++i) { float d = y[i] - mu; m2 += d * d; }
        m2 = rsum8(m2);
        if ((lane & 7) == 0) st[(size_t)row * 16 + hf * 8 + (lane >> 3)] = (f32x2){sm, m2};
      }
    }
  }
}

extern "C" void kernel_launch(void* const* d_in, const int* in_sizes, int n_in, void* d_out, int out_size, void* d_ws,
                              size_t ws_size, hipStream_t stream) {
  const u16* x_in = (const u16*)d_in[0];
  const u16* wqkv = (const u16*)d_in[1];
  const u16* wphi = (const u16*)d_in[2];
  const u16* wo = (const u16*)d_in[3];
  const u16* w1 = (const u16*)d_in[5];
  const u16* w2 = (const u16*)d_in[6];
  const u16* w3 = (const u16*)d_in[7];
  const u16* w4 = (const u16*)d_in[8];
  char* ws = (char*)d_ws;
  size_t off = 0;
  auto alloc = [&](size_t bytes) { char* p = ws + off; off += (bytes + 4095) & ~(size_t)4095; return p; };
  u16* xb = (u16*)alloc((size_t)S_ * D_ * 2);
  u16* x2 = (u16*)alloc((size_t)S_ * D_ * 2);
  u16* x1 = (u16*)alloc((size_t)S_ * D_ * 2);
  u16* hs = (u16*)alloc((size_t)S_ * D_ * 2);
  u16* ob = (u16*)alloc((size_t)S_ * D_ * 2);
  u16* gb = (u16*)alloc((size_t)S_ * F_ * 2);
  u16* phiq = (u16*)alloc((size_t)S_ * 2048 * 2);
  float* kvp = (float*)alloc((size_t)32 * 16 * 2048 * 4);
  float* zp = (float*)alloc((size_t)32 * 16 * 64 * 4);
  f32x2* st_x = (f32x2*)alloc((size_t)S_ * 16 * 8);
  f32x2* st_x2 = (f32x2*)alloc((size_t)S_ * 16 * 8);
  f32x2* st_x1 = (f32x2*)alloc((size_t)S_ * 16 * 8);
  float* cs_qkv = (float*)alloc(16 * 3072 * 4);
  float* cs_w1 = (float*)alloc(16 * 1024 * 4);
  float* cs_w3 = (float*)alloc(16 * F_ * 4);
  float* cf_w1 = (float*)alloc(1024 * 4);
  float* cf_w3 = (float*)alloc(F_ * 4);
  char* wc = alloc(WC_END);

  prep_kernel<<<368 + 176, 256, 0, stream>>>(x_in, wqkv, w1, w3, cs_qkv, cs_w1, cs_w3, st_x, wo, w2, w4, wc);
  for (int layer = 0; layer < 2; ++layer) {
    const u16* x = layer == 0 ? x_in : xb;
    u16* xo = layer == 0 ? xb : (u16*)d_out;
    gemm_kernel<192, 3, 1024, 1024, 3072, EPI_QKV><<<256, 512, 0, stream>>>(x, wqkv, (const u16*)(wc + WC_QKV), cs_qkv, st_x, nullptr, nullptr, phiq, nullptr, wphi, kvp, zp);
    attn_out_kernel<<<layer == 0 ? 256 + 18 : 256, 256, 0, stream>>>(phiq, kvp, zp, ob, cs_w1, cs_w3, cf_w1, cf_w3);
    gemm_kernel<64, 5, 1024, 1024, 1024, EPI_WO><<<256, 512, 0, stream>>>(ob, wo, (const u16*)(wc + WC_WO), nullptr, nullptr, x, nullptr, x2, st_x2, nullptr, nullptr, nullptr);
    gemm_kernel<64, 5, 1024, 1024, 1024, EPI_W1><<<256, 512, 0, stream>>>(x2, w1, (const u16*)(wc + WC_W1), cf_w1, st_x2, nullptr, nullptr, hs, nullptr, nullptr, nullptr, nullptr);
    gemm_kernel<64, 5, 1024, 1024, 1024, EPI_W2><<<256, 512, 0, stream>>>(hs, w2, (const u16*)(wc + WC_W2), nullptr, st_x2, x2, nullptr, x1, st_x1, nullptr, nullptr, nullptr);
    gemm_kernel<224, 3, 1024, 1024, F_, EPI_W3><<<256, 512, 0, stream>>>(x1, w3, nullptr, cf_w3, st_x1, nullptr, nullptr, gb, nullptr, nullptr, nullptr, nullptr);
    gemm_kernel<64, 5, F_, F_, 1024, EPI_W4><<<256, 512, 0, stream>>>(gb, w4, (const u16*)(wc + WC_W4), nullptr, nullptr, x2, x1, xo, st_x, nullptr, nullptr, nullptr);
  }
}
```

```cpp
#include <hip/hip_runtime.h>
#include <hip/hip_bf16.h>
#include <stdint.h>
#include <type_traits>

typedef short bf16x8 __attribute__((ext_vector_type(8)));
typedef short s16x4 __attribute__((ext_vector_type(4)));
typedef float f32x4 __attribute__((ext_vector_type(4)));
typedef float f32x2 __attribute__((ext_vector_type(2)));
typedef unsigned u32x2 __attribute__((ext_vector_type(2)));
typedef unsigned u32x4 __attribute__((ext_vector_type(4)));
typedef __attribute__((address_space(3))) s16x4* lds_s16x4_p;
typedef unsigned short u16;

constexpr int S_ = 2048, D_ = 1024, H_ = 32, F_ = 3584;

__device__ __forceinline__ float bf2f(u16 v) { return __uint_as_float(((unsigned)v) << 16); }
__device__ __forceinline__ float bflo(unsigned v) { return __uint_as_float(v << 16); }
__device__ __forceinline__ float bfhi(unsigned v) { return __uint_as_float(v & 0xffff0000u); }
typedef __bf16 bf16x2_t __attribute__((ext_vector_type(2)));
__device__ __forceinline__ unsigned pk2(float a, float b) {
  return __builtin_bit_cast(unsigned, __builtin_convertvector((f32x2){a, b}, bf16x2_t));
}
__device__ __forceinline__ unsigned pk2v(f32x2 v) { return __builtin_bit_cast(unsigned, __builtin_convertvector(v, bf16x2_t)); }
__device__ __forceinline__ float max2_(float a, float b) { float r; asm("v_max_f32 %0, %1, %2" : "=v"(r) : "v"(a), "v"(b)); return r; }
__device__ __forceinline__ float min2_(float a, float b) { float r; asm("v_min_f32 %0, %1, %2" : "=v"(r) : "v"(a), "v"(b)); return r; }
__device__ __forceinline__ float max3_(float a, float b, float c) { float r; asm("v_max3_f32 %0, %1, %2, %3" : "=v"(r) : "v"(a), "v"(b), "v"(c)); return r; }
__device__ __forceinline__ float min3_(float a, float b, float c) { float r; asm("v_min3_f32 %0, %1, %2, %3" : "=v"(r) : "v"(a), "v"(b), "v"(c)); return r; }
__device__ __forceinline__ float rbf(float a) { return bflo(pk2(a, 0.f)); }
__device__ __forceinline__ float gelu_t(float x) {
  const float c1 = -2.0f * 0.7978845608028654f * 1.4426950408889634f;
  const float c2 = c1 * 0.044715f;
  float t = x * x;
  float w = x * __builtin_fmaf(t, c2, c1);
  float e = __builtin_amdgcn_exp2f(w);
  return x * __builtin_amdgcn_rcpf(1.0f + e);
}

__device__ __forceinline__ f32x2 gelu2(f32x2 x) {
  const float c1 = -2.0f * 0.7978845608028654f * 1.4426950408889634f;
  const float c2 = c1 * 0.044715f;
  f32x2 t = x * x;
  f32x2 w = x * (t * c2 + c1);
  f32x2 d = (f32x2){__builtin_amdgcn_exp2f(w[0]), __builtin_amdgcn_exp2f(w[1])} + 1.0f;
  return x * (f32x2){__builtin_amdgcn_rcpf(d[0]), __builtin_amdgcn_rcpf(d[1])};
}
__device__ __forceinline__ void x16(float v, float& a, float& b) {
  auto r = __builtin_amdgcn_permlane16_swap(__float_as_uint(v), __float_as_uint(v), false, false);
  a = __uint_as_float(r[0]); b = __uint_as_float(r[1]);
}
__device__ __forceinline__ void x32(float v, float& a, float& b) {
  auto r = __builtin_amdgcn_permlane32_swap(__float_as_uint(v), __float_as_uint(v), false, false);
  a = __uint_as_float(r[0]); b = __uint_as_float(r[1]);
}
__device__ __forceinline__ float rmax4(float v) { float a, b; x16(v, a, b); v = max2_(a, b); x32(v, a, b); return max2_(a, b); }
__device__ __forceinline__ float rmin4(float v) { float a, b; x16(v, a, b); v = min2_(a, b); x32(v, a, b); return min2_(a, b); }
__device__ __forceinline__ float rsum4(float v) { float a, b; x16(v, a, b); v = a + b; x32(v, a, b); return a + b; }

__device__ __forceinline__ float rsum8(float v) {
  v += __uint_as_float(__builtin_amdgcn_mov_dpp(__float_as_uint(v), 0xB1, 0xF, 0xF, true));
  v += __uint_as_float(__builtin_amdgcn_mov_dpp(__float_as_uint(v), 0x4E, 0xF, 0xF, true));
  v += __uint_as_float(__builtin_amdgcn_mov_dpp(__float_as_uint(v), 0x141, 0xF, 0xF, true));
  return v;
}

__device__ __forceinline__ void wt_store(u32x4 v, u32x4* p) { asm volatile("global_store_dwordx4 %0, %1, off sc1\n\ts_nop 1" :: "v"(p), "v"(v) : "memory"); }
__device__ __forceinline__ void wt_store(f32x4 v, f32x4* p) { asm volatile("global_store_dwordx4 %0, %1, off sc1\n\ts_nop 1" :: "v"(p), "v"(v) : "memory"); }
__device__ __forceinline__ void wt_store(u32x2 v, u32x2* p) { asm volatile("global_store_dwordx2 %0, %1, off sc1\n\ts_nop 1" :: "v"(p), "v"(v) : "memory"); }
#define WAIT_V(n) asm volatile("s_waitcnt vmcnt(" #n ")" ::: "memory")
#define WAIT_L0() asm volatile("s_waitcnt lgkmcnt(0)" ::: "memory")
template <int N> __device__ __forceinline__ void wait_vm() {
  asm volatile("s_waitcnt vmcnt(%0)" ::"i"(N) : "memory");
}

__device__ __forceinline__ void glds16(unsigned voff, const void* sbase, unsigned lds) {
  unsigned keep;
  asm volatile("s_mov_b32 %0, m0\n\ts_mov_b32 m0, %3\n\ts_nop 0\n\tglobal_load_lds_dwordx4 %1, %2\n\ts_mov_b32 m0, %0"
               : "=&s"(keep) : "v"(voff), "s"(sbase), "s"(lds) : "memory");
}

__device__ __forceinline__ void mfma_acc(f32x4& c, const u32x4& a, const u32x4& b) {
  asm volatile("v_mfma_f32_16x16x32_bf16 %0, %1, %2, %0" : "+a"(c) : "v"(a), "v"(b));
}

#define STAMP(i)

#define XPOS 2
enum { EPI_QKV = 0, EPI_WO = 1, EPI_W1 = 2, EPI_W2 = 3, EPI_W3 = 4, EPI_W4 = 5 };

template <int BN, int NST, int KD, int LDA, int LDB, int EPI>
__global__ __launch_bounds__(512, 2) void gemm_kernel(
    const u16* __restrict__ A, const u16* __restrict__ W,
    const u16* __restrict__ Wc,
    const float* __restrict__ cs,
    const f32x2* __restrict__ st_in,
    const u16* __restrict__ r0,
    const u16* __restrict__ r1,
    u16* __restrict__ out,
    f32x2* __restrict__ st_out,
    const u16* __restrict__ wphi,
    float* __restrict__ kvp,
    float* __restrict__ zp)
{
  constexpr int NFULL = BN / 64, HALF = (BN % 64) / 32;
  constexpr int A_BYTES = 128 * 64 * 2, B_BYTES = BN * 64 * 2, STAGE = A_BYTES + B_BYTES;
  constexpr int NT = KD / 64, NTW = 2 * NFULL + HALF, LPS = 4 + 2 * NFULL + HALF;
  constexpr int PB = BN * 2 + 16;
  constexpr int EPI_BYTES = (EPI == EPI_QKV) ? (4 * 64 * 144 + 2 * (64 * 32 + 64) * 4) : 128 * PB;
  constexpr int MAINB = NST * STAGE;
  constexpr int RS_OFF = (MAINB > EPI_BYTES ? MAINB : EPI_BYTES);
  constexpr int CS_OFF = RS_OFF + 128 * 16;
  constexpr int WP_OFF = CS_OFF + 1024;
  constexpr int QX_OFF = WP_OFF + 4096;
  constexpr int SMEM = WP_OFF + (EPI == EPI_QKV ? 4096 + 32768 : 0);
  constexpr bool HAS_CS = (EPI == EPI_QKV || EPI == EPI_W1 || EPI == EPI_W3);
  constexpr bool HAS_ST = (EPI == EPI_QKV || EPI == EPI_W1 || EPI == EPI_W3 || EPI == EPI_W2);
  constexpr bool HAS_RES = (EPI == EPI_WO || EPI == EPI_W2 || EPI == EPI_W4);
  constexpr int LAG = (BN == 64) ? 1 : 0;
  constexpr int CH = BN / 8, NIT = CH / 4;
  static_assert(SMEM <= 163840, "LDS");
  __shared__ __attribute__((aligned(16))) char smem[SMEM];

  STAMP(0);
  const int tid = threadIdx.x, lane = tid & 63;
  const int wid8 = __builtin_amdgcn_readfirstlane(tid >> 6), wid = wid8 & 3, wm = wid >> 1, wn = wid & 1;
  const int g = lane >> 4, l15 = lane & 15;
  const int xcd = blockIdx.x & 7, idx = blockIdx.x >> 3;
  const int tm = (xcd >> 1) * 4 + (idx & 3), tn = (xcd & 1) * 8 + (idx >> 2);
  const int row0 = tm * 128;
  f32x4* rs = reinterpret_cast<f32x4*>(smem + RS_OFF);

  f32x4 acc[4][NTW];
  u32x4 ra[HAS_RES ? NIT : 1], rb[(EPI == EPI_W4) ? NIT : 1];

  unsigned rof[HAS_RES ? NIT : 1];
  if constexpr (HAS_RES) {
#pragma unroll
    for (int it = 0; it < NIT; ++it) {
      const int c = tid + 512 * it, r = c / CH, ch = c % CH;
      rof[it] = (unsigned)((row0 + r) * D_ + tn * BN + ch * 8) * 2u;
      asm volatile("" : "+v"(rof[it]));
    }
  }
  auto ldres = [&]() {
    if constexpr (HAS_RES) {
#pragma unroll
      for (int it = 0; it < NIT; ++it) {
        ra[it] = *reinterpret_cast<const u32x4*>(reinterpret_cast<const char*>(r0) + rof[it]);
        if constexpr (EPI == EPI_W4) rb[it] = *reinterpret_cast<const u32x4*>(reinterpret_cast<const char*>(r1) + rof[it]);
      }
    }
  };

  if (wid8 >= 4) {
    unsigned voA[4], voB[NFULL > 0 ? NFULL : 1][2], voH = 0;
    const bool pk_ = (HALF == 0) && ((tn & 7) == 3);
    const unsigned pitchB = pk_ ? NFULL * 128 : LDB * 2;
#pragma unroll
    for (int i = 0; i < 4; ++i) {
      int r = (wid * 4 + i) * 8 + (lane >> 3);
      int ch = (lane & 7) ^ ((r >> 1) & 7);
      voA[i] = (unsigned)(row0 + r) * (LDA * 2) + ch * 16;
    }
#pragma unroll
    for (int b = 0; b < NFULL; ++b) {
      int colb = (EPI == EPI_QKV) ? (b * 1024 + tn * 64) : (tn * BN + b * 64);
#pragma unroll
      for (int j = 0; j < 2; ++j) {
        int r = (wid * 2 + j) * 8 + (lane >> 3);
        int f = ((r >> 1) & 1) | (((r >> 3) & 1) << 1);
        int chp = lane & 7;
        int gch = (((chp >> 1) ^ f) << 1) | (chp & 1);
        voB[b][j] = (unsigned)r * pitchB + (pk_ ? b * 128 : colb * 2) + gch * 16;
      }
    }
    if constexpr (HALF) {
      int r = wid * 16 + (lane >> 2);
      int chp = lane & 3;
      int gch = (((chp >> 1) ^ ((r >> 3) & 1)) << 1) | (chp & 1);
      voH = (unsigned)r * (LDB * 2) + (tn * BN + NFULL * 64) * 2 + gch * 16;
    }
    const char* Ab = reinterpret_cast<const char*>(A);
    const char* Wb = pk_ ? reinterpret_cast<const char*>(Wc) + (size_t)(tn >> 3) * (KD * NFULL * 128) : reinterpret_cast<const char*>(W);
    const unsigned lbase = (unsigned)(uintptr_t)(__attribute__((address_space(3))) char*)smem;
    auto stage = [&](int kt, int buf) {
      const char* a = Ab + (size_t)kt * 128;
      const char* w = Wb + (size_t)(kt * (64 * pitchB));
      const unsigned s = lbase + buf * STAGE;
#pragma unroll
      for (int i = 0; i < 4; ++i) glds16(voA[i], a, s + (wid * 4 + i) * 1024);
#pragma unroll
      for (int b = 0; b < NFULL; ++b)
#pragma unroll
        for (int j = 0; j < 2; ++j) glds16(voB[b][j], w, s + A_BYTES + b * 8192 + (wid * 2 + j) * 1024);
      if constexpr (HALF) glds16(voH, w, s + A_BYTES + NFULL * 8192 + wid * 1024);
    };
#pragma unroll
    for (int s = 0; s < NST; ++s) stage(s, s);
    constexpr int XL = 0;
    constexpr int NCS = HAS_CS ? (EPI == EPI_QKV ? 16 : 1) : 0;
    constexpr int XE = NCS + (EPI == EPI_QKV ? 1 : 0) + (HAS_ST ? 4 : 0) + (HAS_RES ? NIT * (EPI == EPI_W4 ? 2 : 1) : 0);
    f32x4 stp[4];
    const int lt = tid - 256;
    u32x4 wpx;
    f32x4 csp[HAS_CS ? NCS : 1];
    constexpr int XP = XPOS;
    wait_vm<(NST - 1 - LAG) * LPS>();
    __builtin_amdgcn_s_barrier();
#pragma unroll
    for (int kt = 0; kt < XP; ++kt) {
      wait_vm<(NST - 2 - LAG) * LPS>();
      __builtin_amdgcn_s_barrier();
      stage(kt + NST, kt % NST);
    }
    asm volatile("" ::: "memory");
    if constexpr (HAS_CS) {
      if (lt < BN / 4) {
        const float* cp = cs + (EPI == EPI_QKV ? (lt >> 4) * 1024 + tn * 64 + (lt & 15) * 4 : tn * BN + lt * 4);
#pragma unroll
        for (int i = 0; i < NCS; ++i) csp[i] = *reinterpret_cast<const f32x4*>(cp + i * LDB);
      }
    }
    if constexpr (EPI == EPI_QKV) wpx = *reinterpret_cast<const u32x4*>(wphi + tn * 2048 + lt * 8);
    if constexpr (HAS_ST) {
      const f32x4* sp = reinterpret_cast<const f32x4*>(st_in + (size_t)(row0 + (lt >> 1)) * 16 + (lt & 1) * 8);
#pragma unroll
      for (int i = 0; i < 4; ++i) stp[i] = sp[i];
    }
    ldres();
    asm volatile("" ::: "memory");
    constexpr int NPEEL = XP + NST - 1 - LAG;
    auto head = [&](auto xe) {
      constexpr int X = decltype(xe)::value;
#pragma unroll
      for (int kt = XP; kt < NPEEL; ++kt) {
        wait_vm<(NST - 2 - LAG) * LPS + X>();
        __builtin_amdgcn_s_barrier();
        stage(kt + NST, kt % NST);
      }
    };
    if (HAS_CS && wid == 0) head(std::integral_constant<int, XE>{});
    else head(std::integral_constant<int, XE - NCS>{});
    int buf = NPEEL % NST;
    for (int kt = NPEEL; kt < NT - NST; ++kt) {
      wait_vm<(NST - 2 - LAG) * LPS>();
      __builtin_amdgcn_s_barrier();
      stage(kt + NST, buf);
      buf = buf + 1 == NST ? 0 : buf + 1;
    }
    asm volatile("" ::: "memory");
#pragma unroll
    for (int r = 0; r < NST - 1; ++r) {
      constexpr int NSL = NST - LAG;
      if (r == 0) wait_vm<(NSL - 2) * LPS + XL>();
      else if (r == 1) wait_vm<(NSL > 3 ? NSL - 3 : 0) * LPS + XL>();
      else if (r == 2) wait_vm<(NSL > 4 ? NSL - 4 : 0) * LPS + XL>();
      else if (r == 3) wait_vm<(NSL > 5 ? NSL - 5 : 0) * LPS + XL>();
      else wait_vm<XL>();
      __builtin_amdgcn_s_barrier();
    }
    if constexpr (HAS_ST) {
#pragma unroll
      for (int i = 0; i < 4; ++i) asm volatile("" : "+v"(stp[i]));
      float sm = 0.f;
#pragma unroll
      for (int i = 0; i < 4; ++i) sm += stp[i][0] + stp[i][2];
      sm += __shfl_xor(sm, 1);
      float mean = sm * (1.0f / 1024.f);
      float m2 = 0.f;
#pragma unroll
      for (int i = 0; i < 4; ++i) {
        float d0 = stp[i][0] * (1.0f / 64.f) - mean, d1 = stp[i][2] * (1.0f / 64.f) - mean;
        m2 += stp[i][1] + stp[i][3] + 64.f * (d0 * d0 + d1 * d1);
      }
      m2 += __shfl_xor(m2, 1);
      float var = m2 * (1.0f / 1024.f);
      float r1_ = 1.0f / sqrtf(var + 1e-5f);
      float sc = r1_;
      if constexpr (EPI == EPI_W1) sc = r1_ / sqrtf(var * r1_ * r1_ + 1e-5f);
      if ((lt & 1) == 0) rs[lt >> 1] = (f32x4){mean, sc, r1_, 0.f};
    }
    if constexpr (HAS_CS) {
      if (lt < BN / 4) {
#pragma unroll
        for (int i = 0; i < NCS; ++i) asm volatile("" : "+v"(csp[i]));
#pragma unroll
        for (int i = NCS / 2; i > 0; i >>= 1)
#pragma unroll
          for (int j = 0; j < i; ++j) csp[j] += csp[j + i];
        *reinterpret_cast<f32x4*>(smem + CS_OFF + lt * 16) = csp[0];
      }
    }
    if constexpr (EPI == EPI_QKV) *reinterpret_cast<u32x4*>(smem + WP_OFF + lt * 16) = wpx;
  } else {
    ldres();
    const int q = l15 >> 2, p = lane & 3;
    const int fB = (q >> 1) | ((g & 1) << 1);
    unsigned aoff[2], boff[2], hoff = 0;
#pragma unroll
    for (int ks = 0; ks < 2; ++ks) aoff[ks] = (wm * 64 + l15) * 128 + (((4 * ks + g) ^ (l15 >> 1)) * 16);
#pragma unroll
    for (int u = 0; u < 2; ++u) boff[u] = A_BYTES + (8 * g + q) * 128 + (((2 * wn + u) ^ fB) * 32) + p * 8;
    if constexpr (HALF) hoff = A_BYTES + NFULL * 8192 + (8 * g + q) * 64 + ((wn ^ (g & 1)) * 32) + p * 8;

    auto ldf = [&](int i, const char* s, int ks, u32x4(&af)[4], u32x4(&bfr)[NTW]) {
      if (i == 0 || i > NTW) {
        const int mt = i == 0 ? 0 : i - NTW;
        af[mt] = *reinterpret_cast<const u32x4*>(s + aoff[ks] + mt * 2048);
      } else if (i - 1 < 2 * NFULL) {
        const int b = (i - 1) >> 1, u = (i - 1) & 1;
        s16x4 lo = __builtin_amdgcn_ds_read_tr16_b64_v4i16((lds_s16x4_p)(s + boff[u] + b * 8192 + ks * 4096));
        s16x4 hi = __builtin_amdgcn_ds_read_tr16_b64_v4i16((lds_s16x4_p)(s + boff[u] + b * 8192 + ks * 4096 + 512));
        bfr[i - 1] = __builtin_shufflevector(__builtin_bit_cast(u32x2, lo), __builtin_bit_cast(u32x2, hi), 0, 1, 2, 3);
      } else {
        s16x4 lo = __builtin_amdgcn_ds_read_tr16_b64_v4i16((lds_s16x4_p)(s + hoff + ks * 2048));
        s16x4 hi = __builtin_amdgcn_ds_read_tr16_b64_v4i16((lds_s16x4_p)(s + hoff + ks * 2048 + 256));
        bfr[NTW - 1] = __builtin_shufflevector(__builtin_bit_cast(u32x2, lo), __builtin_bit_cast(u32x2, hi), 0, 1, 2, 3);
      }
    };
    constexpr int NM = 4 * NTW, NL = NTW + 4;
    auto phase = [&](u32x4(&afc)[4], u32x4(&bfc)[NTW], u32x4(&afn)[4], u32x4(&bfn)[NTW], int nbuf, int nks, auto rd, auto bar) {
      const char* s = smem + nbuf * STAGE;
      constexpr int BAR = decltype(bar)::value, R0 = BAR < 0 ? 0 : BAR + 1;
      constexpr int RSP = (NM - R0 - 2) / NL > 0 ? (NM - R0 - 2) / NL : 1;
      static_assert(R0 + (NL - 1) * RSP < NM, "reads");
#pragma unroll
      for (int mt = 0; mt < 4; ++mt)
#pragma unroll
        for (int j = 0; j < NTW; ++j) {
          const int m = mt * NTW + j;
          if (m == BAR) {
            WAIT_L0();
            __builtin_amdgcn_s_barrier();
            asm volatile("" ::: "memory");
          }
          if (decltype(rd)::value && m >= R0 && (m - R0) % RSP == 0 && (m - R0) / RSP < NL) ldf((m - R0) / RSP, s, nks, afn, bfn);
          if (EPI == EPI_QKV && j >= 4) mfma_acc(acc[mt][j], afc[mt], bfc[j]);
          else mfma_acc(acc[mt][j], bfc[j], afc[mt]);
          __builtin_amdgcn_sched_barrier(0);
        }
    };
    typedef std::integral_constant<int, -1> nobar;
#pragma unroll
    for (int mt = 0; mt < 4; ++mt)
#pragma unroll
      for (int j = 0; j < NTW; ++j) {
        acc[mt][j] = (f32x4){0.f, 0.f, 0.f, 0.f};
        asm volatile("" : "+a"(acc[mt][j]));
      }
    u32x4 af0[4], af1[4], bf0[NTW], bf1[NTW];
    __builtin_amdgcn_s_barrier();
    asm volatile("" ::: "memory");
    STAMP(1);
#pragma unroll
    for (int i = 0; i < NL; ++i) { ldf(i, smem, 0, af0, bf0); __builtin_amdgcn_sched_barrier(0); }
    int buf = 0;
    if constexpr (LAG) {
      phase(af0, bf0, af1, bf1, 0, 1, std::true_type{}, nobar{});
      for (int kt = 0; kt < NT - 1; ++kt) {
        buf = buf + 1 == NST ? 0 : buf + 1;
        phase(af1, bf1, af0, bf0, buf, 0, std::true_type{}, nobar{});
        asm volatile("" ::: "memory");
        __builtin_amdgcn_s_barrier();
        asm volatile("" ::: "memory");
        phase(af0, bf0, af1, bf1, buf, 1, std::true_type{}, nobar{});
      }
      phase(af1, bf1, af0, bf0, buf, 0, std::false_type{}, nobar{});
    } else {
      for (int kt = 0; kt < NT - 1; ++kt) {
        phase(af0, bf0, af1, bf1, buf, 1, std::true_type{}, nobar{});
        buf = buf + 1 == NST ? 0 : buf + 1;
        phase(af1, bf1, af0, bf0, buf, 0, std::true_type{}, std::integral_constant<int, 4>{});
      }
      phase(af0, bf0, af1, bf1, buf, 1, std::true_type{}, nobar{});
      phase(af1, bf1, af0, bf0, buf, 0, std::false_type{}, nobar{});
    }
    asm volatile("s_nop 7\n\ts_nop 7\n\ts_nop 7" ::: "memory");
    if constexpr (EPI == EPI_QKV) {
#pragma unroll
      for (int mt = 0; mt < 4; ++mt)
#pragma unroll
        for (int t = 0; t < 2; ++t) *reinterpret_cast<f32x4*>(smem + QX_OFF + wid * 8192 + (mt * 2 + t) * 1024 + lane * 16) = acc[mt][t];
    }
  }
  WAIT_L0();
  __builtin_amdgcn_s_barrier();
  asm volatile("" ::: "memory");
  STAMP(2);

  if constexpr (EPI == EPI_QKV) {
    const int h = tn * 2 + wn;
    const int q = l15 >> 2, p = lane & 3;
    f32x4 kv[4][2], zz[4];
    float zsel = 0.f;
    float* red = reinterpret_cast<float*>(smem + 4 * 64 * 144) + wn * (64 * 32 + 64);
    bf16x8 wf[2];
#pragma unroll
    for (int ft = 0; ft < 2; ++ft) {
      const char* src = smem + WP_OFF + wn * 2048 + (4 * g + q) * 64 + ft * 32 + p * 8;
      s16x4 lo = __builtin_amdgcn_ds_read_tr16_b64_v4i16((lds_s16x4_p)(src));
      s16x4 hi = __builtin_amdgcn_ds_read_tr16_b64_v4i16((lds_s16x4_p)(src + 16 * 64));
      wf[ft] = __builtin_shufflevector(lo, hi, 0, 1, 2, 3, 4, 5, 6, 7);
    }
    const float* csl = reinterpret_cast<const float*>(smem + CS_OFF);
    auto feat = [&](const bf16x8& xf, u32x2 (&o)[4]) {
      const float L2E = 1.4426950408889634f;
      f32x4 z4 = (f32x4){0.f, 0.f, 0.f, 0.f};
      f32x4 p0 = __builtin_amdgcn_mfma_f32_16x16x32_bf16(wf[0], xf, z4, 0, 0, 0);
      f32x4 p1 = __builtin_amdgcn_mfma_f32_16x16x32_bf16(wf[1], xf, z4, 0, 0, 0);
      f32x2 v[4];
      {
        const unsigned r0_ = pk2(p0[0], p0[1]), r1_ = pk2(p0[2], p0[3]), r2_ = pk2(p1[0], p1[1]), r3_ = pk2(p1[2], p1[3]);
        v[0] = (f32x2){bflo(r0_), bfhi(r0_)}; v[1] = (f32x2){bflo(r1_), bfhi(r1_)};
        v[2] = (f32x2){bflo(r2_), bfhi(r2_)}; v[3] = (f32x2){bflo(r3_), bfhi(r3_)};
      }
      float mx = max2_(max3_(max3_(v[0][0], v[0][1], v[1][0]), v[1][1], v[2][0]), max3_(v[2][1], v[3][0], v[3][1]));
      float mn = min2_(min3_(min3_(v[0][0], v[0][1], v[1][0]), v[1][1], v[2][0]), min3_(v[2][1], v[3][0], v[3][1]));
      mx = rmax4(mx); mn = rmin4(mn);
      const float mxl = -mx * L2E, mnl = mn * L2E;
      f32x2 ep[4], en[4];
#pragma unroll
      for (int i = 0; i < 4; ++i) {
        const f32x2 ap = v[i] * L2E + mxl, an = v[i] * (-L2E) + mnl;
        ep[i] = (f32x2){__builtin_amdgcn_exp2f(ap[0]), __builtin_amdgcn_exp2f(ap[1])};
        en[i] = (f32x2){__builtin_amdgcn_exp2f(an[0]), __builtin_amdgcn_exp2f(an[1])};
      }
      const f32x2 sp2 = (ep[0] + ep[1]) + (ep[2] + ep[3]), sn2 = (en[0] + en[1]) + (en[2] + en[3]);
      const float ip = __builtin_amdgcn_rcpf(rsum4(sp2[0] + sp2[1])), in_ = __builtin_amdgcn_rcpf(rsum4(sn2[0] + sn2[1]));
      o[0] = (u32x2){pk2v(ep[0] * ip), pk2v(ep[1] * ip)};
      o[1] = (u32x2){pk2v(ep[2] * ip), pk2v(ep[3] * ip)};
      o[2] = (u32x2){pk2v(en[0] * in_), pk2v(en[1] * in_)};
      o[3] = (u32x2){pk2v(en[2] * in_), pk2v(en[3] * in_)};
    };
    if (wid8 >= 4) {
      f32x4 cs4[2];
#pragma unroll
      for (int t = 0; t < 2; ++t) cs4[t] = *reinterpret_cast<const f32x4*>(csl + wn * 32 + t * 16 + 4 * g);
      bf16x8 qf[4];
#pragma unroll
      for (int mt = 0; mt < 4; ++mt) {
        f32x4 st = rs[wm * 64 + mt * 16 + l15];
        unsigned pq_[4];
#pragma unroll
        for (int t = 0; t < 2; ++t) {
          const f32x4 ac = *reinterpret_cast<const f32x4*>(smem + QX_OFF + wid * 8192 + (mt * 2 + t) * 1024 + lane * 16);
          float a[4];
#pragma unroll
          for (int i = 0; i < 4; ++i) a[i] = st[1] * (ac[i] - st[0] * cs4[t][i]);
          pq_[t * 2] = pk2(a[0], a[1]); pq_[t * 2 + 1] = pk2(a[2], a[3]);
        }
        qf[mt] = __builtin_bit_cast(bf16x8, (u32x4){pq_[0], pq_[1], pq_[2], pq_[3]});
      }
#pragma unroll
      for (int mt = 0; mt < 4; ++mt) {
        u32x2 o[4];
        feat(qf[mt], o);
        u32x4* dst = reinterpret_cast<u32x4*>(out) + ((size_t)(tm * 8 + wm * 4 + mt) * 32 + h) * 128 + lane;
        wt_store((u32x4){o[0][0], o[0][1], o[1][0], o[1][1]}, dst);
        wt_store((u32x4){o[2][0], o[2][1], o[3][0], o[3][1]}, dst + 64);
      }
    } else {
      f32x4 cs4[2];
      float csv[2];
#pragma unroll
      for (int t = 0; t < 2; ++t) {
        cs4[t] = *reinterpret_cast<const f32x4*>(csl + 64 + wn * 32 + t * 16 + 4 * g);
        csv[t] = csl[128 + wn * 32 + t * 16 + l15];
      }
      bf16x8 kf[4], vf[2][2];
#pragma unroll
      for (int mt = 0; mt < 4; ++mt) {
        f32x4 st = rs[wm * 64 + mt * 16 + l15];
        unsigned pk_[4];
#pragma unroll
        for (int t = 0; t < 2; ++t) {
          float b[4];
#pragma unroll
          for (int i = 0; i < 4; ++i) b[i] = st[1] * (acc[mt][2 + t][i] - st[0] * cs4[t][i]);
          pk_[t * 2] = pk2(b[0], b[1]); pk_[t * 2 + 1] = pk2(b[2], b[3]);
        }
        kf[mt] = __builtin_bit_cast(bf16x8, (u32x4){pk_[0], pk_[1], pk_[2], pk_[3]});
      }
      char* pkl = smem + wid * (64 * 144);
#pragma unroll
      for (int mt = 0; mt < 4; ++mt) {
        u32x2 o[4];
        feat(kf[mt], o);
        char* dst = pkl + (mt * 16 + l15) * 144 + g * 8;
#pragma unroll
        for (int c = 0; c < 4; ++c) *reinterpret_cast<u32x2*>(dst + c * 32) = o[c];
      }
#pragma unroll
      for (int s = 0; s < 2; ++s) {
        unsigned pv[2][4];
#pragma unroll
        for (int m2 = 0; m2 < 2; ++m2) {
          int mt = s * 2 + m2;
          f32x4 s0 = rs[wm * 64 + mt * 16 + 4 * g + 0], s1 = rs[wm * 64 + mt * 16 + 4 * g + 1];
          f32x4 s2 = rs[wm * 64 + mt * 16 + 4 * g + 2], s3 = rs[wm * 64 + mt * 16 + 4 * g + 3];
#pragma unroll
          for (int t = 0; t < 2; ++t) {
            float a0 = s0[1] * (acc[mt][4 + t][0] - s0[0] * csv[t]);
            float a1 = s1[1] * (acc[mt][4 + t][1] - s1[0] * csv[t]);
            float a2 = s2[1] * (acc[mt][4 + t][2] - s2[0] * csv[t]);
            float a3 = s3[1] * (acc[mt][4 + t][3] - s3[0] * csv[t]);
            pv[t][m2 * 2] = pk2(a0, a1); pv[t][m2 * 2 + 1] = pk2(a2, a3);
          }
        }
#pragma unroll
        for (int t = 0; t < 2; ++t) vf[s][t] = __builtin_bit_cast(bf16x8, (u32x4){pv[t][0], pv[t][1], pv[t][2], pv[t][3]});
      }
      WAIT_L0();
      __builtin_amdgcn_sched_barrier(0);
#pragma unroll
      for (int ft = 0; ft < 4; ++ft) { kv[ft][0] = kv[ft][1] = zz[ft] = (f32x4){0.f, 0.f, 0.f, 0.f}; }
      const bf16x8 ones = (bf16x8){0x3F80, 0x3F80, 0x3F80, 0x3F80, 0x3F80, 0x3F80, 0x3F80, 0x3F80};
#pragma unroll
      for (int s = 0; s < 2; ++s)
#pragma unroll
        for (int ft = 0; ft < 4; ++ft) {
          const char* src = pkl + (32 * s + 4 * g + q) * 144 + ft * 32 + p * 8;
          s16x4 lo = __builtin_amdgcn_ds_read_tr16_b64_v4i16((lds_s16x4_p)(src));
          s16x4 hi = __builtin_amdgcn_ds_read_tr16_b64_v4i16((lds_s16x4_p)(src + 16 * 144));
          bf16x8 pf = __builtin_shufflevector(lo, hi, 0, 1, 2, 3, 4, 5, 6, 7);
          kv[ft][0] = __builtin_amdgcn_mfma_f32_16x16x32_bf16(vf[s][0], pf, kv[ft][0], 0, 0, 0);
          kv[ft][1] = __builtin_amdgcn_mfma_f32_16x16x32_bf16(vf[s][1], pf, kv[ft][1], 0, 0, 0);
          zz[ft] = __builtin_amdgcn_mfma_f32_16x16x32_bf16(ones, pf, zz[ft], 0, 0, 0);
        }
      zsel = g == 0 ? zz[0][0] : g == 1 ? zz[1][0] : g == 2 ? zz[2][0] : zz[3][0];
      if (wm == 1) {
#pragma unroll
        for (int ft = 0; ft < 4; ++ft)
#pragma unroll
          for (int dt = 0; dt < 2; ++dt) *reinterpret_cast<f32x4*>(red + ((ft * 2 + dt) * 64 + lane) * 4) = kv[ft][dt];
        red[64 * 32 + lane] = zsel;
      }
    }
    __syncthreads();
    if (wid8 < 2) {
      float* kd = kvp + (size_t)(h * 16 + tm) * 2048;
      float* zd = zp + (size_t)(h * 16 + tm) * 64;
#pragma unroll
      for (int ft = 0; ft < 4; ++ft)
#pragma unroll
        for (int dt = 0; dt < 2; ++dt) {
          f32x4 o = *reinterpret_cast<const f32x4*>(red + ((ft * 2 + dt) * 64 + lane) * 4);
          wt_store(o + kv[ft][dt], reinterpret_cast<f32x4*>(kd + ((ft * 2 + dt) * 64 + lane) * 4));
        }
      zd[lane] = red[64 * 32 + lane] + zsel;
    }
  } else {
    if (wid8 < 4) {
      f32x4 stm[4], cs4[NTW];
      if constexpr (EPI == EPI_W1 || EPI == EPI_W3) {
#pragma unroll
        for (int j = 0; j < NTW; ++j) {
          const int colj = (j < 2 * NFULL) ? ((j >> 1) * 64 + (2 * wn + (j & 1)) * 16) : (NFULL * 64 + wn * 16);
          cs4[j] = *reinterpret_cast<const f32x4*>(smem + CS_OFF + (colj + 4 * g) * 4);
        }
#pragma unroll
        for (int mt = 0; mt < 4; ++mt) stm[mt] = rs[wm * 64 + mt * 16 + l15];
      }
#pragma unroll
      for (int j = 0; j < NTW; ++j) {
        const int colj = (j < 2 * NFULL) ? ((j >> 1) * 64 + (2 * wn + (j & 1)) * 16) : (NFULL * 64 + wn * 16);
#pragma unroll
        for (int mt = 0; mt < 4; ++mt) {
          f32x2 v0 = (f32x2){acc[mt][j][0], acc[mt][j][1]}, v1 = (f32x2){acc[mt][j][2], acc[mt][j][3]};
          if constexpr (EPI == EPI_W1 || EPI == EPI_W3) {
            v0 = gelu2((v0 - stm[mt][0] * (f32x2){cs4[j][0], cs4[j][1]}) * stm[mt][1]);
            v1 = gelu2((v1 - stm[mt][0] * (f32x2){cs4[j][2], cs4[j][3]}) * stm[mt][1]);
          }
          *reinterpret_cast<u32x2*>(smem + (wm * 64 + mt * 16 + l15) * PB + (colj + 4 * g) * 2) = (u32x2){pk2(v0[0], v0[1]), pk2(v1[0], v1[1])};
        }
      }
    }
    __syncthreads();
#pragma unroll
    for (int it = 0; it < NIT; ++it) {
      const int c = tid + 512 * it, r = c / CH, ch = c % CH;
      u32x4 v = *reinterpret_cast<const u32x4*>(smem + r * PB + ch * 16);
      const int grow = row0 + r, gcol = tn * BN + ch * 8;
      if constexpr (EPI == EPI_W3) {
        wt_store(v, reinterpret_cast<u32x4*>(out + (size_t)grow * F_ + gcol));
      } else if constexpr (EPI == EPI_W1) {
        const int grp = tn >> 2;
        const int sh = grp == 0 ? 1 : grp == 1 ? -1 : grp == 2 ? 2 : -2;
        const int dr = grow + sh;
        if (dr >= 0 && dr < S_) wt_store(v, reinterpret_cast<u32x4*>(out + (size_t)dr * D_ + gcol));
        const int zr = grow - sh;
        if (zr < 0 || zr >= S_) *reinterpret_cast<u32x4*>(out + (size_t)grow * D_ + gcol) = (u32x4){0u, 0u, 0u, 0u};
      } else {
        float y[8];
        const u32x4 a = ra[it];
        if constexpr (EPI == EPI_WO) {
#pragma unroll
          for (int i = 0; i < 4; ++i) { y[2 * i] = rbf(bflo(a[i]) + bflo(v[i])); y[2 * i + 1] = rbf(bfhi(a[i]) + bfhi(v[i])); }
        } else if constexpr (EPI == EPI_W2) {
          f32x4 st = rs[r];
#pragma unroll
          for (int i = 0; i < 4; ++i) {
            y[2 * i] = rbf(rbf((bflo(a[i]) - st[0]) * st[2]) + bflo(v[i]));
            y[2 * i + 1] = rbf(rbf((bfhi(a[i]) - st[0]) * st[2]) + bfhi(v[i]));
          }
        } else {
          const u32x4 b = rb[it];
#pragma unroll
          for (int i = 0; i < 4; ++i) {
            y[2 * i] = rbf(bflo(a[i]) + rbf(bflo(b[i]) + bflo(v[i])));
            y[2 * i + 1] = rbf(bfhi(a[i]) + rbf(bfhi(b[i]) + bfhi(v[i])));
          }
        }
        wt_store((u32x4){pk2(y[0], y[1]), pk2(y[2], y[3]), pk2(y[4], y[5]), pk2(y[6], y[7])}, reinterpret_cast<u32x4*>(out + (size_t)grow * D_ + gcol));
        float sm = ((y[0] + y[1]) + (y[2] + y[3])) + ((y[4] + y[5]) + (y[6] + y[7]));
        sm = rsum8(sm);
        float mu = sm * (1.0f / 64.f), m2 = 0.f;
#pragma unroll
        for (int i = 0; i < 8; ++i) { float d = y[i] - mu; m2 += d * d; }
        m2 = rsum8(m2);
        if (ch == 0) st_out[(size_t)grow * 16 + tn] = (f32x2){sm, m2};
      }
    }
  }
  STAMP(3);
}

__global__ __launch_bounds__(256, 1) void attn_out_kernel(const u16* __restrict__ phiq, const float* __restrict__ kvp,
                                                          const float* __restrict__ zp, u16* __restrict__ o,
                                                          const float* __restrict__ cp1, const float* __restrict__ cp3,
                                                          float* __restrict__ cs1, float* __restrict__ cs3) {
  __shared__ __attribute__((aligned(16))) u16 kvT[48 * 72];
  const int tid = threadIdx.x, lane = tid & 63, wid = tid >> 6, g = lane >> 4, l15 = lane & 15;
  if (blockIdx.x >= 256) {
    const int i = (blockIdx.x - 256) * 256 + tid;
    const float* src = i < 1024 ? cp1 + i : cp3 + (i - 1024);
    const int ld = i < 1024 ? 1024 : F_;
    float c[16];
#pragma unroll
    for (int t = 0; t < 16; ++t) c[t] = src[t * ld];
#pragma unroll
    for (int t = 8; t > 0; t >>= 1)
#pragma unroll
      for (int j = 0; j < t; ++j) c[j] += c[j + t];
    *(i < 1024 ? cs1 + i : cs3 + (i - 1024)) = c[0];
    return;
  }
  const int h = blockIdx.x & 31, tg = blockIdx.x >> 5;
  f32x4 kvv[16][2];
  float zv[16];
  bf16x8 pq[4][2];
  {
    const float* src = kvp + (size_t)h * 16 * 2048 + tid * 8;
#pragma unroll
    for (int t = 0; t < 16; ++t) {
      kvv[t][0] = *reinterpret_cast<const f32x4*>(src + t * 2048);
      kvv[t][1] = *reinterpret_cast<const f32x4*>(src + t * 2048 + 4);
    }
#pragma unroll
    for (int t = 0; t < 16; ++t) zv[t] = zp[(size_t)(h * 16 + t) * 64 + lane];
#pragma unroll
    for (int mt = 0; mt < 4; ++mt) {
      const u16* ps = phiq + ((size_t)(tg * 16 + wid * 4 + mt) * 32 + h) * 1024 + lane * 8;
      pq[mt][0] = *reinterpret_cast<const bf16x8*>(ps);
      pq[mt][1] = *reinterpret_cast<const bf16x8*>(ps + 512);
    }
  }
  __builtin_amdgcn_sched_barrier(0);
  {
    for (int i = tid; i < 15 * 64; i += 256) kvT[(33 + i / 64) * 72 + (i & 63)] = 0;
    f32x4 a0 = kvv[0][0], a1 = kvv[0][1];
#pragma unroll
    for (int t = 1; t < 16; ++t) { a0 += kvv[t][0]; a1 += kvv[t][1]; }
    const int ft = tid >> 6, dt = (tid >> 5) & 1, ln = (2 * tid) & 63;
    const int d0 = dt * 16 + 4 * (ln >> 4), pos = (ft >> 1) * 32 + ((ln >> 2) & 3) * 8 + (ft & 1) * 4 + (ln & 3);
#pragma unroll
    for (int i = 0; i < 4; ++i) *reinterpret_cast<unsigned*>(&kvT[(d0 + i) * 72 + pos]) = pk2(a0[i], a1[i]);
    if (tid < 64) {
      float z = 0.f;
#pragma unroll
      for (int t = 0; t < 16; ++t) z += zv[t];
      kvT[32 * 72 + (tid >> 5) * 32 + ((tid >> 2) & 3) * 8 + ((tid >> 4) & 1) * 4 + (tid & 3)] = (u16)(pk2(z, 0.f) & 0xffff);
    }
  }
  __syncthreads();
  bf16x8 kf[3][2];
#pragma unroll
  for (int dt = 0; dt < 3; ++dt)
#pragma unroll
    for (int ks = 0; ks < 2; ++ks) kf[dt][ks] = *reinterpret_cast<const bf16x8*>(&kvT[(dt * 16 + l15) * 72 + ks * 32 + g * 8]);
#pragma unroll
  for (int mt = 0; mt < 4; ++mt) {
    const int tok = tg * 256 + wid * 64 + mt * 16 + l15;
    const bf16x8 p0 = pq[mt][0], p1 = pq[mt][1];
    f32x4 r[3];
#pragma unroll
    for (int dt = 0; dt < 3; ++dt) {
      r[dt] = (f32x4){0.f, 0.f, 0.f, 0.f};
      r[dt] = __builtin_amdgcn_mfma_f32_16x16x32_bf16(kf[dt][0], p0, r[dt], 0, 0, 0);
      r[dt] = __builtin_amdgcn_mfma_f32_16x16x32_bf16(kf[dt][1], p1, r[dt], 0, 0, 0);
    }
    float den = rbf(rbf(__shfl(r[2][0], l15)) + 1e-6f);
    float inv = 1.0f / den;
#pragma unroll
    for (int dt = 0; dt < 2; ++dt) {
      u32x2 w = (u32x2){pk2(rbf(r[dt][0]) * inv, rbf(r[dt][1]) * inv), pk2(rbf(r[dt][2]) * inv, rbf(r[dt][3]) * inv)};
      *reinterpret_cast<u32x2*>(o + (size_t)tok * D_ + h * 32 + dt * 16 + 4 * g) = w;
    }
  }
}

constexpr int WC_QKV = 0, WC_WO = WC_QKV + 2 * 1024 * 384, WC_W1 = WC_WO + 2 * 1024 * 128, WC_W2 = WC_W1 + 2 * 1024 * 128,
              WC_W4 = WC_W2 + 2 * 1024 * 128, WC_END = WC_W4 + 2 * F_ * 128;
__global__ __launch_bounds__(256) void prep_kernel(const u16* __restrict__ x, const u16* __restrict__ wqkv,
                                                   const u16* __restrict__ w1, const u16* __restrict__ w3,
                                                   float* __restrict__ cs_qkv, float* __restrict__ cs_w1,
                                                   float* __restrict__ cs_w3, f32x2* __restrict__ st,
                                                   const u16* __restrict__ wo, const u16* __restrict__ w2,
                                                   const u16* __restrict__ w4, char* __restrict__ wc) {
  __shared__ __attribute__((aligned(16))) float red[4][512];
  const int tid = threadIdx.x, b = blockIdx.x;
  if (b >= 368) {
    const int pb = b - 368;
    const u16* w = pb < 32 ? wo + (size_t)pb * 32768 : pb < 64 ? w2 + (size_t)(pb - 32) * 32768 : w4 + (size_t)(pb - 64) * 32768;
    u32x4 v[16];
#pragma unroll
    for (int r = 0; r < 16; ++r) v[r] = *reinterpret_cast<const u32x4*>(w + r * 2048 + tid * 8);
    if (((tid >> 3) & 7) == 3) {
      const int kd = pb < 64 ? 1024 : F_, row = (pb < 32 ? pb : pb < 64 ? pb - 32 : pb - 64) * 32 + (tid >> 7);
      char* dst = wc + (pb < 32 ? WC_WO : pb < 64 ? WC_W2 : WC_W4) + (size_t)((tid >> 6) & 1) * kd * 128 + (size_t)row * 128 + (tid & 7) * 16;
#pragma unroll
      for (int r = 0; r < 16; ++r) *reinterpret_cast<u32x4*>(dst + r * 256) = v[r];
    }
#pragma unroll
    for (int r = 0; r < 16; ++r) asm volatile("" ::"v"(v[r]));
  } else if (b < 240) {
    const u16* w; float* dst; int ld, ch, rg;
    if (b < 96) { w = wqkv; dst = cs_qkv; ld = 3072; ch = b % 6; rg = b / 6; }
    else if (b < 128) { w = w1; dst = cs_w1; ld = 1024; ch = (b - 96) & 1; rg = (b - 96) >> 1; }
    else { w = w3; dst = cs_w3; ld = F_; ch = (b - 128) % 7; rg = (b - 128) / 7; }
    const int lane = tid & 63, wid = tid >> 6, row = rg * 64 + wid * 16;
    u32x4 v[16];
#pragma unroll
    for (int r = 0; r < 16; ++r) v[r] = *reinterpret_cast<const u32x4*>(w + (size_t)(row + r) * ld + ch * 512 + lane * 8);
    if (b < 128 && (lane >> 3) == 3) {
      const int cpp = b < 96 ? 384 : 128;
      char* cpd = wc + (b < 96 ? WC_QKV + (ch & 1) * (1024 * 384) + (ch >> 1) * 128 : WC_W1 + ch * (1024 * 128)) + row * cpp + (lane & 7) * 16;
#pragma unroll
      for (int r = 0; r < 16; ++r) *reinterpret_cast<u32x4*>(cpd + r * cpp) = v[r];
    }
    f32x4 s0 = (f32x4){0.f, 0.f, 0.f, 0.f}, s1 = s0;
#pragma unroll
    for (int r = 0; r < 16; ++r) {
      s0 += (f32x4){bflo(v[r][0]), bfhi(v[r][0]), bflo(v[r][1]), bfhi(v[r][1])};
      s1 += (f32x4){bflo(v[r][2]), bfhi(v[r][2]), bflo(v[r][3]), bfhi(v[r][3])};
    }
    *reinterpret_cast<f32x4*>(&red[wid][lane * 8]) = s0;
    *reinterpret_cast<f32x4*>(&red[wid][lane * 8 + 4]) = s1;
    __syncthreads();
    f32x2 t = *reinterpret_cast<const f32x2*>(&red[0][tid * 2]);
#pragma unroll
    for (int i = 1; i < 4; ++i) t += *reinterpret_cast<const f32x2*>(&red[i][tid * 2]);
    *reinterpret_cast<f32x2*>(dst + rg * ld + ch * 512 + tid * 2) = t;
  } else {
    const int lane = tid & 63, wid = tid >> 6;
    const int rbase = (b - 240) * 16 + wid * 4;
    u32x4 xv[8];
#pragma unroll
    for (int i = 0; i < 8; ++i) xv[i] = *reinterpret_cast<const u32x4*>(x + (size_t)rbase * D_ + i * 512 + lane * 8);
    __builtin_amdgcn_sched_barrier(0);
#pragma unroll
    for (int rr = 0; rr < 4; ++rr) {
      const int row = rbase + rr;
#pragma unroll
      for (int hf = 0; hf < 2; ++hf) {
        const u32x4 v = xv[rr * 2 + hf];
        float y[8];
#pragma unroll
        for (int i = 0; i < 4; ++i) { y[2 * i] = bflo(v[i]); y[2 * i + 1] = bfhi(v[i]); }
        float sm = ((y[0] + y[1]) + (y[2] + y[3])) + ((y[4] + y[5]) + (y[6] + y[7]));
        sm = rsum8(sm);
        float mu = sm * (1.0f / 64.f), m2 = 0.f;
#pragma unroll
        for (int i = 0; i < 8; ++i) { float d = y[i] - mu; m2 += d * d; }
        m2 = rsum8(m2);
        if ((lane & 7) == 0) st[(size_t)row * 16 + hf * 8 + (lane >> 3)] = (f32x2){sm, m2};
      }
    }
  }
}

extern "C" void kernel_launch(void* const* d_in, const int* in_sizes, int n_in, void* d_out, int out_size, void* d_ws,
                              size_t ws_size, hipStream_t stream) {
  const u16* x_in = (const u16*)d_in[0];
  const u16* wqkv = (const u16*)d_in[1];
  const u16* wphi = (const u16*)d_in[2];
  const u16* wo = (const u16*)d_in[3];
  const u16* w1 = (const u16*)d_in[5];
  const u16* w2 = (const u16*)d_in[6];
  const u16* w3 = (const u16*)d_in[7];
  const u16* w4 = (const u16*)d_in[8];
  char* ws = (char*)d_ws;
  size_t off = 0;
  auto alloc = [&](size_t bytes) { char* p = ws + off; off += (bytes + 4095) & ~(size_t)4095; return p; };
  u16* xb = (u16*)alloc((size_t)S_ * D_ * 2);
  u16* x2 = (u16*)alloc((size_t)S_ * D_ * 2);
  u16* x1 = (u16*)alloc((size_t)S_ * D_ * 2);
  u16* hs = (u16*)alloc((size_t)S_ * D_ * 2);
  u16* ob = (u16*)alloc((size_t)S_ * D_ * 2);
  u16* gb = (u16*)alloc((size_t)S_ * F_ * 2);
  u16* phiq = (u16*)alloc((size_t)S_ * 2048 * 2);
  float* kvp = (float*)alloc((size_t)32 * 16 * 2048 * 4);
  float* zp = (float*)alloc((size_t)32 * 16 * 64 * 4);
  f32x2* st_x = (f32x2*)alloc((size_t)S_ * 16 * 8);
  f32x2* st_x2 = (f32x2*)alloc((size_t)S_ * 16 * 8);
  f32x2* st_x1 = (f32x2*)alloc((size_t)S_ * 16 * 8);
  float* cs_qkv = (float*)alloc(16 * 3072 * 4);
  float* cs_w1 = (float*)alloc(16 * 1024 * 4);
  float* cs_w3 = (float*)alloc(16 * F_ * 4);
  float* cf_w1 = (float*)alloc(1024 * 4);
  float* cf_w3 = (float*)alloc(F_ * 4);
  char* wc = alloc(WC_END);

  prep_kernel<<<368 + 176, 256, 0, stream>>>(x_in, wqkv, w1, w3, cs_qkv, cs_w1, cs_w3, st_x, wo, w2, w4, wc);
  for (int layer = 0; layer < 2; ++layer) {
    const u16* x = layer == 0 ? x_in : xb;
    u16* xo = layer == 0 ? xb : (u16*)d_out;
    gemm_kernel<192, 3, 1024, 1024, 3072, EPI_QKV><<<256, 512, 0, stream>>>(x, wqkv, (const u16*)(wc + WC_QKV), cs_qkv, st_x, nullptr, nullptr, phiq, nullptr, wphi, kvp, zp);
    attn_out_kernel<<<layer == 0 ? 256 + 18 : 256, 256, 0, stream>>>(phiq, kvp, zp, ob, cs_w1, cs_w3, cf_w1, cf_w3);
    gemm_kernel<64, 5, 1024, 1024, 1024, EPI_WO><<<256, 512, 0, stream>>>(ob, wo, (const u16*)(wc + WC_WO), nullptr, nullptr, x, nullptr, x2, st_x2, nullptr, nullptr, nullptr);
    gemm_kernel<64, 5, 1024, 1024, 1024, EPI_W1><<<256, 512, 0, stream>>>(x2, w1, (const u16*)(wc + WC_W1), cf_w1, st_x2, nullptr, nullptr, hs, nullptr, nullptr, nullptr, nullptr);
    gemm_kernel<64, 5, 1024, 1024, 1024, EPI_W2><<<256, 512, 0, stream>>>(hs, w2, (const u16*)(wc + WC_W2), nullptr, st_x2, x2, nullptr, x1, st_x1, nullptr, nullptr, nullptr);
    gemm_kernel<224, 3, 1024, 1024, F_, EPI_W3><<<256, 512, 0, stream>>>(x1, w3, nullptr, cf_w3, st_x1, nullptr, nullptr, gb, nullptr, nullptr, nullptr, nullptr);
    gemm_kernel<64, 5, F_, F_, 1024, EPI_W4><<<256, 512, 0, stream>>>(gb, w4, (const u16*)(wc + WC_W4), nullptr, nullptr, x2, x1, xo, st_x, nullptr, nullptr, nullptr);
  }
}
```

```cpp
#include <hip/hip_runtime.h>
#include <hip/hip_bf16.h>
#include <stdint.h>
#include <type_traits>

typedef short bf16x8 __attribute__((ext_vector_type(8)));
typedef short s16x4 __attribute__((ext_vector_type(4)));
typedef float f32x4 __attribute__((ext_vector_type(4)));
typedef float f32x2 __attribute__((ext_vector_type(2)));
typedef unsigned u32x2 __attribute__((ext_vector_type(2)));
typedef unsigned u32x4 __attribute__((ext_vector_type(4)));
typedef __attribute__((address_space(3))) s16x4* lds_s16x4_p;
typedef unsigned short u16;

constexpr int S_ = 2048, D_ = 1024, H_ = 32, F_ = 3584;

__device__ __forceinline__ float bf2f(u16 v) { return __uint_as_float(((unsigned)v) << 16); }
__device__ __forceinline__ float bflo(unsigned v) { return __uint_as_float(v << 16); }
__device__ __forceinline__ float bfhi(unsigned v) { return __uint_as_float(v & 0xffff0000u); }
typedef __bf16 bf16x2_t __attribute__((ext_vector_type(2)));
__device__ __forceinline__ unsigned pk2(float a, float b) {
  return __builtin_bit_cast(unsigned, __builtin_convertvector((f32x2){a, b}, bf16x2_t));
}
__device__ __forceinline__ unsigned pk2v(f32x2 v) { return __builtin_bit_cast(unsigned, __builtin_convertvector(v, bf16x2_t)); }
__device__ __forceinline__ float max2_(float a, float b) { float r; asm("v_max_f32 %0, %1, %2" : "=v"(r) : "v"(a), "v"(b)); return r; }
__device__ __forceinline__ float min2_(float a, float b) { float r; asm("v_min_f32 %0, %1, %2" : "=v"(r) : "v"(a), "v"(b)); return r; }
__device__ __forceinline__ float max3_(float a, float b, float c) { float r; asm("v_max3_f32 %0, %1, %2, %3" : "=v"(r) : "v"(a), "v"(b), "v"(c)); return r; }
__device__ __forceinline__ float min3_(float a, float b, float c) { float r; asm("v_min3_f32 %0, %1, %2, %3" : "=v"(r) : "v"(a), "v"(b), "v"(c)); return r; }
__device__ __forceinline__ float rbf(float a) { return bflo(pk2(a, 0.f)); }
__device__ __forceinline__ float gelu_t(float x) {
  const float c1 = -2.0f * 0.7978845608028654f * 1.4426950408889634f;
  const float c2 = c1 * 0.044715f;
  float t = x * x;
  float w = x * __builtin_fmaf(t, c2, c1);
  float e = __builtin_amdgcn_exp2f(w);
  return x * __builtin_amdgcn_rcpf(1.0f + e);
}

__device__ __forceinline__ f32x2 gelu2(f32x2 x) {
  const float c1 = -2.0f * 0.7978845608028654f * 1.4426950408889634f;
  const float c2 = c1 * 0.044715f;
  f32x2 t = x * x;
  f32x2 w = x * (t * c2 + c1);
  f32x2 d = (f32x2){__builtin_amdgcn_exp2f(w[0]), __builtin_amdgcn_exp2f(w[1])} + 1.0f;
  return x * (f32x2){__builtin_amdgcn_rcpf(d[0]), __builtin_amdgcn_rcpf(d[1])};
}
__device__ __forceinline__ void x16(float v, float& a, float& b) {
  auto r = __builtin_amdgcn_permlane16_swap(__float_as_uint(v), __float_as_uint(v), false, false);
  a = __uint_as_float(r[0]); b = __uint_as_float(r[1]);
}
__device__ __forceinline__ void x32(float v, float& a, float& b) {
  auto r = __builtin_amdgcn_permlane32_swap(__float_as_uint(v), __float_as_uint(v), false, false);
  a = __uint_as_float(r[0]); b = __uint_as_float(r[1]);
}
__device__ __forceinline__ float rmax4(float v) { float a, b; x16(v, a, b); v = max2_(a, b); x32(v, a, b); return max2_(a, b); }
__device__ __forceinline__ float rmin4(float v) { float a, b; x16(v, a, b); v = min2_(a, b); x32(v, a, b); return min2_(a, b); }
__device__ __forceinline__ float rsum4(float v) { float a, b; x16(v, a, b); v = a + b; x32(v, a, b); return a + b; }

__device__ __forceinline__ float rsum8(float v) {
  v += __uint_as_float(__builtin_amdgcn_mov_dpp(__float_as_uint(v), 0xB1, 0xF, 0xF, true));
  v += __uint_as_float(__builtin_amdgcn_mov_dpp(__float_as_uint(v), 0x4E, 0xF, 0xF, true));
  v += __uint_as_float(__builtin_amdgcn_mov_dpp(__float_as_uint(v), 0x141, 0xF, 0xF, true));
  return v;
}

__device__ __forceinline__ void wt_store(u32x4 v, u32x4* p) { asm volatile("global_store_dwordx4 %0, %1, off sc1\n\ts_nop 1" :: "v"(p), "v"(v) : "memory"); }
__device__ __forceinline__ void wt_store(f32x4 v, f32x4* p) { asm volatile("global_store_dwordx4 %0, %1, off sc1\n\ts_nop 1" :: "v"(p), "v"(v) : "memory"); }
__device__ __forceinline__ void wt_store(u32x2 v, u32x2* p) { asm volatile("global_store_dwordx2 %0, %1, off sc1\n\ts_nop 1" :: "v"(p), "v"(v) : "memory"); }
#define WAIT_V(n) asm volatile("s_waitcnt vmcnt(" #n ")" ::: "memory")
#define WAIT_L0() asm volatile("s_waitcnt lgkmcnt(0)" ::: "memory")
template <int N> __device__ __forceinline__ void wait_vm() {
  asm volatile("s_waitcnt vmcnt(%0)" ::"i"(N) : "memory");
}

__device__ __forceinline__ void glds16(unsigned voff, const void* sbase, unsigned lds) {
  unsigned keep;
  asm volatile("s_mov_b32 %0, m0\n\ts_mov_b32 m0, %3\n\ts_nop 0\n\tglobal_load_lds_dwordx4 %1, %2\n\ts_mov_b32 m0, %0"
               : "=&s"(keep) : "v"(voff), "s"(sbase), "s"(lds) : "memory");
}

__device__ __forceinline__ void mfma_acc(f32x4& c, const u32x4& a, const u32x4& b) {
  asm volatile("v_mfma_f32_16x16x32_bf16 %0, %1, %2, %0" : "+a"(c) : "v"(a), "v"(b));
}

#define STAMP(i)

#define XPOS 2
enum { EPI_QKV = 0, EPI_WO = 1, EPI_W1 = 2, EPI_W2 = 3, EPI_W3 = 4, EPI_W4 = 5 };

template <int BN, int NST, int KD, int LDA, int LDB, int EPI>
__global__ __launch_bounds__(512, 2) void gemm_kernel(
    const u16* __restrict__ A, const u16* __restrict__ W,
    const u16* __restrict__ Wc,
    const float* __restrict__ cs,
    const f32x2* __restrict__ st_in,
    const u16* __restrict__ r0,
    const u16* __restrict__ r1,
    u16* __restrict__ out,
    f32x2* __restrict__ st_out,
    const u16* __restrict__ wphi,
    float* __restrict__ kvp,
    float* __restrict__ zp)
{
  constexpr int NFULL = BN / 64, HALF = (BN % 64) / 32;
  constexpr int A_BYTES = 128 * 64 * 2, B_BYTES = BN * 64 * 2, STAGE = A_BYTES + B_BYTES;
  constexpr int NT = KD / 64, NTW = 2 * NFULL + HALF, LPS = 4 + 2 * NFULL + HALF;
  constexpr int PB = BN * 2 + 16;
  constexpr int EPI_BYTES = (EPI == EPI_QKV) ? (4 * 64 * 144 + 2 * (64 * 32 + 64) * 4) : 128 * PB;
  constexpr int MAINB = NST * STAGE;
  constexpr int RS_OFF = (MAINB > EPI_BYTES ? MAINB : EPI_BYTES);
  constexpr int CS_OFF = RS_OFF + 128 * 16;
  constexpr int WP_OFF = CS_OFF + 1024;
  constexpr int QX_OFF = WP_OFF + 4096;
  constexpr int SMEM = WP_OFF + (EPI == EPI_QKV ? 4096 + 32768 : 0);
  constexpr bool HAS_CS = (EPI == EPI_QKV || EPI == EPI_W1 || EPI == EPI_W3);
  constexpr bool HAS_ST = (EPI == EPI_QKV || EPI == EPI_W1 || EPI == EPI_W3 || EPI == EPI_W2);
  constexpr bool HAS_RES = (EPI == EPI_WO || EPI == EPI_W2 || EPI == EPI_W4);
  constexpr int LAG = (BN == 64) ? 1 : 0;
  constexpr int CH = BN / 8, NIT = CH / 4;
  static_assert(SMEM <= 163840, "LDS");
  __shared__ __attribute__((aligned(16))) char smem[SMEM];

  STAMP(0);
  const int tid = threadIdx.x, lane = tid & 63;
  const int wid8 = __builtin_amdgcn_readfirstlane(tid >> 6), wid = wid8 & 3, wm = wid >> 1, wn = wid & 1;
  const int g = lane >> 4, l15 = lane & 15;
  const int xcd = blockIdx.x & 7, idx = blockIdx.x >> 3;
  const int tm = (xcd >> 1) * 4 + (idx & 3), tn = (xcd & 1) * 8 + (idx >> 2);
  const int row0 = tm * 128;
  f32x4* rs = reinterpret_cast<f32x4*>(smem + RS_OFF);

  f32x4 acc[4][NTW];
  u32x4 ra[HAS_RES ? NIT : 1], rb[(EPI == EPI_W4) ? NIT : 1];

  unsigned rof[HAS_RES ? NIT : 1];
  if constexpr (HAS_RES) {
#pragma unroll
    for (int it = 0; it < NIT; ++it) {
      const int c = tid + 512 * it, r = c / CH, ch = c % CH;
      rof[it] = (unsigned)((row0 + r) * D_ + tn * BN + ch * 8) * 2u;
      asm volatile("" : "+v"(rof[it]));
    }
  }
  auto ldres = [&]() {
    if constexpr (HAS_RES) {
#pragma unroll
      for (int it = 0; it < NIT; ++it) {
        ra[it] = *reinterpret_cast<const u32x4*>(reinterpret_cast<const char*>(r0) + rof[it]);
        if constexpr (EPI == EPI_W4) rb[it] = *reinterpret_cast<const u32x4*>(reinterpret_cast<const char*>(r1) + rof[it]);
      }
    }
  };

  if (wid8 >= 4) {
    unsigned voA[4], voB[NFULL > 0 ? NFULL : 1][2], voH = 0;
    const bool pk_ = (HALF == 0) && ((tn & 7) == 3);
    const unsigned pitchB = pk_ ? NFULL * 128 : LDB * 2;
#pragma unroll
    for (int i = 0; i < 4; ++i) {
      int r = (wid * 4 + i) * 8 + (lane >> 3);
      int ch = (lane & 7) ^ ((r >> 1) & 7);
      voA[i] = (unsigned)(row0 + r) * (LDA * 2) + ch * 16;
      if constexpr (EPI == EPI_WO) voA[i] = (unsigned)((ch >> 2) * S_ + row0 + r) * 64 + (ch & 3) * 16;
    }
#pragma unroll
    for (int b = 0; b < NFULL; ++b) {
      int colb = (EPI == EPI_QKV) ? (b * 1024 + tn * 64) : (tn * BN + b * 64);
#pragma unroll
      for (int j = 0; j < 2; ++j) {
        int r = (wid * 2 + j) * 8 + (lane >> 3);
        int f = ((r >> 1) & 1) | (((r >> 3) & 1) << 1);
        int chp = lane & 7;
        int gch = (((chp >> 1) ^ f) << 1) | (chp & 1);
        const int rs_ = (EPI == EPI_WO) ? ((r & 32) | ((r & 4) << 2) | ((r & 24) >> 1) | (r & 3)) : r;
        voB[b][j] = (unsigned)rs_ * pitchB + (pk_ ? b * 128 : colb * 2) + gch * 16;
      }
    }
    if constexpr (HALF) {
      int r = wid * 16 + (lane >> 2);
      int chp = lane & 3;
      int gch = (((chp >> 1) ^ ((r >> 3) & 1)) << 1) | (chp & 1);
      voH = (unsigned)r * (LDB * 2) + (tn * BN + NFULL * 64) * 2 + gch * 16;
    }
    const char* Ab = reinterpret_cast<const char*>(A);
    const char* Wb = pk_ ? reinterpret_cast<const char*>(Wc) + (size_t)(tn >> 3) * (KD * NFULL * 128) : reinterpret_cast<const char*>(W);
    const unsigned lbase = (unsigned)(uintptr_t)(__attribute__((address_space(3))) char*)smem;
    auto stage = [&](int kt, int buf) {
      const char* a = Ab + (size_t)kt * (EPI == EPI_WO ? 2 * S_ * 64 : 128);
      const char* w = Wb + (size_t)(kt * (64 * pitchB));
      const unsigned s = lbase + buf * STAGE;
#pragma unroll
      for (int i = 0; i < 4; ++i) glds16(voA[i], a, s + (wid * 4 + i) * 1024);
#pragma unroll
      for (int b = 0; b < NFULL; ++b)
#pragma unroll
        for (int j = 0; j < 2; ++j) glds16(voB[b][j], w, s + A_BYTES + b * 8192 + (wid * 2 + j) * 1024);
      if constexpr (HALF) glds16(voH, w, s + A_BYTES + NFULL * 8192 + wid * 1024);
    };
#pragma unroll
    for (int s = 0; s < NST; ++s) stage(s, s);
    constexpr int XL = 0;
    constexpr int NCS = HAS_CS ? (EPI == EPI_QKV ? 16 : 1) : 0;
    constexpr int XE = NCS + (EPI == EPI_QKV ? 1 : 0) + (HAS_ST ? 4 : 0) + (HAS_RES ? NIT * (EPI == EPI_W4 ? 2 : 1) : 0);
    f32x4 stp[4];
    const int lt = tid - 256;
    u32x4 wpx;
    f32x4 csp[HAS_CS ? NCS : 1];
    constexpr int XP = XPOS;
    wait_vm<(NST - 1 - LAG) * LPS>();
    __builtin_amdgcn_s_barrier();
#pragma unroll
    for (int kt = 0; kt < XP; ++kt) {
      wait_vm<(NST - 2 - LAG) * LPS>();
      __builtin_amdgcn_s_barrier();
      stage(kt + NST, kt % NST);
    }
    asm volatile("" ::: "memory");
    if constexpr (HAS_CS) {
      if (lt < BN / 4) {
        const float* cp = cs + (EPI == EPI_QKV ? (lt >> 4) * 1024 + tn * 64 + (lt & 15) * 4 : tn * BN + lt * 4);
#pragma unroll
        for (int i = 0; i < NCS; ++i) csp[i] = *reinterpret_cast<const f32x4*>(cp + i * LDB);
      }
    }
    if constexpr (EPI == EPI_QKV) wpx = *reinterpret_cast<const u32x4*>(wphi + tn * 2048 + lt * 8);
    if constexpr (HAS_ST) {
      const f32x4* sp = reinterpret_cast<const f32x4*>(st_in + (size_t)(row0 + (lt >> 1)) * 16 + (lt & 1) * 8);
#pragma unroll
      for (int i = 0; i < 4; ++i) stp[i] = sp[i];
    }
    ldres();
    asm volatile("" ::: "memory");
    constexpr int NPEEL = XP + NST - 1 - LAG;
    auto head = [&](auto xe) {
      constexpr int X = decltype(xe)::value;
#pragma unroll
      for (int kt = XP; kt < NPEEL; ++kt) {
        wait_vm<(NST - 2 - LAG) * LPS + X>();
        __builtin_amdgcn_s_barrier();
        stage(kt + NST, kt % NST);
      }
    };
    if (HAS_CS && wid == 0) head(std::integral_constant<int, XE>{});
    else head(std::integral_constant<int, XE - NCS>{});
    int buf = NPEEL % NST;
    for (int kt = NPEEL; kt < NT - NST; ++kt) {
      wait_vm<(NST - 2 - LAG) * LPS>();
      __builtin_amdgcn_s_barrier();
      stage(kt + NST, buf);
      buf = buf + 1 == NST ? 0 : buf + 1;
    }
    asm volatile("" ::: "memory");
#pragma unroll
    for (int r = 0; r < NST - 1; ++r) {
      constexpr int NSL = NST - LAG;
      if (r == 0) wait_vm<(NSL - 2) * LPS + XL>();
      else if (r == 1) wait_vm<(NSL > 3 ? NSL - 3 : 0) * LPS + XL>();
      else if (r == 2) wait_vm<(NSL > 4 ? NSL - 4 : 0) * LPS + XL>();
      else if (r == 3) wait_vm<(NSL > 5 ? NSL - 5 : 0) * LPS + XL>();
      else wait_vm<XL>();
      __builtin_amdgcn_s_barrier();
    }
    if constexpr (HAS_ST) {
#pragma unroll
      for (int i = 0; i < 4; ++i) asm volatile("" : "+v"(stp[i]));
      float sm = 0.f;
#pragma unroll
      for (int i = 0; i < 4; ++i) sm += stp[i][0] + stp[i][2];
      sm += __shfl_xor(sm, 1);
      float mean = sm * (1.0f / 1024.f);
      float m2 = 0.f;
#pragma unroll
      for (int i = 0; i < 4; ++i) {
        float d0 = stp[i][0] * (1.0f / 64.f) - mean, d1 = stp[i][2] * (1.0f / 64.f) - mean;
        m2 += stp[i][1] + stp[i][3] + 64.f * (d0 * d0 + d1 * d1);
      }
      m2 += __shfl_xor(m2, 1);
      float var = m2 * (1.0f / 1024.f);
      float r1_ = 1.0f / sqrtf(var + 1e-5f);
      float sc = r1_;
      if constexpr (EPI == EPI_W1) sc = r1_ / sqrtf(var * r1_ * r1_ + 1e-5f);
      if ((lt & 1) == 0) rs[lt >> 1] = (f32x4){mean, sc, r1_, 0.f};
    }
    if constexpr (HAS_CS) {
      if (lt < BN / 4) {
#pragma unroll
        for (int i = 0; i < NCS; ++i) asm volatile("" : "+v"(csp[i]));
#pragma unroll
        for (int i = NCS / 2; i > 0; i >>= 1)
#pragma unroll
          for (int j = 0; j < i; ++j) csp[j] += csp[j + i];
        *reinterpret_cast<f32x4*>(smem + CS_OFF + lt * 16) = csp[0];
      }
    }
    if constexpr (EPI == EPI_QKV) *reinterpret_cast<u32x4*>(smem + WP_OFF + lt * 16) = wpx;
  } else {
    ldres();
    const int q = l15 >> 2, p = lane & 3;
    const int fB = (q >> 1) | ((g & 1) << 1);
    unsigned aoff[2], boff[2], hoff = 0;
#pragma unroll
    for (int ks = 0; ks < 2; ++ks) aoff[ks] = (wm * 64 + l15) * 128 + (((4 * ks + g) ^ (l15 >> 1)) * 16);
#pragma unroll
    for (int u = 0; u < 2; ++u) boff[u] = A_BYTES + (8 * g + q) * 128 + (((2 * wn + u) ^ fB) * 32) + p * 8;
    if constexpr (HALF) hoff = A_BYTES + NFULL * 8192 + (8 * g + q) * 64 + ((wn ^ (g & 1)) * 32) + p * 8;

    auto ldf = [&](int i, const char* s, int ks, u32x4(&af)[4], u32x4(&bfr)[NTW]) {
      if (i == 0 || i > NTW) {
        const int mt = i == 0 ? 0 : i - NTW;
        af[mt] = *reinterpret_cast<const u32x4*>(s + aoff[ks] + mt * 2048);
      } else if (i - 1 < 2 * NFULL) {
        const int b = (i - 1) >> 1, u = (i - 1) & 1;
        s16x4 lo = __builtin_amdgcn_ds_read_tr16_b64_v4i16((lds_s16x4_p)(s + boff[u] + b * 8192 + ks * 4096));
        s16x4 hi = __builtin_amdgcn_ds_read_tr16_b64_v4i16((lds_s16x4_p)(s + boff[u] + b * 8192 + ks * 4096 + 512));
        bfr[i - 1] = __builtin_shufflevector(__builtin_bit_cast(u32x2, lo), __builtin_bit_cast(u32x2, hi), 0, 1, 2, 3);
      } else {
        s16x4 lo = __builtin_amdgcn_ds_read_tr16_b64_v4i16((lds_s16x4_p)(s + hoff + ks * 2048));
        s16x4 hi = __builtin_amdgcn_ds_read_tr16_b64_v4i16((lds_s16x4_p)(s + hoff + ks * 2048 + 256));
        bfr[NTW - 1] = __builtin_shufflevector(__builtin_bit_cast(u32x2, lo), __builtin_bit_cast(u32x2, hi), 0, 1, 2, 3);
      }
    };
    constexpr int NM = 4 * NTW, NL = NTW + 4;
    auto phase = [&](u32x4(&afc)[4], u32x4(&bfc)[NTW], u32x4(&afn)[4], u32x4(&bfn)[NTW], int nbuf, int nks, auto rd, auto bar) {
      const char* s = smem + nbuf * STAGE;
      constexpr int BAR = decltype(bar)::value, R0 = BAR < 0 ? 0 : BAR + 1;
      constexpr int RSP = (NM - R0 - 2) / NL > 0 ? (NM - R0 - 2) / NL : 1;
      static_assert(R0 + (NL - 1) * RSP < NM, "reads");
#pragma unroll
      for (int mt = 0; mt < 4; ++mt)
#pragma unroll
        for (int j = 0; j < NTW; ++j) {
          const int m = mt * NTW + j;
          if (m == BAR) {
            WAIT_L0();
            __builtin_amdgcn_s_barrier();
            asm volatile("" ::: "memory");
          }
          if (decltype(rd)::value && m >= R0 && (m - R0) % RSP == 0 && (m - R0) / RSP < NL) ldf((m - R0) / RSP, s, nks, afn, bfn);
          if (EPI == EPI_QKV && j >= 4) mfma_acc(acc[mt][j], afc[mt], bfc[j]);
          else mfma_acc(acc[mt][j], bfc[j], afc[mt]);
          __builtin_amdgcn_sched_barrier(0);
        }
    };
    typedef std::integral_constant<int, -1> nobar;
#pragma unroll
    for (int mt = 0; mt < 4; ++mt)
#pragma unroll
      for (int j = 0; j < NTW; ++j) {
        acc[mt][j] = (f32x4){0.f, 0.f, 0.f, 0.f};
        asm volatile("" : "+a"(acc[mt][j]));
      }
    u32x4 af0[4], af1[4], bf0[NTW], bf1[NTW];
    __builtin_amdgcn_s_barrier();
    asm volatile("" ::: "memory");
    STAMP(1);
#pragma unroll
    for (int i = 0; i < NL; ++i) { ldf(i, smem, 0, af0, bf0); __builtin_amdgcn_sched_barrier(0); }
    int buf = 0;
    if constexpr (LAG) {
      phase(af0, bf0, af1, bf1, 0, 1, std::true_type{}, nobar{});
      for (int kt = 0; kt < NT - 1; ++kt) {
        buf = buf + 1 == NST ? 0 : buf + 1;
        phase(af1, bf1, af0, bf0, buf, 0, std::true_type{}, nobar{});
        asm volatile("" ::: "memory");
        __builtin_amdgcn_s_barrier();
        asm volatile("" ::: "memory");
        phase(af0, bf0, af1, bf1, buf, 1, std::true_type{}, nobar{});
      }
      phase(af1, bf1, af0, bf0, buf, 0, std::false_type{}, nobar{});
    } else {
      for (int kt = 0; kt < NT - 1; ++kt) {
        phase(af0, bf0, af1, bf1, buf, 1, std::true_type{}, nobar{});
        buf = buf + 1 == NST ? 0 : buf + 1;
        phase(af1, bf1, af0, bf0, buf, 0, std::true_type{}, std::integral_constant<int, 4>{});
      }
      phase(af0, bf0, af1, bf1, buf, 1, std::true_type{}, nobar{});
      phase(af1, bf1, af0, bf0, buf, 0, std::false_type{}, nobar{});
    }
    asm volatile("s_nop 7\n\ts_nop 7\n\ts_nop 7" ::: "memory");
    if constexpr (EPI == EPI_QKV) {
#pragma unroll
      for (int mt = 0; mt < 4; ++mt)
#pragma unroll
        for (int t = 0; t < 2; ++t) *reinterpret_cast<f32x4*>(smem + QX_OFF + wid * 8192 + (mt * 2 + t) * 1024 + lane * 16) = acc[mt][t];
    }
  }
  WAIT_L0();
  __builtin_amdgcn_s_barrier();
  asm volatile("" ::: "memory");
  STAMP(2);

  if constexpr (EPI == EPI_QKV) {
    const int h = tn * 2 + wn;
    const int q = l15 >> 2, p = lane & 3;
    f32x4 kv[4][2], zz[4];
    float zsel = 0.f;
    float* red = reinterpret_cast<float*>(smem + 4 * 64 * 144) + wn * (64 * 32 + 64);
    bf16x8 wf[2];
#pragma unroll
    for (int ft = 0; ft < 2; ++ft) {
      const char* src = smem + WP_OFF + wn * 2048 + (4 * g + q) * 64 + ft * 32 + p * 8;
      s16x4 lo = __builtin_amdgcn_ds_read_tr16_b64_v4i16((lds_s16x4_p)(src));
      s16x4 hi = __builtin_amdgcn_ds_read_tr16_b64_v4i16((lds_s16x4_p)(src + 16 * 64));
      wf[ft] = __builtin_shufflevector(lo, hi, 0, 1, 2, 3, 4, 5, 6, 7);
    }
    const float* csl = reinterpret_cast<const float*>(smem + CS_OFF);
    auto feat = [&](const bf16x8& xf, u32x2 (&o)[4]) {
      const float L2E = 1.4426950408889634f;
      f32x4 z4 = (f32x4){0.f, 0.f, 0.f, 0.f};
      f32x4 p0 = __builtin_amdgcn_mfma_f32_16x16x32_bf16(wf[0], xf, z4, 0, 0, 0);
      f32x4 p1 = __builtin_amdgcn_mfma_f32_16x16x32_bf16(wf[1], xf, z4, 0, 0, 0);
      f32x2 v[4];
      {
        const unsigned r0_ = pk2(p0[0], p0[1]), r1_ = pk2(p0[2], p0[3]), r2_ = pk2(p1[0], p1[1]), r3_ = pk2(p1[2], p1[3]);
        v[0] = (f32x2){bflo(r0_), bfhi(r0_)}; v[1] = (f32x2){bflo(r1_), bfhi(r1_)};
        v[2] = (f32x2){bflo(r2_), bfhi(r2_)}; v[3] = (f32x2){bflo(r3_), bfhi(r3_)};
      }
      float mx = max2_(max3_(max3_(v[0][0], v[0][1], v[1][0]), v[1][1], v[2][0]), max3_(v[2][1], v[3][0], v[3][1]));
      float mn = min2_(min3_(min3_(v[0][0], v[0][1], v[1][0]), v[1][1], v[2][0]), min3_(v[2][1], v[3][0], v[3][1]));
      mx = rmax4(mx); mn = rmin4(mn);
      const float mxl = -mx * L2E, mnl = mn * L2E;
      f32x2 ep[4], en[4];
#pragma unroll
      for (int i = 0; i < 4; ++i) {
        const f32x2 ap = v[i] * L2E + mxl, an = v[i] * (-L2E) + mnl;
        ep[i] = (f32x2){__builtin_amdgcn_exp2f(ap[0]), __builtin_amdgcn_exp2f(ap[1])};
        en[i] = (f32x2){__builtin_amdgcn_exp2f(an[0]), __builtin_amdgcn_exp2f(an[1])};
      }
      const f32x2 sp2 = (ep[0] + ep[1]) + (ep[2] + ep[3]), sn2 = (en[0] + en[1]) + (en[2] + en[3]);
      const float ip = __builtin_amdgcn_rcpf(rsum4(sp2[0] + sp2[1])), in_ = __builtin_amdgcn_rcpf(rsum4(sn2[0] + sn2[1]));
      o[0] = (u32x2){pk2v(ep[0] * ip), pk2v(ep[1] * ip)};
      o[1] = (u32x2){pk2v(ep[2] * ip), pk2v(ep[3] * ip)};
      o[2] = (u32x2){pk2v(en[0] * in_), pk2v(en[1] * in_)};
      o[3] = (u32x2){pk2v(en[2] * in_), pk2v(en[3] * in_)};
    };
    if (wid8 >= 4) {
      f32x4 cs4[2];
#pragma unroll
      for (int t = 0; t < 2; ++t) cs4[t] = *reinterpret_cast<const f32x4*>(csl + wn * 32 + t * 16 + 4 * g);
      bf16x8 qf[4];
#pragma unroll
      for (int mt = 0; mt < 4; ++mt) {
        f32x4 st = rs[wm * 64 + mt * 16 + l15];
        unsigned pq_[4];
#pragma unroll
        for (int t = 0; t < 2; ++t) {
          const f32x4 ac = *reinterpret_cast<const f32x4*>(smem + QX_OFF + wid * 8192 + (mt * 2 + t) * 1024 + lane * 16);
          float a[4];
#pragma unroll
          for (int i = 0; i < 4; ++i) a[i] = st[1] * (ac[i] - st[0] * cs4[t][i]);
          pq_[t * 2] = pk2(a[0], a[1]); pq_[t * 2 + 1] = pk2(a[2], a[3]);
        }
        qf[mt] = __builtin_bit_cast(bf16x8, (u32x4){pq_[0], pq_[1], pq_[2], pq_[3]});
      }
#pragma unroll
      for (int mt = 0; mt < 4; ++mt) {
        u32x2 o[4];
        feat(qf[mt], o);
        u32x4* dst = reinterpret_cast<u32x4*>(out) + ((size_t)(tm * 8 + wm * 4 + mt) * 32 + h) * 128 + lane;
        wt_store((u32x4){o[0][0], o[0][1], o[1][0], o[1][1]}, dst);
        wt_store((u32x4){o[2][0], o[2][1], o[3][0], o[3][1]}, dst + 64);
      }
    } else {
      f32x4 cs4[2];
      float csv[2];
#pragma unroll
      for (int t = 0; t < 2; ++t) {
        cs4[t] = *reinterpret_cast<const f32x4*>(csl + 64 + wn * 32 + t * 16 + 4 * g);
        csv[t] = csl[128 + wn * 32 + t * 16 + l15];
      }
      bf16x8 kf[4], vf[2][2];
#pragma unroll
      for (int mt = 0; mt < 4; ++mt) {
        f32x4 st = rs[wm * 64 + mt * 16 + l15];
        unsigned pk_[4];
#pragma unroll
        for (int t = 0; t < 2; ++t) {
          float b[4];
#pragma unroll
          for (int i = 0; i < 4; ++i) b[i] = st[1] * (acc[mt][2 + t][i] - st[0] * cs4[t][i]);
          pk_[t * 2] = pk2(b[0], b[1]); pk_[t * 2 + 1] = pk2(b[2], b[3]);
        }
        kf[mt] = __builtin_bit_cast(bf16x8, (u32x4){pk_[0], pk_[1], pk_[2], pk_[3]});
      }
      char* pkl = smem + wid * (64 * 144);
#pragma unroll
      for (int mt = 0; mt < 4; ++mt) {
        u32x2 o[4];
        feat(kf[mt], o);
        char* dst = pkl + (mt * 16 + l15) * 144 + g * 8;
#pragma unroll
        for (int c = 0; c < 4; ++c) *reinterpret_cast<u32x2*>(dst + c * 32) = o[c];
      }
#pragma unroll
      for (int s = 0; s < 2; ++s) {
        unsigned pv[2][4];
#pragma unroll
        for (int m2 = 0; m2 < 2; ++m2) {
          int mt = s * 2 + m2;
          f32x4 s0 = rs[wm * 64 + mt * 16 + 4 * g + 0], s1 = rs[wm * 64 + mt * 16 + 4 * g + 1];
          f32x4 s2 = rs[wm * 64 + mt * 16 + 4 * g + 2], s3 = rs[wm * 64 + mt * 16 + 4 * g + 3];
#pragma unroll
          for (int t = 0; t < 2; ++t) {
            float a0 = s0[1] * (acc[mt][4 + t][0] - s0[0] * csv[t]);
            float a1 = s1[1] * (acc[mt][4 + t][1] - s1[0] * csv[t]);
            float a2 = s2[1] * (acc[mt][4 + t][2] - s2[0] * csv[t]);
            float a3 = s3[1] * (acc[mt][4 + t][3] - s3[0] * csv[t]);
            pv[t][m2 * 2] = pk2(a0, a1); pv[t][m2 * 2 + 1] = pk2(a2, a3);
          }
        }
#pragma unroll
        for (int t = 0; t < 2; ++t) vf[s][t] = __builtin_bit_cast(bf16x8, (u32x4){pv[t][0], pv[t][1], pv[t][2], pv[t][3]});
      }
      WAIT_L0();
      __builtin_amdgcn_sched_barrier(0);
#pragma unroll
      for (int ft = 0; ft < 4; ++ft) { kv[ft][0] = kv[ft][1] = zz[ft] = (f32x4){0.f, 0.f, 0.f, 0.f}; }
      const bf16x8 ones = (bf16x8){0x3F80, 0x3F80, 0x3F80, 0x3F80, 0x3F80, 0x3F80, 0x3F80, 0x3F80};
#pragma unroll
      for (int s = 0; s < 2; ++s)
#pragma unroll
        for (int ft = 0; ft < 4; ++ft) {
          const char* src = pkl + (32 * s + 4 * g + q) * 144 + ft * 32 + p * 8;
          s16x4 lo = __builtin_amdgcn_ds_read_tr16_b64_v4i16((lds_s16x4_p)(src));
          s16x4 hi = __builtin_amdgcn_ds_read_tr16_b64_v4i16((lds_s16x4_p)(src + 16 * 144));
          bf16x8 pf = __builtin_shufflevector(lo, hi, 0, 1, 2, 3, 4, 5, 6, 7);
          kv[ft][0] = __builtin_amdgcn_mfma_f32_16x16x32_bf16(vf[s][0], pf, kv[ft][0], 0, 0, 0);
          kv[ft][1] = __builtin_amdgcn_mfma_f32_16x16x32_bf16(vf[s][1], pf, kv[ft][1], 0, 0, 0);
          zz[ft] = __builtin_amdgcn_mfma_f32_16x16x32_bf16(ones, pf, zz[ft], 0, 0, 0);
        }
      zsel = g == 0 ? zz[0][0] : g == 1 ? zz[1][0] : g == 2 ? zz[2][0] : zz[3][0];
      if (wm == 1) {
#pragma unroll
        for (int ft = 0; ft < 4; ++ft)
#pragma unroll
          for (int dt = 0; dt < 2; ++dt) *reinterpret_cast<f32x4*>(red + ((ft * 2 + dt) * 64 + lane) * 4) = kv[ft][dt];
        red[64 * 32 + lane] = zsel;
      }
    }
    __syncthreads();
    if (wid8 < 2) {
      float* kd = kvp + (size_t)(h * 16 + tm) * 2048;
      float* zd = zp + (size_t)(h * 16 + tm) * 64;
#pragma unroll
      for (int ft = 0; ft < 4; ++ft)
#pragma unroll
        for (int dt = 0; dt < 2; ++dt) {
          f32x4 o = *reinterpret_cast<const f32x4*>(red + ((ft * 2 + dt) * 64 + lane) * 4);
          wt_store(o + kv[ft][dt], reinterpret_cast<f32x4*>(kd + ((ft * 2 + dt) * 64 + lane) * 4));
        }
      zd[lane] = red[64 * 32 + lane] + zsel;
    }
  } else {
    if (wid8 < 4) {
      f32x4 stm[4], cs4[NTW];
      if constexpr (EPI == EPI_W1 || EPI == EPI_W3) {
#pragma unroll
        for (int j = 0; j < NTW; ++j) {
          const int colj = (j < 2 * NFULL) ? ((j >> 1) * 64 + (2 * wn + (j & 1)) * 16) : (NFULL * 64 + wn * 16);
          cs4[j] = *reinterpret_cast<const f32x4*>(smem + CS_OFF + (colj + 4 * g) * 4);
        }
#pragma unroll
        for (int mt = 0; mt < 4; ++mt) stm[mt] = rs[wm * 64 + mt * 16 + l15];
      }
#pragma unroll
      for (int j = 0; j < NTW; ++j) {
        const int colj = (j < 2 * NFULL) ? ((j >> 1) * 64 + (2 * wn + (j & 1)) * 16) : (NFULL * 64 + wn * 16);
#pragma unroll
        for (int mt = 0; mt < 4; ++mt) {
          f32x2 v0 = (f32x2){acc[mt][j][0], acc[mt][j][1]}, v1 = (f32x2){acc[mt][j][2], acc[mt][j][3]};
          if constexpr (EPI == EPI_W1 || EPI == EPI_W3) {
            v0 = gelu2((v0 - stm[mt][0] * (f32x2){cs4[j][0], cs4[j][1]}) * stm[mt][1]);
            v1 = gelu2((v1 - stm[mt][0] * (f32x2){cs4[j][2], cs4[j][3]}) * stm[mt][1]);
          }
          *reinterpret_cast<u32x2*>(smem + (wm * 64 + mt * 16 + l15) * PB + (colj + 4 * g) * 2) = (u32x2){pk2(v0[0], v0[1]), pk2(v1[0], v1[1])};
        }
      }
    }
    __syncthreads();
#pragma unroll
    for (int it = 0; it < NIT; ++it) {
      const int c = tid + 512 * it, r = c / CH, ch = c % CH;
      u32x4 v = *reinterpret_cast<const u32x4*>(smem + r * PB + ch * 16);
      const int grow = row0 + r, gcol = tn * BN + ch * 8;
      if constexpr (EPI == EPI_W3) {
        wt_store(v, reinterpret_cast<u32x4*>(out + (size_t)grow * F_ + gcol));
      } else if constexpr (EPI == EPI_W1) {
        const int grp = tn >> 2;
        const int sh = grp == 0 ? 1 : grp == 1 ? -1 : grp == 2 ? 2 : -2;
        const int dr = grow + sh;
        if (dr >= 0 && dr < S_) wt_store(v, reinterpret_cast<u32x4*>(out + (size_t)dr * D_ + gcol));
        const int zr = grow - sh;
        if (zr < 0 || zr >= S_) *reinterpret_cast<u32x4*>(out + (size_t)grow * D_ + gcol) = (u32x4){0u, 0u, 0u, 0u};
      } else {
        float y[8];
        const u32x4 a = ra[it];
        if constexpr (EPI == EPI_WO) {
#pragma unroll
          for (int i = 0; i < 4; ++i) { y[2 * i] = rbf(bflo(a[i]) + bflo(v[i])); y[2 * i + 1] = rbf(bfhi(a[i]) + bfhi(v[i])); }
        } else if constexpr (EPI == EPI_W2) {
          f32x4 st = rs[r];
#pragma unroll
          for (int i = 0; i < 4; ++i) {
            y[2 * i] = rbf(rbf((bflo(a[i]) - st[0]) * st[2]) + bflo(v[i]));
            y[2 * i + 1] = rbf(rbf((bfhi(a[i]) - st[0]) * st[2]) + bfhi(v[i]));
          }
        } else {
          const u32x4 b = rb[it];
#pragma unroll
          for (int i = 0; i < 4; ++i) {
            y[2 * i] = rbf(bflo(a[i]) + rbf(bflo(b[i]) + bflo(v[i])));
            y[2 * i + 1] = rbf(bfhi(a[i]) + rbf(bfhi(b[i]) + bfhi(v[i])));
          }
        }
        wt_store((u32x4){pk2(y[0], y[1]), pk2(y[2], y[3]), pk2(y[4], y[5]), pk2(y[6], y[7])}, reinterpret_cast<u32x4*>(out + (size_t)grow * D_ + gcol));
        float sm = ((y[0] + y[1]) + (y[2] + y[3])) + ((y[4] + y[5]) + (y[6] + y[7]));
        sm = rsum8(sm);
        float mu = sm * (1.0f / 64.f), m2 = 0.f;
#pragma unroll
        for (int i = 0; i < 8; ++i) { float d = y[i] - mu; m2 += d * d; }
        m2 = rsum8(m2);
        if (ch == 0) st_out[(size_t)grow * 16 + tn] = (f32x2){sm, m2};
      }
    }
  }
  STAMP(3);
}

__global__ __launch_bounds__(256, 1) void attn_out_kernel(const u16* __restrict__ phiq, const float* __restrict__ kvp,
                                                          const float* __restrict__ zp, u16* __restrict__ o,
                                                          const float* __restrict__ cp1, const float* __restrict__ cp3,
                                                          float* __restrict__ cs1, float* __restrict__ cs3) {
  __shared__ __attribute__((aligned(16))) u16 kvT[48 * 72];
  const int tid = threadIdx.x, lane = tid & 63, wid = tid >> 6, g = lane >> 4, l15 = lane & 15;
  if (blockIdx.x >= 256) {
    const int i = (blockIdx.x - 256) * 256 + tid;
    const float* src = i < 1024 ? cp1 + i : cp3 + (i - 1024);
    const int ld = i < 1024 ? 1024 : F_;
    float c[16];
#pragma unroll
    for (int t = 0; t < 16; ++t) c[t] = src[t * ld];
#pragma unroll
    for (int t = 8; t > 0; t >>= 1)
#pragma unroll
      for (int j = 0; j < t; ++j) c[j] += c[j + t];
    *(i < 1024 ? cs1 + i : cs3 + (i - 1024)) = c[0];
    return;
  }
  const int h = blockIdx.x & 31, tg = blockIdx.x >> 5;
  f32x4 kvv[16][2];
  float zv[16];
  bf16x8 pq[4][2];
  {
    const float* src = kvp + (size_t)h * 16 * 2048 + tid * 8;
#pragma unroll
    for (int t = 0; t < 16; ++t) {
      kvv[t][0] = *reinterpret_cast<const f32x4*>(src + t * 2048);
      kvv[t][1] = *reinterpret_cast<const f32x4*>(src + t * 2048 + 4);
    }
#pragma unroll
    for (int t = 0; t < 16; ++t) zv[t] = zp[(size_t)(h * 16 + t) * 64 + lane];
#pragma unroll
    for (int mt = 0; mt < 4; ++mt) {
      const u16* ps = phiq + ((size_t)(tg * 16 + wid * 4 + mt) * 32 + h) * 1024 + lane * 8;
      pq[mt][0] = *reinterpret_cast<const bf16x8*>(ps);
      pq[mt][1] = *reinterpret_cast<const bf16x8*>(ps + 512);
    }
  }
  __builtin_amdgcn_sched_barrier(0);
  {
    for (int i = tid; i < 15 * 64; i += 256) kvT[(33 + i / 64) * 72 + (i & 63)] = 0;
    f32x4 a0 = kvv[0][0], a1 = kvv[0][1];
#pragma unroll
    for (int t = 1; t < 16; ++t) { a0 += kvv[t][0]; a1 += kvv[t][1]; }
    const int ft = tid >> 6, dt = (tid >> 5) & 1, ln = (2 * tid) & 63;
    const int d0 = dt * 16 + 4 * (ln >> 4), pos = (ft >> 1) * 32 + ((ln >> 2) & 3) * 8 + (ft & 1) * 4 + (ln & 3);
#pragma unroll
    for (int i = 0; i < 4; ++i) *reinterpret_cast<unsigned*>(&kvT[(d0 + i) * 72 + pos]) = pk2(a0[i], a1[i]);
    if (tid < 64) {
      float z = 0.f;
#pragma unroll
      for (int t = 0; t < 16; ++t) z += zv[t];
      kvT[32 * 72 + (tid >> 5) * 32 + ((tid >> 2) & 3) * 8 + ((tid >> 4) & 1) * 4 + (tid & 3)] = (u16)(pk2(z, 0.f) & 0xffff);
    }
  }
  __syncthreads();
  bf16x8 kf[3][2];
#pragma unroll
  for (int dt = 0; dt < 3; ++dt)
#pragma unroll
    for (int ks = 0; ks < 2; ++ks) kf[dt][ks] = *reinterpret_cast<const bf16x8*>(&kvT[(dt * 16 + l15) * 72 + ks * 32 + g * 8]);
#pragma unroll
  for (int mt = 0; mt < 4; ++mt) {
    const int tok = tg * 256 + wid * 64 + mt * 16 + l15;
    const bf16x8 p0 = pq[mt][0], p1 = pq[mt][1];
    f32x4 r[3];
#pragma unroll
    for (int dt = 0; dt < 3; ++dt) {
      r[dt] = (f32x4){0.f, 0.f, 0.f, 0.f};
      r[dt] = __builtin_amdgcn_mfma_f32_16x16x32_bf16(kf[dt][0], p0, r[dt], 0, 0, 0);
      r[dt] = __builtin_amdgcn_mfma_f32_16x16x32_bf16(kf[dt][1], p1, r[dt], 0, 0, 0);
    }
    float den = rbf(rbf(__shfl(r[2][0], l15)) + 1e-6f);
    float inv = 1.0f / den;
    const u32x4 w = (u32x4){pk2(rbf(r[0][0]) * inv, rbf(r[0][1]) * inv), pk2(rbf(r[0][2]) * inv, rbf(r[0][3]) * inv),
                            pk2(rbf(r[1][0]) * inv, rbf(r[1][1]) * inv), pk2(rbf(r[1][2]) * inv, rbf(r[1][3]) * inv)};
    wt_store(w, reinterpret_cast<u32x4*>(o + ((size_t)h * S_ + tok) * 32 + 8 * g));
  }
}

constexpr int WC_QKV = 0, WC_WO = WC_QKV + 2 * 1024 * 384, WC_W1 = WC_WO + 2 * 1024 * 128, WC_W2 = WC_W1 + 2 * 1024 * 128,
              WC_W4 = WC_W2 + 2 * 1024 * 128, WC_END = WC_W4 + 2 * F_ * 128;
__global__ __launch_bounds__(256) void prep_kernel(const u16* __restrict__ x, const u16* __restrict__ wqkv,
                                                   const u16* __restrict__ w1, const u16* __restrict__ w3,
                                                   float* __restrict__ cs_qkv, float* __restrict__ cs_w1,
                                                   float* __restrict__ cs_w3, f32x2* __restrict__ st,
                                                   const u16* __restrict__ wo, const u16* __restrict__ w2,
                                                   const u16* __restrict__ w4, char* __restrict__ wc) {
  __shared__ __attribute__((aligned(16))) float red[4][512];
  const int tid = threadIdx.x, b = blockIdx.x;
  if (b >= 368) {
    const int pb = b - 368;
    const u16* w = pb < 32 ? wo + (size_t)pb * 32768 : pb < 64 ? w2 + (size_t)(pb - 32) * 32768 : w4 + (size_t)(pb - 64) * 32768;
    u32x4 v[16];
#pragma unroll
    for (int r = 0; r < 16; ++r) v[r] = *reinterpret_cast<const u32x4*>(w + r * 2048 + tid * 8);
    if (((tid >> 3) & 7) == 3) {
      const int kd = pb < 64 ? 1024 : F_, row = (pb < 32 ? pb : pb < 64 ? pb - 32 : pb - 64) * 32 + (tid >> 7);
      char* dst = wc + (pb < 32 ? WC_WO : pb < 64 ? WC_W2 : WC_W4) + (size_t)((tid >> 6) & 1) * kd * 128 + (size_t)row * 128 + (tid & 7) * 16;
#pragma unroll
      for (int r = 0; r < 16; ++r) *reinterpret_cast<u32x4*>(dst + r * 256) = v[r];
    }
#pragma unroll
    for (int r = 0; r < 16; ++r) asm volatile("" ::"v"(v[r]));
  } else if (b < 240) {
    const u16* w; float* dst; int ld, ch, rg;
    if (b < 96) { w = wqkv; dst = cs_qkv; ld = 3072; ch = b % 6; rg = b / 6; }
    else if (b < 128) { w = w1; dst = cs_w1; ld = 1024; ch = (b - 96) & 1; rg = (b - 96) >> 1; }
    else { w = w3; dst = cs_w3; ld = F_; ch = (b - 128) % 7; rg = (b - 128) / 7; }
    const int lane = tid & 63, wid = tid >> 6, row = rg * 64 + wid * 16;
    u32x4 v[16];
#pragma unroll
    for (int r = 0; r < 16; ++r) v[r] = *reinterpret_cast<const u32x4*>(w + (size_t)(row + r) * ld + ch * 512 + lane * 8);
    if (b < 128 && (lane >> 3) == 3) {
      const int cpp = b < 96 ? 384 : 128;
      char* cpd = wc + (b < 96 ? WC_QKV + (ch & 1) * (1024 * 384) + (ch >> 1) * 128 : WC_W1 + ch * (1024 * 128)) + row * cpp + (lane & 7) * 16;
#pragma unroll
      for (int r = 0; r < 16; ++r) *reinterpret_cast<u32x4*>(cpd + r * cpp) = v[r];
    }
    f32x4 s0 = (f32x4){0.f, 0.f, 0.f, 0.f}, s1 = s0;
#pragma unroll
    for (int r = 0; r < 16; ++r) {
      s0 += (f32x4){bflo(v[r][0]), bfhi(v[r][0]), bflo(v[r][1]), bfhi(v[r][1])};
      s1 += (f32x4){bflo(v[r][2]), bfhi(v[r][2]), bflo(v[r][3]), bfhi(v[r][3])};
    }
    *reinterpret_cast<f32x4*>(&red[wid][lane * 8]) = s0;
    *reinterpret_cast<f32x4*>(&red[wid][lane * 8 + 4]) = s1;
    __syncthreads();
    f32x2 t = *reinterpret_cast<const f32x2*>(&red[0][tid * 2]);
#pragma unroll
    for (int i = 1; i < 4; ++i) t += *reinterpret_cast<const f32x2*>(&red[i][tid * 2]);
    *reinterpret_cast<f32x2*>(dst + rg * ld + ch * 512 + tid * 2) = t;
  } else {
    const int lane = tid & 63, wid = tid >> 6;
    const int rbase = (b - 240) * 16 + wid * 4;
    u32x4 xv[8];
#pragma unroll
    for (int i = 0; i < 8; ++i) xv[i] = *reinterpret_cast<const u32x4*>(x + (size_t)rbase * D_ + i * 512 + lane * 8);
    __builtin_amdgcn_sched_barrier(0);
#pragma unroll
    for (int rr = 0; rr < 4; ++rr) {
      const int row = rbase + rr;
#pragma unroll
      for (int hf = 0; hf < 2; ++hf) {
        const u32x4 v = xv[rr * 2 + hf];
        float y[8];
#pragma unroll
        for (int i = 0; i < 4; ++i) { y[2 * i] = bflo(v[i]); y[2 * i + 1] = bfhi(v[i]); }
        float sm = ((y[0] + y[1]) + (y[2] + y[3])) + ((y[4] + y[5]) + (y[6] + y[7]));
        sm = rsum8(sm);
        float mu = sm * (1.0f / 64.f), m2 = 0.f;
#pragma unroll
        for (int i = 0; i < 8; ++i) { float d = y[i] - mu; m2 += d * d; }
        m2 = rsum8(m2);
        if ((lane & 7) == 0) st[(size_t)row * 16 + hf * 8 + (lane >> 3)] = (f32x2){sm, m2};
      }
    }
  }
}

extern "C" void kernel_launch(void* const* d_in, const int* in_sizes, int n_in, void* d_out, int out_size, void* d_ws,
                              size_t ws_size, hipStream_t stream) {
  const u16* x_in = (const u16*)d_in[0];
  const u16* wqkv = (const u16*)d_in[1];
  const u16* wphi = (const u16*)d_in[2];
  const u16* wo = (const u16*)d_in[3];
  const u16* w1 = (const u16*)d_in[5];
  const u16* w2 = (const u16*)d_in[6];
  const u16* w3 = (const u16*)d_in[7];
  const u16* w4 = (const u16*)d_in[8];
  char* ws = (char*)d_ws;
  size_t off = 0;
  auto alloc = [&](size_t bytes) { char* p = ws + off; off += (bytes + 4095) & ~(size_t)4095; return p; };
  u16* xb = (u16*)alloc((size_t)S_ * D_ * 2);
  u16* x2 = (u16*)alloc((size_t)S_ * D_ * 2);
  u16* x1 = (u16*)alloc((size_t)S_ * D_ * 2);
  u16* hs = (u16*)alloc((size_t)S_ * D_ * 2);
  u16* ob = (u16*)alloc((size_t)S_ * D_ * 2);
  u16* gb = (u16*)alloc((size_t)S_ * F_ * 2);
  u16* phiq = (u16*)alloc((size_t)S_ * 2048 * 2);
  float* kvp = (float*)alloc((size_t)32 * 16 * 2048 * 4);
  float* zp = (float*)alloc((size_t)32 * 16 * 64 * 4);
  f32x2* st_x = (f32x2*)alloc((size_t)S_ * 16 * 8);
  f32x2* st_x2 = (f32x2*)alloc((size_t)S_ * 16 * 8);
  f32x2* st_x1 = (f32x2*)alloc((size_t)S_ * 16 * 8);
  float* cs_qkv = (float*)alloc(16 * 3072 * 4);
  float* cs_w1 = (float*)alloc(16 * 1024 * 4);
  float* cs_w3 = (float*)alloc(16 * F_ * 4);
  float* cf_w1 = (float*)alloc(1024 * 4);
  float* cf_w3 = (float*)alloc(F_ * 4);
  char* wc = alloc(WC_END);

  prep_kernel<<<368 + 176, 256, 0, stream>>>(x_in, wqkv, w1, w3, cs_qkv, cs_w1, cs_w3, st_x, wo, w2, w4, wc);
  for (int layer = 0; layer < 2; ++layer) {
    const u16* x = layer == 0 ? x_in : xb;
    u16* xo = layer == 0 ? xb : (u16*)d_out;
    gemm_kernel<192, 3, 1024, 1024, 3072, EPI_QKV><<<256, 512, 0, stream>>>(x, wqkv, (const u16*)(wc + WC_QKV), cs_qkv, st_x, nullptr, nullptr, phiq, nullptr, wphi, kvp, zp);
    attn_out_kernel<<<layer == 0 ? 256 + 18 : 256, 256, 0, stream>>>(phiq, kvp, zp, ob, cs_w1, cs_w3, cf_w1, cf_w3);
    gemm_kernel<64, 5, 1024, 1024, 1024, EPI_WO><<<256, 512, 0, stream>>>(ob, wo, (const u16*)(wc + WC_WO), nullptr, nullptr, x, nullptr, x2, st_x2, nullptr, nullptr, nullptr);
    gemm_kernel<64, 5, 1024, 1024, 1024, EPI_W1><<<256, 512, 0, stream>>>(x2, w1, (const u16*)(wc + WC_W1), cf_w1, st_x2, nullptr, nullptr, hs, nullptr, nullptr, nullptr, nullptr);
    gemm_kernel<64, 5, 1024, 1024, 1024, EPI_W2><<<256, 512, 0, stream>>>(hs, w2, (const u16*)(wc + WC_W2), nullptr, st_x2, x2, nullptr, x1, st_x1, nullptr, nullptr, nullptr);
    gemm_kernel<224, 3, 1024, 1024, F_, EPI_W3><<<256, 512, 0, stream>>>(x1, w3, nullptr, cf_w3, st_x1, nullptr, nullptr, gb, nullptr, nullptr, nullptr, nullptr);
    gemm_kernel<64, 5, F_, F_, 1024, EPI_W4><<<256, 512, 0, stream>>>(gb, w4, (const u16*)(wc + WC_W4), nullptr, nullptr, x2, x1, xo, st_x, nullptr, nullptr, nullptr);
  }
}
```

```cpp
#include <hip/hip_runtime.h>
#include <hip/hip_bf16.h>
#include <stdint.h>
#include <type_traits>

typedef short bf16x8 __attribute__((ext_vector_type(8)));
typedef short s16x4 __attribute__((ext_vector_type(4)));
typedef float f32x4 __attribute__((ext_vector_type(4)));
typedef float f32x2 __attribute__((ext_vector_type(2)));
typedef unsigned u32x2 __attribute__((ext_vector_type(2)));
typedef unsigned u32x4 __attribute__((ext_vector_type(4)));
typedef __attribute__((address_space(3))) s16x4* lds_s16x4_p;
typedef unsigned short u16;

constexpr int S_ = 2048, D_ = 1024, H_ = 32, F_ = 3584;

__device__ __forceinline__ float bf2f(u16 v) { return __uint_as_float(((unsigned)v) << 16); }
__device__ __forceinline__ float bflo(unsigned v) { return __uint_as_float(v << 16); }
__device__ __forceinline__ float bfhi(unsigned v) { return __uint_as_float(v & 0xffff0000u); }
typedef __bf16 bf16x2_t __attribute__((ext_vector_type(2)));
__device__ __forceinline__ unsigned pk2(float a, float b) {
  return __builtin_bit_cast(unsigned, __builtin_convertvector((f32x2){a, b}, bf16x2_t));
}
__device__ __forceinline__ unsigned pk2v(f32x2 v) { return __builtin_bit_cast(unsigned, __builtin_convertvector(v, bf16x2_t)); }
__device__ __forceinline__ float max2_(float a, float b) { float r; asm("v_max_f32 %0, %1, %2" : "=v"(r) : "v"(a), "v"(b)); return r; }
__device__ __forceinline__ float min2_(float a, float b) { float r; asm("v_min_f32 %0, %1, %2" : "=v"(r) : "v"(a), "v"(b)); return r; }
__device__ __forceinline__ float max3_(float a, float b, float c) { float r; asm("v_max3_f32 %0, %1, %2, %3" : "=v"(r) : "v"(a), "v"(b), "v"(c)); return r; }
__device__ __forceinline__ float min3_(float a, float b, float c) { float r; asm("v_min3_f32 %0, %1, %2, %3" : "=v"(r) : "v"(a), "v"(b), "v"(c)); return r; }
__device__ __forceinline__ float rbf(float a) { return bflo(pk2(a, 0.f)); }
__device__ __forceinline__ float gelu_t(float x) {
  const float c1 = -2.0f * 0.7978845608028654f * 1.4426950408889634f;
  const float c2 = c1 * 0.044715f;
  float t = x * x;
  float w = x * __builtin_fmaf(t, c2, c1);
  float e = __builtin_amdgcn_exp2f(w);
  return x * __builtin_amdgcn_rcpf(1.0f + e);
}

__device__ __forceinline__ f32x2 gelu2(f32x2 x) {
  const float c1 = -2.0f * 0.7978845608028654f * 1.4426950408889634f;
  const float c2 = c1 * 0.044715f;
  f32x2 t = x * x;
  f32x2 w = x * (t * c2 + c1);
  f32x2 d = (f32x2){__builtin_amdgcn_exp2f(w[0]), __builtin_amdgcn_exp2f(w[1])} + 1.0f;
  return x * (f32x2){__builtin_amdgcn_rcpf(d[0]), __builtin_amdgcn_rcpf(d[1])};
}
__device__ __forceinline__ void x16(float v, float& a, float& b) {
  auto r = __builtin_amdgcn_permlane16_swap(__float_as_uint(v), __float_as_uint(v), false, false);
  a = __uint_as_float(r[0]); b = __uint_as_float(r[1]);
}
__device__ __forceinline__ void x32(float v, float& a, float& b) {
  auto r = __builtin_amdgcn_permlane32_swap(__float_as_uint(v), __float_as_uint(v), false, false);
  a = __uint_as_float(r[0]); b = __uint_as_float(r[1]);
}
__device__ __forceinline__ float rmax4(float v) { float a, b; x16(v, a, b); v = max2_(a, b); x32(v, a, b); return max2_(a, b); }
__device__ __forceinline__ float rmin4(float v) { float a, b; x16(v, a, b); v = min2_(a, b); x32(v, a, b); return min2_(a, b); }
__device__ __forceinline__ float rsum4(float v) { float a, b; x16(v, a, b); v = a + b; x32(v, a, b); return a + b; }

__device__ __forceinline__ float rsum8(float v) {
  v += __uint_as_float(__builtin_amdgcn_mov_dpp(__float_as_uint(v), 0xB1, 0xF, 0xF, true));
  v += __uint_as_float(__builtin_amdgcn_mov_dpp(__float_as_uint(v), 0x4E, 0xF, 0xF, true));
  v += __uint_as_float(__builtin_amdgcn_mov_dpp(__float_as_uint(v), 0x141, 0xF, 0xF, true));
  return v;
}

__device__ __forceinline__ void wt_store(u32x4 v, u32x4* p) { asm volatile("global_store_dwordx4 %0, %1, off sc1\n\ts_nop 1" :: "v"(p), "v"(v) : "memory"); }
__device__ __forceinline__ void wt_store(f32x4 v, f32x4* p) { asm volatile("global_store_dwordx4 %0, %1, off sc1\n\ts_nop 1" :: "v"(p), "v"(v) : "memory"); }
__device__ __forceinline__ void wt_store(u32x2 v, u32x2* p) { asm volatile("global_store_dwordx2 %0, %1, off sc1\n\ts_nop 1" :: "v"(p), "v"(v) : "memory"); }
#define WAIT_V(n) asm volatile("s_waitcnt vmcnt(" #n ")" ::: "memory")
#define WAIT_L0() asm volatile("s_waitcnt lgkmcnt(0)" ::: "memory")
template <int N> __device__ __forceinline__ void wait_vm() {
  asm volatile("s_waitcnt vmcnt(%0)" ::"i"(N) : "memory");
}

__device__ __forceinline__ void glds16(unsigned voff, const void* sbase, unsigned lds) {
  unsigned keep;
  asm volatile("s_mov_b32 %0, m0\n\ts_mov_b32 m0, %3\n\ts_nop 0\n\tglobal_load_lds_dwordx4 %1, %2\n\ts_mov_b32 m0, %0"
               : "=&s"(keep) : "v"(voff), "s"(sbase), "s"(lds) : "memory");
}

__device__ __forceinline__ void mfma_acc(f32x4& c, const u32x4& a, const u32x4& b) {
  asm volatile("v_mfma_f32_16x16x32_bf16 %0, %1, %2, %0" : "+a"(c) : "v"(a), "v"(b));
}

#define STAMP(i)

#define W4NST 4
#define W4NT1 19
#define W4FK 8
#define W4RK 4
#define XPOS 2
enum { EPI_QKV = 0, EPI_WO = 1, EPI_W1 = 2, EPI_W2 = 3, EPI_W3 = 4, EPI_W4 = 5 };

template <int BN, int NST, int KD, int LDA, int LDB, int EPI, int SPLIT = 0>
__global__ __launch_bounds__(512, 2) void gemm_kernel(
    const u16* __restrict__ A, const u16* __restrict__ W,
    const u16* __restrict__ Wc,
    const float* __restrict__ cs,
    const f32x2* __restrict__ st_in,
    const u16* __restrict__ r0,
    const u16* __restrict__ r1,
    u16* __restrict__ out,
    f32x2* __restrict__ st_out,
    const u16* __restrict__ wphi,
    float* __restrict__ kvp,
    float* __restrict__ zp)
{
  constexpr int NFULL = BN / 64, HALF = (BN % 64) / 32;
  constexpr int A_BYTES = 128 * 64 * 2, B_BYTES = BN * 64 * 2, STAGE = A_BYTES + B_BYTES;
  constexpr int NT1 = SPLIT ? W4NT1 : 0, NT2 = KD / 64 - 2 * NT1, BNE = SPLIT ? BN / 2 : BN;
  constexpr int NT = NT1 + NT2, NTW = 2 * NFULL + HALF, LPS = 4 + 2 * NFULL + HALF;
  constexpr int PB = BNE * 2 + 16;
  constexpr int EPI_BYTES = (EPI == EPI_QKV) ? (4 * 64 * 144 + 2 * (64 * 32 + 64) * 4) : 128 * PB;
  constexpr int MAINB = NST * STAGE;
  constexpr int RS_OFF = (MAINB > EPI_BYTES ? MAINB : EPI_BYTES);
  constexpr int CS_OFF = RS_OFF + 128 * 16;
  constexpr int WP_OFF = CS_OFF + 1024;
  constexpr int QX_OFF = WP_OFF + 4096;
  constexpr int SMEM = WP_OFF + (EPI == EPI_QKV ? 4096 + 32768 : 0);
  constexpr bool HAS_CS = (EPI == EPI_QKV || EPI == EPI_W1 || EPI == EPI_W3);
  constexpr bool HAS_ST = (EPI == EPI_QKV || EPI == EPI_W1 || EPI == EPI_W3 || EPI == EPI_W2);
  constexpr bool HAS_RES = (EPI == EPI_WO || EPI == EPI_W2 || EPI == EPI_W4);
  constexpr int LAG = (BN == 64) ? 1 : 0;
  constexpr int CH = BNE / 8, NIT = CH / 4;
  static_assert(SMEM <= 163840, "LDS");
  __shared__ __attribute__((aligned(16))) char smem[SMEM];

  STAMP(0);
  const int tid = threadIdx.x, lane = tid & 63;
  const int wid8 = __builtin_amdgcn_readfirstlane(tid >> 6), wid = wid8 & 3, wm = wid >> 1, wn = wid & 1;
  const int g = lane >> 4, l15 = lane & 15;
  const int xcd = blockIdx.x & 7, idx = blockIdx.x >> 3;
  const int tm = (xcd >> 1) * 4 + (idx & 3), tn = SPLIT ? (idx >> 2) : (xcd & 1) * 8 + (idx >> 2);
  const int kh = SPLIT ? (xcd & 1) : 0, tne = SPLIT ? 2 * tn + kh : tn;
  const int row0 = tm * 128;
  f32x4* rs = reinterpret_cast<f32x4*>(smem + RS_OFF);

  f32x4 acc[4][NTW];
  u32x4 ra[HAS_RES ? NIT : 1], rb[(EPI == EPI_W4) ? NIT : 1];

  unsigned rof[HAS_RES ? NIT : 1];
  if constexpr (HAS_RES) {
#pragma unroll
    for (int it = 0; it < NIT; ++it) {
      const int c = tid + 512 * it, r = c / CH, ch = c % CH;
      rof[it] = (unsigned)((row0 + r) * D_ + tne * BNE + ch * 8) * 2u;
      asm volatile("" : "+v"(rof[it]));
    }
  }
  auto ldres = [&]() {
    if constexpr (HAS_RES) {
#pragma unroll
      for (int it = 0; it < NIT; ++it) {
        ra[it] = *reinterpret_cast<const u32x4*>(reinterpret_cast<const char*>(r0) + rof[it]);
        if constexpr (EPI == EPI_W4) rb[it] = *reinterpret_cast<const u32x4*>(reinterpret_cast<const char*>(r1) + rof[it]);
      }
    }
  };

  if (wid8 >= 4) {
    unsigned voA[4], voB[NFULL > 0 ? NFULL : 1][2], voH = 0;
    const bool pk_ = (HALF == 0) && ((tn & 7) == 3);
    const unsigned pitchB = pk_ ? NFULL * 128 : LDB * 2;
    bool pkS[NFULL > 0 ? NFULL : 1];
    unsigned pitchS[NFULL > 0 ? NFULL : 1];
    const char* WbS[NFULL > 0 ? NFULL : 1];
    if constexpr (SPLIT) {
#pragma unroll
      for (int b = 0; b < NFULL; ++b) {
        const int t64 = tn * NFULL + (b ^ kh);
        pkS[b] = (t64 & 7) == 3;
        pitchS[b] = pkS[b] ? 128 : LDB * 2;
        WbS[b] = pkS[b] ? reinterpret_cast<const char*>(Wc) + (size_t)(t64 >> 3) * (KD * 128) : reinterpret_cast<const char*>(W) + t64 * 128;
      }
    }
#pragma unroll
    for (int i = 0; i < 4; ++i) {
      int r = (wid * 4 + i) * 8 + (lane >> 3);
      int ch = (lane & 7) ^ ((r >> 1) & 7);
      voA[i] = (unsigned)(row0 + r) * (LDA * 2) + ch * 16;
      if constexpr (EPI == EPI_WO) voA[i] = (unsigned)((ch >> 2) * S_ + row0 + r) * 64 + (ch & 3) * 16;
    }
#pragma unroll
    for (int b = 0; b < NFULL; ++b) {
      int colb = (EPI == EPI_QKV) ? (b * 1024 + tn * 64) : (tn * BN + b * 64);
#pragma unroll
      for (int j = 0; j < 2; ++j) {
        int r = (wid * 2 + j) * 8 + (lane >> 3);
        int f = ((r >> 1) & 1) | (((r >> 3) & 1) << 1);
        int chp = lane & 7;
        int gch = (((chp >> 1) ^ f) << 1) | (chp & 1);
        const int rs_ = (EPI == EPI_WO) ? ((r & 32) | ((r & 4) << 2) | ((r & 24) >> 1) | (r & 3)) : r;
        voB[b][j] = (unsigned)rs_ * pitchB + (pk_ ? b * 128 : colb * 2) + gch * 16;
        if constexpr (SPLIT) voB[b][j] = (unsigned)rs_ * pitchS[b] + gch * 16;
      }
    }
    if constexpr (HALF) {
      int r = wid * 16 + (lane >> 2);
      int chp = lane & 3;
      int gch = (((chp >> 1) ^ ((r >> 3) & 1)) << 1) | (chp & 1);
      voH = (unsigned)r * (LDB * 2) + (tn * BN + NFULL * 64) * 2 + gch * 16;
    }
    const char* Ab = reinterpret_cast<const char*>(A);
    const char* Wb = pk_ ? reinterpret_cast<const char*>(Wc) + (size_t)(tn >> 3) * (KD * NFULL * 128) : reinterpret_cast<const char*>(W);
    const unsigned lbase = (unsigned)(uintptr_t)(__attribute__((address_space(3))) char*)smem;
    auto stage = [&](int kt, int buf) {
      if constexpr (SPLIT) kt += kh * NT1;
      const char* a = Ab + (size_t)kt * (EPI == EPI_WO ? 2 * S_ * 64 : 128);
      const char* w = Wb + (size_t)(kt * (64 * pitchB));
      const unsigned s = lbase + buf * STAGE;
#pragma unroll
      for (int i = 0; i < 4; ++i) glds16(voA[i], a, s + (wid * 4 + i) * 1024);
#pragma unroll
      for (int b = 0; b < NFULL; ++b)
#pragma unroll
        for (int j = 0; j < 2; ++j) glds16(voB[b][j], SPLIT ? WbS[b] + (size_t)(kt * (64 * pitchS[b])) : w, s + A_BYTES + b * 8192 + (wid * 2 + j) * 1024);
      if constexpr (HALF) glds16(voH, w, s + A_BYTES + NFULL * 8192 + wid * 1024);
    };
    auto stage2 = [&](int kt, int buf) {
      kt += NT1;
      const char* a = Ab + (size_t)kt * 128;
      const unsigned s = lbase + buf * STAGE;
#pragma unroll
      for (int i = 0; i < 4; ++i) glds16(voA[i], a, s + (wid * 4 + i) * 1024);
#pragma unroll
      for (int j = 0; j < 2; ++j) glds16(voB[0][j], WbS[0] + (size_t)(kt * (64 * pitchS[0])), s + A_BYTES + (wid * 2 + j) * 1024);
    };
    constexpr int LPT = SPLIT ? 6 : LPS;
#pragma unroll
    for (int s = 0; s < NST; ++s) stage(s, s);
    constexpr int XL = 0;
    constexpr int NCS = HAS_CS ? (EPI == EPI_QKV ? 16 : 1) : 0;
    constexpr int XE = NCS + (EPI == EPI_QKV ? 1 : 0) + (HAS_ST ? 4 : 0) + (HAS_RES ? NIT * (EPI == EPI_W4 ? 2 : 1) : 0);
    f32x4 stp[4];
    const int lt = tid - 256;
    u32x4 wpx;
    f32x4 csp[HAS_CS ? NCS : 1];
    constexpr int XP = XPOS;
    wait_vm<(NST - 1 - LAG) * LPS>();
    __builtin_amdgcn_s_barrier();
#pragma unroll
    for (int kt = 0; kt < XP; ++kt) {
      wait_vm<(NST - 2 - LAG) * LPS>();
      __builtin_amdgcn_s_barrier();
      stage(kt + NST, kt % NST);
    }
    asm volatile("" ::: "memory");
    if constexpr (HAS_CS) {
      if (lt < BN / 4) {
        const float* cp = cs + (EPI == EPI_QKV ? (lt >> 4) * 1024 + tn * 64 + (lt & 15) * 4 : tn * BN + lt * 4);
#pragma unroll
        for (int i = 0; i < NCS; ++i) csp[i] = *reinterpret_cast<const f32x4*>(cp + i * LDB);
      }
    }
    if constexpr (EPI == EPI_QKV) wpx = *reinterpret_cast<const u32x4*>(wphi + tn * 2048 + lt * 8);
    if constexpr (HAS_ST) {
      const f32x4* sp = reinterpret_cast<const f32x4*>(st_in + (size_t)(row0 + (lt >> 1)) * 16 + (lt & 1) * 8);
#pragma unroll
      for (int i = 0; i < 4; ++i) stp[i] = sp[i];
    }
    ldres();
    asm volatile("" ::: "memory");
    constexpr int NPEEL = XP + NST - 1 - LAG;
    auto head = [&](auto xe) {
      constexpr int X = decltype(xe)::value;
#pragma unroll
      for (int kt = XP; kt < NPEEL; ++kt) {
        wait_vm<(NST - 2 - LAG) * LPS + X>();
        __builtin_amdgcn_s_barrier();
        stage(kt + NST, kt % NST);
      }
    };
    if (HAS_CS && wid == 0) head(std::integral_constant<int, XE>{});
    else head(std::integral_constant<int, XE - NCS>{});
    int buf = NPEEL % NST;
    for (int kt = NPEEL; kt < (SPLIT ? NT1 : NT) - NST; ++kt) {
      wait_vm<(NST - 2 - LAG) * LPS>();
      __builtin_amdgcn_s_barrier();
      stage(kt + NST, buf);
      buf = buf + 1 == NST ? 0 : buf + 1;
    }
    if constexpr (SPLIT) {
      for (int kt = NT1 - NST; kt < NT - NST; ++kt) {
        wait_vm<(NST - 2 - LAG) * LPT>();
        __builtin_amdgcn_s_barrier();
        stage2(kt + NST, buf);
        buf = buf + 1 == NST ? 0 : buf + 1;
      }
    }
    asm volatile("" ::: "memory");
#pragma unroll
    for (int r = 0; r < NST - 1; ++r) {
      constexpr int NSL = NST - LAG;
      if (r == 0) wait_vm<(NSL - 2) * LPT + XL>();
      else if (r == 1) wait_vm<(NSL > 3 ? NSL - 3 : 0) * LPT + XL>();
      else if (r == 2) wait_vm<(NSL > 4 ? NSL - 4 : 0) * LPT + XL>();
      else if (r == 3) wait_vm<(NSL > 5 ? NSL - 5 : 0) * LPT + XL>();
      else wait_vm<XL>();
      __builtin_amdgcn_s_barrier();
    }
    if constexpr (HAS_ST) {
#pragma unroll
      for (int i = 0; i < 4; ++i) asm volatile("" : "+v"(stp[i]));
      float sm = 0.f;
#pragma unroll
      for (int i = 0; i < 4; ++i) sm += stp[i][0] + stp[i][2];
      sm += __shfl_xor(sm, 1);
      float mean = sm * (1.0f / 1024.f);
      float m2 = 0.f;
#pragma unroll
      for (int i = 0; i < 4; ++i) {
        float d0 = stp[i][0] * (1.0f / 64.f) - mean, d1 = stp[i][2] * (1.0f / 64.f) - mean;
        m2 += stp[i][1] + stp[i][3] + 64.f * (d0 * d0 + d1 * d1);
      }
      m2 += __shfl_xor(m2, 1);
      float var = m2 * (1.0f / 1024.f);
      float r1_ = 1.0f / sqrtf(var + 1e-5f);
      float sc = r1_;
      if constexpr (EPI == EPI_W1) sc = r1_ / sqrtf(var * r1_ * r1_ + 1e-5f);
      if ((lt & 1) == 0) rs[lt >> 1] = (f32x4){mean, sc, r1_, 0.f};
    }
    if constexpr (HAS_CS) {
      if (lt < BN / 4) {
#pragma unroll
        for (int i = 0; i < NCS; ++i) asm volatile("" : "+v"(csp[i]));
#pragma unroll
        for (int i = NCS / 2; i > 0; i >>= 1)
#pragma unroll
          for (int j = 0; j < i; ++j) csp[j] += csp[j + i];
        *reinterpret_cast<f32x4*>(smem + CS_OFF + lt * 16) = csp[0];
      }
    }
    if constexpr (EPI == EPI_QKV) *reinterpret_cast<u32x4*>(smem + WP_OFF + lt * 16) = wpx;
  } else {
    ldres();
    const int q = l15 >> 2, p = lane & 3;
    const int fB = (q >> 1) | ((g & 1) << 1);
    unsigned aoff[2], boff[2], hoff = 0;
#pragma unroll
    for (int ks = 0; ks < 2; ++ks) aoff[ks] = (wm * 64 + l15) * 128 + (((4 * ks + g) ^ (l15 >> 1)) * 16);
#pragma unroll
    for (int u = 0; u < 2; ++u) boff[u] = A_BYTES + (8 * g + q) * 128 + (((2 * wn + u) ^ fB) * 32) + p * 8;
    if constexpr (HALF) hoff = A_BYTES + NFULL * 8192 + (8 * g + q) * 64 + ((wn ^ (g & 1)) * 32) + p * 8;

    auto ldf = [&](int i, const char* s, int ks, u32x4(&af)[4], u32x4(&bfr)[NTW]) {
      if (i == 0 || i > NTW) {
        const int mt = i == 0 ? 0 : i - NTW;
        af[mt] = *reinterpret_cast<const u32x4*>(s + aoff[ks] + mt * 2048);
      } else if (i - 1 < 2 * NFULL) {
        const int b = (i - 1) >> 1, u = (i - 1) & 1;
        s16x4 lo = __builtin_amdgcn_ds_read_tr16_b64_v4i16((lds_s16x4_p)(s + boff[u] + b * 8192 + ks * 4096));
        s16x4 hi = __builtin_amdgcn_ds_read_tr16_b64_v4i16((lds_s16x4_p)(s + boff[u] + b * 8192 + ks * 4096 + 512));
        bfr[i - 1] = __builtin_shufflevector(__builtin_bit_cast(u32x2, lo), __builtin_bit_cast(u32x2, hi), 0, 1, 2, 3);
      } else {
        s16x4 lo = __builtin_amdgcn_ds_read_tr16_b64_v4i16((lds_s16x4_p)(s + hoff + ks * 2048));
        s16x4 hi = __builtin_amdgcn_ds_read_tr16_b64_v4i16((lds_s16x4_p)(s + hoff + ks * 2048 + 256));
        bfr[NTW - 1] = __builtin_shufflevector(__builtin_bit_cast(u32x2, lo), __builtin_bit_cast(u32x2, hi), 0, 1, 2, 3);
      }
    };
    constexpr int NM = 4 * NTW, NL = NTW + 4;
    auto phase = [&](u32x4(&afc)[4], u32x4(&bfc)[NTW], u32x4(&afn)[4], u32x4(&bfn)[NTW], int nbuf, int nks, auto rd, auto bar) {
      const char* s = smem + nbuf * STAGE;
      constexpr int BAR = decltype(bar)::value, R0 = BAR < 0 ? 0 : BAR + 1;
      constexpr int RSP = (NM - R0 - 2) / NL > 0 ? (NM - R0 - 2) / NL : 1;
      static_assert(R0 + (NL - 1) * RSP < NM, "reads");
#pragma unroll
      for (int mt = 0; mt < 4; ++mt)
#pragma unroll
        for (int j = 0; j < NTW; ++j) {
          const int m = mt * NTW + j;
          if (m == BAR) {
            WAIT_L0();
            __builtin_amdgcn_s_barrier();
            asm volatile("" ::: "memory");
          }
          if (decltype(rd)::value && m >= R0 && (m - R0) % RSP == 0 && (m - R0) / RSP < NL) ldf((m - R0) / RSP, s, nks, afn, bfn);
          if (EPI == EPI_QKV && j >= 4) mfma_acc(acc[mt][j], afc[mt], bfc[j]);
          else mfma_acc(acc[mt][j], bfc[j], afc[mt]);
          __builtin_amdgcn_sched_barrier(0);
        }
    };
    typedef std::integral_constant<int, -1> nobar;
    auto phase2 = [&](u32x4(&afc)[4], u32x4(&bfc)[NTW], u32x4(&afn)[4], u32x4(&bfn)[NTW], int nbuf, int nks, auto rd, auto bar, auto nc) {
      const char* s = smem + nbuf * STAGE;
      constexpr int BAR = decltype(bar)::value, R0 = BAR < 0 ? 0 : BAR + 1, NC = decltype(nc)::value, NMC = 4 * NC, NLN = 6;
      constexpr bool DENSE = (NMC - R0 - 2) / NLN < 1;
#pragma unroll
      for (int mt = 0; mt < 4; ++mt)
#pragma unroll
        for (int j = 0; j < NC; ++j) {
          const int m = mt * NC + j;
          if (m == BAR) {
            WAIT_L0();
            __builtin_amdgcn_s_barrier();
            asm volatile("" ::: "memory");
          }
          if (decltype(rd)::value) {
#pragma unroll
            for (int i = 0; i < NLN; ++i) {
              const int slot = DENSE ? R0 + (i * (NMC - R0)) / NLN : R0 + i * ((NMC - R0 - 2) / NLN);
              if (slot == m) ldf(i <= 2 ? i : i + NTW - 2, s, nks, afn, bfn);
            }
          }
          mfma_acc(acc[mt][j], bfc[j], afc[mt]);
          __builtin_amdgcn_sched_barrier(0);
        }
    };
#pragma unroll
    for (int mt = 0; mt < 4; ++mt)
#pragma unroll
      for (int j = 0; j < NTW; ++j) {
        acc[mt][j] = (f32x4){0.f, 0.f, 0.f, 0.f};
        asm volatile("" : "+a"(acc[mt][j]));
      }
    u32x4 af0[4], af1[4], bf0[NTW], bf1[NTW];
    __builtin_amdgcn_s_barrier();
    asm volatile("" ::: "memory");
    STAMP(1);
#pragma unroll
    for (int i = 0; i < NL; ++i) { ldf(i, smem, 0, af0, bf0); __builtin_amdgcn_sched_barrier(0); }
    int buf = 0;
    if constexpr (SPLIT) {
      typedef std::integral_constant<int, 2> c2;
      typedef std::integral_constant<int, 4> c4;
      f32x4* xs = reinterpret_cast<f32x4*>(kvp) + (size_t)(blockIdx.x * 4 + wid) * 512 + lane;
      const f32x4* xr = reinterpret_cast<const f32x4*>(kvp) + (size_t)((blockIdx.x ^ 1) * 4 + wid) * 512 + lane;
      unsigned* fl = reinterpret_cast<unsigned*>(zp);
      for (int kt = 0; kt < NT1 - 1; ++kt) {
        phase(af0, bf0, af1, bf1, buf, 1, std::true_type{}, nobar{});
        buf = buf + 1 == NST ? 0 : buf + 1;
        phase(af1, bf1, af0, bf0, buf, 0, std::true_type{}, c4{});
      }
      phase(af0, bf0, af1, bf1, buf, 1, std::true_type{}, nobar{});
      buf = buf + 1 == NST ? 0 : buf + 1;
      phase2(af1, bf1, af0, bf0, buf, 0, std::true_type{}, c4{}, c4{});
      auto run2 = [&](int n) {
        for (int kt = 0; kt < n; ++kt) {
          phase2(af0, bf0, af1, bf1, buf, 1, std::true_type{}, nobar{}, c2{});
          buf = buf + 1 == NST ? 0 : buf + 1;
          phase2(af1, bf1, af0, bf0, buf, 0, std::true_type{}, c2{}, c2{});
        }
      };
      run2(1);
#pragma unroll
      for (int mt = 0; mt < 4; ++mt)
#pragma unroll
        for (int u = 0; u < 2; ++u)
          asm volatile("global_store_dwordx4 %0, %1, off sc1\n\ts_nop 1" :: "v"(xs + (mt * 2 + u) * 64), "a"(acc[mt][2 + u]) : "memory");
      run2(W4FK);
      WAIT_V(0);
      if (lane == 0) __hip_atomic_store(fl + blockIdx.x * 4 + wid, 1u, __ATOMIC_RELAXED, __HIP_MEMORY_SCOPE_AGENT);
      run2(NT2 - 2 - W4FK - W4RK);
      while (__hip_atomic_load(fl + (blockIdx.x ^ 1) * 4 + wid, __ATOMIC_RELAXED, __HIP_MEMORY_SCOPE_AGENT) == 0u) __builtin_amdgcn_s_sleep(1);
      asm volatile("" ::: "memory");
#pragma unroll
      for (int mt = 0; mt < 4; ++mt)
#pragma unroll
        for (int u = 0; u < 2; ++u)
          asm volatile("global_load_dwordx4 %0, %1, off sc1" : "=a"(acc[mt][2 + u]) : "v"(xr + (mt * 2 + u) * 64) : "memory");
      run2(W4RK);
      phase2(af0, bf0, af1, bf1, buf, 1, std::true_type{}, nobar{}, c2{});
      phase2(af1, bf1, af0, bf0, buf, 0, std::false_type{}, nobar{}, c2{});
      asm volatile("s_waitcnt vmcnt(0)\n\ts_nop 7\n\ts_nop 7\n\ts_nop 7"
                   : "+a"(acc[0][2]), "+a"(acc[0][3]), "+a"(acc[1][2]), "+a"(acc[1][3]), "+a"(acc[2][2]), "+a"(acc[2][3]), "+a"(acc[3][2]), "+a"(acc[3][3]),
                     "+a"(acc[0][0]), "+a"(acc[0][1]), "+a"(acc[1][0]), "+a"(acc[1][1]), "+a"(acc[2][0]), "+a"(acc[2][1]), "+a"(acc[3][0]), "+a"(acc[3][1]) :: "memory");
    } else if constexpr (LAG) {
      phase(af0, bf0, af1, bf1, 0, 1, std::true_type{}, nobar{});
      for (int kt = 0; kt < NT - 1; ++kt) {
        buf = buf + 1 == NST ? 0 : buf + 1;
        phase(af1, bf1, af0, bf0, buf, 0, std::true_type{}, nobar{});
        asm volatile("" ::: "memory");
        __builtin_amdgcn_s_barrier();
        asm volatile("" ::: "memory");
        phase(af0, bf0, af1, bf1, buf, 1, std::true_type{}, nobar{});
      }
      phase(af1, bf1, af0, bf0, buf, 0, std::false_type{}, nobar{});
    } else {
      for (int kt = 0; kt < NT - 1; ++kt) {
        phase(af0, bf0, af1, bf1, buf, 1, std::true_type{}, nobar{});
        buf = buf + 1 == NST ? 0 : buf + 1;
        phase(af1, bf1, af0, bf0, buf, 0, std::true_type{}, std::integral_constant<int, 4>{});
      }
      phase(af0, bf0, af1, bf1, buf, 1, std::true_type{}, nobar{});
      phase(af1, bf1, af0, bf0, buf, 0, std::false_type{}, nobar{});
    }
    asm volatile("s_nop 7\n\ts_nop 7\n\ts_nop 7" ::: "memory");
    if constexpr (SPLIT) {
#pragma unroll
      for (int mt = 0; mt < 4; ++mt)
#pragma unroll
        for (int u = 0; u < 2; ++u) acc[mt][u] += acc[mt][2 + u];
    }
    if constexpr (EPI == EPI_QKV) {
#pragma unroll
      for (int mt = 0; mt < 4; ++mt)
#pragma unroll
        for (int t = 0; t < 2; ++t) *reinterpret_cast<f32x4*>(smem + QX_OFF + wid * 8192 + (mt * 2 + t) * 1024 + lane * 16) = acc[mt][t];
    }
  }
  WAIT_L0();
  __builtin_amdgcn_s_barrier();
  asm volatile("" ::: "memory");
  STAMP(2);

  if constexpr (EPI == EPI_QKV) {
    const int h = tn * 2 + wn;
    const int q = l15 >> 2, p = lane & 3;
    f32x4 kv[4][2], zz[4];
    float zsel = 0.f;
    float* red = reinterpret_cast<float*>(smem + 4 * 64 * 144) + wn * (64 * 32 + 64);
    bf16x8 wf[2];
#pragma unroll
    for (int ft = 0; ft < 2; ++ft) {
      const char* src = smem + WP_OFF + wn * 2048 + (4 * g + q) * 64 + ft * 32 + p * 8;
      s16x4 lo = __builtin_amdgcn_ds_read_tr16_b64_v4i16((lds_s16x4_p)(src));
      s16x4 hi = __builtin_amdgcn_ds_read_tr16_b64_v4i16((lds_s16x4_p)(src + 16 * 64));
      wf[ft] = __builtin_shufflevector(lo, hi, 0, 1, 2, 3, 4, 5, 6, 7);
    }
    const float* csl = reinterpret_cast<const float*>(smem + CS_OFF);
    auto feat = [&](const bf16x8& xf, u32x2 (&o)[4]) {
      const float L2E = 1.4426950408889634f;
      f32x4 z4 = (f32x4){0.f, 0.f, 0.f, 0.f};
      f32x4 p0 = __builtin_amdgcn_mfma_f32_16x16x32_bf16(wf[0], xf, z4, 0, 0, 0);
      f32x4 p1 = __builtin_amdgcn_mfma_f32_16x16x32_bf16(wf[1], xf, z4, 0, 0, 0);
      f32x2 v[4];
      {
        const unsigned r0_ = pk2(p0[0], p0[1]), r1_ = pk2(p0[2], p0[3]), r2_ = pk2(p1[0], p1[1]), r3_ = pk2(p1[2], p1[3]);
        v[0] = (f32x2){bflo(r0_), bfhi(r0_)}; v[1] = (f32x2){bflo(r1_), bfhi(r1_)};
        v[2] = (f32x2){bflo(r2_), bfhi(r2_)}; v[3] = (f32x2){bflo(r3_), bfhi(r3_)};
      }
      float mx = max2_(max3_(max3_(v[0][0], v[0][1], v[1][0]), v[1][1], v[2][0]), max3_(v[2][1], v[3][0], v[3][1]));
      float mn = min2_(min3_(min3_(v[0][0], v[0][1], v[1][0]), v[1][1], v[2][0]), min3_(v[2][1], v[3][0], v[3][1]));
      mx = rmax4(mx); mn = rmin4(mn);
      const float mxl = -mx * L2E, mnl = mn * L2E;
      f32x2 ep[4], en[4];
#pragma unroll
      for (int i = 0; i < 4; ++i) {
        const f32x2 ap = v[i] * L2E + mxl, an = v[i] * (-L2E) + mnl;
        ep[i] = (f32x2){__builtin_amdgcn_exp2f(ap[0]), __builtin_amdgcn_exp2f(ap[1])};
        en[i] = (f32x2){__builtin_amdgcn_exp2f(an[0]), __builtin_amdgcn_exp2f(an[1])};
      }
      const f32x2 sp2 = (ep[0] + ep[1]) + (ep[2] + ep[3]), sn2 = (en[0] + en[1]) + (en[2] + en[3]);
      const float ip = __builtin_amdgcn_rcpf(rsum4(sp2[0] + sp2[1])), in_ = __builtin_amdgcn_rcpf(rsum4(sn2[0] + sn2[1]));
      o[0] = (u32x2){pk2v(ep[0] * ip), pk2v(ep[1] * ip)};
      o[1] = (u32x2){pk2v(ep[2] * ip), pk2v(ep[3] * ip)};
      o[2] = (u32x2){pk2v(en[0] * in_), pk2v(en[1] * in_)};
      o[3] = (u32x2){pk2v(en[2] * in_), pk2v(en[3] * in_)};
    };
    if (wid8 >= 4) {
      f32x4 cs4[2];
#pragma unroll
      for (int t = 0; t < 2; ++t) cs4[t] = *reinterpret_cast<const f32x4*>(csl + wn * 32 + t * 16 + 4 * g);
      bf16x8 qf[4];
#pragma unroll
      for (int mt = 0; mt < 4; ++mt) {
        f32x4 st = rs[wm * 64 + mt * 16 + l15];
        unsigned pq_[4];
#pragma unroll
        for (int t = 0; t < 2; ++t) {
          const f32x4 ac = *reinterpret_cast<const f32x4*>(smem + QX_OFF + wid * 8192 + (mt * 2 + t) * 1024 + lane * 16);
          float a[4];
#pragma unroll
          for (int i = 0; i < 4; ++i) a[i] = st[1] * (ac[i] - st[0] * cs4[t][i]);
          pq_[t * 2] = pk2(a[0], a[1]); pq_[t * 2 + 1] = pk2(a[2], a[3]);
        }
        qf[mt] = __builtin_bit_cast(bf16x8, (u32x4){pq_[0], pq_[1], pq_[2], pq_[3]});
      }
#pragma unroll
      for (int mt = 0; mt < 4; ++mt) {
        u32x2 o[4];
        feat(qf[mt], o);
        u32x4* dst = reinterpret_cast<u32x4*>(out) + ((size_t)(tm * 8 + wm * 4 + mt) * 32 + h) * 128 + lane;
        wt_store((u32x4){o[0][0], o[0][1], o[1][0], o[1][1]}, dst);
        wt_store((u32x4){o[2][0], o[2][1], o[3][0], o[3][1]}, dst + 64);
      }
    } else {
      f32x4 cs4[2];
      float csv[2];
#pragma unroll
      for (int t = 0; t < 2; ++t) {
        cs4[t] = *reinterpret_cast<const f32x4*>(csl + 64 + wn * 32 + t * 16 + 4 * g);
        csv[t] = csl[128 + wn * 32 + t * 16 + l15];
      }
      bf16x8 kf[4], vf[2][2];
#pragma unroll
      for (int mt = 0; mt < 4; ++mt) {
        f32x4 st = rs[wm * 64 + mt * 16 + l15];
        unsigned pk_[4];
#pragma unroll
        for (int t = 0; t < 2; ++t) {
          float b[4];
#pragma unroll
          for (int i = 0; i < 4; ++i) b[i] = st[1] * (acc[mt][2 + t][i] - st[0] * cs4[t][i]);
          pk_[t * 2] = pk2(b[0], b[1]); pk_[t * 2 + 1] = pk2(b[2], b[3]);
        }
        kf[mt] = __builtin_bit_cast(bf16x8, (u32x4){pk_[0], pk_[1], pk_[2], pk_[3]});
      }
      char* pkl = smem + wid * (64 * 144);
#pragma unroll
      for (int mt = 0; mt < 4; ++mt) {
        u32x2 o[4];
        feat(kf[mt], o);
        char* dst = pkl + (mt * 16 + l15) * 144 + g * 8;
#pragma unroll
        for (int c = 0; c < 4; ++c) *reinterpret_cast<u32x2*>(dst + c * 32) = o[c];
      }
#pragma unroll
      for (int s = 0; s < 2; ++s) {
        unsigned pv[2][4];
#pragma unroll
        for (int m2 = 0; m2 < 2; ++m2) {
          int mt = s * 2 + m2;
          f32x4 s0 = rs[wm * 64 + mt * 16 + 4 * g + 0], s1 = rs[wm * 64 + mt * 16 + 4 * g + 1];
          f32x4 s2 = rs[wm * 64 + mt * 16 + 4 * g + 2], s3 = rs[wm * 64 + mt * 16 + 4 * g + 3];
#pragma unroll
          for (int t = 0; t < 2; ++t) {
            float a0 = s0[1] * (acc[mt][4 + t][0] - s0[0] * csv[t]);
            float a1 = s1[1] * (acc[mt][4 + t][1] - s1[0] * csv[t]);
            float a2 = s2[1] * (acc[mt][4 + t][2] - s2[0] * csv[t]);
            float a3 = s3[1] * (acc[mt][4 + t][3] - s3[0] * csv[t]);
            pv[t][m2 * 2] = pk2(a0, a1); pv[t][m2 * 2 + 1] = pk2(a2, a3);
          }
        }
#pragma unroll
        for (int t = 0; t < 2; ++t) vf[s][t] = __builtin_bit_cast(bf16x8, (u32x4){pv[t][0], pv[t][1], pv[t][2], pv[t][3]});
      }
      WAIT_L0();
      __builtin_amdgcn_sched_barrier(0);
#pragma unroll
      for (int ft = 0; ft < 4; ++ft) { kv[ft][0] = kv[ft][1] = zz[ft] = (f32x4){0.f, 0.f, 0.f, 0.f}; }
      const bf16x8 ones = (bf16x8){0x3F80, 0x3F80, 0x3F80, 0x3F80, 0x3F80, 0x3F80, 0x3F80, 0x3F80};
#pragma unroll
      for (int s = 0; s < 2; ++s)
#pragma unroll
        for (int ft = 0; ft < 4; ++ft) {
          const char* src = pkl + (32 * s + 4 * g + q) * 144 + ft * 32 + p * 8;
          s16x4 lo = __builtin_amdgcn_ds_read_tr16_b64_v4i16((lds_s16x4_p)(src));
          s16x4 hi = __builtin_amdgcn_ds_read_tr16_b64_v4i16((lds_s16x4_p)(src + 16 * 144));
          bf16x8 pf = __builtin_shufflevector(lo, hi, 0, 1, 2, 3, 4, 5, 6, 7);
          kv[ft][0] = __builtin_amdgcn_mfma_f32_16x16x32_bf16(vf[s][0], pf, kv[ft][0], 0, 0, 0);
          kv[ft][1] = __builtin_amdgcn_mfma_f32_16x16x32_bf16(vf[s][1], pf, kv[ft][1], 0, 0, 0);
          zz[ft] = __builtin_amdgcn_mfma_f32_16x16x32_bf16(ones, pf, zz[ft], 0, 0, 0);
        }
      zsel = g == 0 ? zz[0][0] : g == 1 ? zz[1][0] : g == 2 ? zz[2][0] : zz[3][0];
      if (wm == 1) {
#pragma unroll
        for (int ft = 0; ft < 4; ++ft)
#pragma unroll
          for (int dt = 0; dt < 2; ++dt) *reinterpret_cast<f32x4*>(red + ((ft * 2 + dt) * 64 + lane) * 4) = kv[ft][dt];
        red[64 * 32 + lane] = zsel;
      }
    }
    __syncthreads();
    if (wid8 < 2) {
      float* kd = kvp + (size_t)(h * 16 + tm) * 2048;
      float* zd = zp + (size_t)(h * 16 + tm) * 64;
#pragma unroll
      for (int ft = 0; ft < 4; ++ft)
#pragma unroll
        for (int dt = 0; dt < 2; ++dt) {
          f32x4 o = *reinterpret_cast<const f32x4*>(red + ((ft * 2 + dt) * 64 + lane) * 4);
          wt_store(o + kv[ft][dt], reinterpret_cast<f32x4*>(kd + ((ft * 2 + dt) * 64 + lane) * 4));
        }
      zd[lane] = red[64 * 32 + lane] + zsel;
    }
  } else {
    if (wid8 < 4) {
      f32x4 stm[4], cs4[NTW];
      if constexpr (EPI == EPI_W1 || EPI == EPI_W3) {
#pragma unroll
        for (int j = 0; j < NTW; ++j) {
          const int colj = (j < 2 * NFULL) ? ((j >> 1) * 64 + (2 * wn + (j & 1)) * 16) : (NFULL * 64 + wn * 16);
          cs4[j] = *reinterpret_cast<const f32x4*>(smem + CS_OFF + (colj + 4 * g) * 4);
        }
#pragma unroll
        for (int mt = 0; mt < 4; ++mt) stm[mt] = rs[wm * 64 + mt * 16 + l15];
      }
#pragma unroll
      for (int j = 0; j < (SPLIT ? NTW / 2 : NTW); ++j) {
        const int colj = (j < 2 * NFULL) ? ((j >> 1) * 64 + (2 * wn + (j & 1)) * 16) : (NFULL * 64 + wn * 16);
#pragma unroll
        for (int mt = 0; mt < 4; ++mt) {
          f32x2 v0 = (f32x2){acc[mt][j][0], acc[mt][j][1]}, v1 = (f32x2){acc[mt][j][2], acc[mt][j][3]};
          if constexpr (EPI == EPI_W1 || EPI == EPI_W3) {
            v0 = gelu2((v0 - stm[mt][0] * (f32x2){cs4[j][0], cs4[j][1]}) * stm[mt][1]);
            v1 = gelu2((v1 - stm[mt][0] * (f32x2){cs4[j][2], cs4[j][3]}) * stm[mt][1]);
          }
          *reinterpret_cast<u32x2*>(smem + (wm * 64 + mt * 16 + l15) * PB + (colj + 4 * g) * 2) = (u32x2){pk2(v0[0], v0[1]), pk2(v1[0], v1[1])};
        }
      }
    }
    __syncthreads();
#pragma unroll
    for (int it = 0; it < NIT; ++it) {
      const int c = tid + 512 * it, r = c / CH, ch = c % CH;
      u32x4 v = *reinterpret_cast<const u32x4*>(smem + r * PB + ch * 16);
      const int grow = row0 + r, gcol = tne * BNE + ch * 8;
      if constexpr (EPI == EPI_W3) {
        wt_store(v, reinterpret_cast<u32x4*>(out + (size_t)grow * F_ + gcol));
      } else if constexpr (EPI == EPI_W1) {
        const int grp = tn >> 2;
        const int sh = grp == 0 ? 1 : grp == 1 ? -1 : grp == 2 ? 2 : -2;
        const int dr = grow + sh;
        if (dr >= 0 && dr < S_) wt_store(v, reinterpret_cast<u32x4*>(out + (size_t)dr * D_ + gcol));
        const int zr = grow - sh;
        if (zr < 0 || zr >= S_) *reinterpret_cast<u32x4*>(out + (size_t)grow * D_ + gcol) = (u32x4){0u, 0u, 0u, 0u};
      } else {
        float y[8];
        const u32x4 a = ra[it];
        if constexpr (EPI == EPI_WO) {
#pragma unroll
          for (int i = 0; i < 4; ++i) { y[2 * i] = rbf(bflo(a[i]) + bflo(v[i])); y[2 * i + 1] = rbf(bfhi(a[i]) + bfhi(v[i])); }
        } else if constexpr (EPI == EPI_W2) {
          f32x4 st = rs[r];
#pragma unroll
          for (int i = 0; i < 4; ++i) {
            y[2 * i] = rbf(rbf((bflo(a[i]) - st[0]) * st[2]) + bflo(v[i]));
            y[2 * i + 1] = rbf(rbf((bfhi(a[i]) - st[0]) * st[2]) + bfhi(v[i]));
          }
        } else {
          const u32x4 b = rb[it];
#pragma unroll
          for (int i = 0; i < 4; ++i) {
            y[2 * i] = rbf(bflo(a[i]) + rbf(bflo(b[i]) + bflo(v[i])));
            y[2 * i + 1] = rbf(bfhi(a[i]) + rbf(bfhi(b[i]) + bfhi(v[i])));
          }
        }
        wt_store((u32x4){pk2(y[0], y[1]), pk2(y[2], y[3]), pk2(y[4], y[5]), pk2(y[6], y[7])}, reinterpret_cast<u32x4*>(out + (size_t)grow * D_ + gcol));
        float sm = ((y[0] + y[1]) + (y[2] + y[3])) + ((y[4] + y[5]) + (y[6] + y[7]));
        sm = rsum8(sm);
        float mu = sm * (1.0f / 64.f), m2 = 0.f;
#pragma unroll
        for (int i = 0; i < 8; ++i) { float d = y[i] - mu; m2 += d * d; }
        m2 = rsum8(m2);
        if (ch == 0) st_out[(size_t)grow * 16 + tne] = (f32x2){sm, m2};
      }
    }
  }
  STAMP(3);
}

__global__ __launch_bounds__(256, 1) void attn_out_kernel(const u16* __restrict__ phiq, const float* __restrict__ kvp,
                                                          const float* __restrict__ zp, u16* __restrict__ o,
                                                          const float* __restrict__ cp1, const float* __restrict__ cp3,
                                                          float* __restrict__ cs1, float* __restrict__ cs3) {
  __shared__ __attribute__((aligned(16))) u16 kvT[48 * 72];
  const int tid = threadIdx.x, lane = tid & 63, wid = tid >> 6, g = lane >> 4, l15 = lane & 15;
  if (blockIdx.x >= 256) {
    const int i = (blockIdx.x - 256) * 256 + tid;
    const float* src = i < 1024 ? cp1 + i : cp3 + (i - 1024);
    const int ld = i < 1024 ? 1024 : F_;
    float c[16];
#pragma unroll
    for (int t = 0; t < 16; ++t) c[t] = src[t * ld];
#pragma unroll
    for (int t = 8; t > 0; t >>= 1)
#pragma unroll
      for (int j = 0; j < t; ++j) c[j] += c[j + t];
    *(i < 1024 ? cs1 + i : cs3 + (i - 1024)) = c[0];
    return;
  }
  const int h = blockIdx.x & 31, tg = blockIdx.x >> 5;
  f32x4 kvv[16][2];
  float zv[16];
  bf16x8 pq[4][2];
  {
    const float* src = kvp + (size_t)h * 16 * 2048 + tid * 8;
#pragma unroll
    for (int t = 0; t < 16; ++t) {
      kvv[t][0] = *reinterpret_cast<const f32x4*>(src + t * 2048);
      kvv[t][1] = *reinterpret_cast<const f32x4*>(src + t * 2048 + 4);
    }
#pragma unroll
    for (int t = 0; t < 16; ++t) zv[t] = zp[(size_t)(h * 16 + t) * 64 + lane];
#pragma unroll
    for (int mt = 0; mt < 4; ++mt) {
      const u16* ps = phiq + ((size_t)(tg * 16 + wid * 4 + mt) * 32 + h) * 1024 + lane * 8;
      pq[mt][0] = *reinterpret_cast<const bf16x8*>(ps);
      pq[mt][1] = *reinterpret_cast<const bf16x8*>(ps + 512);
    }
  }
  __builtin_amdgcn_sched_barrier(0);
  {
    for (int i = tid; i < 15 * 64; i += 256) kvT[(33 + i / 64) * 72 + (i & 63)] = 0;
    f32x4 a0 = kvv[0][0], a1 = kvv[0][1];
#pragma unroll
    for (int t = 1; t < 16; ++t) { a0 += kvv[t][0]; a1 += kvv[t][1]; }
    const int ft = tid >> 6, dt = (tid >> 5) & 1, ln = (2 * tid) & 63;
    const int d0 = dt * 16 + 4 * (ln >> 4), pos = (ft >> 1) * 32 + ((ln >> 2) & 3) * 8 + (ft & 1) * 4 + (ln & 3);
#pragma unroll
    for (int i = 0; i < 4; ++i) *reinterpret_cast<unsigned*>(&kvT[(d0 + i) * 72 + pos]) = pk2(a0[i], a1[i]);
    if (tid < 64) {
      float z = 0.f;
#pragma unroll
      for (int t = 0; t < 16; ++t) z += zv[t];
      kvT[32 * 72 + (tid >> 5) * 32 + ((tid >> 2) & 3) * 8 + ((tid >> 4) & 1) * 4 + (tid & 3)] = (u16)(pk2(z, 0.f) & 0xffff);
    }
  }
  __syncthreads();
  bf16x8 kf[3][2];
#pragma unroll
  for (int dt = 0; dt < 3; ++dt)
#pragma unroll
    for (int ks = 0; ks < 2; ++ks) kf[dt][ks] = *reinterpret_cast<const bf16x8*>(&kvT[(dt * 16 + l15) * 72 + ks * 32 + g * 8]);
#pragma unroll
  for (int mt = 0; mt < 4; ++mt) {
    const int tok = tg * 256 + wid * 64 + mt * 16 + l15;
    const bf16x8 p0 = pq[mt][0], p1 = pq[mt][1];
    f32x4 r[3];
#pragma unroll
    for (int dt = 0; dt < 3; ++dt) {
      r[dt] = (f32x4){0.f, 0.f, 0.f, 0.f};
      r[dt] = __builtin_amdgcn_mfma_f32_16x16x32_bf16(kf[dt][0], p0, r[dt], 0, 0, 0);
      r[dt] = __builtin_amdgcn_mfma_f32_16x16x32_bf16(kf[dt][1], p1, r[dt], 0, 0, 0);
    }
    float den = rbf(rbf(__shfl(r[2][0], l15)) + 1e-6f);
    float inv = 1.0f / den;
    const u32x4 w = (u32x4){pk2(rbf(r[0][0]) * inv, rbf(r[0][1]) * inv), pk2(rbf(r[0][2]) * inv, rbf(r[0][3]) * inv),
                            pk2(rbf(r[1][0]) * inv, rbf(r[1][1]) * inv), pk2(rbf(r[1][2]) * inv, rbf(r[1][3]) * inv)};
    wt_store(w, reinterpret_cast<u32x4*>(o + ((size_t)h * S_ + tok) * 32 + 8 * g));
  }
}

constexpr int WC_QKV = 0, WC_WO = WC_QKV + 2 * 1024 * 384, WC_W1 = WC_WO + 2 * 1024 * 128, WC_W2 = WC_W1 + 2 * 1024 * 128,
              WC_W4 = WC_W2 + 2 * 1024 * 128, WC_END = WC_W4 + 2 * F_ * 128;
__global__ __launch_bounds__(256) void prep_kernel(const u16* __restrict__ x, const u16* __restrict__ wqkv,
                                                   const u16* __restrict__ w1, const u16* __restrict__ w3,
                                                   float* __restrict__ cs_qkv, float* __restrict__ cs_w1,
                                                   float* __restrict__ cs_w3, f32x2* __restrict__ st,
                                                   const u16* __restrict__ wo, const u16* __restrict__ w2,
                                                   const u16* __restrict__ w4, char* __restrict__ wc, unsigned* __restrict__ flags) {
  __shared__ __attribute__((aligned(16))) float red[4][512];
  const int tid = threadIdx.x, b = blockIdx.x;
  if (b >= 368) {
    const int pb = b - 368;
    const u16* w = pb < 32 ? wo + (size_t)pb * 32768 : pb < 64 ? w2 + (size_t)(pb - 32) * 32768 : w4 + (size_t)(pb - 64) * 32768;
    u32x4 v[16];
#pragma unroll
    for (int r = 0; r < 16; ++r) v[r] = *reinterpret_cast<const u32x4*>(w + r * 2048 + tid * 8);
    if (((tid >> 3) & 7) == 3) {
      const int kd = pb < 64 ? 1024 : F_, row = (pb < 32 ? pb : pb < 64 ? pb - 32 : pb - 64) * 32 + (tid >> 7);
      char* dst = wc + (pb < 32 ? WC_WO : pb < 64 ? WC_W2 : WC_W4) + (size_t)((tid >> 6) & 1) * kd * 128 + (size_t)row * 128 + (tid & 7) * 16;
#pragma unroll
      for (int r = 0; r < 16; ++r) *reinterpret_cast<u32x4*>(dst + r * 256) = v[r];
    }
#pragma unroll
    for (int r = 0; r < 16; ++r) asm volatile("" ::"v"(v[r]));
  } else if (b < 240) {
    const u16* w; float* dst; int ld, ch, rg;
    if (b < 96) { w = wqkv; dst = cs_qkv; ld = 3072; ch = b % 6; rg = b / 6; }
    else if (b < 128) { w = w1; dst = cs_w1; ld = 1024; ch = (b - 96) & 1; rg = (b - 96) >> 1; }
    else { w = w3; dst = cs_w3; ld = F_; ch = (b - 128) % 7; rg = (b - 128) / 7; }
    const int lane = tid & 63, wid = tid >> 6, row = rg * 64 + wid * 16;
    u32x4 v[16];
#pragma unroll
    for (int r = 0; r < 16; ++r) v[r] = *reinterpret_cast<const u32x4*>(w + (size_t)(row + r) * ld + ch * 512 + lane * 8);
    if (b < 128 && (lane >> 3) == 3) {
      const int cpp = b < 96 ? 384 : 128;
      char* cpd = wc + (b < 96 ? WC_QKV + (ch & 1) * (1024 * 384) + (ch >> 1) * 128 : WC_W1 + ch * (1024 * 128)) + row * cpp + (lane & 7) * 16;
#pragma unroll
      for (int r = 0; r < 16; ++r) *reinterpret_cast<u32x4*>(cpd + r * cpp) = v[r];
    }
    f32x4 s0 = (f32x4){0.f, 0.f, 0.f, 0.f}, s1 = s0;
#pragma unroll
    for (int r = 0; r < 16; ++r) {
      s0 += (f32x4){bflo(v[r][0]), bfhi(v[r][0]), bflo(v[r][1]), bfhi(v[r][1])};
      s1 += (f32x4){bflo(v[r][2]), bfhi(v[r][2]), bflo(v[r][3]), bfhi(v[r][3])};
    }
    *reinterpret_cast<f32x4*>(&red[wid][lane * 8]) = s0;
    *reinterpret_cast<f32x4*>(&red[wid][lane * 8 + 4]) = s1;
    __syncthreads();
    f32x2 t = *reinterpret_cast<const f32x2*>(&red[0][tid * 2]);
#pragma unroll
    for (int i = 1; i < 4; ++i) t += *reinterpret_cast<const f32x2*>(&red[i][tid * 2]);
    *reinterpret_cast<f32x2*>(dst + rg * ld + ch * 512 + tid * 2) = t;
  } else {
    const int lane = tid & 63, wid = tid >> 6;
    const int rbase = (b - 240) * 16 + wid * 4;
    if (b < 248) flags[(b - 240) * 256 + tid] = 0u;
    u32x4 xv[8];
#pragma unroll
    for (int i = 0; i < 8; ++i) xv[i] = *reinterpret_cast<const u32x4*>(x + (size_t)rbase * D_ + i * 512 + lane * 8);
    __builtin_amdgcn_sched_barrier(0);
#pragma unroll
    for (int rr = 0; rr < 4; ++rr) {
      const int row = rbase + rr;
#pragma unroll
      for (int hf = 0; hf < 2; ++hf) {
        const u32x4 v = xv[rr * 2 + hf];
        float y[8];
#pragma unroll
        for (int i = 0; i < 4; ++i) { y[2 * i] = bflo(v[i]); y[2 * i + 1] = bfhi(v[i]); }
        float sm = ((y[0] + y[1]) + (y[2] + y[3])) + ((y[4] + y[5]) + (y[6] + y[7]));
        sm = rsum8(sm);
        float mu = sm * (1.0f / 64.f), m2 = 0.f;
#pragma unroll
        for (int i = 0; i < 8; ++i) { float d = y[i] - mu; m2 += d * d; }
        m2 = rsum8(m2);
        if ((lane & 7) == 0) st[(size_t)row * 16 + hf * 8 + (lane >> 3)] = (f32x2){sm, m2};
      }
    }
  }
}

extern "C" void kernel_launch(void* const* d_in, const int* in_sizes, int n_in, void* d_out, int out_size, void* d_ws,
                              size_t ws_size, hipStream_t stream) {
  const u16* x_in = (const u16*)d_in[0];
  const u16* wqkv = (const u16*)d_in[1];
  const u16* wphi = (const u16*)d_in[2];
  const u16* wo = (const u16*)d_in[3];
  const u16* w1 = (const u16*)d_in[5];
  const u16* w2 = (const u16*)d_in[6];
  const u16* w3 = (const u16*)d_in[7];
  const u16* w4 = (const u16*)d_in[8];
  char* ws = (char*)d_ws;
  size_t off = 0;
  auto alloc = [&](size_t bytes) { char* p = ws + off; off += (bytes + 4095) & ~(size_t)4095; return p; };
  u16* xb = (u16*)alloc((size_t)S_ * D_ * 2);
  u16* x2 = (u16*)alloc((size_t)S_ * D_ * 2);
  u16* x1 = (u16*)alloc((size_t)S_ * D_ * 2);
  u16* hs = (u16*)alloc((size_t)S_ * D_ * 2);
  u16* ob = (u16*)alloc((size_t)S_ * D_ * 2);
  u16* gb = (u16*)alloc((size_t)S_ * F_ * 2);
  u16* phiq = (u16*)alloc((size_t)S_ * 2048 * 2);
  float* kvp = (float*)alloc((size_t)32 * 16 * 2048 * 4);
  float* zp = (float*)alloc((size_t)32 * 16 * 64 * 4);
  f32x2* st_x = (f32x2*)alloc((size_t)S_ * 16 * 8);
  f32x2* st_x2 = (f32x2*)alloc((size_t)S_ * 16 * 8);
  f32x2* st_x1 = (f32x2*)alloc((size_t)S_ * 16 * 8);
  float* cs_qkv = (float*)alloc(16 * 3072 * 4);
  float* cs_w1 = (float*)alloc(16 * 1024 * 4);
  float* cs_w3 = (float*)alloc(16 * F_ * 4);
  float* cf_w1 = (float*)alloc(1024 * 4);
  float* cf_w3 = (float*)alloc(F_ * 4);
  char* wc = alloc(WC_END);
  float* xch = (float*)alloc((size_t)256 * 4 * 8192);
  unsigned* flags = (unsigned*)alloc(2 * 1024 * 4);

  prep_kernel<<<368 + 176, 256, 0, stream>>>(x_in, wqkv, w1, w3, cs_qkv, cs_w1, cs_w3, st_x, wo, w2, w4, wc, flags);
  for (int layer = 0; layer < 2; ++layer) {
    const u16* x = layer == 0 ? x_in : xb;
    u16* xo = layer == 0 ? xb : (u16*)d_out;
    gemm_kernel<192, 3, 1024, 1024, 3072, EPI_QKV><<<256, 512, 0, stream>>>(x, wqkv, (const u16*)(wc + WC_QKV), cs_qkv, st_x, nullptr, nullptr, phiq, nullptr, wphi, kvp, zp);
    attn_out_kernel<<<layer == 0 ? 256 + 18 : 256, 256, 0, stream>>>(phiq, kvp, zp, ob, cs_w1, cs_w3, cf_w1, cf_w3);
    gemm_kernel<64, 5, 1024, 1024, 1024, EPI_WO><<<256, 512, 0, stream>>>(ob, wo, (const u16*)(wc + WC_WO), nullptr, nullptr, x, nullptr, x2, st_x2, nullptr, nullptr, nullptr);
    gemm_kernel<64, 5, 1024, 1024, 1024, EPI_W1><<<256, 512, 0, stream>>>(x2, w1, (const u16*)(wc + WC_W1), cf_w1, st_x2, nullptr, nullptr, hs, nullptr, nullptr, nullptr, nullptr);
    gemm_kernel<64, 5, 1024, 1024, 1024, EPI_W2><<<256, 512, 0, stream>>>(hs, w2, (const u16*)(wc + WC_W2), nullptr, st_x2, x2, nullptr, x1, st_x1, nullptr, nullptr, nullptr);
    gemm_kernel<224, 3, 1024, 1024, F_, EPI_W3><<<256, 512, 0, stream>>>(x1, w3, nullptr, cf_w3, st_x1, nullptr, nullptr, gb, nullptr, nullptr, nullptr, nullptr);
    gemm_kernel<128, W4NST, F_, F_, 1024, EPI_W4, 1><<<256, 512, 0, stream>>>(gb, w4, (const u16*)(wc + WC_W4), nullptr, nullptr, x2, x1, xo, st_x, nullptr, xch, (float*)(flags + layer * 1024));
  }
}
```

```cpp
#include <hip/hip_runtime.h>
#include <hip/hip_bf16.h>
#include <stdint.h>
#include <type_traits>

typedef short bf16x8 __attribute__((ext_vector_type(8)));
typedef short s16x4 __attribute__((ext_vector_type(4)));
typedef float f32x4 __attribute__((ext_vector_type(4)));
typedef float f32x2 __attribute__((ext_vector_type(2)));
typedef unsigned u32x2 __attribute__((ext_vector_type(2)));
typedef unsigned u32x4 __attribute__((ext_vector_type(4)));
typedef __attribute__((address_space(3))) s16x4* lds_s16x4_p;
typedef unsigned short u16;

constexpr int S_ = 2048, D_ = 1024, H_ = 32, F_ = 3584;

__device__ __forceinline__ float bf2f(u16 v) { return __uint_as_float(((unsigned)v) << 16); }
__device__ __forceinline__ float bflo(unsigned v) { return __uint_as_float(v << 16); }
__device__ __forceinline__ float bfhi(unsigned v) { return __uint_as_float(v & 0xffff0000u); }
typedef __bf16 bf16x2_t __attribute__((ext_vector_type(2)));
__device__ __forceinline__ unsigned pk2(float a, float b) {
  return __builtin_bit_cast(unsigned, __builtin_convertvector((f32x2){a, b}, bf16x2_t));
}
__device__ __forceinline__ unsigned pk2v(f32x2 v) { return __builtin_bit_cast(unsigned, __builtin_convertvector(v, bf16x2_t)); }
__device__ __forceinline__ float max2_(float a, float b) { float r; asm("v_max_f32 %0, %1, %2" : "=v"(r) : "v"(a), "v"(b)); return r; }
__device__ __forceinline__ float min2_(float a, float b) { float r; asm("v_min_f32 %0, %1, %2" : "=v"(r) : "v"(a), "v"(b)); return r; }
__device__ __forceinline__ float max3_(float a, float b, float c) { float r; asm("v_max3_f32 %0, %1, %2, %3" : "=v"(r) : "v"(a), "v"(b), "v"(c)); return r; }
__device__ __forceinline__ float min3_(float a, float b, float c) { float r; asm("v_min3_f32 %0, %1, %2, %3" : "=v"(r) : "v"(a), "v"(b), "v"(c)); return r; }
__device__ __forceinline__ float rbf(float a) { return bflo(pk2(a, 0.f)); }
__device__ __forceinline__ float gelu_t(float x) {
  const float c1 = -2.0f * 0.7978845608028654f * 1.4426950408889634f;
  const float c2 = c1 * 0.044715f;
  float t = x * x;
  float w = x * __builtin_fmaf(t, c2, c1);
  float e = __builtin_amdgcn_exp2f(w);
  return x * __builtin_amdgcn_rcpf(1.0f + e);
}

__device__ __forceinline__ f32x2 gelu2(f32x2 x) {
  const float c1 = -2.0f * 0.7978845608028654f * 1.4426950408889634f;
  const float c2 = c1 * 0.044715f;
  f32x2 t = x * x;
  f32x2 w = x * (t * c2 + c1);
  f32x2 d = (f32x2){__builtin_amdgcn_exp2f(w[0]), __builtin_amdgcn_exp2f(w[1])} + 1.0f;
  return x * (f32x2){__builtin_amdgcn_rcpf(d[0]), __builtin_amdgcn_rcpf(d[1])};
}
__device__ __forceinline__ void x16(float v, float& a, float& b) {
  auto r = __builtin_amdgcn_permlane16_swap(__float_as_uint(v), __float_as_uint(v), false, false);
  a = __uint_as_float(r[0]); b = __uint_as_float(r[1]);
}
__device__ __forceinline__ void x32(float v, float& a, float& b) {
  auto r = __builtin_amdgcn_permlane32_swap(__float_as_uint(v), __float_as_uint(v), false, false);
  a = __uint_as_float(r[0]); b = __uint_as_float(r[1]);
}
__device__ __forceinline__ float rmax4(float v) { float a, b; x16(v, a, b); v = max2_(a, b); x32(v, a, b); return max2_(a, b); }
__device__ __forceinline__ float rmin4(float v) { float a, b; x16(v, a, b); v = min2_(a, b); x32(v, a, b); return min2_(a, b); }
__device__ __forceinline__ float rsum4(float v) { float a, b; x16(v, a, b); v = a + b; x32(v, a, b); return a + b; }

__device__ __forceinline__ float rsum8(float v) {
  v += __uint_as_float(__builtin_amdgcn_mov_dpp(__float_as_uint(v), 0xB1, 0xF, 0xF, true));
  v += __uint_as_float(__builtin_amdgcn_mov_dpp(__float_as_uint(v), 0x4E, 0xF, 0xF, true));
  v += __uint_as_float(__builtin_amdgcn_mov_dpp(__float_as_uint(v), 0x141, 0xF, 0xF, true));
  return v;
}

__device__ __forceinline__ void wt_store(u32x4 v, u32x4* p) { asm volatile("global_store_dwordx4 %0, %1, off sc1\n\ts_nop 1" :: "v"(p), "v"(v) : "memory"); }
__device__ __forceinline__ void wt_store(f32x4 v, f32x4* p) { asm volatile("global_store_dwordx4 %0, %1, off sc1\n\ts_nop 1" :: "v"(p), "v"(v) : "memory"); }
__device__ __forceinline__ void wt_store(u32x2 v, u32x2* p) { asm volatile("global_store_dwordx2 %0, %1, off sc1\n\ts_nop 1" :: "v"(p), "v"(v) : "memory"); }
#define WAIT_V(n) asm volatile("s_waitcnt vmcnt(" #n ")" ::: "memory")
#define WAIT_L0() asm volatile("s_waitcnt lgkmcnt(0)" ::: "memory")
template <int N> __device__ __forceinline__ void wait_vm() {
  asm volatile("s_waitcnt vmcnt(%0)" ::"i"(N) : "memory");
}

__device__ __forceinline__ void glds16(unsigned voff, const void* sbase, unsigned lds) {
  unsigned keep;
  asm volatile("s_mov_b32 %0, m0\n\ts_mov_b32 m0, %3\n\ts_nop 0\n\tglobal_load_lds_dwordx4 %1, %2\n\ts_mov_b32 m0, %0"
               : "=&s"(keep) : "v"(voff), "s"(sbase), "s"(lds) : "memory");
}

__device__ __forceinline__ void mfma_acc(f32x4& c, const u32x4& a, const u32x4& b) {
  asm volatile("v_mfma_f32_16x16x32_bf16 %0, %1, %2, %0" : "+v"(c) : "v"(a), "v"(b));
}

#define STAMP(i)

#define XPOS 2
enum { EPI_QKV = 0, EPI_WO = 1, EPI_W1 = 2, EPI_W2 = 3, EPI_W3 = 4, EPI_W4 = 5 };

template <int BN, int NST, int KD, int LDA, int LDB, int EPI>
__global__ __launch_bounds__(512, 2) void gemm_kernel(
    const u16* __restrict__ A, const u16* __restrict__ W,
    const u16* __restrict__ Wc,
    const float* __restrict__ cs,
    const f32x2* __restrict__ st_in,
    const u16* __restrict__ r0,
    const u16* __restrict__ r1,
    u16* __restrict__ out,
    f32x2* __restrict__ st_out,
    const u16* __restrict__ wphi,
    float* __restrict__ kvp,
    float* __restrict__ zp)
{
  constexpr int NFULL = BN / 64, HALF = (BN % 64) / 32;
  constexpr int A_BYTES = 128 * 64 * 2, B_BYTES = BN * 64 * 2, STAGE = A_BYTES + B_BYTES;
  constexpr int NT = KD / 64, NTW = 2 * NFULL + HALF, LPS = 4 + 2 * NFULL + HALF;
  constexpr int PB = BN * 2 + 16;
  constexpr int EPI_BYTES = (EPI == EPI_QKV) ? (4 * 64 * 144 + 2 * (64 * 32 + 64) * 4) : 128 * PB;
  constexpr int MAINB = NST * STAGE;
  constexpr int RS_OFF = (MAINB > EPI_BYTES ? MAINB : EPI_BYTES);
  constexpr int CS_OFF = RS_OFF + 128 * 16;
  constexpr int WP_OFF = CS_OFF + 1024;
  constexpr int QX_OFF = WP_OFF + 4096;
  constexpr int SMEM = WP_OFF + (EPI == EPI_QKV ? 4096 + 32768 : 0);
  constexpr bool HAS_CS = (EPI == EPI_QKV || EPI == EPI_W1 || EPI == EPI_W3);
  constexpr bool HAS_ST = (EPI == EPI_QKV || EPI == EPI_W1 || EPI == EPI_W3 || EPI == EPI_W2);
  constexpr bool HAS_RES = (EPI == EPI_WO || EPI == EPI_W2 || EPI == EPI_W4);
  constexpr int LAG = (BN == 64) ? 1 : 0;
  constexpr int CH = BN / 8, NIT = CH / 4;
  static_assert(SMEM <= 163840, "LDS");
  __shared__ __attribute__((aligned(16))) char smem[SMEM];

  STAMP(0);
  const int tid = threadIdx.x, lane = tid & 63;
  const int wid8 = __builtin_amdgcn_readfirstlane(tid >> 6), wid = wid8 & 3, wm = wid >> 1, wn = wid & 1;
  const int g = lane >> 4, l15 = lane & 15;
  const int xcd = blockIdx.x & 7, idx = blockIdx.x >> 3;
  const int tm = (xcd >> 1) * 4 + (idx & 3), tn = (xcd & 1) * 8 + (idx >> 2);
  const int row0 = tm * 128;
  f32x4* rs = reinterpret_cast<f32x4*>(smem + RS_OFF);

  f32x4 acc[4][NTW];
  u32x4 ra[HAS_RES ? NIT : 1], rb[(EPI == EPI_W4) ? NIT : 1];

  unsigned rof[HAS_RES ? NIT : 1];
  if constexpr (HAS_RES) {
#pragma unroll
    for (int it = 0; it < NIT; ++it) {
      const int c = tid + 512 * it, r = c / CH, ch = c % CH;
      rof[it] = (unsigned)((row0 + r) * D_ + tn * BN + ch * 8) * 2u;
      asm volatile("" : "+v"(rof[it]));
    }
  }
  auto ldres = [&]() {
    if constexpr (HAS_RES) {
#pragma unroll
      for (int it = 0; it < NIT; ++it) {
        ra[it] = *reinterpret_cast<const u32x4*>(reinterpret_cast<const char*>(r0) + rof[it]);
        if constexpr (EPI == EPI_W4) rb[it] = *reinterpret_cast<const u32x4*>(reinterpret_cast<const char*>(r1) + rof[it]);
      }
    }
  };

  if (wid8 >= 4) {
    unsigned voA[4], voB[NFULL > 0 ? NFULL : 1][2], voH = 0;
    const bool pk_ = (HALF == 0) && ((tn & 7) == 3);
    const unsigned pitchB = pk_ ? NFULL * 128 : LDB * 2;
#pragma unroll
    for (int i = 0; i < 4; ++i) {
      int r = (wid * 4 + i) * 8 + (lane >> 3);
      int ch = (lane & 7) ^ ((r >> 1) & 7);
      voA[i] = (unsigned)(row0 + r) * (LDA * 2) + ch * 16;
      if constexpr (EPI == EPI_WO) voA[i] = (unsigned)((ch >> 2) * S_ + row0 + r) * 64 + (ch & 3) * 16;
    }
#pragma unroll
    for (int b = 0; b < NFULL; ++b) {
      int colb = (EPI == EPI_QKV) ? (b * 1024 + tn * 64) : (tn * BN + b * 64);
#pragma unroll
      for (int j = 0; j < 2; ++j) {
        int r = (wid * 2 + j) * 8 + (lane >> 3);
        int f = ((r >> 1) & 1) | (((r >> 3) & 1) << 1);
        int chp = lane & 7;
        int gch = (((chp >> 1) ^ f) << 1) | (chp & 1);
        const int rs_ = (EPI == EPI_WO) ? ((r & 32) | ((r & 4) << 2) | ((r & 24) >> 1) | (r & 3)) : r;
        voB[b][j] = (unsigned)rs_ * pitchB + (pk_ ? b * 128 : colb * 2) + gch * 16;
      }
    }
    if constexpr (HALF) {
      int r = wid * 16 + (lane >> 2);
      int chp = lane & 3;
      int gch = (((chp >> 1) ^ ((r >> 3) & 1)) << 1) | (chp & 1);
      voH = (unsigned)r * (LDB * 2) + (tn * BN + NFULL * 64) * 2 + gch * 16;
    }
    const char* Ab = reinterpret_cast<const char*>(A);
    const char* Wb = pk_ ? reinterpret_cast<const char*>(Wc) + (size_t)(tn >> 3) * (KD * NFULL * 128) : reinterpret_cast<const char*>(W);
    const unsigned lbase = (unsigned)(uintptr_t)(__attribute__((address_space(3))) char*)smem;
    auto stage = [&](int kt, int buf) {
      const char* a = Ab + (size_t)kt * (EPI == EPI_WO ? 2 * S_ * 64 : 128);
      const char* w = Wb + (size_t)(kt * (64 * pitchB));
      const unsigned s = lbase + buf * STAGE;
#pragma unroll
      for (int i = 0; i < 4; ++i) glds16(voA[i], a, s + (wid * 4 + i) * 1024);
#pragma unroll
      for (int b = 0; b < NFULL; ++b)
#pragma unroll
        for (int j = 0; j < 2; ++j) glds16(voB[b][j], w, s + A_BYTES + b * 8192 + (wid * 2 + j) * 1024);
      if constexpr (HALF) glds16(voH, w, s + A_BYTES + NFULL * 8192 + wid * 1024);
    };
#pragma unroll
    for (int s = 0; s < NST; ++s) stage(s, s);
    constexpr int XL = 0;
    constexpr int NCS = HAS_CS ? (EPI == EPI_QKV ? 16 : 1) : 0;
    constexpr int XE = NCS + (EPI == EPI_QKV ? 1 : 0) + (HAS_ST ? 4 : 0) + (HAS_RES ? NIT * (EPI == EPI_W4 ? 2 : 1) : 0);
    f32x4 stp[4];
    const int lt = tid - 256;
    u32x4 wpx;
    f32x4 csp[HAS_CS ? NCS : 1];
    constexpr int XP = XPOS;
    wait_vm<(NST - 1 - LAG) * LPS>();
    __builtin_amdgcn_s_barrier();
#pragma unroll
    for (int kt = 0; kt < XP; ++kt) {
      wait_vm<(NST - 2 - LAG) * LPS>();
      __builtin_amdgcn_s_barrier();
      stage(kt + NST, kt % NST);
    }
    asm volatile("" ::: "memory");
    if constexpr (HAS_CS) {
      if (lt < BN / 4) {
        const float* cp = cs + (EPI == EPI_QKV ? (lt >> 4) * 1024 + tn * 64 + (lt & 15) * 4 : tn * BN + lt * 4);
#pragma unroll
        for (int i = 0; i < NCS; ++i) csp[i] = *reinterpret_cast<const f32x4*>(cp + i * LDB);
      }
    }
    if constexpr (EPI == EPI_QKV) wpx = *reinterpret_cast<const u32x4*>(wphi + tn * 2048 + lt * 8);
    if constexpr (HAS_ST) {
      const f32x4* sp = reinterpret_cast<const f32x4*>(st_in + (size_t)(row0 + (lt >> 1)) * 16 + (lt & 1) * 8);
#pragma unroll
      for (int i = 0; i < 4; ++i) stp[i] = sp[i];
    }
    ldres();
    asm volatile("" ::: "memory");
    constexpr int NPEEL = XP + NST - 1 - LAG;
    auto head = [&](auto xe) {
      constexpr int X = decltype(xe)::value;
#pragma unroll
      for (int kt = XP; kt < NPEEL; ++kt) {
        wait_vm<(NST - 2 - LAG) * LPS + X>();
        __builtin_amdgcn_s_barrier();
        stage(kt + NST, kt % NST);
      }
    };
    if (HAS_CS && wid == 0) head(std::integral_constant<int, XE>{});
    else head(std::integral_constant<int, XE - NCS>{});
    int buf = NPEEL % NST;
    for (int kt = NPEEL; kt < NT - NST; ++kt) {
      wait_vm<(NST - 2 - LAG) * LPS>();
      __builtin_amdgcn_s_barrier();
      stage(kt + NST, buf);
      buf = buf + 1 == NST ? 0 : buf + 1;
    }
    asm volatile("" ::: "memory");
#pragma unroll
    for (int r = 0; r < NST - 1; ++r) {
      constexpr int NSL = NST - LAG;
      if (r == 0) wait_vm<(NSL - 2) * LPS + XL>();
      else if (r == 1) wait_vm<(NSL > 3 ? NSL - 3 : 0) * LPS + XL>();
      else if (r == 2) wait_vm<(NSL > 4 ? NSL - 4 : 0) * LPS + XL>();
      else if (r == 3) wait_vm<(NSL > 5 ? NSL - 5 : 0) * LPS + XL>();
      else wait_vm<XL>();
      __builtin_amdgcn_s_barrier();
    }
    if constexpr (HAS_ST) {
#pragma unroll
      for (int i = 0; i < 4; ++i) asm volatile("" : "+v"(stp[i]));
      float sm = 0.f;
#pragma unroll
      for (int i = 0; i < 4; ++i) sm += stp[i][0] + stp[i][2];
      sm += __shfl_xor(sm, 1);
      float mean = sm * (1.0f / 1024.f);
      float m2 = 0.f;
#pragma unroll
      for (int i = 0; i < 4; ++i) {
        float d0 = stp[i][0] * (1.0f / 64.f) - mean, d1 = stp[i][2] * (1.0f / 64.f) - mean;
        m2 += stp[i][1] + stp[i][3] + 64.f * (d0 * d0 + d1 * d1);
      }
      m2 += __shfl_xor(m2, 1);
      float var = m2 * (1.0f / 1024.f);
      float r1_ = 1.0f / sqrtf(var + 1e-5f);
      float sc = r1_;
      if constexpr (EPI == EPI_W1) sc = r1_ / sqrtf(var * r1_ * r1_ + 1e-5f);
      if ((lt & 1) == 0) rs[lt >> 1] = (f32x4){mean, sc, r1_, 0.f};
    }
    if constexpr (HAS_CS) {
      if (lt < BN / 4) {
#pragma unroll
        for (int i = 0; i < NCS; ++i) asm volatile("" : "+v"(csp[i]));
#pragma unroll
        for (int i = NCS / 2; i > 0; i >>= 1)
#pragma unroll
          for (int j = 0; j < i; ++j) csp[j] += csp[j + i];
        *reinterpret_cast<f32x4*>(smem + CS_OFF + lt * 16) = csp[0];
      }
    }
    if constexpr (EPI == EPI_QKV) *reinterpret_cast<u32x4*>(smem + WP_OFF + lt * 16) = wpx;
  } else {
    ldres();
    const int q = l15 >> 2, p = lane & 3;
    const int fB = (q >> 1) | ((g & 1) << 1);
    unsigned aoff[2], boff[2], hoff = 0;
#pragma unroll
    for (int ks = 0; ks < 2; ++ks) aoff[ks] = (wm * 64 + l15) * 128 + (((4 * ks + g) ^ (l15 >> 1)) * 16);
#pragma unroll
    for (int u = 0; u < 2; ++u) boff[u] = A_BYTES + (8 * g + q) * 128 + (((2 * wn + u) ^ fB) * 32) + p * 8;
    if constexpr (HALF) hoff = A_BYTES + NFULL * 8192 + (8 * g + q) * 64 + ((wn ^ (g & 1)) * 32) + p * 8;

    auto ldf = [&](int i, const char* s, int ks, u32x4(&af)[4], u32x4(&bfr)[NTW]) {
      if (i == 0 || i > NTW) {
        const int mt = i == 0 ? 0 : i - NTW;
        af[mt] = *reinterpret_cast<const u32x4*>(s + aoff[ks] + mt * 2048);
      } else if (i - 1 < 2 * NFULL) {
        const int b = (i - 1) >> 1, u = (i - 1) & 1;
        s16x4 lo = __builtin_amdgcn_ds_read_tr16_b64_v4i16((lds_s16x4_p)(s + boff[u] + b * 8192 + ks * 4096));
        s16x4 hi = __builtin_amdgcn_ds_read_tr16_b64_v4i16((lds_s16x4_p)(s + boff[u] + b * 8192 + ks * 4096 + 512));
        bfr[i - 1] = __builtin_shufflevector(__builtin_bit_cast(u32x2, lo), __builtin_bit_cast(u32x2, hi), 0, 1, 2, 3);
      } else {
        s16x4 lo = __builtin_amdgcn_ds_read_tr16_b64_v4i16((lds_s16x4_p)(s + hoff + ks * 2048));
        s16x4 hi = __builtin_amdgcn_ds_read_tr16_b64_v4i16((lds_s16x4_p)(s + hoff + ks * 2048 + 256));
        bfr[NTW - 1] = __builtin_shufflevector(__builtin_bit_cast(u32x2, lo), __builtin_bit_cast(u32x2, hi), 0, 1, 2, 3);
      }
    };
    constexpr int NM = 4 * NTW, NL = NTW + 4;
    auto phase = [&](u32x4(&afc)[4], u32x4(&bfc)[NTW], u32x4(&afn)[4], u32x4(&bfn)[NTW], int nbuf, int nks, auto rd, auto bar) {
      const char* s = smem + nbuf * STAGE;
      constexpr int BAR = decltype(bar)::value, R0 = BAR < 0 ? 0 : BAR + 1;
      constexpr int RSP = (NM - R0 - 2) / NL > 0 ? (NM - R0 - 2) / NL : 1;
      static_assert(R0 + (NL - 1) * RSP < NM, "reads");
#pragma unroll
      for (int mt = 0; mt < 4; ++mt)
#pragma unroll
        for (int j = 0; j < NTW; ++j) {
          const int m = mt * NTW + j;
          if (m == BAR) {
            WAIT_L0();
            __builtin_amdgcn_s_barrier();
            asm volatile("" ::: "memory");
          }
          if (decltype(rd)::value && m >= R0 && (m - R0) % RSP == 0 && (m - R0) / RSP < NL) ldf((m - R0) / RSP, s, nks, afn, bfn);
          if (EPI == EPI_QKV && j >= 4) mfma_acc(acc[mt][j], afc[mt], bfc[j]);
          else mfma_acc(acc[mt][j], bfc[j], afc[mt]);
          __builtin_amdgcn_sched_barrier(0);
        }
    };
    typedef std::integral_constant<int, -1> nobar;
#pragma unroll
    for (int mt = 0; mt < 4; ++mt)
#pragma unroll
      for (int j = 0; j < NTW; ++j) {
        acc[mt][j] = (f32x4){0.f, 0.f, 0.f, 0.f};
        asm volatile("" : "+v"(acc[mt][j]));
      }
    u32x4 af0[4], af1[4], bf0[NTW], bf1[NTW];
    __builtin_amdgcn_s_barrier();
    asm volatile("" ::: "memory");
    STAMP(1);
#pragma unroll
    for (int i = 0; i < NL; ++i) { ldf(i, smem, 0, af0, bf0); __builtin_amdgcn_sched_barrier(0); }
    int buf = 0;
    if constexpr (LAG) {
      phase(af0, bf0, af1, bf1, 0, 1, std::true_type{}, nobar{});
      for (int kt = 0; kt < NT - 1; ++kt) {
        buf = buf + 1 == NST ? 0 : buf + 1;
        phase(af1, bf1, af0, bf0, buf, 0, std::true_type{}, nobar{});
        asm volatile("" ::: "memory");
        __builtin_amdgcn_s_barrier();
        asm volatile("" ::: "memory");
        phase(af0, bf0, af1, bf1, buf, 1, std::true_type{}, nobar{});
      }
      phase(af1, bf1, af0, bf0, buf, 0, std::false_type{}, nobar{});
    } else {
      for (int kt = 0; kt < NT - 1; ++kt) {
        phase(af0, bf0, af1, bf1, buf, 1, std::true_type{}, nobar{});
        buf = buf + 1 == NST ? 0 : buf + 1;
        phase(af1, bf1, af0, bf0, buf, 0, std::true_type{}, std::integral_constant<int, 4>{});
      }
      phase(af0, bf0, af1, bf1, buf, 1, std::true_type{}, nobar{});
      phase(af1, bf1, af0, bf0, buf, 0, std::false_type{}, nobar{});
    }
    asm volatile("s_nop 7\n\ts_nop 7\n\ts_nop 7" ::: "memory");
#pragma unroll
    for (int mt = 0; mt < 4; ++mt)
#pragma unroll
      for (int j = 0; j < NTW; ++j) asm volatile("" : "+v"(acc[mt][j]));
    if constexpr (EPI == EPI_QKV) {
#pragma unroll
      for (int mt = 0; mt < 4; ++mt)
#pragma unroll
        for (int t = 0; t < 2; ++t) *reinterpret_cast<f32x4*>(smem + QX_OFF + wid * 8192 + (mt * 2 + t) * 1024 + lane * 16) = acc[mt][t];
    }
  }
  WAIT_L0();
  __builtin_amdgcn_s_barrier();
  asm volatile("" ::: "memory");
  STAMP(2);

  if constexpr (EPI == EPI_QKV) {
    const int h = tn * 2 + wn;
    const int q = l15 >> 2, p = lane & 3;
    f32x4 kv[4][2], zz[4];
    float zsel = 0.f;
    float* red = reinterpret_cast<float*>(smem + 4 * 64 * 144) + wn * (64 * 32 + 64);
    bf16x8 wf[2];
#pragma unroll
    for (int ft = 0; ft < 2; ++ft) {
      const char* src = smem + WP_OFF + wn * 2048 + (4 * g + q) * 64 + ft * 32 + p * 8;
      s16x4 lo = __builtin_amdgcn_ds_read_tr16_b64_v4i16((lds_s16x4_p)(src));
      s16x4 hi = __builtin_amdgcn_ds_read_tr16_b64_v4i16((lds_s16x4_p)(src + 16 * 64));
      wf[ft] = __builtin_shufflevector(lo, hi, 0, 1, 2, 3, 4, 5, 6, 7);
    }
    const float* csl = reinterpret_cast<const float*>(smem + CS_OFF);
    auto feat = [&](const bf16x8& xf, u32x2 (&o)[4]) {
      const float L2E = 1.4426950408889634f;
      f32x4 z4 = (f32x4){0.f, 0.f, 0.f, 0.f};
      f32x4 p0 = __builtin_amdgcn_mfma_f32_16x16x32_bf16(wf[0], xf, z4, 0, 0, 0);
      f32x4 p1 = __builtin_amdgcn_mfma_f32_16x16x32_bf16(wf[1], xf, z4, 0, 0, 0);
      f32x2 v[4];
      {
        const unsigned r0_ = pk2(p0[0], p0[1]), r1_ = pk2(p0[2], p0[3]), r2_ = pk2(p1[0], p1[1]), r3_ = pk2(p1[2], p1[3]);
        v[0] = (f32x2){bflo(r0_), bfhi(r0_)}; v[1] = (f32x2){bflo(r1_), bfhi(r1_)};
        v[2] = (f32x2){bflo(r2_), bfhi(r2_)}; v[3] = (f32x2){bflo(r3_), bfhi(r3_)};
      }
      float mx = max2_(max3_(max3_(v[0][0], v[0][1], v[1][0]), v[1][1], v[2][0]), max3_(v[2][1], v[3][0], v[3][1]));
      float mn = min2_(min3_(min3_(v[0][0], v[0][1], v[1][0]), v[1][1], v[2][0]), min3_(v[2][1], v[3][0], v[3][1]));
      mx = rmax4(mx); mn = rmin4(mn);
      const float mxl = -mx * L2E, mnl = mn * L2E;
      f32x2 ep[4], en[4];
#pragma unroll
      for (int i = 0; i < 4; ++i) {
        const f32x2 ap = v[i] * L2E + mxl, an = v[i] * (-L2E) + mnl;
        ep[i] = (f32x2){__builtin_amdgcn_exp2f(ap[0]), __builtin_amdgcn_exp2f(ap[1])};
        en[i] = (f32x2){__builtin_amdgcn_exp2f(an[0]), __builtin_amdgcn_exp2f(an[1])};
      }
      const f32x2 sp2 = (ep[0] + ep[1]) + (ep[2] + ep[3]), sn2 = (en[0] + en[1]) + (en[2] + en[3]);
      const float ip = __builtin_amdgcn_rcpf(rsum4(sp2[0] + sp2[1])), in_ = __builtin_amdgcn_rcpf(rsum4(sn2[0] + sn2[1]));
      o[0] = (u32x2){pk2v(ep[0] * ip), pk2v(ep[1] * ip)};
      o[1] = (u32x2){pk2v(ep[2] * ip), pk2v(ep[3] * ip)};
      o[2] = (u32x2){pk2v(en[0] * in_), pk2v(en[1] * in_)};
      o[3] = (u32x2){pk2v(en[2] * in_), pk2v(en[3] * in_)};
    };
    if (wid8 >= 4) {
      f32x4 cs4[2];
#pragma unroll
      for (int t = 0; t < 2; ++t) cs4[t] = *reinterpret_cast<const f32x4*>(csl + wn * 32 + t * 16 + 4 * g);
      bf16x8 qf[4];
#pragma unroll
      for (int mt = 0; mt < 4; ++mt) {
        f32x4 st = rs[wm * 64 + mt * 16 + l15];
        unsigned pq_[4];
#pragma unroll
        for (int t = 0; t < 2; ++t) {
          const f32x4 ac = *reinterpret_cast<const f32x4*>(smem + QX_OFF + wid * 8192 + (mt * 2 + t) * 1024 + lane * 16);
          float a[4];
#pragma unroll
          for (int i = 0; i < 4; ++i) a[i] = st[1] * (ac[i] - st[0] * cs4[t][i]);
          pq_[t * 2] = pk2(a[0], a[1]); pq_[t * 2 + 1] = pk2(a[2], a[3]);
        }
        qf[mt] = __builtin_bit_cast(bf16x8, (u32x4){pq_[0], pq_[1], pq_[2], pq_[3]});
      }
#pragma unroll
      for (int mt = 0; mt < 4; ++mt) {
        u32x2 o[4];
        feat(qf[mt], o);
        u32x4* dst = reinterpret_cast<u32x4*>(out) + ((size_t)(tm * 8 + wm * 4 + mt) * 32 + h) * 128 + lane;
        wt_store((u32x4){o[0][0], o[0][1], o[1][0], o[1][1]}, dst);
        wt_store((u32x4){o[2][0], o[2][1], o[3][0], o[3][1]}, dst + 64);
      }
    } else {
      f32x4 cs4[2];
      float csv[2];
#pragma unroll
      for (int t = 0; t < 2; ++t) {
        cs4[t] = *reinterpret_cast<const f32x4*>(csl + 64 + wn * 32 + t * 16 + 4 * g);
        csv[t] = csl[128 + wn * 32 + t * 16 + l15];
      }
      bf16x8 kf[4], vf[2][2];
#pragma unroll
      for (int mt = 0; mt < 4; ++mt) {
        f32x4 st = rs[wm * 64 + mt * 16 + l15];
        unsigned pk_[4];
#pragma unroll
        for (int t = 0; t < 2; ++t) {
          float b[4];
#pragma unroll
          for (int i = 0; i < 4; ++i) b[i] = st[1] * (acc[mt][2 + t][i] - st[0] * cs4[t][i]);
          pk_[t * 2] = pk2(b[0], b[1]); pk_[t * 2 + 1] = pk2(b[2], b[3]);
        }
        kf[mt] = __builtin_bit_cast(bf16x8, (u32x4){pk_[0], pk_[1], pk_[2], pk_[3]});
      }
      char* pkl = smem + wid * (64 * 144);
#pragma unroll
      for (int mt = 0; mt < 4; ++mt) {
        u32x2 o[4];
        feat(kf[mt], o);
        char* dst = pkl + (mt * 16 + l15) * 144 + g * 8;
#pragma unroll
        for (int c = 0; c < 4; ++c) *reinterpret_cast<u32x2*>(dst + c * 32) = o[c];
      }
#pragma unroll
      for (int s = 0; s < 2; ++s) {
        unsigned pv[2][4];
#pragma unroll
        for (int m2 = 0; m2 < 2; ++m2) {
          int mt = s * 2 + m2;
          f32x4 s0 = rs[wm * 64 + mt * 16 + 4 * g + 0], s1 = rs[wm * 64 + mt * 16 + 4 * g + 1];
          f32x4 s2 = rs[wm * 64 + mt * 16 + 4 * g + 2], s3 = rs[wm * 64 + mt * 16 + 4 * g + 3];
#pragma unroll
          for (int t = 0; t < 2; ++t) {
            float a0 = s0[1] * (acc[mt][4 + t][0] - s0[0] * csv[t]);
            float a1 = s1[1] * (acc[mt][4 + t][1] - s1[0] * csv[t]);
            float a2 = s2[1] * (acc[mt][4 + t][2] - s2[0] * csv[t]);
            float a3 = s3[1] * (acc[mt][4 + t][3] - s3[0] * csv[t]);
            pv[t][m2 * 2] = pk2(a0, a1); pv[t][m2 * 2 + 1] = pk2(a2, a3);
          }
        }
#pragma unroll
        for (int t = 0; t < 2; ++t) vf[s][t] = __builtin_bit_cast(bf16x8, (u32x4){pv[t][0], pv[t][1], pv[t][2], pv[t][3]});
      }
      WAIT_L0();
      __builtin_amdgcn_sched_barrier(0);
#pragma unroll
      for (int ft = 0; ft < 4; ++ft) { kv[ft][0] = kv[ft][1] = zz[ft] = (f32x4){0.f, 0.f, 0.f, 0.f}; }
      const bf16x8 ones = (bf16x8){0x3F80, 0x3F80, 0x3F80, 0x3F80, 0x3F80, 0x3F80, 0x3F80, 0x3F80};
#pragma unroll
      for (int s = 0; s < 2; ++s)
#pragma unroll
        for (int ft = 0; ft < 4; ++ft) {
          const char* src = pkl + (32 * s + 4 * g + q) * 144 + ft * 32 + p * 8;
          s16x4 lo = __builtin_amdgcn_ds_read_tr16_b64_v4i16((lds_s16x4_p)(src));
          s16x4 hi = __builtin_amdgcn_ds_read_tr16_b64_v4i16((lds_s16x4_p)(src + 16 * 144));
          bf16x8 pf = __builtin_shufflevector(lo, hi, 0, 1, 2, 3, 4, 5, 6, 7);
          kv[ft][0] = __builtin_amdgcn_mfma_f32_16x16x32_bf16(vf[s][0], pf, kv[ft][0], 0, 0, 0);
          kv[ft][1] = __builtin_amdgcn_mfma_f32_16x16x32_bf16(vf[s][1], pf, kv[ft][1], 0, 0, 0);
          zz[ft] = __builtin_amdgcn_mfma_f32_16x16x32_bf16(ones, pf, zz[ft], 0, 0, 0);
        }
      zsel = g == 0 ? zz[0][0] : g == 1 ? zz[1][0] : g == 2 ? zz[2][0] : zz[3][0];
      if (wm == 1) {
#pragma unroll
        for (int ft = 0; ft < 4; ++ft)
#pragma unroll
          for (int dt = 0; dt < 2; ++dt) *reinterpret_cast<f32x4*>(red + ((ft * 2 + dt) * 64 + lane) * 4) = kv[ft][dt];
        red[64 * 32 + lane] = zsel;
      }
    }
    __syncthreads();
    if (wid8 < 2) {
      float* kd = kvp + (size_t)(h * 16 + tm) * 2048;
      float* zd = zp + (size_t)(h * 16 + tm) * 64;
#pragma unroll
      for (int ft = 0; ft < 4; ++ft)
#pragma unroll
        for (int dt = 0; dt < 2; ++dt) {
          f32x4 o = *reinterpret_cast<const f32x4*>(red + ((ft * 2 + dt) * 64 + lane) * 4);
          wt_store(o + kv[ft][dt], reinterpret_cast<f32x4*>(kd + ((ft * 2 + dt) * 64 + lane) * 4));
        }
      zd[lane] = red[64 * 32 + lane] + zsel;
    }
  } else {
    if (wid8 < 4) {
      f32x4 stm[4], cs4[NTW];
      if constexpr (EPI == EPI_W1 || EPI == EPI_W3) {
#pragma unroll
        for (int j = 0; j < NTW; ++j) {
          const int colj = (j < 2 * NFULL) ? ((j >> 1) * 64 + (2 * wn + (j & 1)) * 16) : (NFULL * 64 + wn * 16);
          cs4[j] = *reinterpret_cast<const f32x4*>(smem + CS_OFF + (colj + 4 * g) * 4);
        }
#pragma unroll
        for (int mt = 0; mt < 4; ++mt) stm[mt] = rs[wm * 64 + mt * 16 + l15];
      }
#pragma unroll
      for (int j = 0; j < NTW; ++j) {
        const int colj = (j < 2 * NFULL) ? ((j >> 1) * 64 + (2 * wn + (j & 1)) * 16) : (NFULL * 64 + wn * 16);
#pragma unroll
        for (int mt = 0; mt < 4; ++mt) {
          f32x2 v0 = (f32x2){acc[mt][j][0], acc[mt][j][1]}, v1 = (f32x2){acc[mt][j][2], acc[mt][j][3]};
          if constexpr (EPI == EPI_W1 || EPI == EPI_W3) {
            v0 = gelu2((v0 - stm[mt][0] * (f32x2){cs4[j][0], cs4[j][1]}) * stm[mt][1]);
            v1 = gelu2((v1 - stm[mt][0] * (f32x2){cs4[j][2], cs4[j][3]}) * stm[mt][1]);
          }
          *reinterpret_cast<u32x2*>(smem + (wm * 64 + mt * 16 + l15) * PB + (colj + 4 * g) * 2) = (u32x2){pk2(v0[0], v0[1]), pk2(v1[0], v1[1])};
        }
      }
    }
    __syncthreads();
#pragma unroll
    for (int it = 0; it < NIT; ++it) {
      const int c = tid + 512 * it, r = c / CH, ch = c % CH;
      u32x4 v = *reinterpret_cast<const u32x4*>(smem + r * PB + ch * 16);
      const int grow = row0 + r, gcol = tn * BN + ch * 8;
      if constexpr (EPI == EPI_W3) {
        wt_store(v, reinterpret_cast<u32x4*>(out + (size_t)grow * F_ + gcol));
      } else if constexpr (EPI == EPI_W1) {
        const int grp = tn >> 2;
        const int sh = grp == 0 ? 1 : grp == 1 ? -1 : grp == 2 ? 2 : -2;
        const int dr = grow + sh;
        if (dr >= 0 && dr < S_) wt_store(v, reinterpret_cast<u32x4*>(out + (size_t)dr * D_ + gcol));
        const int zr = grow - sh;
        if (zr < 0 || zr >= S_) *reinterpret_cast<u32x4*>(out + (size_t)grow * D_ + gcol) = (u32x4){0u, 0u, 0u, 0u};
      } else {
        float y[8];
        const u32x4 a = ra[it];
        if constexpr (EPI == EPI_WO) {
#pragma unroll
          for (int i = 0; i < 4; ++i) { y[2 * i] = rbf(bflo(a[i]) + bflo(v[i])); y[2 * i + 1] = rbf(bfhi(a[i]) + bfhi(v[i])); }
        } else if constexpr (EPI == EPI_W2) {
          f32x4 st = rs[r];
#pragma unroll
          for (int i = 0; i < 4; ++i) {
            y[2 * i] = rbf(rbf((bflo(a[i]) - st[0]) * st[2]) + bflo(v[i]));
            y[2 * i + 1] = rbf(rbf((bfhi(a[i]) - st[0]) * st[2]) + bfhi(v[i]));
          }
        } else {
          const u32x4 b = rb[it];
#pragma unroll
          for (int i = 0; i < 4; ++i) {
            y[2 * i] = rbf(bflo(a[i]) + rbf(bflo(b[i]) + bflo(v[i])));
            y[2 * i + 1] = rbf(bfhi(a[i]) + rbf(bfhi(b[i]) + bfhi(v[i])));
          }
        }
        wt_store((u32x4){pk2(y[0], y[1]), pk2(y[2], y[3]), pk2(y[4], y[5]), pk2(y[6], y[7])}, reinterpret_cast<u32x4*>(out + (size_t)grow * D_ + gcol));
        float sm = ((y[0] + y[1]) + (y[2] + y[3])) + ((y[4] + y[5]) + (y[6] + y[7]));
        sm = rsum8(sm);
        float mu = sm * (1.0f / 64.f), m2 = 0.f;
#pragma unroll
        for (int i = 0; i < 8; ++i) { float d = y[i] - mu; m2 += d * d; }
        m2 = rsum8(m2);
        if (ch == 0) st_out[(size_t)grow * 16 + tn] = (f32x2){sm, m2};
      }
    }
  }
  STAMP(3);
}

__global__ __launch_bounds__(256, 1) void attn_out_kernel(const u16* __restrict__ phiq, const float* __restrict__ kvp,
                                                          const float* __restrict__ zp, u16* __restrict__ o,
                                                          const float* __restrict__ cp1, const float* __restrict__ cp3,
                                                          float* __restrict__ cs1, float* __restrict__ cs3) {
  __shared__ __attribute__((aligned(16))) u16 kvT[48 * 72];
  const int tid = threadIdx.x, lane = tid & 63, wid = tid >> 6, g = lane >> 4, l15 = lane & 15;
  if (blockIdx.x >= 256) {
    const int i = (blockIdx.x - 256) * 256 + tid;
    const float* src = i < 1024 ? cp1 + i : cp3 + (i - 1024);
    const int ld = i < 1024 ? 1024 : F_;
    float c[16];
#pragma unroll
    for (int t = 0; t < 16; ++t) c[t] = src[t * ld];
#pragma unroll
    for (int t = 8; t > 0; t >>= 1)
#pragma unroll
      for (int j = 0; j < t; ++j) c[j] += c[j + t];
    *(i < 1024 ? cs1 + i : cs3 + (i - 1024)) = c[0];
    return;
  }
  const int h = blockIdx.x & 31, tg = blockIdx.x >> 5;
  f32x4 kvv[16][2];
  float zv[16];
  bf16x8 pq[4][2];
  {
    const float* src = kvp + (size_t)h * 16 * 2048 + tid * 8;
#pragma unroll
    for (int t = 0; t < 16; ++t) {
      kvv[t][0] = *reinterpret_cast<const f32x4*>(src + t * 2048);
      kvv[t][1] = *reinterpret_cast<const f32x4*>(src + t * 2048 + 4);
    }
#pragma unroll
    for (int t = 0; t < 16; ++t) zv[t] = zp[(size_t)(h * 16 + t) * 64 + lane];
#pragma unroll
    for (int mt = 0; mt < 4; ++mt) {
      const u16* ps = phiq + ((size_t)(tg * 16 + wid * 4 + mt) * 32 + h) * 1024 + lane * 8;
      pq[mt][0] = *reinterpret_cast<const bf16x8*>(ps);
      pq[mt][1] = *reinterpret_cast<const bf16x8*>(ps + 512);
    }
  }
  __builtin_amdgcn_sched_barrier(0);
  {
    for (int i = tid; i < 15 * 64; i += 256) kvT[(33 + i / 64) * 72 + (i & 63)] = 0;
    f32x4 a0 = kvv[0][0], a1 = kvv[0][1];
#pragma unroll
    for (int t = 1; t < 16; ++t) { a0 += kvv[t][0]; a1 += kvv[t][1]; }
    const int ft = tid >> 6, dt = (tid >> 5) & 1, ln = (2 * tid) & 63;
    const int d0 = dt * 16 + 4 * (ln >> 4), pos = (ft >> 1) * 32 + ((ln >> 2) & 3) * 8 + (ft & 1) * 4 + (ln & 3);
#pragma unroll
    for (int i = 0; i < 4; ++i) *reinterpret_cast<unsigned*>(&kvT[(d0 + i) * 72 + pos]) = pk2(a0[i], a1[i]);
    if (tid < 64) {
      float z = 0.f;
#pragma unroll
      for (int t = 0; t < 16; ++t) z += zv[t];
      kvT[32 * 72 + (tid >> 5) * 32 + ((tid >> 2) & 3) * 8 + ((tid >> 4) & 1) * 4 + (tid & 3)] = (u16)(pk2(z, 0.f) & 0xffff);
    }
  }
  __syncthreads();
  bf16x8 kf[3][2];
#pragma unroll
  for (int dt = 0; dt < 3; ++dt)
#pragma unroll
    for (int ks = 0; ks < 2; ++ks) kf[dt][ks] = *reinterpret_cast<const bf16x8*>(&kvT[(dt * 16 + l15) * 72 + ks * 32 + g * 8]);
#pragma unroll
  for (int mt = 0; mt < 4; ++mt) {
    const int tok = tg * 256 + wid * 64 + mt * 16 + l15;
    const bf16x8 p0 = pq[mt][0], p1 = pq[mt][1];
    f32x4 r[3];
#pragma unroll
    for (int dt = 0; dt < 3; ++dt) {
      r[dt] = (f32x4){0.f, 0.f, 0.f, 0.f};
      r[dt] = __builtin_amdgcn_mfma_f32_16x16x32_bf16(kf[dt][0], p0, r[dt], 0, 0, 0);
      r[dt] = __builtin_amdgcn_mfma_f32_16x16x32_bf16(kf[dt][1], p1, r[dt], 0, 0, 0);
    }
    float den = rbf(rbf(__shfl(r[2][0], l15)) + 1e-6f);
    float inv = 1.0f / den;
    const u32x4 w = (u32x4){pk2(rbf(r[0][0]) * inv, rbf(r[0][1]) * inv), pk2(rbf(r[0][2]) * inv, rbf(r[0][3]) * inv),
                            pk2(rbf(r[1][0]) * inv, rbf(r[1][1]) * inv), pk2(rbf(r[1][2]) * inv, rbf(r[1][3]) * inv)};
    wt_store(w, reinterpret_cast<u32x4*>(o + ((size_t)h * S_ + tok) * 32 + 8 * g));
  }
}

constexpr int WC_QKV = 0, WC_WO = WC_QKV + 2 * 1024 * 384, WC_W1 = WC_WO + 2 * 1024 * 128, WC_W2 = WC_W1 + 2 * 1024 * 128,
              WC_W4 = WC_W2 + 2 * 1024 * 128, WC_END = WC_W4 + 2 * F_ * 128;
__global__ __launch_bounds__(256) void prep_kernel(const u16* __restrict__ x, const u16* __restrict__ wqkv,
                                                   const u16* __restrict__ w1, const u16* __restrict__ w3,
                                                   float* __restrict__ cs_qkv, float* __restrict__ cs_w1,
                                                   float* __restrict__ cs_w3, f32x2* __restrict__ st,
                                                   const u16* __restrict__ wo, const u16* __restrict__ w2,
                                                   const u16* __restrict__ w4, char* __restrict__ wc) {
  __shared__ __attribute__((aligned(16))) float red[4][512];
  const int tid = threadIdx.x, b = blockIdx.x;
  if (b >= 368) {
    const int pb = b - 368;
    const u16* w = pb < 32 ? wo + (size_t)pb * 32768 : pb < 64 ? w2 + (size_t)(pb - 32) * 32768 : w4 + (size_t)(pb - 64) * 32768;
    u32x4 v[16];
#pragma unroll
    for (int r = 0; r < 16; ++r) v[r] = *reinterpret_cast<const u32x4*>(w + r * 2048 + tid * 8);
    if (((tid >> 3) & 7) == 3) {
      const int kd = pb < 64 ? 1024 : F_, row = (pb < 32 ? pb : pb < 64 ? pb - 32 : pb - 64) * 32 + (tid >> 7);
      char* dst = wc + (pb < 32 ? WC_WO : pb < 64 ? WC_W2 : WC_W4) + (size_t)((tid >> 6) & 1) * kd * 128 + (size_t)row * 128 + (tid & 7) * 16;
#pragma unroll
      for (int r = 0; r < 16; ++r) *reinterpret_cast<u32x4*>(dst + r * 256) = v[r];
    }
#pragma unroll
    for (int r = 0; r < 16; ++r) asm volatile("" ::"v"(v[r]));
  } else if (b < 240) {
    const u16* w; float* dst; int ld, ch, rg;
    if (b < 96) { w = wqkv; dst = cs_qkv; ld = 3072; ch = b % 6; rg = b / 6; }
    else if (b < 128) { w = w1; dst = cs_w1; ld = 1024; ch = (b - 96) & 1; rg = (b - 96) >> 1; }
    else { w = w3; dst = cs_w3; ld = F_; ch = (b - 128) % 7; rg = (b - 128) / 7; }
    const int lane = tid & 63, wid = tid >> 6, row = rg * 64 + wid * 16;
    u32x4 v[16];
#pragma unroll
    for (int r = 0; r < 16; ++r) v[r] = *reinterpret_cast<const u32x4*>(w + (size_t)(row + r) * ld + ch * 512 + lane * 8);
    if (b < 128 && (lane >> 3) == 3) {
      const int cpp = b < 96 ? 384 : 128;
      char* cpd = wc + (b < 96 ? WC_QKV + (ch & 1) * (1024 * 384) + (ch >> 1) * 128 : WC_W1 + ch * (1024 * 128)) + row * cpp + (lane & 7) * 16;
#pragma unroll
      for (int r = 0; r < 16; ++r) *reinterpret_cast<u32x4*>(cpd + r * cpp) = v[r];
    }
    f32x4 s0 = (f32x4){0.f, 0.f, 0.f, 0.f}, s1 = s0;
#pragma unroll
    for (int r = 0; r < 16; ++r) {
      s0 += (f32x4){bflo(v[r][0]), bfhi(v[r][0]), bflo(v[r][1]), bfhi(v[r][1])};
      s1 += (f32x4){bflo(v[r][2]), bfhi(v[r][2]), bflo(v[r][3]), bfhi(v[r][3])};
    }
    *reinterpret_cast<f32x4*>(&red[wid][lane * 8]) = s0;
    *reinterpret_cast<f32x4*>(&red[wid][lane * 8 + 4]) = s1;
    __syncthreads();
    f32x2 t = *reinterpret_cast<const f32x2*>(&red[0][tid * 2]);
#pragma unroll
    for (int i = 1; i < 4; ++i) t += *reinterpret_cast<const f32x2*>(&red[i][tid * 2]);
    *reinterpret_cast<f32x2*>(dst + rg * ld + ch * 512 + tid * 2) = t;
  } else {
    const int lane = tid & 63, wid = tid >> 6;
    const int rbase = (b - 240) * 16 + wid * 4;
    u32x4 xv[8];
#pragma unroll
    for (int i = 0; i < 8; ++i) xv[i] = *reinterpret_cast<const u32x4*>(x + (size_t)rbase * D_ + i * 512 + lane * 8);
    __builtin_amdgcn_sched_barrier(0);
#pragma unroll
    for (int rr = 0; rr < 4; ++rr) {
      const int row = rbase + rr;
#pragma unroll
      for (int hf = 0; hf < 2; ++hf) {
        const u32x4 v = xv[rr * 2 + hf];
        float y[8];
#pragma unroll
        for (int i = 0; i < 4; ++i) { y[2 * i] = bflo(v[i]); y[2 * i + 1] = bfhi(v[i]); }
        float sm = ((y[0] + y[1]) + (y[2] + y[3])) + ((y[4] + y[5]) + (y[6] + y[7]));
        sm = rsum8(sm);
        float mu = sm * (1.0f / 64.f), m2 = 0.f;
#pragma unroll
        for (int i = 0; i < 8; ++i) { float d = y[i] - mu; m2 += d * d; }
        m2 = rsum8(m2);
        if ((lane & 7) == 0) st[(size_t)row * 16 + hf * 8 + (lane >> 3)] = (f32x2){sm, m2};
      }
    }
  }
}

extern "C" void kernel_launch(void* const* d_in, const int* in_sizes, int n_in, void* d_out, int out_size, void* d_ws,
                              size_t ws_size, hipStream_t stream) {
  const u16* x_in = (const u16*)d_in[0];
  const u16* wqkv = (const u16*)d_in[1];
  const u16* wphi = (const u16*)d_in[2];
  const u16* wo = (const u16*)d_in[3];
  const u16* w1 = (const u16*)d_in[5];
  const u16* w2 = (const u16*)d_in[6];
  const u16* w3 = (const u16*)d_in[7];
  const u16* w4 = (const u16*)d_in[8];
  char* ws = (char*)d_ws;
  size_t off = 0;
  auto alloc = [&](size_t bytes) { char* p = ws + off; off += (bytes + 4095) & ~(size_t)4095; return p; };
  u16* xb = (u16*)alloc((size_t)S_ * D_ * 2);
  u16* x2 = (u16*)alloc((size_t)S_ * D_ * 2);
  u16* x1 = (u16*)alloc((size_t)S_ * D_ * 2);
  u16* hs = (u16*)alloc((size_t)S_ * D_ * 2);
  u16* ob = (u16*)alloc((size_t)S_ * D_ * 2);
  u16* gb = (u16*)alloc((size_t)S_ * F_ * 2);
  u16* phiq = (u16*)alloc((size_t)S_ * 2048 * 2);
  float* kvp = (float*)alloc((size_t)32 * 16 * 2048 * 4);
  float* zp = (float*)alloc((size_t)32 * 16 * 64 * 4);
  f32x2* st_x = (f32x2*)alloc((size_t)S_ * 16 * 8);
  f32x2* st_x2 = (f32x2*)alloc((size_t)S_ * 16 * 8);
  f32x2* st_x1 = (f32x2*)alloc((size_t)S_ * 16 * 8);
  float* cs_qkv = (float*)alloc(16 * 3072 * 4);
  float* cs_w1 = (float*)alloc(16 * 1024 * 4);
  float* cs_w3 = (float*)alloc(16 * F_ * 4);
  float* cf_w1 = (float*)alloc(1024 * 4);
  float* cf_w3 = (float*)alloc(F_ * 4);
  char* wc = alloc(WC_END);

  prep_kernel<<<368 + 176, 256, 0, stream>>>(x_in, wqkv, w1, w3, cs_qkv, cs_w1, cs_w3, st_x, wo, w2, w4, wc);
  for (int layer = 0; layer < 2; ++layer) {
    const u16* x = layer == 0 ? x_in : xb;
    u16* xo = layer == 0 ? xb : (u16*)d_out;
    gemm_kernel<192, 3, 1024, 1024, 3072, EPI_QKV><<<256, 512, 0, stream>>>(x, wqkv, (const u16*)(wc + WC_QKV), cs_qkv, st_x, nullptr, nullptr, phiq, nullptr, wphi, kvp, zp);
    attn_out_kernel<<<layer == 0 ? 256 + 18 : 256, 256, 0, stream>>>(phiq, kvp, zp, ob, cs_w1, cs_w3, cf_w1, cf_w3);
    gemm_kernel<64, 5, 1024, 1024, 1024, EPI_WO><<<256, 512, 0, stream>>>(ob, wo, (const u16*)(wc + WC_WO), nullptr, nullptr, x, nullptr, x2, st_x2, nullptr, nullptr, nullptr);
    gemm_kernel<64, 5, 1024, 1024, 1024, EPI_W1><<<256, 512, 0, stream>>>(x2, w1, (const u16*)(wc + WC_W1), cf_w1, st_x2, nullptr, nullptr, hs, nullptr, nullptr, nullptr, nullptr);
    gemm_kernel<64, 5, 1024, 1024, 1024, EPI_W2><<<256, 512, 0, stream>>>(hs, w2, (const u16*)(wc + WC_W2), nullptr, st_x2, x2, nullptr, x1, st_x1, nullptr, nullptr, nullptr);
    gemm_kernel<224, 3, 1024, 1024, F_, EPI_W3><<<256, 512, 0, stream>>>(x1, w3, nullptr, cf_w3, st_x1, nullptr, nullptr, gb, nullptr, nullptr, nullptr, nullptr);
    gemm_kernel<64, 5, F_, F_, 1024, EPI_W4><<<256, 512, 0, stream>>>(gb, w4, (const u16*)(wc + WC_W4), nullptr, nullptr, x2, x1, xo, st_x, nullptr, nullptr, nullptr);
  }
}
```

```cpp
#include <hip/hip_runtime.h>
#include <hip/hip_bf16.h>
#include <stdint.h>
#include <type_traits>

typedef short bf16x8 __attribute__((ext_vector_type(8)));
typedef short s16x4 __attribute__((ext_vector_type(4)));
typedef float f32x4 __attribute__((ext_vector_type(4)));
typedef float f32x2 __attribute__((ext_vector_type(2)));
typedef unsigned u32x2 __attribute__((ext_vector_type(2)));
typedef unsigned u32x4 __attribute__((ext_vector_type(4)));
typedef __attribute__((address_space(3))) s16x4* lds_s16x4_p;
typedef unsigned short u16;

constexpr int S_ = 2048, D_ = 1024, H_ = 32, F_ = 3584;

__device__ __forceinline__ float bf2f(u16 v) { return __uint_as_float(((unsigned)v) << 16); }
__device__ __forceinline__ float bflo(unsigned v) { return __uint_as_float(v << 16); }
__device__ __forceinline__ float bfhi(unsigned v) { return __uint_as_float(v & 0xffff0000u); }
typedef __bf16 bf16x2_t __attribute__((ext_vector_type(2)));
__device__ __forceinline__ unsigned pk2(float a, float b) {
  return __builtin_bit_cast(unsigned, __builtin_convertvector((f32x2){a, b}, bf16x2_t));
}
__device__ __forceinline__ unsigned pk2v(f32x2 v) { return __builtin_bit_cast(unsigned, __builtin_convertvector(v, bf16x2_t)); }
__device__ __forceinline__ float max2_(float a, float b) { float r; asm("v_max_f32 %0, %1, %2" : "=v"(r) : "v"(a), "v"(b)); return r; }
__device__ __forceinline__ float min2_(float a, float b) { float r; asm("v_min_f32 %0, %1, %2" : "=v"(r) : "v"(a), "v"(b)); return r; }
__device__ __forceinline__ float max3_(float a, float b, float c) { float r; asm("v_max3_f32 %0, %1, %2, %3" : "=v"(r) : "v"(a), "v"(b), "v"(c)); return r; }
__device__ __forceinline__ float min3_(float a, float b, float c) { float r; asm("v_min3_f32 %0, %1, %2, %3" : "=v"(r) : "v"(a), "v"(b), "v"(c)); return r; }
__device__ __forceinline__ float rbf(float a) { return bflo(pk2(a, 0.f)); }
__device__ __forceinline__ float gelu_t(float x) {
  const float c1 = -2.0f * 0.7978845608028654f * 1.4426950408889634f;
  const float c2 = c1 * 0.044715f;
  float t = x * x;
  float w = x * __builtin_fmaf(t, c2, c1);
  float e = __builtin_amdgcn_exp2f(w);
  return x * __builtin_amdgcn_rcpf(1.0f + e);
}

__device__ __forceinline__ f32x2 gelu2(f32x2 x) {
  const float c1 = -2.0f * 0.7978845608028654f * 1.4426950408889634f;
  const float c2 = c1 * 0.044715f;
  f32x2 t = x * x;
  f32x2 w = x * (t * c2 + c1);
  f32x2 d = (f32x2){__builtin_amdgcn_exp2f(w[0]), __builtin_amdgcn_exp2f(w[1])} + 1.0f;
  return x * (f32x2){__builtin_amdgcn_rcpf(d[0]), __builtin_amdgcn_rcpf(d[1])};
}
__device__ __forceinline__ void x16(float v, float& a, float& b) {
  auto r = __builtin_amdgcn_permlane16_swap(__float_as_uint(v), __float_as_uint(v), false, false);
  a = __uint_as_float(r[0]); b = __uint_as_float(r[1]);
}
__device__ __forceinline__ void x32(float v, float& a, float& b) {
  auto r = __builtin_amdgcn_permlane32_swap(__float_as_uint(v), __float_as_uint(v), false, false);
  a = __uint_as_float(r[0]); b = __uint_as_float(r[1]);
}
__device__ __forceinline__ float rmax4(float v) { float a, b; x16(v, a, b); v = max2_(a, b); x32(v, a, b); return max2_(a, b); }
__device__ __forceinline__ float rmin4(float v) { float a, b; x16(v, a, b); v = min2_(a, b); x32(v, a, b); return min2_(a, b); }
__device__ __forceinline__ float rsum4(float v) { float a, b; x16(v, a, b); v = a + b; x32(v, a, b); return a + b; }

__device__ __forceinline__ float rsum8(float v) {
  v += __uint_as_float(__builtin_amdgcn_mov_dpp(__float_as_uint(v), 0xB1, 0xF, 0xF, true));
  v += __uint_as_float(__builtin_amdgcn_mov_dpp(__float_as_uint(v), 0x4E, 0xF, 0xF, true));
  v += __uint_as_float(__builtin_amdgcn_mov_dpp(__float_as_uint(v), 0x141, 0xF, 0xF, true));
  return v;
}

__device__ __forceinline__ void wt_store(u32x4 v, u32x4* p) { asm volatile("global_store_dwordx4 %0, %1, off sc1\n\ts_nop 1" :: "v"(p), "v"(v) : "memory"); }
__device__ __forceinline__ void wt_store(f32x4 v, f32x4* p) { asm volatile("global_store_dwordx4 %0, %1, off sc1\n\ts_nop 1" :: "v"(p), "v"(v) : "memory"); }
__device__ __forceinline__ void wt_store(u32x2 v, u32x2* p) { asm volatile("global_store_dwordx2 %0, %1, off sc1\n\ts_nop 1" :: "v"(p), "v"(v) : "memory"); }
#define WAIT_V(n) asm volatile("s_waitcnt vmcnt(" #n ")" ::: "memory")
#define WAIT_L0() asm volatile("s_waitcnt lgkmcnt(0)" ::: "memory")
template <int N> __device__ __forceinline__ void wait_vm() {
  asm volatile("s_waitcnt vmcnt(%0)" ::"i"(N) : "memory");
}

__device__ __forceinline__ void glds16(unsigned voff, const void* sbase, unsigned lds) {
  unsigned keep;
  asm volatile("s_mov_b32 %0, m0\n\ts_mov_b32 m0, %3\n\ts_nop 0\n\tglobal_load_lds_dwordx4 %1, %2\n\ts_mov_b32 m0, %0"
               : "=&s"(keep) : "v"(voff), "s"(sbase), "s"(lds) : "memory");
}

__device__ __forceinline__ void mfma_acc(f32x4& c, const u32x4& a, const u32x4& b) {
  asm volatile("v_mfma_f32_16x16x32_bf16 %0, %1, %2, %0" : "+v"(c) : "v"(a), "v"(b));
}

#define STAMP(i)

#define XPOS 2
enum { EPI_QKV = 0, EPI_WO = 1, EPI_W1 = 2, EPI_W2 = 3, EPI_W3 = 4, EPI_W4 = 5 };

template <int BN, int NST, int KD, int LDA, int LDB, int EPI>
__global__ __launch_bounds__(512, 2) void gemm_kernel(
    const u16* __restrict__ A, const u16* __restrict__ W,
    const u16* __restrict__ Wc,
    const float* __restrict__ cs,
    const f32x2* __restrict__ st_in,
    const u16* __restrict__ r0,
    const u16* __restrict__ r1,
    u16* __restrict__ out,
    f32x2* __restrict__ st_out,
    const u16* __restrict__ wphi,
    float* __restrict__ kvp,
    float* __restrict__ zp)
{
  constexpr int NFULL = BN / 64, HALF = (BN % 64) / 32;
  constexpr int A_BYTES = 128 * 64 * 2, B_BYTES = BN * 64 * 2, STAGE = A_BYTES + B_BYTES;
  constexpr int NT = KD / 64, NTW = 2 * NFULL + HALF, LPS = 4 + 2 * NFULL + HALF;
  constexpr int PB = BN * 2 + 16;
  constexpr int EPI_BYTES = (EPI == EPI_QKV) ? (4 * 64 * 144 + 2 * (64 * 32 + 64) * 4) : 128 * PB;
  constexpr int MAINB = NST * STAGE;
  constexpr int RS_OFF = (MAINB > EPI_BYTES ? MAINB : EPI_BYTES);
  constexpr int CS_OFF = RS_OFF + 128 * 16;
  constexpr int WP_OFF = CS_OFF + 1024;
  constexpr int QX_OFF = WP_OFF + 4096;
  constexpr int SMEM = WP_OFF + (EPI == EPI_QKV ? 4096 + 32768 : 0);
  constexpr bool HAS_CS = (EPI == EPI_QKV || EPI == EPI_W1 || EPI == EPI_W3);
  constexpr bool HAS_ST = (EPI == EPI_QKV || EPI == EPI_W1 || EPI == EPI_W3 || EPI == EPI_W2);
  constexpr bool HAS_RES = (EPI == EPI_WO || EPI == EPI_W2 || EPI == EPI_W4);
  constexpr int LAG = (BN == 64) ? 1 : 0;
  constexpr int CH = BN / 8, NIT = CH / 4;
  static_assert(SMEM <= 163840, "LDS");
  __shared__ __attribute__((aligned(16))) char smem[SMEM];

  STAMP(0);
  const int tid = threadIdx.x, lane = tid & 63;
  const int wid8 = __builtin_amdgcn_readfirstlane(tid >> 6), wid = wid8 & 3, wm = wid >> 1, wn = wid & 1;
  const int g = lane >> 4, l15 = lane & 15;
  const int xcd = blockIdx.x & 7, idx = blockIdx.x >> 3;
  const int tm = (xcd >> 1) * 4 + (idx & 3), tn = (xcd & 1) * 8 + (idx >> 2);
  const int row0 = tm * 128;
  f32x4* rs = reinterpret_cast<f32x4*>(smem + RS_OFF);

  f32x4 acc[4][NTW];
  unsigned pft[3] = {0u, 0u, 0u};
  u32x4 ra[HAS_RES ? NIT : 1], rb[(EPI == EPI_W4) ? NIT : 1];

  unsigned rof[HAS_RES ? NIT : 1];
  if constexpr (HAS_RES) {
#pragma unroll
    for (int it = 0; it < NIT; ++it) {
      const int c = tid + 512 * it, r = c / CH, ch = c % CH;
      rof[it] = (unsigned)((row0 + r) * D_ + tn * BN + ch * 8) * 2u;
      asm volatile("" : "+v"(rof[it]));
    }
  }
  auto ldres = [&]() {
    if constexpr (HAS_RES) {
#pragma unroll
      for (int it = 0; it < NIT; ++it) {
        ra[it] = *reinterpret_cast<const u32x4*>(reinterpret_cast<const char*>(r0) + rof[it]);
        if constexpr (EPI == EPI_W4) rb[it] = *reinterpret_cast<const u32x4*>(reinterpret_cast<const char*>(r1) + rof[it]);
      }
    }
  };

  if (wid8 >= 4) {
    unsigned voA[4], voB[NFULL > 0 ? NFULL : 1][2], voH = 0;
    const bool pk_ = (HALF == 0) && ((tn & 7) == 3);
    const unsigned pitchB = pk_ ? NFULL * 128 : LDB * 2;
#pragma unroll
    for (int i = 0; i < 4; ++i) {
      int r = (wid * 4 + i) * 8 + (lane >> 3);
      int ch = (lane & 7) ^ ((r >> 1) & 7);
      voA[i] = (unsigned)(row0 + r) * (LDA * 2) + ch * 16;
      if constexpr (EPI == EPI_WO) voA[i] = (unsigned)((ch >> 2) * S_ + row0 + r) * 64 + (ch & 3) * 16;
    }
#pragma unroll
    for (int b = 0; b < NFULL; ++b) {
      int colb = (EPI == EPI_QKV) ? (b * 1024 + tn * 64) : (tn * BN + b * 64);
#pragma unroll
      for (int j = 0; j < 2; ++j) {
        int r = (wid * 2 + j) * 8 + (lane >> 3);
        int f = ((r >> 1) & 1) | (((r >> 3) & 1) << 1);
        int chp = lane & 7;
        int gch = (((chp >> 1) ^ f) << 1) | (chp & 1);
        const int rs_ = (EPI == EPI_WO) ? ((r & 32) | ((r & 4) << 2) | ((r & 24) >> 1) | (r & 3)) : r;
        voB[b][j] = (unsigned)rs_ * pitchB + (pk_ ? b * 128 : colb * 2) + gch * 16;
      }
    }
    if constexpr (HALF) {
      int r = wid * 16 + (lane >> 2);
      int chp = lane & 3;
      int gch = (((chp >> 1) ^ ((r >> 3) & 1)) << 1) | (chp & 1);
      voH = (unsigned)r * (LDB * 2) + (tn * BN + NFULL * 64) * 2 + gch * 16;
    }
    const char* Ab = reinterpret_cast<const char*>(A);
    const char* Wb = pk_ ? reinterpret_cast<const char*>(Wc) + (size_t)(tn >> 3) * (KD * NFULL * 128) : reinterpret_cast<const char*>(W);
    const unsigned lbase = (unsigned)(uintptr_t)(__attribute__((address_space(3))) char*)smem;
    auto stage = [&](int kt, int buf) {
      const char* a = Ab + (size_t)kt * (EPI == EPI_WO ? 2 * S_ * 64 : 128);
      const char* w = Wb + (size_t)(kt * (64 * pitchB));
      const unsigned s = lbase + buf * STAGE;
#pragma unroll
      for (int i = 0; i < 4; ++i) glds16(voA[i], a, s + (wid * 4 + i) * 1024);
#pragma unroll
      for (int b = 0; b < NFULL; ++b)
#pragma unroll
        for (int j = 0; j < 2; ++j) glds16(voB[b][j], w, s + A_BYTES + b * 8192 + (wid * 2 + j) * 1024);
      if constexpr (HALF) glds16(voH, w, s + A_BYTES + NFULL * 8192 + wid * 1024);
    };
#pragma unroll
    for (int s = 0; s < NST; ++s) stage(s, s);
    constexpr int XL = 0;
    constexpr int NCS = HAS_CS ? (EPI == EPI_QKV ? 16 : 1) : 0;
    constexpr int XE = NCS + (EPI == EPI_QKV ? 1 : 0) + (HAS_ST ? 4 : 0) + (HAS_RES ? NIT * (EPI == EPI_W4 ? 2 : 1) : 0);
    f32x4 stp[4];
    const int lt = tid - 256;
    u32x4 wpx;
    f32x4 csp[HAS_CS ? NCS : 1];
    constexpr int XP = XPOS;
    wait_vm<(NST - 1 - LAG) * LPS>();
    __builtin_amdgcn_s_barrier();
#pragma unroll
    for (int kt = 0; kt < XP; ++kt) {
      wait_vm<(NST - 2 - LAG) * LPS>();
      __builtin_amdgcn_s_barrier();
      stage(kt + NST, kt % NST);
    }
    asm volatile("" ::: "memory");
    if constexpr (HAS_CS) {
      if (lt < BN / 4) {
        const float* cp = cs + (EPI == EPI_QKV ? (lt >> 4) * 1024 + tn * 64 + (lt & 15) * 4 : tn * BN + lt * 4);
#pragma unroll
        for (int i = 0; i < NCS; ++i) csp[i] = *reinterpret_cast<const f32x4*>(cp + i * LDB);
      }
    }
    if constexpr (EPI == EPI_QKV) wpx = *reinterpret_cast<const u32x4*>(wphi + tn * 2048 + lt * 8);
    if constexpr (HAS_ST) {
      const f32x4* sp = reinterpret_cast<const f32x4*>(st_in + (size_t)(row0 + (lt >> 1)) * 16 + (lt & 1) * 8);
#pragma unroll
      for (int i = 0; i < 4; ++i) stp[i] = sp[i];
    }
    ldres();
    asm volatile("" ::: "memory");
    constexpr int NPEEL = XP + NST - 1 - LAG;
    auto head = [&](auto xe) {
      constexpr int X = decltype(xe)::value;
#pragma unroll
      for (int kt = XP; kt < NPEEL; ++kt) {
        wait_vm<(NST - 2 - LAG) * LPS + X>();
        __builtin_amdgcn_s_barrier();
        stage(kt + NST, kt % NST);
        }
    };
    if (HAS_CS && wid == 0) head(std::integral_constant<int, XE>{});
    else head(std::integral_constant<int, XE - NCS>{});
    int buf = NPEEL % NST;
    for (int kt = NPEEL; kt < NT - NST; ++kt) {
      wait_vm<(NST - 2 - LAG) * LPS>();
      __builtin_amdgcn_s_barrier();
      stage(kt + NST, buf);
      buf = buf + 1 == NST ? 0 : buf + 1;
    }
    asm volatile("" ::: "memory");
#pragma unroll
    for (int r = 0; r < NST - 1; ++r) {
      constexpr int NSL = NST - LAG;
      if (r == 0) wait_vm<(NSL - 2) * LPS + XL>();
      else if (r == 1) wait_vm<(NSL > 3 ? NSL - 3 : 0) * LPS + XL>();
      else if (r == 2) wait_vm<(NSL > 4 ? NSL - 4 : 0) * LPS + XL>();
      else if (r == 3) wait_vm<(NSL > 5 ? NSL - 5 : 0) * LPS + XL>();
      else wait_vm<XL>();
      __builtin_amdgcn_s_barrier();
    }
    if constexpr (HAS_ST) {
#pragma unroll
      for (int i = 0; i < 4; ++i) asm volatile("" : "+v"(stp[i]));
      float sm = 0.f;
#pragma unroll
      for (int i = 0; i < 4; ++i) sm += stp[i][0] + stp[i][2];
      sm += __shfl_xor(sm, 1);
      float mean = sm * (1.0f / 1024.f);
      float m2 = 0.f;
#pragma unroll
      for (int i = 0; i < 4; ++i) {
        float d0 = stp[i][0] * (1.0f / 64.f) - mean, d1 = stp[i][2] * (1.0f / 64.f) - mean;
        m2 += stp[i][1] + stp[i][3] + 64.f * (d0 * d0 + d1 * d1);
      }
      m2 += __shfl_xor(m2, 1);
      float var = m2 * (1.0f / 1024.f);
      float r1_ = 1.0f / sqrtf(var + 1e-5f);
      float sc = r1_;
      if constexpr (EPI == EPI_W1) sc = r1_ / sqrtf(var * r1_ * r1_ + 1e-5f);
      if ((lt & 1) == 0) rs[lt >> 1] = (f32x4){mean, sc, r1_, 0.f};
    }
    if constexpr (HAS_CS) {
      if (lt < BN / 4) {
#pragma unroll
        for (int i = 0; i < NCS; ++i) asm volatile("" : "+v"(csp[i]));
#pragma unroll
        for (int i = NCS / 2; i > 0; i >>= 1)
#pragma unroll
          for (int j = 0; j < i; ++j) csp[j] += csp[j + i];
        *reinterpret_cast<f32x4*>(smem + CS_OFF + lt * 16) = csp[0];
      }
    }
    if constexpr (EPI == EPI_QKV) *reinterpret_cast<u32x4*>(smem + WP_OFF + lt * 16) = wpx;
  } else {
    ldres();
    const int q = l15 >> 2, p = lane & 3;
    const int fB = (q >> 1) | ((g & 1) << 1);
    unsigned aoff[2], boff[2], hoff = 0;
#pragma unroll
    for (int ks = 0; ks < 2; ++ks) aoff[ks] = (wm * 64 + l15) * 128 + (((4 * ks + g) ^ (l15 >> 1)) * 16);
#pragma unroll
    for (int u = 0; u < 2; ++u) boff[u] = A_BYTES + (8 * g + q) * 128 + (((2 * wn + u) ^ fB) * 32) + p * 8;
    if constexpr (HALF) hoff = A_BYTES + NFULL * 8192 + (8 * g + q) * 64 + ((wn ^ (g & 1)) * 32) + p * 8;

    auto ldf = [&](int i, const char* s, int ks, u32x4(&af)[4], u32x4(&bfr)[NTW]) {
      if (i == 0 || i > NTW) {
        const int mt = i == 0 ? 0 : i - NTW;
        af[mt] = *reinterpret_cast<const u32x4*>(s + aoff[ks] + mt * 2048);
      } else if (i - 1 < 2 * NFULL) {
        const int b = (i - 1) >> 1, u = (i - 1) & 1;
        s16x4 lo = __builtin_amdgcn_ds_read_tr16_b64_v4i16((lds_s16x4_p)(s + boff[u] + b * 8192 + ks * 4096));
        s16x4 hi = __builtin_amdgcn_ds_read_tr16_b64_v4i16((lds_s16x4_p)(s + boff[u] + b * 8192 + ks * 4096 + 512));
        bfr[i - 1] = __builtin_shufflevector(__builtin_bit_cast(u32x2, lo), __builtin_bit_cast(u32x2, hi), 0, 1, 2, 3);
      } else {
        s16x4 lo = __builtin_amdgcn_ds_read_tr16_b64_v4i16((lds_s16x4_p)(s + hoff + ks * 2048));
        s16x4 hi = __builtin_amdgcn_ds_read_tr16_b64_v4i16((lds_s16x4_p)(s + hoff + ks * 2048 + 256));
        bfr[NTW - 1] = __builtin_shufflevector(__builtin_bit_cast(u32x2, lo), __builtin_bit_cast(u32x2, hi), 0, 1, 2, 3);
      }
    };
    constexpr int NM = 4 * NTW, NL = NTW + 4;
    auto phase = [&](u32x4(&afc)[4], u32x4(&bfc)[NTW], u32x4(&afn)[4], u32x4(&bfn)[NTW], int nbuf, int nks, auto rd, auto bar) {
      const char* s = smem + nbuf * STAGE;
      constexpr int BAR = decltype(bar)::value, R0 = BAR < 0 ? 0 : BAR + 1;
      constexpr int RSP = (NM - R0 - 2) / NL > 0 ? (NM - R0 - 2) / NL : 1;
      static_assert(R0 + (NL - 1) * RSP < NM, "reads");
#pragma unroll
      for (int mt = 0; mt < 4; ++mt)
#pragma unroll
        for (int j = 0; j < NTW; ++j) {
          const int m = mt * NTW + j;
          if (m == BAR) {
            WAIT_L0();
            __builtin_amdgcn_s_barrier();
            asm volatile("" ::: "memory");
          }
          if (decltype(rd)::value && m >= R0 && (m - R0) % RSP == 0 && (m - R0) / RSP < NL) ldf((m - R0) / RSP, s, nks, afn, bfn);
          if (EPI == EPI_QKV && j >= 4) mfma_acc(acc[mt][j], afc[mt], bfc[j]);
          else mfma_acc(acc[mt][j], bfc[j], afc[mt]);
          __builtin_amdgcn_sched_barrier(0);
        }
    };
    typedef std::integral_constant<int, -1> nobar;
#pragma unroll
    for (int mt = 0; mt < 4; ++mt)
#pragma unroll
      for (int j = 0; j < NTW; ++j) {
        acc[mt][j] = (f32x4){0.f, 0.f, 0.f, 0.f};
        asm volatile("" : "+v"(acc[mt][j]));
      }
    u32x4 af0[4], af1[4], bf0[NTW], bf1[NTW];
    __builtin_amdgcn_s_barrier();
    asm volatile("" ::: "memory");
    STAMP(1);
#pragma unroll
    for (int i = 0; i < NL; ++i) { ldf(i, smem, 0, af0, bf0); __builtin_amdgcn_sched_barrier(0); }
    int buf = 0;
    if constexpr (LAG) {
      phase(af0, bf0, af1, bf1, 0, 1, std::true_type{}, nobar{});
      for (int kt = 0; kt < NT - 1; ++kt) {
        buf = buf + 1 == NST ? 0 : buf + 1;
        phase(af1, bf1, af0, bf0, buf, 0, std::true_type{}, nobar{});
        asm volatile("" ::: "memory");
        __builtin_amdgcn_s_barrier();
        asm volatile("" ::: "memory");
        phase(af0, bf0, af1, bf1, buf, 1, std::true_type{}, nobar{});
      }
      phase(af1, bf1, af0, bf0, buf, 0, std::false_type{}, nobar{});
    } else {
      for (int kt = 0; kt < NT - 1; ++kt) {
        phase(af0, bf0, af1, bf1, buf, 1, std::true_type{}, nobar{});
        buf = buf + 1 == NST ? 0 : buf + 1;
        phase(af1, bf1, af0, bf0, buf, 0, std::true_type{}, std::integral_constant<int, 4>{});
      }
      phase(af0, bf0, af1, bf1, buf, 1, std::true_type{}, nobar{});
      phase(af1, bf1, af0, bf0, buf, 0, std::false_type{}, nobar{});
    }
    asm volatile("s_nop 7\n\ts_nop 7\n\ts_nop 7" ::: "memory");
    if constexpr (EPI == EPI_QKV) {
      if (r0 != nullptr) {
        const unsigned ln = (blockIdx.x * 4 + wid) * 64 + lane;
        const char* p01 = reinterpret_cast<const char*>(ln < 32768 ? r0 : r1) + (size_t)(ln & 32767) * 64;
        const char* p2 = reinterpret_cast<const char*>(st_out) + (size_t)ln * 64;
        pft[0] = *reinterpret_cast<const unsigned*>(p01);
        pft[1] = *reinterpret_cast<const unsigned*>(p2);
        if (ln + 65536 < F_ * 1024 * 2 / 64) pft[2] = *reinterpret_cast<const unsigned*>(p2 + 65536 * 64);
      }
    }
#pragma unroll
    for (int mt = 0; mt < 4; ++mt)
#pragma unroll
      for (int j = 0; j < NTW; ++j) asm volatile("" : "+v"(acc[mt][j]));
    if constexpr (EPI == EPI_QKV) {
#pragma unroll
      for (int mt = 0; mt < 4; ++mt)
#pragma unroll
        for (int t = 0; t < 2; ++t) *reinterpret_cast<f32x4*>(smem + QX_OFF + wid * 8192 + (mt * 2 + t) * 1024 + lane * 16) = acc[mt][t];
    }
  }
  WAIT_L0();
  __builtin_amdgcn_s_barrier();
  asm volatile("" ::: "memory");
  STAMP(2);

  if constexpr (EPI == EPI_QKV) {
    const int h = tn * 2 + wn;
    const int q = l15 >> 2, p = lane & 3;
    f32x4 kv[4][2], zz[4];
    float zsel = 0.f;
    float* red = reinterpret_cast<float*>(smem + 4 * 64 * 144) + wn * (64 * 32 + 64);
    bf16x8 wf[2];
#pragma unroll
    for (int ft = 0; ft < 2; ++ft) {
      const char* src = smem + WP_OFF + wn * 2048 + (4 * g + q) * 64 + ft * 32 + p * 8;
      s16x4 lo = __builtin_amdgcn_ds_read_tr16_b64_v4i16((lds_s16x4_p)(src));
      s16x4 hi = __builtin_amdgcn_ds_read_tr16_b64_v4i16((lds_s16x4_p)(src + 16 * 64));
      wf[ft] = __builtin_shufflevector(lo, hi, 0, 1, 2, 3, 4, 5, 6, 7);
    }
    const float* csl = reinterpret_cast<const float*>(smem + CS_OFF);
    auto feat = [&](const bf16x8& xf, u32x2 (&o)[4]) {
      const float L2E = 1.4426950408889634f;
      f32x4 z4 = (f32x4){0.f, 0.f, 0.f, 0.f};
      f32x4 p0 = __builtin_amdgcn_mfma_f32_16x16x32_bf16(wf[0], xf, z4, 0, 0, 0);
      f32x4 p1 = __builtin_amdgcn_mfma_f32_16x16x32_bf16(wf[1], xf, z4, 0, 0, 0);
      f32x2 v[4];
      {
        const unsigned r0_ = pk2(p0[0], p0[1]), r1_ = pk2(p0[2], p0[3]), r2_ = pk2(p1[0], p1[1]), r3_ = pk2(p1[2], p1[3]);
        v[0] = (f32x2){bflo(r0_), bfhi(r0_)}; v[1] = (f32x2){bflo(r1_), bfhi(r1_)};
        v[2] = (f32x2){bflo(r2_), bfhi(r2_)}; v[3] = (f32x2){bflo(r3_), bfhi(r3_)};
      }
      float mx = max2_(max3_(max3_(v[0][0], v[0][1], v[1][0]), v[1][1], v[2][0]), max3_(v[2][1], v[3][0], v[3][1]));
      float mn = min2_(min3_(min3_(v[0][0], v[0][1], v[1][0]), v[1][1], v[2][0]), min3_(v[2][1], v[3][0], v[3][1]));
      mx = rmax4(mx); mn = rmin4(mn);
      const float mxl = -mx * L2E, mnl = mn * L2E;
      f32x2 ep[4], en[4];
#pragma unroll
      for (int i = 0; i < 4; ++i) {
        const f32x2 ap = v[i] * L2E + mxl, an = v[i] * (-L2E) + mnl;
        ep[i] = (f32x2){__builtin_amdgcn_exp2f(ap[0]), __builtin_amdgcn_exp2f(ap[1])};
        en[i] = (f32x2){__builtin_amdgcn_exp2f(an[0]), __builtin_amdgcn_exp2f(an[1])};
      }
      const f32x2 sp2 = (ep[0] + ep[1]) + (ep[2] + ep[3]), sn2 = (en[0] + en[1]) + (en[2] + en[3]);
      const float ip = __builtin_amdgcn_rcpf(rsum4(sp2[0] + sp2[1])), in_ = __builtin_amdgcn_rcpf(rsum4(sn2[0] + sn2[1]));
      o[0] = (u32x2){pk2v(ep[0] * ip), pk2v(ep[1] * ip)};
      o[1] = (u32x2){pk2v(ep[2] * ip), pk2v(ep[3] * ip)};
      o[2] = (u32x2){pk2v(en[0] * in_), pk2v(en[1] * in_)};
      o[3] = (u32x2){pk2v(en[2] * in_), pk2v(en[3] * in_)};
    };
    if (wid8 >= 4) {
      f32x4 cs4[2];
#pragma unroll
      for (int t = 0; t < 2; ++t) cs4[t] = *reinterpret_cast<const f32x4*>(csl + wn * 32 + t * 16 + 4 * g);
      bf16x8 qf[4];
#pragma unroll
      for (int mt = 0; mt < 4; ++mt) {
        f32x4 st = rs[wm * 64 + mt * 16 + l15];
        unsigned pq_[4];
#pragma unroll
        for (int t = 0; t < 2; ++t) {
          const f32x4 ac = *reinterpret_cast<const f32x4*>(smem + QX_OFF + wid * 8192 + (mt * 2 + t) * 1024 + lane * 16);
          float a[4];
#pragma unroll
          for (int i = 0; i < 4; ++i) a[i] = st[1] * (ac[i] - st[0] * cs4[t][i]);
          pq_[t * 2] = pk2(a[0], a[1]); pq_[t * 2 + 1] = pk2(a[2], a[3]);
        }
        qf[mt] = __builtin_bit_cast(bf16x8, (u32x4){pq_[0], pq_[1], pq_[2], pq_[3]});
      }
#pragma unroll
      for (int mt = 0; mt < 4; ++mt) {
        u32x2 o[4];
        feat(qf[mt], o);
        u32x4* dst = reinterpret_cast<u32x4*>(out) + ((size_t)(tm * 8 + wm * 4 + mt) * 32 + h) * 128 + lane;
        wt_store((u32x4){o[0][0], o[0][1], o[1][0], o[1][1]}, dst);
        wt_store((u32x4){o[2][0], o[2][1], o[3][0], o[3][1]}, dst + 64);
      }
    } else {
      f32x4 cs4[2];
      float csv[2];
#pragma unroll
      for (int t = 0; t < 2; ++t) {
        cs4[t] = *reinterpret_cast<const f32x4*>(csl + 64 + wn * 32 + t * 16 + 4 * g);
        csv[t] = csl[128 + wn * 32 + t * 16 + l15];
      }
      bf16x8 kf[4], vf[2][2];
#pragma unroll
      for (int mt = 0; mt < 4; ++mt) {
        f32x4 st = rs[wm * 64 + mt * 16 + l15];
        unsigned pk_[4];
#pragma unroll
        for (int t = 0; t < 2; ++t) {
          float b[4];
#pragma unroll
          for (int i = 0; i < 4; ++i) b[i] = st[1] * (acc[mt][2 + t][i] - st[0] * cs4[t][i]);
          pk_[t * 2] = pk2(b[0], b[1]); pk_[t * 2 + 1] = pk2(b[2], b[3]);
        }
        kf[mt] = __builtin_bit_cast(bf16x8, (u32x4){pk_[0], pk_[1], pk_[2], pk_[3]});
      }
      char* pkl = smem + wid * (64 * 144);
#pragma unroll
      for (int mt = 0; mt < 4; ++mt) {
        u32x2 o[4];
        feat(kf[mt], o);
        char* dst = pkl + (mt * 16 + l15) * 144 + g * 8;
#pragma unroll
        for (int c = 0; c < 4; ++c) *reinterpret_cast<u32x2*>(dst + c * 32) = o[c];
      }
#pragma unroll
      for (int s = 0; s < 2; ++s) {
        unsigned pv[2][4];
#pragma unroll
        for (int m2 = 0; m2 < 2; ++m2) {
          int mt = s * 2 + m2;
          f32x4 s0 = rs[wm * 64 + mt * 16 + 4 * g + 0], s1 = rs[wm * 64 + mt * 16 + 4 * g + 1];
          f32x4 s2 = rs[wm * 64 + mt * 16 + 4 * g + 2], s3 = rs[wm * 64 + mt * 16 + 4 * g + 3];
#pragma unroll
          for (int t = 0; t < 2; ++t) {
            float a0 = s0[1] * (acc[mt][4 + t][0] - s0[0] * csv[t]);
            float a1 = s1[1] * (acc[mt][4 + t][1] - s1[0] * csv[t]);
            float a2 = s2[1] * (acc[mt][4 + t][2] - s2[0] * csv[t]);
            float a3 = s3[1] * (acc[mt][4 + t][3] - s3[0] * csv[t]);
            pv[t][m2 * 2] = pk2(a0, a1); pv[t][m2 * 2 + 1] = pk2(a2, a3);
          }
        }
#pragma unroll
        for (int t = 0; t < 2; ++t) vf[s][t] = __builtin_bit_cast(bf16x8, (u32x4){pv[t][0], pv[t][1], pv[t][2], pv[t][3]});
      }
      WAIT_L0();
      __builtin_amdgcn_sched_barrier(0);
#pragma unroll
      for (int ft = 0; ft < 4; ++ft) { kv[ft][0] = kv[ft][1] = zz[ft] = (f32x4){0.f, 0.f, 0.f, 0.f}; }
      const bf16x8 ones = (bf16x8){0x3F80, 0x3F80, 0x3F80, 0x3F80, 0x3F80, 0x3F80, 0x3F80, 0x3F80};
#pragma unroll
      for (int s = 0; s < 2; ++s)
#pragma unroll
        for (int ft = 0; ft < 4; ++ft) {
          const char* src = pkl + (32 * s + 4 * g + q) * 144 + ft * 32 + p * 8;
          s16x4 lo = __builtin_amdgcn_ds_read_tr16_b64_v4i16((lds_s16x4_p)(src));
          s16x4 hi = __builtin_amdgcn_ds_read_tr16_b64_v4i16((lds_s16x4_p)(src + 16 * 144));
          bf16x8 pf = __builtin_shufflevector(lo, hi, 0, 1, 2, 3, 4, 5, 6, 7);
          kv[ft][0] = __builtin_amdgcn_mfma_f32_16x16x32_bf16(vf[s][0], pf, kv[ft][0], 0, 0, 0);
          kv[ft][1] = __builtin_amdgcn_mfma_f32_16x16x32_bf16(vf[s][1], pf, kv[ft][1], 0, 0, 0);
          zz[ft] = __builtin_amdgcn_mfma_f32_16x16x32_bf16(ones, pf, zz[ft], 0, 0, 0);
        }
      zsel = g == 0 ? zz[0][0] : g == 1 ? zz[1][0] : g == 2 ? zz[2][0] : zz[3][0];
      if (wm == 1) {
#pragma unroll
        for (int ft = 0; ft < 4; ++ft)
#pragma unroll
          for (int dt = 0; dt < 2; ++dt) *reinterpret_cast<f32x4*>(red + ((ft * 2 + dt) * 64 + lane) * 4) = kv[ft][dt];
        red[64 * 32 + lane] = zsel;
      }
    }
    __syncthreads();
    if (wid8 < 2) {
      float* kd = kvp + (size_t)(h * 16 + tm) * 2048;
      float* zd = zp + (size_t)(h * 16 + tm) * 64;
#pragma unroll
      for (int ft = 0; ft < 4; ++ft)
#pragma unroll
        for (int dt = 0; dt < 2; ++dt) {
          f32x4 o = *reinterpret_cast<const f32x4*>(red + ((ft * 2 + dt) * 64 + lane) * 4);
          wt_store(o + kv[ft][dt], reinterpret_cast<f32x4*>(kd + ((ft * 2 + dt) * 64 + lane) * 4));
        }
      zd[lane] = red[64 * 32 + lane] + zsel;
    }
    asm volatile("" ::"v"(pft[0]), "v"(pft[1]), "v"(pft[2]));
  } else {
    if (wid8 < 4) {
      f32x4 stm[4], cs4[NTW];
      if constexpr (EPI == EPI_W1 || EPI == EPI_W3) {
#pragma unroll
        for (int j = 0; j < NTW; ++j) {
          const int colj = (j < 2 * NFULL) ? ((j >> 1) * 64 + (2 * wn + (j & 1)) * 16) : (NFULL * 64 + wn * 16);
          cs4[j] = *reinterpret_cast<const f32x4*>(smem + CS_OFF + (colj + 4 * g) * 4);
        }
#pragma unroll
        for (int mt = 0; mt < 4; ++mt) stm[mt] = rs[wm * 64 + mt * 16 + l15];
      }
#pragma unroll
      for (int j = 0; j < NTW; ++j) {
        const int colj = (j < 2 * NFULL) ? ((j >> 1) * 64 + (2 * wn + (j & 1)) * 16) : (NFULL * 64 + wn * 16);
#pragma unroll
        for (int mt = 0; mt < 4; ++mt) {
          f32x2 v0 = (f32x2){acc[mt][j][0], acc[mt][j][1]}, v1 = (f32x2){acc[mt][j][2], acc[mt][j][3]};
          if constexpr (EPI == EPI_W1 || EPI == EPI_W3) {
            v0 = gelu2((v0 - stm[mt][0] * (f32x2){cs4[j][0], cs4[j][1]}) * stm[mt][1]);
            v1 = gelu2((v1 - stm[mt][0] * (f32x2){cs4[j][2], cs4[j][3]}) * stm[mt][1]);
          }
          *reinterpret_cast<u32x2*>(smem + (wm * 64 + mt * 16 + l15) * PB + (colj + 4 * g) * 2) = (u32x2){pk2(v0[0], v0[1]), pk2(v1[0], v1[1])};
        }
      }
    }
    __syncthreads();
#pragma unroll
    for (int it = 0; it < NIT; ++it) {
      const int c = tid + 512 * it, r = c / CH, ch = c % CH;
      u32x4 v = *reinterpret_cast<const u32x4*>(smem + r * PB + ch * 16);
      const int grow = row0 + r, gcol = tn * BN + ch * 8;
      if constexpr (EPI == EPI_W3) {
        wt_store(v, reinterpret_cast<u32x4*>(out + (size_t)grow * F_ + gcol));
      } else if constexpr (EPI == EPI_W1) {
        const int grp = tn >> 2;
        const int sh = grp == 0 ? 1 : grp == 1 ? -1 : grp == 2 ? 2 : -2;
        const int dr = grow + sh;
        if (dr >= 0 && dr < S_) wt_store(v, reinterpret_cast<u32x4*>(out + (size_t)dr * D_ + gcol));
        const int zr = grow - sh;
        if (zr < 0 || zr >= S_) *reinterpret_cast<u32x4*>(out + (size_t)grow * D_ + gcol) = (u32x4){0u, 0u, 0u, 0u};
      } else {
        float y[8];
        const u32x4 a = ra[it];
        if constexpr (EPI == EPI_WO) {
#pragma unroll
          for (int i = 0; i < 4; ++i) { y[2 * i] = rbf(bflo(a[i]) + bflo(v[i])); y[2 * i + 1] = rbf(bfhi(a[i]) + bfhi(v[i])); }
        } else if constexpr (EPI == EPI_W2) {
          f32x4 st = rs[r];
#pragma unroll
          for (int i = 0; i < 4; ++i) {
            y[2 * i] = rbf(rbf((bflo(a[i]) - st[0]) * st[2]) + bflo(v[i]));
            y[2 * i + 1] = rbf(rbf((bfhi(a[i]) - st[0]) * st[2]) + bfhi(v[i]));
          }
        } else {
          const u32x4 b = rb[it];
#pragma unroll
          for (int i = 0; i < 4; ++i) {
            y[2 * i] = rbf(bflo(a[i]) + rbf(bflo(b[i]) + bflo(v[i])));
            y[2 * i + 1] = rbf(bfhi(a[i]) + rbf(bfhi(b[i]) + bfhi(v[i])));
          }
        }
        wt_store((u32x4){pk2(y[0], y[1]), pk2(y[2], y[3]), pk2(y[4], y[5]), pk2(y[6], y[7])}, reinterpret_cast<u32x4*>(out + (size_t)grow * D_ + gcol));
        float sm = ((y[0] + y[1]) + (y[2] + y[3])) + ((y[4] + y[5]) + (y[6] + y[7]));
        sm = rsum8(sm);
        float mu = sm * (1.0f / 64.f), m2 = 0.f;
#pragma unroll
        for (int i = 0; i < 8; ++i) { float d = y[i] - mu; m2 += d * d; }
        m2 = rsum8(m2);
        if (ch == 0) st_out[(size_t)grow * 16 + tn] = (f32x2){sm, m2};
      }
    }
  }
  STAMP(3);
}

constexpr int WC_QKV = 0, WC_WO = WC_QKV + 2 * 1024 * 384, WC_W1 = WC_WO + 2 * 1024 * 128, WC_W2 = WC_W1 + 2 * 1024 * 128,
              WC_W4 = WC_W2 + 2 * 1024 * 128, WC_END = WC_W4 + 2 * F_ * 128;
__global__ __launch_bounds__(256, 1) void attn_out_kernel(const u16* __restrict__ phiq, const float* __restrict__ kvp,
                                                          const float* __restrict__ zp, u16* __restrict__ o,
                                                          const float* __restrict__ cp1, const float* __restrict__ cp3,
                                                          float* __restrict__ cs1, float* __restrict__ cs3,
                                                          const u16* __restrict__ wo, const u16* __restrict__ w2,
                                                          const u16* __restrict__ w4, char* __restrict__ wc) {
  __shared__ __attribute__((aligned(16))) u16 kvT[48 * 72];
  const int tid = threadIdx.x, lane = tid & 63, wid = tid >> 6, g = lane >> 4, l15 = lane & 15;
  if (blockIdx.x >= 256 + 18) {
    const int e = blockIdx.x - (256 + 18);
    const u16* w = e < 8 ? wo : e < 16 ? w2 : w4;
    const int kd = e < 16 ? 1024 : F_, row = (e < 8 ? e : e < 16 ? e - 8 : e - 16) * 128 + (tid >> 4), t = (tid >> 3) & 1, c = tid & 7;
    char* dst = wc + (e < 8 ? WC_WO : e < 16 ? WC_W2 : WC_W4) + (size_t)t * kd * 128 + (size_t)row * 128 + c * 16;
    u32x4 v[8];
#pragma unroll
    for (int p = 0; p < 8; ++p) v[p] = *reinterpret_cast<const u32x4*>(w + (size_t)(row + p * 16) * 1024 + (3 + 8 * t) * 64 + c * 8);
#pragma unroll
    for (int p = 0; p < 8; ++p) *reinterpret_cast<u32x4*>(dst + p * 2048) = v[p];
    return;
  }
  if (blockIdx.x >= 256) {
    const int i = (blockIdx.x - 256) * 256 + tid;
    const float* src = i < 1024 ? cp1 + i : cp3 + (i - 1024);
    const int ld = i < 1024 ? 1024 : F_;
    float c[16];
#pragma unroll
    for (int t = 0; t < 16; ++t) c[t] = src[t * ld];
#pragma unroll
    for (int t = 8; t > 0; t >>= 1)
#pragma unroll
      for (int j = 0; j < t; ++j) c[j] += c[j + t];
    *(i < 1024 ? cs1 + i : cs3 + (i - 1024)) = c[0];
    return;
  }
  const int h = blockIdx.x & 31, tg = blockIdx.x >> 5;
  f32x4 kvv[16][2];
  float zv[16];
  bf16x8 pq[4][2];
  {
    const float* src = kvp + (size_t)h * 16 * 2048 + tid * 8;
#pragma unroll
    for (int t = 0; t < 16; ++t) {
      kvv[t][0] = *reinterpret_cast<const f32x4*>(src + t * 2048);
      kvv[t][1] = *reinterpret_cast<const f32x4*>(src + t * 2048 + 4);
    }
#pragma unroll
    for (int t = 0; t < 16; ++t) zv[t] = zp[(size_t)(h * 16 + t) * 64 + lane];
#pragma unroll
    for (int mt = 0; mt < 4; ++mt) {
      const u16* ps = phiq + ((size_t)(tg * 16 + wid * 4 + mt) * 32 + h) * 1024 + lane * 8;
      pq[mt][0] = *reinterpret_cast<const bf16x8*>(ps);
      pq[mt][1] = *reinterpret_cast<const bf16x8*>(ps + 512);
    }
  }
  __builtin_amdgcn_sched_barrier(0);
  {
    for (int i = tid; i < 15 * 64; i += 256) kvT[(33 + i / 64) * 72 + (i & 63)] = 0;
    f32x4 a0 = kvv[0][0], a1 = kvv[0][1];
#pragma unroll
    for (int t = 1; t < 16; ++t) { a0 += kvv[t][0]; a1 += kvv[t][1]; }
    const int ft = tid >> 6, dt = (tid >> 5) & 1, ln = (2 * tid) & 63;
    const int d0 = dt * 16 + 4 * (ln >> 4), pos = (ft >> 1) * 32 + ((ln >> 2) & 3) * 8 + (ft & 1) * 4 + (ln & 3);
#pragma unroll
    for (int i = 0; i < 4; ++i) *reinterpret_cast<unsigned*>(&kvT[(d0 + i) * 72 + pos]) = pk2(a0[i], a1[i]);
    if (tid < 64) {
      float z = 0.f;
#pragma unroll
      for (int t = 0; t < 16; ++t) z += zv[t];
      kvT[32 * 72 + (tid >> 5) * 32 + ((tid >> 2) & 3) * 8 + ((tid >> 4) & 1) * 4 + (tid & 3)] = (u16)(pk2(z, 0.f) & 0xffff);
    }
  }
  __syncthreads();
  bf16x8 kf[3][2];
#pragma unroll
  for (int dt = 0; dt < 3; ++dt)
#pragma unroll
    for (int ks = 0; ks < 2; ++ks) kf[dt][ks] = *reinterpret_cast<const bf16x8*>(&kvT[(dt * 16 + l15) * 72 + ks * 32 + g * 8]);
#pragma unroll
  for (int mt = 0; mt < 4; ++mt) {
    const int tok = tg * 256 + wid * 64 + mt * 16 + l15;
    const bf16x8 p0 = pq[mt][0], p1 = pq[mt][1];
    f32x4 r[3];
#pragma unroll
    for (int dt = 0; dt < 3; ++dt) {
      r[dt] = (f32x4){0.f, 0.f, 0.f, 0.f};
      r[dt] = __builtin_amdgcn_mfma_f32_16x16x32_bf16(kf[dt][0], p0, r[dt], 0, 0, 0);
      r[dt] = __builtin_amdgcn_mfma_f32_16x16x32_bf16(kf[dt][1], p1, r[dt], 0, 0, 0);
    }
    float den = rbf(rbf(__shfl(r[2][0], l15)) + 1e-6f);
    float inv = 1.0f / den;
    const u32x4 w = (u32x4){pk2(rbf(r[0][0]) * inv, rbf(r[0][1]) * inv), pk2(rbf(r[0][2]) * inv, rbf(r[0][3]) * inv),
                            pk2(rbf(r[1][0]) * inv, rbf(r[1][1]) * inv), pk2(rbf(r[1][2]) * inv, rbf(r[1][3]) * inv)};
    wt_store(w, reinterpret_cast<u32x4*>(o + ((size_t)h * S_ + tok) * 32 + 8 * g));
  }
}

__global__ __launch_bounds__(256) void prep_kernel(const u16* __restrict__ x, const u16* __restrict__ wqkv,
                                                   const u16* __restrict__ w1, const u16* __restrict__ w3,
                                                   float* __restrict__ cs_qkv, float* __restrict__ cs_w1,
                                                   float* __restrict__ cs_w3, f32x2* __restrict__ st,
                                                   const u16* __restrict__ wo, const u16* __restrict__ w2,
                                                   const u16* __restrict__ w4, char* __restrict__ wc) {
  __shared__ __attribute__((aligned(16))) float red[4][512];
  const int tid = threadIdx.x, b = blockIdx.x;
  if (b >= 368) {
    unsigned long long c0, c1;
    asm volatile("s_getpc_b64 s[90:91]\n\ts_add_u32 s90, s90, _Z15attn_out_kernelPKtPKfS2_PtS2_S2_PfS4_S0_S0_S0_Pc@rel32@lo+4\n\t"
                 "s_addc_u32 s91, s91, _Z15attn_out_kernelPKtPKfS2_PtS2_S2_PfS4_S0_S0_S0_Pc@rel32@hi+12\n\ts_mov_b64 %0, s[90:91]"
                 : "=s"(c0) :: "s90", "s91", "scc");
    asm volatile("s_getpc_b64 s[90:91]\n\ts_add_u32 s90, s90, _Z11gemm_kernelILi64ELi5ELi3584ELi3584ELi1024ELi5EEvPKtS1_S1_PKfPKDv2_fS1_S1_PtPS4_S1_PfS9_@rel32@lo+4\n\t"
                 "s_addc_u32 s91, s91, _Z11gemm_kernelILi64ELi5ELi3584ELi3584ELi1024ELi5EEvPKtS1_S1_PKfPKDv2_fS1_S1_PtPS4_S1_PfS9_@rel32@hi+12\n\ts_mov_b64 %0, s[90:91]"
                 : "=s"(c1) :: "s90", "s91", "scc");
    c1 += 5632;
    if (c1 > c0 && c1 - c0 < 6 * 256 * 64) {
      unsigned v[6];
#pragma unroll
      for (int r = 0; r < 6; ++r) {
        const unsigned long long a = c0 + (unsigned long long)(r * 256 + tid) * 64;
        v[r] = a < c1 ? *reinterpret_cast<const unsigned*>(a) : 0u;
      }
#pragma unroll
      for (int r = 0; r < 6; ++r) asm volatile("" ::"v"(v[r]));
    }
  } else if (b < 240) {
    const u16* w; float* dst; int ld, ch, rg;
    if (b < 96) { w = wqkv; dst = cs_qkv; ld = 3072; ch = b % 6; rg = b / 6; }
    else if (b < 128) { w = w1; dst = cs_w1; ld = 1024; ch = (b - 96) & 1; rg = (b - 96) >> 1; }
    else { w = w3; dst = cs_w3; ld = F_; ch = (b - 128) % 7; rg = (b - 128) / 7; }
    const int lane = tid & 63, wid = tid >> 6, row = rg * 64 + wid * 16;
    u32x4 v[16];
#pragma unroll
    for (int r = 0; r < 16; ++r) v[r] = *reinterpret_cast<const u32x4*>(w + (size_t)(row + r) * ld + ch * 512 + lane * 8);
    if (b < 128 && (lane >> 3) == 3) {
      const int cpp = b < 96 ? 384 : 128;
      char* cpd = wc + (b < 96 ? WC_QKV + (ch & 1) * (1024 * 384) + (ch >> 1) * 128 : WC_W1 + ch * (1024 * 128)) + row * cpp + (lane & 7) * 16;
#pragma unroll
      for (int r = 0; r < 16; ++r) *reinterpret_cast<u32x4*>(cpd + r * cpp) = v[r];
    }
    f32x4 s0 = (f32x4){0.f, 0.f, 0.f, 0.f}, s1 = s0;
#pragma unroll
    for (int r = 0; r < 16; ++r) {
      s0 += (f32x4){bflo(v[r][0]), bfhi(v[r][0]), bflo(v[r][1]), bfhi(v[r][1])};
      s1 += (f32x4){bflo(v[r][2]), bfhi(v[r][2]), bflo(v[r][3]), bfhi(v[r][3])};
    }
    *reinterpret_cast<f32x4*>(&red[wid][lane * 8]) = s0;
    *reinterpret_cast<f32x4*>(&red[wid][lane * 8 + 4]) = s1;
    __syncthreads();
    f32x2 t = *reinterpret_cast<const f32x2*>(&red[0][tid * 2]);
#pragma unroll
    for (int i = 1; i < 4; ++i) t += *reinterpret_cast<const f32x2*>(&red[i][tid * 2]);
    *reinterpret_cast<f32x2*>(dst + rg * ld + ch * 512 + tid * 2) = t;
  } else {
    const int lane = tid & 63, wid = tid >> 6;
    const int rbase = (b - 240) * 16 + wid * 4;
    u32x4 xv[8];
#pragma unroll
    for (int i = 0; i < 8; ++i) xv[i] = *reinterpret_cast<const u32x4*>(x + (size_t)rbase * D_ + i * 512 + lane * 8);
    __builtin_amdgcn_sched_barrier(0);
#pragma unroll
    for (int rr = 0; rr < 4; ++rr) {
      const int row = rbase + rr;
#pragma unroll
      for (int hf = 0; hf < 2; ++hf) {
        const u32x4 v = xv[rr * 2 + hf];
        float y[8];
#pragma unroll
        for (int i = 0; i < 4; ++i) { y[2 * i] = bflo(v[i]); y[2 * i + 1] = bfhi(v[i]); }
        float sm = ((y[0] + y[1]) + (y[2] + y[3])) + ((y[4] + y[5]) + (y[6] + y[7]));
        sm = rsum8(sm);
        float mu = sm * (1.0f / 64.f), m2 = 0.f;
#pragma unroll
        for (int i = 0; i < 8; ++i) { float d = y[i] - mu; m2 += d * d; }
        m2 = rsum8(m2);
        if ((lane & 7) == 0) st[(size_t)row * 16 + hf * 8 + (lane >> 3)] = (f32x2){sm, m2};
      }
    }
  }
}

extern "C" void kernel_launch(void* const* d_in, const int* in_sizes, int n_in, void* d_out, int out_size, void* d_ws,
                              size_t ws_size, hipStream_t stream) {
  const u16* x_in = (const u16*)d_in[0];
  const u16* wqkv = (const u16*)d_in[1];
  const u16* wphi = (const u16*)d_in[2];
  const u16* wo = (const u16*)d_in[3];
  const u16* w1 = (const u16*)d_in[5];
  const u16* w2 = (const u16*)d_in[6];
  const u16* w3 = (const u16*)d_in[7];
  const u16* w4 = (const u16*)d_in[8];
  char* ws = (char*)d_ws;
  size_t off = 0;
  auto alloc = [&](size_t bytes) { char* p = ws + off; off += (bytes + 4095) & ~(size_t)4095; return p; };
  u16* xb = (u16*)alloc((size_t)S_ * D_ * 2);
  u16* x2 = (u16*)alloc((size_t)S_ * D_ * 2);
  u16* x1 = (u16*)alloc((size_t)S_ * D_ * 2);
  u16* hs = (u16*)alloc((size_t)S_ * D_ * 2);
  u16* ob = (u16*)alloc((size_t)S_ * D_ * 2);
  u16* gb = (u16*)alloc((size_t)S_ * F_ * 2);
  u16* phiq = (u16*)alloc((size_t)S_ * 2048 * 2);
  float* kvp = (float*)alloc((size_t)32 * 16 * 2048 * 4);
  float* zp = (float*)alloc((size_t)32 * 16 * 64 * 4);
  f32x2* st_x = (f32x2*)alloc((size_t)S_ * 16 * 8);
  f32x2* st_x2 = (f32x2*)alloc((size_t)S_ * 16 * 8);
  f32x2* st_x1 = (f32x2*)alloc((size_t)S_ * 16 * 8);
  float* cs_qkv = (float*)alloc(16 * 3072 * 4);
  float* cs_w1 = (float*)alloc(16 * 1024 * 4);
  float* cs_w3 = (float*)alloc(16 * F_ * 4);
  float* cf_w1 = (float*)alloc(1024 * 4);
  float* cf_w3 = (float*)alloc(F_ * 4);
  char* wc = alloc(WC_END);

  prep_kernel<<<368 + 16, 256, 0, stream>>>(x_in, wqkv, w1, w3, cs_qkv, cs_w1, cs_w3, st_x, wo, w2, w4, wc);
  for (int layer = 0; layer < 2; ++layer) {
    const u16* x = layer == 0 ? x_in : xb;
    u16* xo = layer == 0 ? xb : (u16*)d_out;
    gemm_kernel<192, 3, 1024, 1024, 3072, EPI_QKV><<<256, 512, 0, stream>>>(x, wqkv, (const u16*)(wc + WC_QKV), cs_qkv, st_x, layer == 0 ? wo : nullptr, layer == 0 ? w2 : nullptr, phiq, layer == 0 ? (f32x2*)w4 : nullptr, wphi, kvp, zp);
    attn_out_kernel<<<layer == 0 ? 256 + 18 + 44 : 256, 256, 0, stream>>>(phiq, kvp, zp, ob, cs_w1, cs_w3, cf_w1, cf_w3, wo, w2, w4, wc);
    gemm_kernel<64, 5, 1024, 1024, 1024, EPI_WO><<<256, 512, 0, stream>>>(ob, wo, (const u16*)(wc + WC_WO), nullptr, nullptr, x, nullptr, x2, st_x2, nullptr, nullptr, nullptr);
    gemm_kernel<64, 5, 1024, 1024, 1024, EPI_W1><<<256, 512, 0, stream>>>(x2, w1, (const u16*)(wc + WC_W1), cf_w1, st_x2, nullptr, nullptr, hs, nullptr, nullptr, nullptr, nullptr);
    gemm_kernel<64, 5, 1024, 1024, 1024, EPI_W2><<<256, 512, 0, stream>>>(hs, w2, (const u16*)(wc + WC_W2), nullptr, st_x2, x2, nullptr, x1, st_x1, nullptr, nullptr, nullptr);
    gemm_kernel<224, 3, 1024, 1024, F_, EPI_W3><<<256, 512, 0, stream>>>(x1, w3, nullptr, cf_w3, st_x1, nullptr, nullptr, gb, nullptr, nullptr, nullptr, nullptr);
    gemm_kernel<64, 5, F_, F_, 1024, EPI_W4><<<256, 512, 0, stream>>>(gb, w4, (const u16*)(wc + WC_W4), nullptr, nullptr, x2, x1, xo, st_x, nullptr, nullptr, nullptr);
  }
}
```

```cpp
#include <hip/hip_runtime.h>
#include <hip/hip_bf16.h>
#include <stdint.h>
#include <type_traits>

typedef short bf16x8 __attribute__((ext_vector_type(8)));
typedef short s16x4 __attribute__((ext_vector_type(4)));
typedef float f32x4 __attribute__((ext_vector_type(4)));
typedef float f32x2 __attribute__((ext_vector_type(2)));
typedef unsigned u32x2 __attribute__((ext_vector_type(2)));
typedef unsigned u32x4 __attribute__((ext_vector_type(4)));
typedef __attribute__((address_space(3))) s16x4* lds_s16x4_p;
typedef unsigned short u16;

constexpr int S_ = 2048, D_ = 1024, H_ = 32, F_ = 3584;

__device__ __forceinline__ float bf2f(u16 v) { return __uint_as_float(((unsigned)v) << 16); }
__device__ __forceinline__ float bflo(unsigned v) { return __uint_as_float(v << 16); }
__device__ __forceinline__ float bfhi(unsigned v) { return __uint_as_float(v & 0xffff0000u); }
typedef __bf16 bf16x2_t __attribute__((ext_vector_type(2)));
__device__ __forceinline__ unsigned pk2(float a, float b) {
  return __builtin_bit_cast(unsigned, __builtin_convertvector((f32x2){a, b}, bf16x2_t));
}
__device__ __forceinline__ unsigned pk2v(f32x2 v) { return __builtin_bit_cast(unsigned, __builtin_convertvector(v, bf16x2_t)); }
__device__ __forceinline__ float max2_(float a, float b) { float r; asm("v_max_f32 %0, %1, %2" : "=v"(r) : "v"(a), "v"(b)); return r; }
__device__ __forceinline__ float min2_(float a, float b) { float r; asm("v_min_f32 %0, %1, %2" : "=v"(r) : "v"(a), "v"(b)); return r; }
__device__ __forceinline__ float max3_(float a, float b, float c) { float r; asm("v_max3_f32 %0, %1, %2, %3" : "=v"(r) : "v"(a), "v"(b), "v"(c)); return r; }
__device__ __forceinline__ float min3_(float a, float b, float c) { float r; asm("v_min3_f32 %0, %1, %2, %3" : "=v"(r) : "v"(a), "v"(b), "v"(c)); return r; }
__device__ __forceinline__ float rbf(float a) { return bflo(pk2(a, 0.f)); }
__device__ __forceinline__ float gelu_t(float x) {
  const float c1 = -2.0f * 0.7978845608028654f * 1.4426950408889634f;
  const float c2 = c1 * 0.044715f;
  float t = x * x;
  float w = x * __builtin_fmaf(t, c2, c1);
  float e = __builtin_amdgcn_exp2f(w);
  return x * __builtin_amdgcn_rcpf(1.0f + e);
}

__device__ __forceinline__ f32x2 gelu2(f32x2 x) {
  const float c1 = -2.0f * 0.7978845608028654f * 1.4426950408889634f;
  const float c2 = c1 * 0.044715f;
  f32x2 t = x * x;
  f32x2 w = x * (t * c2 + c1);
  f32x2 d = (f32x2){__builtin_amdgcn_exp2f(w[0]), __builtin_amdgcn_exp2f(w[1])} + 1.0f;
  return x * (f32x2){__builtin_amdgcn_rcpf(d[0]), __builtin_amdgcn_rcpf(d[1])};
}
__device__ __forceinline__ void x16(float v, float& a, float& b) {
  auto r = __builtin_amdgcn_permlane16_swap(__float_as_uint(v), __float_as_uint(v), false, false);
  a = __uint_as_float(r[0]); b = __uint_as_float(r[1]);
}
__device__ __forceinline__ void x32(float v, float& a, float& b) {
  auto r = __builtin_amdgcn_permlane32_swap(__float_as_uint(v), __float_as_uint(v), false, false);
  a = __uint_as_float(r[0]); b = __uint_as_float(r[1]);
}
__device__ __forceinline__ float rmax4(float v) { float a, b; x16(v, a, b); v = max2_(a, b); x32(v, a, b); return max2_(a, b); }
__device__ __forceinline__ float rmin4(float v) { float a, b; x16(v, a, b); v = min2_(a, b); x32(v, a, b); return min2_(a, b); }
__device__ __forceinline__ float rsum4(float v) { float a, b; x16(v, a, b); v = a + b; x32(v, a, b); return a + b; }

__device__ __forceinline__ float rsum8(float v) {
  v += __uint_as_float(__builtin_amdgcn_mov_dpp(__float_as_uint(v), 0xB1, 0xF, 0xF, true));
  v += __uint_as_float(__builtin_amdgcn_mov_dpp(__float_as_uint(v), 0x4E, 0xF, 0xF, true));
  v += __uint_as_float(__builtin_amdgcn_mov_dpp(__float_as_uint(v), 0x141, 0xF, 0xF, true));
  return v;
}

__device__ __forceinline__ void wt_store(u32x4 v, u32x4* p) { asm volatile("global_store_dwordx4 %0, %1, off sc1\n\ts_nop 1" :: "v"(p), "v"(v) : "memory"); }
__device__ __forceinline__ void wt_store(f32x4 v, f32x4* p) { asm volatile("global_store_dwordx4 %0, %1, off sc1\n\ts_nop 1" :: "v"(p), "v"(v) : "memory"); }
__device__ __forceinline__ void wt_store(u32x2 v, u32x2* p) { asm volatile("global_store_dwordx2 %0, %1, off sc1\n\ts_nop 1" :: "v"(p), "v"(v) : "memory"); }
#define WAIT_V(n) asm volatile("s_waitcnt vmcnt(" #n ")" ::: "memory")
#define WAIT_L0() asm volatile("s_waitcnt lgkmcnt(0)" ::: "memory")
template <int N> __device__ __forceinline__ void wait_vm() {
  asm volatile("s_waitcnt vmcnt(%0)" ::"i"(N) : "memory");
}

__device__ __forceinline__ void glds16(unsigned voff, const void* sbase, unsigned lds) {
  unsigned keep;
  asm volatile("s_mov_b32 %0, m0\n\ts_mov_b32 m0, %3\n\ts_nop 0\n\tglobal_load_lds_dwordx4 %1, %2\n\ts_mov_b32 m0, %0"
               : "=&s"(keep) : "v"(voff), "s"(sbase), "s"(lds) : "memory");
}

__device__ __forceinline__ void mfma_acc(f32x4& c, const u32x4& a, const u32x4& b) {
  asm volatile("v_mfma_f32_16x16x32_bf16 %0, %1, %2, %0" : "+v"(c) : "v"(a), "v"(b));
}

#define STAMP(i)

#define XPOS 2
enum { EPI_QKV = 0, EPI_WO = 1, EPI_W1 = 2, EPI_W2 = 3, EPI_W3 = 4, EPI_W4 = 5 };

#define CODE_SYM(dst, sym) asm volatile("s_getpc_b64 s[90:91]\n\ts_add_u32 s90, s90, " sym "@rel32@lo+4\n\ts_addc_u32 s91, s91, " sym "@rel32@hi+12\n\ts_mov_b64 %0, s[90:91]" : "=s"(dst) :: "s90", "s91", "scc")
#define CODE_FIRST "_Z15attn_out_kernelPKtPKfS2_PtS2_S2_PfS4_S0_S0_S0_Pc"
#define CODE_LAST "_Z11gemm_kernelILi64ELi5ELi3584ELi3584ELi1024ELi5EEvPKtS1_S1_PKfPKDv2_fS1_S1_PtPS4_S1_PfS9_"
typedef const __attribute__((address_space(1))) unsigned* gptr_u;
template <int BN, int NST, int KD, int LDA, int LDB, int EPI>
__global__ __launch_bounds__(512, 2) void gemm_kernel(
    const u16* __restrict__ A, const u16* __restrict__ W,
    const u16* __restrict__ Wc,
    const float* __restrict__ cs,
    const f32x2* __restrict__ st_in,
    const u16* __restrict__ r0,
    const u16* __restrict__ r1,
    u16* __restrict__ out,
    f32x2* __restrict__ st_out,
    const u16* __restrict__ wphi,
    float* __restrict__ kvp,
    float* __restrict__ zp)
{
  constexpr int NFULL = BN / 64, HALF = (BN % 64) / 32;
  constexpr int A_BYTES = 128 * 64 * 2, B_BYTES = BN * 64 * 2, STAGE = A_BYTES + B_BYTES;
  constexpr int NT = KD / 64, NTW = 2 * NFULL + HALF, LPS = 4 + 2 * NFULL + HALF;
  constexpr int PB = BN * 2 + 16;
  constexpr int EPI_BYTES = (EPI == EPI_QKV) ? (4 * 64 * 144 + 2 * (64 * 32 + 64) * 4) : 128 * PB;
  constexpr int MAINB = NST * STAGE;
  constexpr int RS_OFF = (MAINB > EPI_BYTES ? MAINB : EPI_BYTES);
  constexpr int CS_OFF = RS_OFF + 128 * 16;
  constexpr int WP_OFF = CS_OFF + 1024;
  constexpr int QX_OFF = WP_OFF + 4096;
  constexpr int SMEM = WP_OFF + (EPI == EPI_QKV ? 4096 + 32768 : 0);
  constexpr bool HAS_CS = (EPI == EPI_QKV || EPI == EPI_W1 || EPI == EPI_W3);
  constexpr bool HAS_ST = (EPI == EPI_QKV || EPI == EPI_W1 || EPI == EPI_W3 || EPI == EPI_W2);
  constexpr bool HAS_RES = (EPI == EPI_WO || EPI == EPI_W2 || EPI == EPI_W4);
  constexpr int LAG = (BN == 64) ? 1 : 0;
  constexpr int CH = BN / 8, NIT = CH / 4;
  static_assert(SMEM <= 163840, "LDS");
  __shared__ __attribute__((aligned(16))) char smem[SMEM];

  STAMP(0);
  unsigned long long pc0;
  asm volatile("s_getpc_b64 %0" : "=s"(pc0));
  const int tid = threadIdx.x, lane = tid & 63;
  const int wid8 = __builtin_amdgcn_readfirstlane(tid >> 6), wid = wid8 & 3, wm = wid >> 1, wn = wid & 1;
  const int g = lane >> 4, l15 = lane & 15;
  const int xcd = blockIdx.x & 7, idx = blockIdx.x >> 3;
  const int tm = (xcd >> 1) * 4 + (idx & 3), tn = (xcd & 1) * 8 + (idx >> 2);
  const int row0 = tm * 128;
  f32x4* rs = reinterpret_cast<f32x4*>(smem + RS_OFF);

  f32x4 acc[4][NTW];
  unsigned pft[3] = {0u, 0u, 0u};
  u32x4 ra[HAS_RES ? NIT : 1], rb[(EPI == EPI_W4) ? NIT : 1];

  unsigned rof[HAS_RES ? NIT : 1];
  if constexpr (HAS_RES) {
#pragma unroll
    for (int it = 0; it < NIT; ++it) {
      const int c = tid + 512 * it, r = c / CH, ch = c % CH;
      rof[it] = (unsigned)((row0 + r) * D_ + tn * BN + ch * 8) * 2u;
      asm volatile("" : "+v"(rof[it]));
    }
  }
  auto ldres = [&]() {
    if constexpr (HAS_RES) {
#pragma unroll
      for (int it = 0; it < NIT; ++it) {
        ra[it] = *reinterpret_cast<const u32x4*>(reinterpret_cast<const char*>(r0) + rof[it]);
        if constexpr (EPI == EPI_W4) rb[it] = *reinterpret_cast<const u32x4*>(reinterpret_cast<const char*>(r1) + rof[it]);
      }
    }
  };

  if (wid8 >= 4) {
    unsigned voA[4], voB[NFULL > 0 ? NFULL : 1][2], voH = 0;
    const bool pk_ = (HALF == 0) && ((tn & 7) == 3);
    const unsigned pitchB = pk_ ? NFULL * 128 : LDB * 2;
#pragma unroll
    for (int i = 0; i < 4; ++i) {
      int r = (wid * 4 + i) * 8 + (lane >> 3);
      int ch = (lane & 7) ^ ((r >> 1) & 7);
      voA[i] = (unsigned)(row0 + r) * (LDA * 2) + ch * 16;
      if constexpr (EPI == EPI_WO) voA[i] = (unsigned)((ch >> 2) * S_ + row0 + r) * 64 + (ch & 3) * 16;
    }
#pragma unroll
    for (int b = 0; b < NFULL; ++b) {
      int colb = (EPI == EPI_QKV) ? (b * 1024 + tn * 64) : (tn * BN + b * 64);
#pragma unroll
      for (int j = 0; j < 2; ++j) {
        int r = (wid * 2 + j) * 8 + (lane >> 3);
        int f = ((r >> 1) & 1) | (((r >> 3) & 1) << 1);
        int chp = lane & 7;
        int gch = (((chp >> 1) ^ f) << 1) | (chp & 1);
        const int rs_ = (EPI == EPI_WO) ? ((r & 32) | ((r & 4) << 2) | ((r & 24) >> 1) | (r & 3)) : r;
        voB[b][j] = (unsigned)rs_ * pitchB + (pk_ ? b * 128 : colb * 2) + gch * 16;
      }
    }
    if constexpr (HALF) {
      int r = wid * 16 + (lane >> 2);
      int chp = lane & 3;
      int gch = (((chp >> 1) ^ ((r >> 3) & 1)) << 1) | (chp & 1);
      voH = (unsigned)r * (LDB * 2) + (tn * BN + NFULL * 64) * 2 + gch * 16;
    }
    const char* Ab = reinterpret_cast<const char*>(A);
    const char* Wb = pk_ ? reinterpret_cast<const char*>(Wc) + (size_t)(tn >> 3) * (KD * NFULL * 128) : reinterpret_cast<const char*>(W);
    const unsigned lbase = (unsigned)(uintptr_t)(__attribute__((address_space(3))) char*)smem;
    auto stage = [&](int kt, int buf) {
      const char* a = Ab + (size_t)kt * (EPI == EPI_WO ? 2 * S_ * 64 : 128);
      const char* w = Wb + (size_t)(kt * (64 * pitchB));
      const unsigned s = lbase + buf * STAGE;
#pragma unroll
      for (int i = 0; i < 4; ++i) glds16(voA[i], a, s + (wid * 4 + i) * 1024);
#pragma unroll
      for (int b = 0; b < NFULL; ++b)
#pragma unroll
        for (int j = 0; j < 2; ++j) glds16(voB[b][j], w, s + A_BYTES + b * 8192 + (wid * 2 + j) * 1024);
      if constexpr (HALF) glds16(voH, w, s + A_BYTES + NFULL * 8192 + wid * 1024);
    };
#pragma unroll
    for (int s = 0; s < NST; ++s) stage(s, s);
    constexpr int XL = 0;
    constexpr int NCS = HAS_CS ? (EPI == EPI_QKV ? 16 : 1) : 0;
    constexpr int XE = NCS + (EPI == EPI_QKV ? 1 : 0) + (HAS_ST ? 4 : 0) + (HAS_RES ? NIT * (EPI == EPI_W4 ? 2 : 1) : 0);
    f32x4 stp[4];
    const int lt = tid - 256;
    u32x4 wpx;
    f32x4 csp[HAS_CS ? NCS : 1];
    constexpr int XP = XPOS;
    wait_vm<(NST - 1 - LAG) * LPS>();
    __builtin_amdgcn_s_barrier();
#pragma unroll
    for (int kt = 0; kt < XP; ++kt) {
      wait_vm<(NST - 2 - LAG) * LPS>();
      __builtin_amdgcn_s_barrier();
      stage(kt + NST, kt % NST);
    }
    asm volatile("" ::: "memory");
    if constexpr (HAS_CS) {
      if (lt < BN / 4) {
        const float* cp = cs + (EPI == EPI_QKV ? (lt >> 4) * 1024 + tn * 64 + (lt & 15) * 4 : tn * BN + lt * 4);
#pragma unroll
        for (int i = 0; i < NCS; ++i) csp[i] = *reinterpret_cast<const f32x4*>(cp + i * LDB);
      }
    }
    if constexpr (EPI == EPI_QKV) wpx = *reinterpret_cast<const u32x4*>(wphi + tn * 2048 + lt * 8);
    if constexpr (HAS_ST) {
      const f32x4* sp = reinterpret_cast<const f32x4*>(st_in + (size_t)(row0 + (lt >> 1)) * 16 + (lt & 1) * 8);
#pragma unroll
      for (int i = 0; i < 4; ++i) stp[i] = sp[i];
    }
    ldres();
    asm volatile("" ::: "memory");
    constexpr int NPEEL = XP + NST - 1 - LAG;
    auto head = [&](auto xe) {
      constexpr int X = decltype(xe)::value;
#pragma unroll
      for (int kt = XP; kt < NPEEL; ++kt) {
        wait_vm<(NST - 2 - LAG) * LPS + X>();
        __builtin_amdgcn_s_barrier();
        stage(kt + NST, kt % NST);
        }
    };
    if (HAS_CS && wid == 0) head(std::integral_constant<int, XE>{});
    else head(std::integral_constant<int, XE - NCS>{});
    int buf = NPEEL % NST;
    for (int kt = NPEEL; kt < NT - NST; ++kt) {
      wait_vm<(NST - 2 - LAG) * LPS>();
      __builtin_amdgcn_s_barrier();
      stage(kt + NST, buf);
      buf = buf + 1 == NST ? 0 : buf + 1;
    }
    asm volatile("" ::: "memory");
#pragma unroll
    for (int r = 0; r < NST - 1; ++r) {
      constexpr int NSL = NST - LAG;
      if (r == 0) wait_vm<(NSL - 2) * LPS + XL>();
      else if (r == 1) wait_vm<(NSL > 3 ? NSL - 3 : 0) * LPS + XL>();
      else if (r == 2) wait_vm<(NSL > 4 ? NSL - 4 : 0) * LPS + XL>();
      else if (r == 3) wait_vm<(NSL > 5 ? NSL - 5 : 0) * LPS + XL>();
      else wait_vm<XL>();
      __builtin_amdgcn_s_barrier();
    }
    if constexpr (HAS_ST) {
#pragma unroll
      for (int i = 0; i < 4; ++i) asm volatile("" : "+v"(stp[i]));
      float sm = 0.f;
#pragma unroll
      for (int i = 0; i < 4; ++i) sm += stp[i][0] + stp[i][2];
      sm += __shfl_xor(sm, 1);
      float mean = sm * (1.0f / 1024.f);
      float m2 = 0.f;
#pragma unroll
      for (int i = 0; i < 4; ++i) {
        float d0 = stp[i][0] * (1.0f / 64.f) - mean, d1 = stp[i][2] * (1.0f / 64.f) - mean;
        m2 += stp[i][1] + stp[i][3] + 64.f * (d0 * d0 + d1 * d1);
      }
      m2 += __shfl_xor(m2, 1);
      float var = m2 * (1.0f / 1024.f);
      float r1_ = 1.0f / sqrtf(var + 1e-5f);
      float sc = r1_;
      if constexpr (EPI == EPI_W1) sc = r1_ / sqrtf(var * r1_ * r1_ + 1e-5f);
      if ((lt & 1) == 0) rs[lt >> 1] = (f32x4){mean, sc, r1_, 0.f};
    }
    if constexpr (HAS_CS) {
      if (lt < BN / 4) {
#pragma unroll
        for (int i = 0; i < NCS; ++i) asm volatile("" : "+v"(csp[i]));
#pragma unroll
        for (int i = NCS / 2; i > 0; i >>= 1)
#pragma unroll
          for (int j = 0; j < i; ++j) csp[j] += csp[j + i];
        *reinterpret_cast<f32x4*>(smem + CS_OFF + lt * 16) = csp[0];
      }
    }
    if constexpr (EPI == EPI_QKV) *reinterpret_cast<u32x4*>(smem + WP_OFF + lt * 16) = wpx;
  } else {
    unsigned ctv = 0;
    if (blockIdx.x < 16) {
      unsigned long long c1;
      CODE_SYM(c1, CODE_LAST);
      const unsigned long long a = (pc0 & ~63ull) + (unsigned long long)tid * 64;
      if (a < c1 + 5632) ctv = *(gptr_u)a;
    }
    ldres();
    const int q = l15 >> 2, p = lane & 3;
    const int fB = (q >> 1) | ((g & 1) << 1);
    unsigned aoff[2], boff[2], hoff = 0;
#pragma unroll
    for (int ks = 0; ks < 2; ++ks) aoff[ks] = (wm * 64 + l15) * 128 + (((4 * ks + g) ^ (l15 >> 1)) * 16);
#pragma unroll
    for (int u = 0; u < 2; ++u) boff[u] = A_BYTES + (8 * g + q) * 128 + (((2 * wn + u) ^ fB) * 32) + p * 8;
    if constexpr (HALF) hoff = A_BYTES + NFULL * 8192 + (8 * g + q) * 64 + ((wn ^ (g & 1)) * 32) + p * 8;

    auto ldf = [&](int i, const char* s, int ks, u32x4(&af)[4], u32x4(&bfr)[NTW]) {
      if (i == 0 || i > NTW) {
        const int mt = i == 0 ? 0 : i - NTW;
        af[mt] = *reinterpret_cast<const u32x4*>(s + aoff[ks] + mt * 2048);
      } else if (i - 1 < 2 * NFULL) {
        const int b = (i - 1) >> 1, u = (i - 1) & 1;
        s16x4 lo = __builtin_amdgcn_ds_read_tr16_b64_v4i16((lds_s16x4_p)(s + boff[u] + b * 8192 + ks * 4096));
        s16x4 hi = __builtin_amdgcn_ds_read_tr16_b64_v4i16((lds_s16x4_p)(s + boff[u] + b * 8192 + ks * 4096 + 512));
        bfr[i - 1] = __builtin_shufflevector(__builtin_bit_cast(u32x2, lo), __builtin_bit_cast(u32x2, hi), 0, 1, 2, 3);
      } else {
        s16x4 lo = __builtin_amdgcn_ds_read_tr16_b64_v4i16((lds_s16x4_p)(s + hoff + ks * 2048));
        s16x4 hi = __builtin_amdgcn_ds_read_tr16_b64_v4i16((lds_s16x4_p)(s + hoff + ks * 2048 + 256));
        bfr[NTW - 1] = __builtin_shufflevector(__builtin_bit_cast(u32x2, lo), __builtin_bit_cast(u32x2, hi), 0, 1, 2, 3);
      }
    };
    constexpr int NM = 4 * NTW, NL = NTW + 4;
    auto phase = [&](u32x4(&afc)[4], u32x4(&bfc)[NTW], u32x4(&afn)[4], u32x4(&bfn)[NTW], int nbuf, int nks, auto rd, auto bar) {
      const char* s = smem + nbuf * STAGE;
      constexpr int BAR = decltype(bar)::value, R0 = BAR < 0 ? 0 : BAR + 1;
      constexpr int RSP = (NM - R0 - 2) / NL > 0 ? (NM - R0 - 2) / NL : 1;
      static_assert(R0 + (NL - 1) * RSP < NM, "reads");
#pragma unroll
      for (int mt = 0; mt < 4; ++mt)
#pragma unroll
        for (int j = 0; j < NTW; ++j) {
          const int m = mt * NTW + j;
          if (m == BAR) {
            WAIT_L0();
            __builtin_amdgcn_s_barrier();
            asm volatile("" ::: "memory");
          }
          if (decltype(rd)::value && m >= R0 && (m - R0) % RSP == 0 && (m - R0) / RSP < NL) ldf((m - R0) / RSP, s, nks, afn, bfn);
          if (EPI == EPI_QKV && j >= 4) mfma_acc(acc[mt][j], afc[mt], bfc[j]);
          else mfma_acc(acc[mt][j], bfc[j], afc[mt]);
          __builtin_amdgcn_sched_barrier(0);
        }
    };
    typedef std::integral_constant<int, -1> nobar;
#pragma unroll
    for (int mt = 0; mt < 4; ++mt)
#pragma unroll
      for (int j = 0; j < NTW; ++j) {
        acc[mt][j] = (f32x4){0.f, 0.f, 0.f, 0.f};
        asm volatile("" : "+v"(acc[mt][j]));
      }
    u32x4 af0[4], af1[4], bf0[NTW], bf1[NTW];
    __builtin_amdgcn_s_barrier();
    asm volatile("" ::: "memory");
    STAMP(1);
#pragma unroll
    for (int i = 0; i < NL; ++i) { ldf(i, smem, 0, af0, bf0); __builtin_amdgcn_sched_barrier(0); }
    int buf = 0;
    if constexpr (LAG) {
      phase(af0, bf0, af1, bf1, 0, 1, std::true_type{}, nobar{});
      for (int kt = 0; kt < NT - 1; ++kt) {
        buf = buf + 1 == NST ? 0 : buf + 1;
        phase(af1, bf1, af0, bf0, buf, 0, std::true_type{}, nobar{});
        asm volatile("" ::: "memory");
        __builtin_amdgcn_s_barrier();
        asm volatile("" ::: "memory");
        phase(af0, bf0, af1, bf1, buf, 1, std::true_type{}, nobar{});
      }
      phase(af1, bf1, af0, bf0, buf, 0, std::false_type{}, nobar{});
    } else {
      for (int kt = 0; kt < NT - 1; ++kt) {
        phase(af0, bf0, af1, bf1, buf, 1, std::true_type{}, nobar{});
        buf = buf + 1 == NST ? 0 : buf + 1;
        phase(af1, bf1, af0, bf0, buf, 0, std::true_type{}, std::integral_constant<int, 4>{});
      }
      phase(af0, bf0, af1, bf1, buf, 1, std::true_type{}, nobar{});
      phase(af1, bf1, af0, bf0, buf, 0, std::false_type{}, nobar{});
    }
    asm volatile("s_nop 7\n\ts_nop 7\n\ts_nop 7" ::: "memory");
    asm volatile("" ::"v"(ctv));
    if constexpr (EPI == EPI_QKV) {
      if (r0 != nullptr) {
        const unsigned ln = (blockIdx.x * 4 + wid) * 64 + lane;
        const char* p01 = reinterpret_cast<const char*>(ln < 32768 ? r0 : r1) + (size_t)(ln & 32767) * 64;
        const char* p2 = reinterpret_cast<const char*>(st_out) + (size_t)ln * 64;
        pft[0] = *reinterpret_cast<const unsigned*>(p01);
        pft[1] = *reinterpret_cast<const unsigned*>(p2);
        if (ln + 65536 < F_ * 1024 * 2 / 64) pft[2] = *reinterpret_cast<const unsigned*>(p2 + 65536 * 64);
      }
    }
#pragma unroll
    for (int mt = 0; mt < 4; ++mt)
#pragma unroll
      for (int j = 0; j < NTW; ++j) asm volatile("" : "+v"(acc[mt][j]));
    if constexpr (EPI == EPI_QKV) {
#pragma unroll
      for (int mt = 0; mt < 4; ++mt)
#pragma unroll
        for (int t = 0; t < 2; ++t) *reinterpret_cast<f32x4*>(smem + QX_OFF + wid * 8192 + (mt * 2 + t) * 1024 + lane * 16) = acc[mt][t];
    }
  }
  WAIT_L0();
  __builtin_amdgcn_s_barrier();
  asm volatile("" ::: "memory");
  STAMP(2);

  if constexpr (EPI == EPI_QKV) {
    const int h = tn * 2 + wn;
    const int q = l15 >> 2, p = lane & 3;
    f32x4 kv[4][2], zz[4];
    float zsel = 0.f;
    float* red = reinterpret_cast<float*>(smem + 4 * 64 * 144) + wn * (64 * 32 + 64);
    bf16x8 wf[2];
#pragma unroll
    for (int ft = 0; ft < 2; ++ft) {
      const char* src = smem + WP_OFF + wn * 2048 + (4 * g + q) * 64 + ft * 32 + p * 8;
      s16x4 lo = __builtin_amdgcn_ds_read_tr16_b64_v4i16((lds_s16x4_p)(src));
      s16x4 hi = __builtin_amdgcn_ds_read_tr16_b64_v4i16((lds_s16x4_p)(src + 16 * 64));
      wf[ft] = __builtin_shufflevector(lo, hi, 0, 1, 2, 3, 4, 5, 6, 7);
    }
    const float* csl = reinterpret_cast<const float*>(smem + CS_OFF);
    auto feat = [&](const bf16x8& xf, u32x2 (&o)[4]) {
      const float L2E = 1.4426950408889634f;
      f32x4 z4 = (f32x4){0.f, 0.f, 0.f, 0.f};
      f32x4 p0 = __builtin_amdgcn_mfma_f32_16x16x32_bf16(wf[0], xf, z4, 0, 0, 0);
      f32x4 p1 = __builtin_amdgcn_mfma_f32_16x16x32_bf16(wf[1], xf, z4, 0, 0, 0);
      f32x2 v[4];
      {
        const unsigned r0_ = pk2(p0[0], p0[1]), r1_ = pk2(p0[2], p0[3]), r2_ = pk2(p1[0], p1[1]), r3_ = pk2(p1[2], p1[3]);
        v[0] = (f32x2){bflo(r0_), bfhi(r0_)}; v[1] = (f32x2){bflo(r1_), bfhi(r1_)};
        v[2] = (f32x2){bflo(r2_), bfhi(r2_)}; v[3] = (f32x2){bflo(r3_), bfhi(r3_)};
      }
      float mx = max2_(max3_(max3_(v[0][0], v[0][1], v[1][0]), v[1][1], v[2][0]), max3_(v[2][1], v[3][0], v[3][1]));
      float mn = min2_(min3_(min3_(v[0][0], v[0][1], v[1][0]), v[1][1], v[2][0]), min3_(v[2][1], v[3][0], v[3][1]));
      mx = rmax4(mx); mn = rmin4(mn);
      const float mxl = -mx * L2E, mnl = mn * L2E;
      f32x2 ep[4], en[4];
#pragma unroll
      for (int i = 0; i < 4; ++i) {
        const f32x2 ap = v[i] * L2E + mxl, an = v[i] * (-L2E) + mnl;
        ep[i] = (f32x2){__builtin_amdgcn_exp2f(ap[0]), __builtin_amdgcn_exp2f(ap[1])};
        en[i] = (f32x2){__builtin_amdgcn_exp2f(an[0]), __builtin_amdgcn_exp2f(an[1])};
      }
      const f32x2 sp2 = (ep[0] + ep[1]) + (ep[2] + ep[3]), sn2 = (en[0] + en[1]) + (en[2] + en[3]);
      const float ip = __builtin_amdgcn_rcpf(rsum4(sp2[0] + sp2[1])), in_ = __builtin_amdgcn_rcpf(rsum4(sn2[0] + sn2[1]));
      o[0] = (u32x2){pk2v(ep[0] * ip), pk2v(ep[1] * ip)};
      o[1] = (u32x2){pk2v(ep[2] * ip), pk2v(ep[3] * ip)};
      o[2] = (u32x2){pk2v(en[0] * in_), pk2v(en[1] * in_)};
      o[3] = (u32x2){pk2v(en[2] * in_), pk2v(en[3] * in_)};
    };
    if (wid8 >= 4) {
      f32x4 cs4[2];
#pragma unroll
      for (int t = 0; t < 2; ++t) cs4[t] = *reinterpret_cast<const f32x4*>(csl + wn * 32 + t * 16 + 4 * g);
      bf16x8 qf[4];
#pragma unroll
      for (int mt = 0; mt < 4; ++mt) {
        f32x4 st = rs[wm * 64 + mt * 16 + l15];
        unsigned pq_[4];
#pragma unroll
        for (int t = 0; t < 2; ++t) {
          const f32x4 ac = *reinterpret_cast<const f32x4*>(smem + QX_OFF + wid * 8192 + (mt * 2 + t) * 1024 + lane * 16);
          float a[4];
#pragma unroll
          for (int i = 0; i < 4; ++i) a[i] = st[1] * (ac[i] - st[0] * cs4[t][i]);
          pq_[t * 2] = pk2(a[0], a[1]); pq_[t * 2 + 1] = pk2(a[2], a[3]);
        }
        qf[mt] = __builtin_bit_cast(bf16x8, (u32x4){pq_[0], pq_[1], pq_[2], pq_[3]});
      }
#pragma unroll
      for (int mt = 0; mt < 4; ++mt) {
        u32x2 o[4];
        feat(qf[mt], o);
        u32x4* dst = reinterpret_cast<u32x4*>(out) + ((size_t)(tm * 8 + wm * 4 + mt) * 32 + h) * 128 + lane;
        wt_store((u32x4){o[0][0], o[0][1], o[1][0], o[1][1]}, dst);
        wt_store((u32x4){o[2][0], o[2][1], o[3][0], o[3][1]}, dst + 64);
      }
    } else {
      f32x4 cs4[2];
      float csv[2];
#pragma unroll
      for (int t = 0; t < 2; ++t) {
        cs4[t] = *reinterpret_cast<const f32x4*>(csl + 64 + wn * 32 + t * 16 + 4 * g);
        csv[t] = csl[128 + wn * 32 + t * 16 + l15];
      }
      bf16x8 kf[4], vf[2][2];
#pragma unroll
      for (int mt = 0; mt < 4; ++mt) {
        f32x4 st = rs[wm * 64 + mt * 16 + l15];
        unsigned pk_[4];
#pragma unroll
        for (int t = 0; t < 2; ++t) {
          float b[4];
#pragma unroll
          for (int i = 0; i < 4; ++i) b[i] = st[1] * (acc[mt][2 + t][i] - st[0] * cs4[t][i]);
          pk_[t * 2] = pk2(b[0], b[1]); pk_[t * 2 + 1] = pk2(b[2], b[3]);
        }
        kf[mt] = __builtin_bit_cast(bf16x8, (u32x4){pk_[0], pk_[1], pk_[2], pk_[3]});
      }
      char* pkl = smem + wid * (64 * 144);
#pragma unroll
      for (int mt = 0; mt < 4; ++mt) {
        u32x2 o[4];
        feat(kf[mt], o);
        char* dst = pkl + (mt * 16 + l15) * 144 + g * 8;
#pragma unroll
        for (int c = 0; c < 4; ++c) *reinterpret_cast<u32x2*>(dst + c * 32) = o[c];
      }
#pragma unroll
      for (int s = 0; s < 2; ++s) {
        unsigned pv[2][4];
#pragma unroll
        for (int m2 = 0; m2 < 2; ++m2) {
          int mt = s * 2 + m2;
          f32x4 s0 = rs[wm * 64 + mt * 16 + 4 * g + 0], s1 = rs[wm * 64 + mt * 16 + 4 * g + 1];
          f32x4 s2 = rs[wm * 64 + mt * 16 + 4 * g + 2], s3 = rs[wm * 64 + mt * 16 + 4 * g + 3];
#pragma unroll
          for (int t = 0; t < 2; ++t) {
            float a0 = s0[1] * (acc[mt][4 + t][0] - s0[0] * csv[t]);
            float a1 = s1[1] * (acc[mt][4 + t][1] - s1[0] * csv[t]);
            float a2 = s2[1] * (acc[mt][4 + t][2] - s2[0] * csv[t]);
            float a3 = s3[1] * (acc[mt][4 + t][3] - s3[0] * csv[t]);
            pv[t][m2 * 2] = pk2(a0, a1); pv[t][m2 * 2 + 1] = pk2(a2, a3);
          }
        }
#pragma unroll
        for (int t = 0; t < 2; ++t) vf[s][t] = __builtin_bit_cast(bf16x8, (u32x4){pv[t][0], pv[t][1], pv[t][2], pv[t][3]});
      }
      WAIT_L0();
      __builtin_amdgcn_sched_barrier(0);
#pragma unroll
      for (int ft = 0; ft < 4; ++ft) { kv[ft][0] = kv[ft][1] = zz[ft] = (f32x4){0.f, 0.f, 0.f, 0.f}; }
      const bf16x8 ones = (bf16x8){0x3F80, 0x3F80, 0x3F80, 0x3F80, 0x3F80, 0x3F80, 0x3F80, 0x3F80};
#pragma unroll
      for (int s = 0; s < 2; ++s)
#pragma unroll
        for (int ft = 0; ft < 4; ++ft) {
          const char* src = pkl + (32 * s + 4 * g + q) * 144 + ft * 32 + p * 8;
          s16x4 lo = __builtin_amdgcn_ds_read_tr16_b64_v4i16((lds_s16x4_p)(src));
          s16x4 hi = __builtin_amdgcn_ds_read_tr16_b64_v4i16((lds_s16x4_p)(src + 16 * 144));
          bf16x8 pf = __builtin_shufflevector(lo, hi, 0, 1, 2, 3, 4, 5, 6, 7);
          kv[ft][0] = __builtin_amdgcn_mfma_f32_16x16x32_bf16(vf[s][0], pf, kv[ft][0], 0, 0, 0);
          kv[ft][1] = __builtin_amdgcn_mfma_f32_16x16x32_bf16(vf[s][1], pf, kv[ft][1], 0, 0, 0);
          zz[ft] = __builtin_amdgcn_mfma_f32_16x16x32_bf16(ones, pf, zz[ft], 0, 0, 0);
        }
      zsel = g == 0 ? zz[0][0] : g == 1 ? zz[1][0] : g == 2 ? zz[2][0] : zz[3][0];
      if (wm == 1) {
#pragma unroll
        for (int ft = 0; ft < 4; ++ft)
#pragma unroll
          for (int dt = 0; dt < 2; ++dt) *reinterpret_cast<f32x4*>(red + ((ft * 2 + dt) * 64 + lane) * 4) = kv[ft][dt];
        red[64 * 32 + lane] = zsel;
      }
    }
    __syncthreads();
    if (wid8 < 2) {
      float* kd = kvp + (size_t)(h * 16 + tm) * 2048;
      float* zd = zp + (size_t)(h * 16 + tm) * 64;
#pragma unroll
      for (int ft = 0; ft < 4; ++ft)
#pragma unroll
        for (int dt = 0; dt < 2; ++dt) {
          f32x4 o = *reinterpret_cast<const f32x4*>(red + ((ft * 2 + dt) * 64 + lane) * 4);
          wt_store(o + kv[ft][dt], reinterpret_cast<f32x4*>(kd + ((ft * 2 + dt) * 64 + lane) * 4));
        }
      zd[lane] = red[64 * 32 + lane] + zsel;
    }
    asm volatile("" ::"v"(pft[0]), "v"(pft[1]), "v"(pft[2]));
  } else {
    if (wid8 < 4) {
      f32x4 stm[4], cs4[NTW];
      if constexpr (EPI == EPI_W1 || EPI == EPI_W3) {
#pragma unroll
        for (int j = 0; j < NTW; ++j) {
          const int colj = (j < 2 * NFULL) ? ((j >> 1) * 64 + (2 * wn + (j & 1)) * 16) : (NFULL * 64 + wn * 16);
          cs4[j] = *reinterpret_cast<const f32x4*>(smem + CS_OFF + (colj + 4 * g) * 4);
        }
#pragma unroll
        for (int mt = 0; mt < 4; ++mt) stm[mt] = rs[wm * 64 + mt * 16 + l15];
      }
#pragma unroll
      for (int j = 0; j < NTW; ++j) {
        const int colj = (j < 2 * NFULL) ? ((j >> 1) * 64 + (2 * wn + (j & 1)) * 16) : (NFULL * 64 + wn * 16);
#pragma unroll
        for (int mt = 0; mt < 4; ++mt) {
          f32x2 v0 = (f32x2){acc[mt][j][0], acc[mt][j][1]}, v1 = (f32x2){acc[mt][j][2], acc[mt][j][3]};
          if constexpr (EPI == EPI_W1 || EPI == EPI_W3) {
            v0 = gelu2((v0 - stm[mt][0] * (f32x2){cs4[j][0], cs4[j][1]}) * stm[mt][1]);
            v1 = gelu2((v1 - stm[mt][0] * (f32x2){cs4[j][2], cs4[j][3]}) * stm[mt][1]);
          }
          *reinterpret_cast<u32x2*>(smem + (wm * 64 + mt * 16 + l15) * PB + (colj + 4 * g) * 2) = (u32x2){pk2(v0[0], v0[1]), pk2(v1[0], v1[1])};
        }
      }
    }
    __syncthreads();
#pragma unroll
    for (int it = 0; it < NIT; ++it) {
      const int c = tid + 512 * it, r = c / CH, ch = c % CH;
      u32x4 v = *reinterpret_cast<const u32x4*>(smem + r * PB + ch * 16);
      const int grow = row0 + r, gcol = tn * BN + ch * 8;
      if constexpr (EPI == EPI_W3) {
        wt_store(v, reinterpret_cast<u32x4*>(out + (size_t)grow * F_ + gcol));
      } else if constexpr (EPI == EPI_W1) {
        const int grp = tn >> 2;
        const int sh = grp == 0 ? 1 : grp == 1 ? -1 : grp == 2 ? 2 : -2;
        const int dr = grow + sh;
        if (dr >= 0 && dr < S_) wt_store(v, reinterpret_cast<u32x4*>(out + (size_t)dr * D_ + gcol));
        const int zr = grow - sh;
        if (zr < 0 || zr >= S_) *reinterpret_cast<u32x4*>(out + (size_t)grow * D_ + gcol) = (u32x4){0u, 0u, 0u, 0u};
      } else {
        float y[8];
        const u32x4 a = ra[it];
        if constexpr (EPI == EPI_WO) {
#pragma unroll
          for (int i = 0; i < 4; ++i) { y[2 * i] = rbf(bflo(a[i]) + bflo(v[i])); y[2 * i + 1] = rbf(bfhi(a[i]) + bfhi(v[i])); }
        } else if constexpr (EPI == EPI_W2) {
          f32x4 st = rs[r];
#pragma unroll
          for (int i = 0; i < 4; ++i) {
            y[2 * i] = rbf(rbf((bflo(a[i]) - st[0]) * st[2]) + bflo(v[i]));
            y[2 * i + 1] = rbf(rbf((bfhi(a[i]) - st[0]) * st[2]) + bfhi(v[i]));
          }
        } else {
          const u32x4 b = rb[it];
#pragma unroll
          for (int i = 0; i < 4; ++i) {
            y[2 * i] = rbf(bflo(a[i]) + rbf(bflo(b[i]) + bflo(v[i])));
            y[2 * i + 1] = rbf(bfhi(a[i]) + rbf(bfhi(b[i]) + bfhi(v[i])));
          }
        }
        wt_store((u32x4){pk2(y[0], y[1]), pk2(y[2], y[3]), pk2(y[4], y[5]), pk2(y[6], y[7])}, reinterpret_cast<u32x4*>(out + (size_t)grow * D_ + gcol));
        float sm = ((y[0] + y[1]) + (y[2] + y[3])) + ((y[4] + y[5]) + (y[6] + y[7]));
        sm = rsum8(sm);
        float mu = sm * (1.0f / 64.f), m2 = 0.f;
#pragma unroll
        for (int i = 0; i < 8; ++i) { float d = y[i] - mu; m2 += d * d; }
        m2 = rsum8(m2);
        if (ch == 0) st_out[(size_t)grow * 16 + tn] = (f32x2){sm, m2};
      }
    }
  }
  STAMP(3);
}

constexpr int WC_QKV = 0, WC_WO = WC_QKV + 2 * 1024 * 384, WC_W1 = WC_WO + 2 * 1024 * 128, WC_W2 = WC_W1 + 2 * 1024 * 128,
              WC_W4 = WC_W2 + 2 * 1024 * 128, WC_END = WC_W4 + 2 * F_ * 128;
__global__ __launch_bounds__(256, 1) void attn_out_kernel(const u16* __restrict__ phiq, const float* __restrict__ kvp,
                                                          const float* __restrict__ zp, u16* __restrict__ o,
                                                          const float* __restrict__ cp1, const float* __restrict__ cp3,
                                                          float* __restrict__ cs1, float* __restrict__ cs3,
                                                          const u16* __restrict__ wo, const u16* __restrict__ w2,
                                                          const u16* __restrict__ w4, char* __restrict__ wc) {
  __shared__ __attribute__((aligned(16))) u16 kvT[48 * 72];
  const int tid = threadIdx.x, lane = tid & 63, wid = tid >> 6, g = lane >> 4, l15 = lane & 15;
  if (blockIdx.x >= 256 + 18) {
    const int e = blockIdx.x - (256 + 18);
    const u16* w = e < 8 ? wo : e < 16 ? w2 : w4;
    const int kd = e < 16 ? 1024 : F_, row = (e < 8 ? e : e < 16 ? e - 8 : e - 16) * 128 + (tid >> 4), t = (tid >> 3) & 1, c = tid & 7;
    char* dst = wc + (e < 8 ? WC_WO : e < 16 ? WC_W2 : WC_W4) + (size_t)t * kd * 128 + (size_t)row * 128 + c * 16;
    u32x4 v[8];
#pragma unroll
    for (int p = 0; p < 8; ++p) v[p] = *reinterpret_cast<const u32x4*>(w + (size_t)(row + p * 16) * 1024 + (3 + 8 * t) * 64 + c * 8);
#pragma unroll
    for (int p = 0; p < 8; ++p) *reinterpret_cast<u32x4*>(dst + p * 2048) = v[p];
    return;
  }
  if (blockIdx.x >= 256) {
    const int i = (blockIdx.x - 256) * 256 + tid;
    const float* src = i < 1024 ? cp1 + i : cp3 + (i - 1024);
    const int ld = i < 1024 ? 1024 : F_;
    float c[16];
#pragma unroll
    for (int t = 0; t < 16; ++t) c[t] = src[t * ld];
#pragma unroll
    for (int t = 8; t > 0; t >>= 1)
#pragma unroll
      for (int j = 0; j < t; ++j) c[j] += c[j + t];
    *(i < 1024 ? cs1 + i : cs3 + (i - 1024)) = c[0];
    return;
  }
  const int h = blockIdx.x & 31, tg = blockIdx.x >> 5;
  f32x4 kvv[16][2];
  float zv[16];
  bf16x8 pq[4][2];
  {
    const float* src = kvp + (size_t)h * 16 * 2048 + tid * 8;
#pragma unroll
    for (int t = 0; t < 16; ++t) {
      kvv[t][0] = *reinterpret_cast<const f32x4*>(src + t * 2048);
      kvv[t][1] = *reinterpret_cast<const f32x4*>(src + t * 2048 + 4);
    }
#pragma unroll
    for (int t = 0; t < 16; ++t) zv[t] = zp[(size_t)(h * 16 + t) * 64 + lane];
#pragma unroll
    for (int mt = 0; mt < 4; ++mt) {
      const u16* ps = phiq + ((size_t)(tg * 16 + wid * 4 + mt) * 32 + h) * 1024 + lane * 8;
      pq[mt][0] = *reinterpret_cast<const bf16x8*>(ps);
      pq[mt][1] = *reinterpret_cast<const bf16x8*>(ps + 512);
    }
  }
  __builtin_amdgcn_sched_barrier(0);
  {
    for (int i = tid; i < 15 * 64; i += 256) kvT[(33 + i / 64) * 72 + (i & 63)] = 0;
    f32x4 a0 = kvv[0][0], a1 = kvv[0][1];
#pragma unroll
    for (int t = 1; t < 16; ++t) { a0 += kvv[t][0]; a1 += kvv[t][1]; }
    const int ft = tid >> 6, dt = (tid >> 5) & 1, ln = (2 * tid) & 63;
    const int d0 = dt * 16 + 4 * (ln >> 4), pos = (ft >> 1) * 32 + ((ln >> 2) & 3) * 8 + (ft & 1) * 4 + (ln & 3);
#pragma unroll
    for (int i = 0; i < 4; ++i) *reinterpret_cast<unsigned*>(&kvT[(d0 + i) * 72 + pos]) = pk2(a0[i], a1[i]);
    if (tid < 64) {
      float z = 0.f;
#pragma unroll
      for (int t = 0; t < 16; ++t) z += zv[t];
      kvT[32 * 72 + (tid >> 5) * 32 + ((tid >> 2) & 3) * 8 + ((tid >> 4) & 1) * 4 + (tid & 3)] = (u16)(pk2(z, 0.f) & 0xffff);
    }
  }
  __syncthreads();
  bf16x8 kf[3][2];
#pragma unroll
  for (int dt = 0; dt < 3; ++dt)
#pragma unroll
    for (int ks = 0; ks < 2; ++ks) kf[dt][ks] = *reinterpret_cast<const bf16x8*>(&kvT[(dt * 16 + l15) * 72 + ks * 32 + g * 8]);
#pragma unroll
  for (int mt = 0; mt < 4; ++mt) {
    const int tok = tg * 256 + wid * 64 + mt * 16 + l15;
    const bf16x8 p0 = pq[mt][0], p1 = pq[mt][1];
    f32x4 r[3];
#pragma unroll
    for (int dt = 0; dt < 3; ++dt) {
      r[dt] = (f32x4){0.f, 0.f, 0.f, 0.f};
      r[dt] = __builtin_amdgcn_mfma_f32_16x16x32_bf16(kf[dt][0], p0, r[dt], 0, 0, 0);
      r[dt] = __builtin_amdgcn_mfma_f32_16x16x32_bf16(kf[dt][1], p1, r[dt], 0, 0, 0);
    }
    float den = rbf(rbf(__shfl(r[2][0], l15)) + 1e-6f);
    float inv = 1.0f / den;
    const u32x4 w = (u32x4){pk2(rbf(r[0][0]) * inv, rbf(r[0][1]) * inv), pk2(rbf(r[0][2]) * inv, rbf(r[0][3]) * inv),
                            pk2(rbf(r[1][0]) * inv, rbf(r[1][1]) * inv), pk2(rbf(r[1][2]) * inv, rbf(r[1][3]) * inv)};
    wt_store(w, reinterpret_cast<u32x4*>(o + ((size_t)h * S_ + tok) * 32 + 8 * g));
  }
}

__global__ __launch_bounds__(256) void prep_kernel(const u16* __restrict__ x, const u16* __restrict__ wqkv,
                                                   const u16* __restrict__ w1, const u16* __restrict__ w3,
                                                   float* __restrict__ cs_qkv, float* __restrict__ cs_w1,
                                                   float* __restrict__ cs_w3, f32x2* __restrict__ st,
                                                   const u16* __restrict__ wo, const u16* __restrict__ w2,
                                                   const u16* __restrict__ w4, char* __restrict__ wc) {
  __shared__ __attribute__((aligned(16))) float red[4][512];
  const int tid = threadIdx.x, b = blockIdx.x;
  if (b >= 368) {
    unsigned long long c0, c1;
    asm volatile("s_getpc_b64 s[90:91]\n\ts_add_u32 s90, s90, _Z15attn_out_kernelPKtPKfS2_PtS2_S2_PfS4_S0_S0_S0_Pc@rel32@lo+4\n\t"
                 "s_addc_u32 s91, s91, _Z15attn_out_kernelPKtPKfS2_PtS2_S2_PfS4_S0_S0_S0_Pc@rel32@hi+12\n\ts_mov_b64 %0, s[90:91]"
                 : "=s"(c0) :: "s90", "s91", "scc");
    asm volatile("s_getpc_b64 s[90:91]\n\ts_add_u32 s90, s90, _Z11gemm_kernelILi64ELi5ELi3584ELi3584ELi1024ELi5EEvPKtS1_S1_PKfPKDv2_fS1_S1_PtPS4_S1_PfS9_@rel32@lo+4\n\t"
                 "s_addc_u32 s91, s91, _Z11gemm_kernelILi64ELi5ELi3584ELi3584ELi1024ELi5EEvPKtS1_S1_PKfPKDv2_fS1_S1_PtPS4_S1_PfS9_@rel32@hi+12\n\ts_mov_b64 %0, s[90:91]"
                 : "=s"(c1) :: "s90", "s91", "scc");
    c1 += 5632;
    if (c1 > c0 && c1 - c0 < 6 * 256 * 64) {
      unsigned v[6];
#pragma unroll
      for (int r = 0; r < 6; ++r) {
        const unsigned long long a = c0 + (unsigned long long)(r * 256 + tid) * 64;
        v[r] = a < c1 ? *reinterpret_cast<const unsigned*>(a) : 0u;
      }
#pragma unroll
      for (int r = 0; r < 6; ++r) asm volatile("" ::"v"(v[r]));
    }
  } else if (b < 240) {
    const u16* w; float* dst; int ld, ch, rg;
    if (b < 96) { w = wqkv; dst = cs_qkv; ld = 3072; ch = b % 6; rg = b / 6; }
    else if (b < 128) { w = w1; dst = cs_w1; ld = 1024; ch = (b - 96) & 1; rg = (b - 96) >> 1; }
    else { w = w3; dst = cs_w3; ld = F_; ch = (b - 128) % 7; rg = (b - 128) / 7; }
    const int lane = tid & 63, wid = tid >> 6, row = rg * 64 + wid * 16;
    u32x4 v[16];
#pragma unroll
    for (int r = 0; r < 16; ++r) v[r] = *reinterpret_cast<const u32x4*>(w + (size_t)(row + r) * ld + ch * 512 + lane * 8);
    if (b < 128 && (lane >> 3) == 3) {
      const int cpp = b < 96 ? 384 : 128;
      char* cpd = wc + (b < 96 ? WC_QKV + (ch & 1) * (1024 * 384) + (ch >> 1) * 128 : WC_W1 + ch * (1024 * 128)) + row * cpp + (lane & 7) * 16;
#pragma unroll
      for (int r = 0; r < 16; ++r) *reinterpret_cast<u32x4*>(cpd + r * cpp) = v[r];
    }
    f32x4 s0 = (f32x4){0.f, 0.f, 0.f, 0.f}, s1 = s0;
#pragma unroll
    for (int r = 0; r < 16; ++r) {
      s0 += (f32x4){bflo(v[r][0]), bfhi(v[r][0]), bflo(v[r][1]), bfhi(v[r][1])};
      s1 += (f32x4){bflo(v[r][2]), bfhi(v[r][2]), bflo(v[r][3]), bfhi(v[r][3])};
    }
    *reinterpret_cast<f32x4*>(&red[wid][lane * 8]) = s0;
    *reinterpret_cast<f32x4*>(&red[wid][lane * 8 + 4]) = s1;
    __syncthreads();
    f32x2 t = *reinterpret_cast<const f32x2*>(&red[0][tid * 2]);
#pragma unroll
    for (int i = 1; i < 4; ++i) t += *reinterpret_cast<const f32x2*>(&red[i][tid * 2]);
    *reinterpret_cast<f32x2*>(dst + rg * ld + ch * 512 + tid * 2) = t;
  } else {
    const int lane = tid & 63, wid = tid >> 6;
    const int rbase = (b - 240) * 16 + wid * 4;
    u32x4 xv[8];
#pragma unroll
    for (int i = 0; i < 8; ++i) xv[i] = *reinterpret_cast<const u32x4*>(x + (size_t)rbase * D_ + i * 512 + lane * 8);
    __builtin_amdgcn_sched_barrier(0);
#pragma unroll
    for (int rr = 0; rr < 4; ++rr) {
      const int row = rbase + rr;
#pragma unroll
      for (int hf = 0; hf < 2; ++hf) {
        const u32x4 v = xv[rr * 2 + hf];
        float y[8];
#pragma unroll
        for (int i = 0; i < 4; ++i) { y[2 * i] = bflo(v[i]); y[2 * i + 1] = bfhi(v[i]); }
        float sm = ((y[0] + y[1]) + (y[2] + y[3])) + ((y[4] + y[5]) + (y[6] + y[7]));
        sm = rsum8(sm);
        float mu = sm * (1.0f / 64.f), m2 = 0.f;
#pragma unroll
        for (int i = 0; i < 8; ++i) { float d = y[i] - mu; m2 += d * d; }
        m2 = rsum8(m2);
        if ((lane & 7) == 0) st[(size_t)row * 16 + hf * 8 + (lane >> 3)] = (f32x2){sm, m2};
      }
    }
  }
}

extern "C" void kernel_launch(void* const* d_in, const int* in_sizes, int n_in, void* d_out, int out_size, void* d_ws,
                              size_t ws_size, hipStream_t stream) {
  const u16* x_in = (const u16*)d_in[0];
  const u16* wqkv = (const u16*)d_in[1];
  const u16* wphi = (const u16*)d_in[2];
  const u16* wo = (const u16*)d_in[3];
  const u16* w1 = (const u16*)d_in[5];
  const u16* w2 = (const u16*)d_in[6];
  const u16* w3 = (const u16*)d_in[7];
  const u16* w4 = (const u16*)d_in[8];
  char* ws = (char*)d_ws;
  size_t off = 0;
  auto alloc = [&](size_t bytes) { char* p = ws + off; off += (bytes + 4095) & ~(size_t)4095; return p; };
  u16* xb = (u16*)alloc((size_t)S_ * D_ * 2);
  u16* x2 = (u16*)alloc((size_t)S_ * D_ * 2);
  u16* x1 = (u16*)alloc((size_t)S_ * D_ * 2);
  u16* hs = (u16*)alloc((size_t)S_ * D_ * 2);
  u16* ob = (u16*)alloc((size_t)S_ * D_ * 2);
  u16* gb = (u16*)alloc((size_t)S_ * F_ * 2);
  u16* phiq = (u16*)alloc((size_t)S_ * 2048 * 2);
  float* kvp = (float*)alloc((size_t)32 * 16 * 2048 * 4);
  float* zp = (float*)alloc((size_t)32 * 16 * 64 * 4);
  f32x2* st_x = (f32x2*)alloc((size_t)S_ * 16 * 8);
  f32x2* st_x2 = (f32x2*)alloc((size_t)S_ * 16 * 8);
  f32x2* st_x1 = (f32x2*)alloc((size_t)S_ * 16 * 8);
  float* cs_qkv = (float*)alloc(16 * 3072 * 4);
  float* cs_w1 = (float*)alloc(16 * 1024 * 4);
  float* cs_w3 = (float*)alloc(16 * F_ * 4);
  float* cf_w1 = (float*)alloc(1024 * 4);
  float* cf_w3 = (float*)alloc(F_ * 4);
  char* wc = alloc(WC_END);

  prep_kernel<<<368 + 16, 256, 0, stream>>>(x_in, wqkv, w1, w3, cs_qkv, cs_w1, cs_w3, st_x, wo, w2, w4, wc);
  for (int layer = 0; layer < 2; ++layer) {
    const u16* x = layer == 0 ? x_in : xb;
    u16* xo = layer == 0 ? xb : (u16*)d_out;
    gemm_kernel<192, 3, 1024, 1024, 3072, EPI_QKV><<<256, 512, 0, stream>>>(x, wqkv, (const u16*)(wc + WC_QKV), cs_qkv, st_x, layer == 0 ? wo : nullptr, layer == 0 ? w2 : nullptr, phiq, layer == 0 ? (f32x2*)w4 : nullptr, wphi, kvp, zp);
    attn_out_kernel<<<layer == 0 ? 256 + 18 + 44 : 256, 256, 0, stream>>>(phiq, kvp, zp, ob, cs_w1, cs_w3, cf_w1, cf_w3, wo, w2, w4, wc);
    gemm_kernel<64, 5, 1024, 1024, 1024, EPI_WO><<<256, 512, 0, stream>>>(ob, wo, (const u16*)(wc + WC_WO), nullptr, nullptr, x, nullptr, x2, st_x2, nullptr, nullptr, nullptr);
    gemm_kernel<64, 5, 1024, 1024, 1024, EPI_W1><<<256, 512, 0, stream>>>(x2, w1, (const u16*)(wc + WC_W1), cf_w1, st_x2, nullptr, nullptr, hs, nullptr, nullptr, nullptr, nullptr);
    gemm_kernel<64, 5, 1024, 1024, 1024, EPI_W2><<<256, 512, 0, stream>>>(hs, w2, (const u16*)(wc + WC_W2), nullptr, st_x2, x2, nullptr, x1, st_x1, nullptr, nullptr, nullptr);
    gemm_kernel<224, 3, 1024, 1024, F_, EPI_W3><<<256, 512, 0, stream>>>(x1, w3, nullptr, cf_w3, st_x1, nullptr, nullptr, gb, nullptr, nullptr, nullptr, nullptr);
    gemm_kernel<64, 5, F_, F_, 1024, EPI_W4><<<256, 512, 0, stream>>>(gb, w4, (const u16*)(wc + WC_W4), nullptr, nullptr, x2, x1, xo, st_x, nullptr, nullptr, nullptr);
  }
}
```

```cpp
#include <hip/hip_runtime.h>
#include <hip/hip_bf16.h>
#include <stdint.h>
#include <type_traits>

typedef short bf16x8 __attribute__((ext_vector_type(8)));
typedef short s16x4 __attribute__((ext_vector_type(4)));
typedef float f32x4 __attribute__((ext_vector_type(4)));
typedef float f32x2 __attribute__((ext_vector_type(2)));
typedef unsigned u32x2 __attribute__((ext_vector_type(2)));
typedef unsigned u32x4 __attribute__((ext_vector_type(4)));
typedef __attribute__((address_space(3))) s16x4* lds_s16x4_p;
typedef unsigned short u16;

constexpr int S_ = 2048, D_ = 1024, H_ = 32, F_ = 3584;

__device__ __forceinline__ float bf2f(u16 v) { return __uint_as_float(((unsigned)v) << 16); }
__device__ __forceinline__ float bflo(unsigned v) { return __uint_as_float(v << 16); }
__device__ __forceinline__ float bfhi(unsigned v) { return __uint_as_float(v & 0xffff0000u); }
typedef __bf16 bf16x2_t __attribute__((ext_vector_type(2)));
__device__ __forceinline__ unsigned pk2(float a, float b) {
  return __builtin_bit_cast(unsigned, __builtin_convertvector((f32x2){a, b}, bf16x2_t));
}
__device__ __forceinline__ unsigned pk2v(f32x2 v) { return __builtin_bit_cast(unsigned, __builtin_convertvector(v, bf16x2_t)); }
__device__ __forceinline__ float max2_(float a, float b) { float r; asm("v_max_f32 %0, %1, %2" : "=v"(r) : "v"(a), "v"(b)); return r; }
__device__ __forceinline__ float min2_(float a, float b) { float r; asm("v_min_f32 %0, %1, %2" : "=v"(r) : "v"(a), "v"(b)); return r; }
__device__ __forceinline__ float max3_(float a, float b, float c) { float r; asm("v_max3_f32 %0, %1, %2, %3" : "=v"(r) : "v"(a), "v"(b), "v"(c)); return r; }
__device__ __forceinline__ float min3_(float a, float b, float c) { float r; asm("v_min3_f32 %0, %1, %2, %3" : "=v"(r) : "v"(a), "v"(b), "v"(c)); return r; }
__device__ __forceinline__ float rbf(float a) { return bflo(pk2(a, 0.f)); }
__device__ __forceinline__ float gelu_t(float x) {
  const float c1 = -2.0f * 0.7978845608028654f * 1.4426950408889634f;
  const float c2 = c1 * 0.044715f;
  float t = x * x;
  float w = x * __builtin_fmaf(t, c2, c1);
  float e = __builtin_amdgcn_exp2f(w);
  return x * __builtin_amdgcn_rcpf(1.0f + e);
}

__device__ __forceinline__ f32x2 gelu2(f32x2 x) {
  const float c1 = -2.0f * 0.7978845608028654f * 1.4426950408889634f;
  const float c2 = c1 * 0.044715f;
  f32x2 t = x * x;
  f32x2 w = x * (t * c2 + c1);
  f32x2 d = (f32x2){__builtin_amdgcn_exp2f(w[0]), __builtin_amdgcn_exp2f(w[1])} + 1.0f;
  return x * (f32x2){__builtin_amdgcn_rcpf(d[0]), __builtin_amdgcn_rcpf(d[1])};
}
__device__ __forceinline__ void x16(float v, float& a, float& b) {
  auto r = __builtin_amdgcn_permlane16_swap(__float_as_uint(v), __float_as_uint(v), false, false);
  a = __uint_as_float(r[0]); b = __uint_as_float(r[1]);
}
__device__ __forceinline__ void x32(float v, float& a, float& b) {
  auto r = __builtin_amdgcn_permlane32_swap(__float_as_uint(v), __float_as_uint(v), false, false);
  a = __uint_as_float(r[0]); b = __uint_as_float(r[1]);
}
__device__ __forceinline__ float rmax4(float v) { float a, b; x16(v, a, b); v = max2_(a, b); x32(v, a, b); return max2_(a, b); }
__device__ __forceinline__ float rmin4(float v) { float a, b; x16(v, a, b); v = min2_(a, b); x32(v, a, b); return min2_(a, b); }
__device__ __forceinline__ float rsum4(float v) { float a, b; x16(v, a, b); v = a + b; x32(v, a, b); return a + b; }

__device__ __forceinline__ float rsum8(float v) {
  v += __uint_as_float(__builtin_amdgcn_mov_dpp(__float_as_uint(v), 0xB1, 0xF, 0xF, true));
  v += __uint_as_float(__builtin_amdgcn_mov_dpp(__float_as_uint(v), 0x4E, 0xF, 0xF, true));
  v += __uint_as_float(__builtin_amdgcn_mov_dpp(__float_as_uint(v), 0x141, 0xF, 0xF, true));
  return v;
}

__device__ __forceinline__ void wt_store(u32x4 v, u32x4* p) { asm volatile("global_store_dwordx4 %0, %1, off sc1\n\ts_nop 1" :: "v"(p), "v"(v) : "memory"); }
__device__ __forceinline__ void wt_store(f32x4 v, f32x4* p) { asm volatile("global_store_dwordx4 %0, %1, off sc1\n\ts_nop 1" :: "v"(p), "v"(v) : "memory"); }
__device__ __forceinline__ void wt_store(u32x2 v, u32x2* p) { asm volatile("global_store_dwordx2 %0, %1, off sc1\n\ts_nop 1" :: "v"(p), "v"(v) : "memory"); }
#define WAIT_V(n) asm volatile("s_waitcnt vmcnt(" #n ")" ::: "memory")
#define WAIT_L0() asm volatile("s_waitcnt lgkmcnt(0)" ::: "memory")
template <int N> __device__ __forceinline__ void wait_vm() {
  asm volatile("s_waitcnt vmcnt(%0)" ::"i"(N) : "memory");
}

__device__ __forceinline__ void glds16(unsigned voff, const void* sbase, unsigned lds) {
  unsigned keep;
  asm volatile("s_mov_b32 %0, m0\n\ts_mov_b32 m0, %3\n\ts_nop 0\n\tglobal_load_lds_dwordx4 %1, %2\n\ts_mov_b32 m0, %0"
               : "=&s"(keep) : "v"(voff), "s"(sbase), "s"(lds) : "memory");
}

__device__ __forceinline__ void mfma_acc(f32x4& c, const u32x4& a, const u32x4& b) {
  asm volatile("v_mfma_f32_16x16x32_bf16 %0, %1, %2, %0" : "+v"(c) : "v"(a), "v"(b));
}

#define STAMP(i)

#define XPOS 2
enum { EPI_QKV = 0, EPI_WO = 1, EPI_W1 = 2, EPI_W2 = 3, EPI_W3 = 4, EPI_W4 = 5 };

#define CODE_SYM(dst, sym) asm volatile("s_getpc_b64 s[90:91]\n\ts_add_u32 s90, s90, " sym "@rel32@lo+4\n\ts_addc_u32 s91, s91, " sym "@rel32@hi+12\n\ts_mov_b64 %0, s[90:91]" : "=s"(dst) :: "s90", "s91", "scc")
#define CODE_FIRST "_Z15attn_out_kernelPKtPKfS2_PtS2_S2_PfS4_S0_S0_S0_Pc"
#define CODE_LAST "_Z11gemm_kernelILi64ELi5ELi3584ELi3584ELi1024ELi5EEvPKtS1_S1_PKfPKDv2_fS1_S1_PtPS4_S1_PfS9_"
typedef const __attribute__((address_space(1))) unsigned* gptr_u;
template <int BN, int NST, int KD, int LDA, int LDB, int EPI>
__global__ __launch_bounds__(512, 2) void gemm_kernel(
    const u16* __restrict__ A, const u16* __restrict__ W,
    const u16* __restrict__ Wc,
    const float* __restrict__ cs,
    const f32x2* __restrict__ st_in,
    const u16* __restrict__ r0,
    const u16* __restrict__ r1,
    u16* __restrict__ out,
    f32x2* __restrict__ st_out,
    const u16* __restrict__ wphi,
    float* __restrict__ kvp,
    float* __restrict__ zp)
{
  constexpr int NFULL = BN / 64, HALF = (BN % 64) / 32;
  constexpr int A_BYTES = 128 * 64 * 2, B_BYTES = BN * 64 * 2, STAGE = A_BYTES + B_BYTES;
  constexpr int NT = KD / 64, NTW = 2 * NFULL + HALF, LPS = 4 + 2 * NFULL + HALF;
  constexpr int PB = BN * 2 + 16;
  constexpr int EPI_BYTES = (EPI == EPI_QKV) ? (4 * 64 * 144 + 2 * (64 * 32 + 64) * 4) : 128 * PB;
  constexpr int MAINB = NST * STAGE;
  constexpr int RS_OFF = (MAINB > EPI_BYTES ? MAINB : EPI_BYTES);
  constexpr int CS_OFF = RS_OFF + 128 * 16;
  constexpr int WP_OFF = CS_OFF + 1024;
  constexpr int QX_OFF = WP_OFF + 4096;
  constexpr int SMEM = WP_OFF + (EPI == EPI_QKV ? 4096 + 32768 : 0);
  constexpr bool HAS_CS = (EPI == EPI_QKV || EPI == EPI_W1 || EPI == EPI_W3);
  constexpr bool HAS_ST = (EPI == EPI_QKV || EPI == EPI_W1 || EPI == EPI_W3 || EPI == EPI_W2);
  constexpr bool HAS_RES = (EPI == EPI_WO || EPI == EPI_W2 || EPI == EPI_W4);
  constexpr int LAG = (BN == 64) ? 1 : 0;
  constexpr int CH = BN / 8, NIT = CH / 4;
  static_assert(SMEM <= 163840, "LDS");
  __shared__ __attribute__((aligned(16))) char smem[SMEM];

  STAMP(0);
  unsigned long long pc0;
  asm volatile("s_getpc_b64 %0" : "=s"(pc0));
  const int tid = threadIdx.x, lane = tid & 63;
  const int wid8 = __builtin_amdgcn_readfirstlane(tid >> 6), wid = wid8 & 3, wm = wid >> 1, wn = wid & 1;
  const int g = lane >> 4, l15 = lane & 15;
  const int xcd = blockIdx.x & 7, idx = blockIdx.x >> 3;
  const int tm = (xcd >> 1) * 4 + (idx & 3), tn = (xcd & 1) * 8 + (idx >> 2);
  const int row0 = tm * 128;
  f32x4* rs = reinterpret_cast<f32x4*>(smem + RS_OFF);

  f32x4 acc[4][NTW];
  unsigned pft[3] = {0u, 0u, 0u};
  u32x4 ra[HAS_RES ? NIT : 1], rb[(EPI == EPI_W4) ? NIT : 1];

  unsigned rof[HAS_RES ? NIT : 1];
  if constexpr (HAS_RES) {
#pragma unroll
    for (int it = 0; it < NIT; ++it) {
      const int c = tid + 512 * it, r = c / CH, ch = c % CH;
      rof[it] = (unsigned)((row0 + r) * D_ + tn * BN + ch * 8) * 2u;
      asm volatile("" : "+v"(rof[it]));
    }
  }
  auto ldres = [&]() {
    if constexpr (HAS_RES) {
#pragma unroll
      for (int it = 0; it < NIT; ++it) {
        ra[it] = *reinterpret_cast<const u32x4*>(reinterpret_cast<const char*>(r0) + rof[it]);
        if constexpr (EPI == EPI_W4) rb[it] = *reinterpret_cast<const u32x4*>(reinterpret_cast<const char*>(r1) + rof[it]);
      }
    }
  };

  if (wid8 >= 4) {
    unsigned voA[4], voB[NFULL > 0 ? NFULL : 1][2], voH = 0;
    const bool pk_ = (HALF == 0) && ((tn & 7) == 3);
    const unsigned pitchB = pk_ ? NFULL * 128 : LDB * 2;
#pragma unroll
    for (int i = 0; i < 4; ++i) {
      int r = (wid * 4 + i) * 8 + (lane >> 3);
      int ch = (lane & 7) ^ ((r >> 1) & 7);
      voA[i] = (unsigned)(row0 + r) * (LDA * 2) + ch * 16;
      if constexpr (EPI == EPI_WO) voA[i] = (unsigned)((ch >> 2) * S_ + row0 + r) * 64 + (ch & 3) * 16;
    }
#pragma unroll
    for (int b = 0; b < NFULL; ++b) {
      int colb = (EPI == EPI_QKV) ? (b * 1024 + tn * 64) : (tn * BN + b * 64);
#pragma unroll
      for (int j = 0; j < 2; ++j) {
        int r = (wid * 2 + j) * 8 + (lane >> 3);
        int f = ((r >> 1) & 1) | (((r >> 3) & 1) << 1);
        int chp = lane & 7;
        int gch = (((chp >> 1) ^ f) << 1) | (chp & 1);
        const int rs_ = (EPI == EPI_WO) ? ((r & 32) | ((r & 4) << 2) | ((r & 24) >> 1) | (r & 3)) : r;
        voB[b][j] = (unsigned)rs_ * pitchB + (pk_ ? b * 128 : colb * 2) + gch * 16;
      }
    }
    if constexpr (HALF) {
      int r = wid * 16 + (lane >> 2);
      int chp = lane & 3;
      int gch = (((chp >> 1) ^ ((r >> 3) & 1)) << 1) | (chp & 1);
      voH = (unsigned)r * (LDB * 2) + (tn * BN + NFULL * 64) * 2 + gch * 16;
    }
    const char* Ab = reinterpret_cast<const char*>(A);
    const char* Wb = pk_ ? reinterpret_cast<const char*>(Wc) + (size_t)(tn >> 3) * (KD * NFULL * 128) : reinterpret_cast<const char*>(W);
    const unsigned lbase = (unsigned)(uintptr_t)(__attribute__((address_space(3))) char*)smem;
    auto stage = [&](int kt, int buf) {
      const char* a = Ab + (size_t)kt * (EPI == EPI_WO ? 2 * S_ * 64 : 128);
      const char* w = Wb + (size_t)(kt * (64 * pitchB));
      const unsigned s = lbase + buf * STAGE;
#pragma unroll
      for (int i = 0; i < 4; ++i) glds16(voA[i], a, s + (wid * 4 + i) * 1024);
#pragma unroll
      for (int b = 0; b < NFULL; ++b)
#pragma unroll
        for (int j = 0; j < 2; ++j) glds16(voB[b][j], w, s + A_BYTES + b * 8192 + (wid * 2 + j) * 1024);
      if constexpr (HALF) glds16(voH, w, s + A_BYTES + NFULL * 8192 + wid * 1024);
    };
#pragma unroll
    for (int s = 0; s < NST; ++s) stage(s, s);
    constexpr int XL = 0;
    constexpr int NCS = HAS_CS ? (EPI == EPI_QKV ? 16 : 1) : 0;
    constexpr int XE = NCS + (EPI == EPI_QKV ? 1 : 0) + (HAS_ST ? 4 : 0) + (HAS_RES ? NIT * (EPI == EPI_W4 ? 2 : 1) : 0);
    f32x4 stp[4];
    const int lt = tid - 256;
    u32x4 wpx;
    f32x4 csp[HAS_CS ? NCS : 1];
    constexpr int XP = XPOS;
    wait_vm<(NST - 1 - LAG) * LPS>();
    __builtin_amdgcn_s_barrier();
#pragma unroll
    for (int kt = 0; kt < XP; ++kt) {
      wait_vm<(NST - 2 - LAG) * LPS>();
      __builtin_amdgcn_s_barrier();
      stage(kt + NST, kt % NST);
    }
    asm volatile("" ::: "memory");
    if constexpr (HAS_CS) {
      if (lt < BN / 4) {
        const float* cp = cs + (EPI == EPI_QKV ? (lt >> 4) * 1024 + tn * 64 + (lt & 15) * 4 : tn * BN + lt * 4);
#pragma unroll
        for (int i = 0; i < NCS; ++i) csp[i] = *reinterpret_cast<const f32x4*>(cp + i * LDB);
      }
    }
    if constexpr (EPI == EPI_QKV) wpx = *reinterpret_cast<const u32x4*>(wphi + tn * 2048 + lt * 8);
    if constexpr (HAS_ST) {
      const f32x4* sp = reinterpret_cast<const f32x4*>(st_in + (size_t)(row0 + (lt >> 1)) * 16 + (lt & 1) * 8);
#pragma unroll
      for (int i = 0; i < 4; ++i) stp[i] = sp[i];
    }
    ldres();
    asm volatile("" ::: "memory");
    constexpr int NPEEL = XP + NST - 1 - LAG;
    auto head = [&](auto xe) {
      constexpr int X = decltype(xe)::value;
#pragma unroll
      for (int kt = XP; kt < NPEEL; ++kt) {
        wait_vm<(NST - 2 - LAG) * LPS + X>();
        __builtin_amdgcn_s_barrier();
        stage(kt + NST, kt % NST);
        }
    };
    if (HAS_CS && wid == 0) head(std::integral_constant<int, XE>{});
    else head(std::integral_constant<int, XE - NCS>{});
    int buf = NPEEL % NST;
    for (int kt = NPEEL; kt < NT - NST; ++kt) {
      wait_vm<(NST - 2 - LAG) * LPS>();
      __builtin_amdgcn_s_barrier();
      stage(kt + NST, buf);
      buf = buf + 1 == NST ? 0 : buf + 1;
    }
    asm volatile("" ::: "memory");
#pragma unroll
    for (int r = 0; r < NST - 1; ++r) {
      constexpr int NSL = NST - LAG;
      if (r == 0) wait_vm<(NSL - 2) * LPS + XL>();
      else if (r == 1) wait_vm<(NSL > 3 ? NSL - 3 : 0) * LPS + XL>();
      else if (r == 2) wait_vm<(NSL > 4 ? NSL - 4 : 0) * LPS + XL>();
      else if (r == 3) wait_vm<(NSL > 5 ? NSL - 5 : 0) * LPS + XL>();
      else wait_vm<XL>();
      __builtin_amdgcn_s_barrier();
    }
    if constexpr (HAS_ST) {
#pragma unroll
      for (int i = 0; i < 4; ++i) asm volatile("" : "+v"(stp[i]));
      float sm = 0.f;
#pragma unroll
      for (int i = 0; i < 4; ++i) sm += stp[i][0] + stp[i][2];
      sm += __shfl_xor(sm, 1);
      float mean = sm * (1.0f / 1024.f);
      float m2 = 0.f;
#pragma unroll
      for (int i = 0; i < 4; ++i) {
        float d0 = stp[i][0] * (1.0f / 64.f) - mean, d1 = stp[i][2] * (1.0f / 64.f) - mean;
        m2 += stp[i][1] + stp[i][3] + 64.f * (d0 * d0 + d1 * d1);
      }
      m2 += __shfl_xor(m2, 1);
      float var = m2 * (1.0f / 1024.f);
      float r1_ = 1.0f / sqrtf(var + 1e-5f);
      float sc = r1_;
      if constexpr (EPI == EPI_W1) sc = r1_ / sqrtf(var * r1_ * r1_ + 1e-5f);
      if ((lt & 1) == 0) rs[lt >> 1] = (f32x4){mean, sc, r1_, 0.f};
    }
    if constexpr (HAS_CS) {
      if (lt < BN / 4) {
#pragma unroll
        for (int i = 0; i < NCS; ++i) asm volatile("" : "+v"(csp[i]));
#pragma unroll
        for (int i = NCS / 2; i > 0; i >>= 1)
#pragma unroll
          for (int j = 0; j < i; ++j) csp[j] += csp[j + i];
        *reinterpret_cast<f32x4*>(smem + CS_OFF + lt * 16) = csp[0];
      }
    }
    if constexpr (EPI == EPI_QKV) *reinterpret_cast<u32x4*>(smem + WP_OFF + lt * 16) = wpx;
  } else {
    unsigned ctv = 0;
    if (blockIdx.x < 16) {
      unsigned long long c1;
      CODE_SYM(c1, CODE_LAST);
      const unsigned long long a = (pc0 & ~63ull) + (unsigned long long)tid * 64;
      if (a < c1 + 5632) ctv = *(gptr_u)a;
    }
    ldres();
    const int q = l15 >> 2, p = lane & 3;
    const int fB = (q >> 1) | ((g & 1) << 1);
    unsigned aoff[2], boff[2], hoff = 0;
#pragma unroll
    for (int ks = 0; ks < 2; ++ks) aoff[ks] = (wm * 64 + l15) * 128 + (((4 * ks + g) ^ (l15 >> 1)) * 16);
#pragma unroll
    for (int u = 0; u < 2; ++u) boff[u] = A_BYTES + (8 * g + q) * 128 + (((2 * wn + u) ^ fB) * 32) + p * 8;
    if constexpr (HALF) hoff = A_BYTES + NFULL * 8192 + (8 * g + q) * 64 + ((wn ^ (g & 1)) * 32) + p * 8;

    auto ldf = [&](int i, const char* s, int ks, u32x4(&af)[4], u32x4(&bfr)[NTW]) {
      if (i == 0 || i > NTW) {
        const int mt = i == 0 ? 0 : i - NTW;
        af[mt] = *reinterpret_cast<const u32x4*>(s + aoff[ks] + mt * 2048);
      } else if (i - 1 < 2 * NFULL) {
        const int b = (i - 1) >> 1, u = (i - 1) & 1;
        s16x4 lo = __builtin_amdgcn_ds_read_tr16_b64_v4i16((lds_s16x4_p)(s + boff[u] + b * 8192 + ks * 4096));
        s16x4 hi = __builtin_amdgcn_ds_read_tr16_b64_v4i16((lds_s16x4_p)(s + boff[u] + b * 8192 + ks * 4096 + 512));
        bfr[i - 1] = __builtin_shufflevector(__builtin_bit_cast(u32x2, lo), __builtin_bit_cast(u32x2, hi), 0, 1, 2, 3);
      } else {
        s16x4 lo = __builtin_amdgcn_ds_read_tr16_b64_v4i16((lds_s16x4_p)(s + hoff + ks * 2048));
        s16x4 hi = __builtin_amdgcn_ds_read_tr16_b64_v4i16((lds_s16x4_p)(s + hoff + ks * 2048 + 256));
        bfr[NTW - 1] = __builtin_shufflevector(__builtin_bit_cast(u32x2, lo), __builtin_bit_cast(u32x2, hi), 0, 1, 2, 3);
      }
    };
    constexpr int NM = 4 * NTW, NL = NTW + 4;
    auto phase = [&](u32x4(&afc)[4], u32x4(&bfc)[NTW], u32x4(&afn)[4], u32x4(&bfn)[NTW], int nbuf, int nks, auto rd, auto bar) {
      const char* s = smem + nbuf * STAGE;
      constexpr int BAR = decltype(bar)::value, R0 = BAR < 0 ? 0 : BAR + 1;
      constexpr int RSP = (NM - R0 - 2) / NL > 0 ? (NM - R0 - 2) / NL : 1;
      static_assert(R0 + (NL - 1) * RSP < NM, "reads");
#pragma unroll
      for (int mt = 0; mt < 4; ++mt)
#pragma unroll
        for (int j = 0; j < NTW; ++j) {
          const int m = mt * NTW + j;
          if (m == BAR) {
            WAIT_L0();
            __builtin_amdgcn_s_barrier();
            asm volatile("" ::: "memory");
          }
          if (decltype(rd)::value && m >= R0 && (m - R0) % RSP == 0 && (m - R0) / RSP < NL) ldf((m - R0) / RSP, s, nks, afn, bfn);
          if (EPI == EPI_QKV && j >= 4) mfma_acc(acc[mt][j], afc[mt], bfc[j]);
          else mfma_acc(acc[mt][j], bfc[j], afc[mt]);
          __builtin_amdgcn_sched_barrier(0);
        }
    };
    typedef std::integral_constant<int, -1> nobar;
#pragma unroll
    for (int mt = 0; mt < 4; ++mt)
#pragma unroll
      for (int j = 0; j < NTW; ++j) {
        acc[mt][j] = (f32x4){0.f, 0.f, 0.f, 0.f};
        asm volatile("" : "+v"(acc[mt][j]));
      }
    u32x4 af0[4], af1[4], bf0[NTW], bf1[NTW];
    __builtin_amdgcn_s_barrier();
    asm volatile("" ::: "memory");
    STAMP(1);
#pragma unroll
    for (int i = 0; i < NL; ++i) { ldf(i, smem, 0, af0, bf0); __builtin_amdgcn_sched_barrier(0); }
    int buf = 0;
    if constexpr (LAG) {
      phase(af0, bf0, af1, bf1, 0, 1, std::true_type{}, nobar{});
      for (int kt = 0; kt < NT - 1; ++kt) {
        buf = buf + 1 == NST ? 0 : buf + 1;
        phase(af1, bf1, af0, bf0, buf, 0, std::true_type{}, nobar{});
        asm volatile("" ::: "memory");
        __builtin_amdgcn_s_barrier();
        asm volatile("" ::: "memory");
        phase(af0, bf0, af1, bf1, buf, 1, std::true_type{}, nobar{});
      }
      phase(af1, bf1, af0, bf0, buf, 0, std::false_type{}, nobar{});
    } else {
      for (int kt = 0; kt < NT - 1; ++kt) {
        phase(af0, bf0, af1, bf1, buf, 1, std::true_type{}, nobar{});
        buf = buf + 1 == NST ? 0 : buf + 1;
        phase(af1, bf1, af0, bf0, buf, 0, std::true_type{}, std::integral_constant<int, 4>{});
      }
      phase(af0, bf0, af1, bf1, buf, 1, std::true_type{}, nobar{});
      phase(af1, bf1, af0, bf0, buf, 0, std::false_type{}, nobar{});
    }
    asm volatile("s_nop 7\n\ts_nop 7\n\ts_nop 7" ::: "memory");
    asm volatile("" ::"v"(ctv));
    if constexpr (EPI == EPI_QKV) {
      if (r0 != nullptr) {
        const unsigned ln = (blockIdx.x * 4 + wid) * 64 + lane;
        const char* p01 = reinterpret_cast<const char*>(ln < 32768 ? r0 : r1) + (size_t)(ln & 32767) * 64;
        const char* p2 = reinterpret_cast<const char*>(st_out) + (size_t)ln * 64;
        pft[0] = *reinterpret_cast<const unsigned*>(p01);
        pft[1] = *reinterpret_cast<const unsigned*>(p2);
        if (ln + 65536 < F_ * 1024 * 2 / 64) pft[2] = *reinterpret_cast<const unsigned*>(p2 + 65536 * 64);
      }
    }
#pragma unroll
    for (int mt = 0; mt < 4; ++mt)
#pragma unroll
      for (int j = 0; j < NTW; ++j) asm volatile("" : "+v"(acc[mt][j]));
    if constexpr (EPI == EPI_QKV) {
#pragma unroll
      for (int mt = 0; mt < 4; ++mt)
#pragma unroll
        for (int t = 0; t < 2; ++t) *reinterpret_cast<f32x4*>(smem + QX_OFF + wid * 8192 + (mt * 2 + t) * 1024 + lane * 16) = acc[mt][t];
    }
  }
  WAIT_L0();
  __builtin_amdgcn_s_barrier();
  asm volatile("" ::: "memory");
  STAMP(2);

  if constexpr (EPI == EPI_QKV) {
    const int h = tn * 2 + wn;
    const int q = l15 >> 2, p = lane & 3;
    f32x4 kv[4][2], zz[4];
    float zsel = 0.f;
    float* red = reinterpret_cast<float*>(smem + 4 * 64 * 144) + wn * (64 * 32 + 64);
    bf16x8 wf[2];
#pragma unroll
    for (int ft = 0; ft < 2; ++ft) {
      const char* src = smem + WP_OFF + wn * 2048 + (4 * g + q) * 64 + ft * 32 + p * 8;
      s16x4 lo = __builtin_amdgcn_ds_read_tr16_b64_v4i16((lds_s16x4_p)(src));
      s16x4 hi = __builtin_amdgcn_ds_read_tr16_b64_v4i16((lds_s16x4_p)(src + 16 * 64));
      wf[ft] = __builtin_shufflevector(lo, hi, 0, 1, 2, 3, 4, 5, 6, 7);
    }
    const float* csl = reinterpret_cast<const float*>(smem + CS_OFF);
    auto feat = [&](const bf16x8& xf, u32x2 (&o)[4]) {
      const float L2E = 1.4426950408889634f;
      f32x4 z4 = (f32x4){0.f, 0.f, 0.f, 0.f};
      f32x4 p0 = __builtin_amdgcn_mfma_f32_16x16x32_bf16(wf[0], xf, z4, 0, 0, 0);
      f32x4 p1 = __builtin_amdgcn_mfma_f32_16x16x32_bf16(wf[1], xf, z4, 0, 0, 0);
      f32x2 v[4];
      {
        const unsigned r0_ = pk2(p0[0], p0[1]), r1_ = pk2(p0[2], p0[3]), r2_ = pk2(p1[0], p1[1]), r3_ = pk2(p1[2], p1[3]);
        v[0] = (f32x2){bflo(r0_), bfhi(r0_)}; v[1] = (f32x2){bflo(r1_), bfhi(r1_)};
        v[2] = (f32x2){bflo(r2_), bfhi(r2_)}; v[3] = (f32x2){bflo(r3_), bfhi(r3_)};
      }
      float mx = max2_(max3_(max3_(v[0][0], v[0][1], v[1][0]), v[1][1], v[2][0]), max3_(v[2][1], v[3][0], v[3][1]));
      float mn = min2_(min3_(min3_(v[0][0], v[0][1], v[1][0]), v[1][1], v[2][0]), min3_(v[2][1], v[3][0], v[3][1]));
      mx = rmax4(mx); mn = rmin4(mn);
      const float mxl = -mx * L2E, mnl = mn * L2E;
      f32x2 ep[4], en[4];
#pragma unroll
      for (int i = 0; i < 4; ++i) {
        const f32x2 ap = v[i] * L2E + mxl, an = v[i] * (-L2E) + mnl;
        ep[i] = (f32x2){__builtin_amdgcn_exp2f(ap[0]), __builtin_amdgcn_exp2f(ap[1])};
        en[i] = (f32x2){__builtin_amdgcn_exp2f(an[0]), __builtin_amdgcn_exp2f(an[1])};
      }
      const f32x2 sp2 = (ep[0] + ep[1]) + (ep[2] + ep[3]), sn2 = (en[0] + en[1]) + (en[2] + en[3]);
      const float ip = __builtin_amdgcn_rcpf(rsum4(sp2[0] + sp2[1])), in_ = __builtin_amdgcn_rcpf(rsum4(sn2[0] + sn2[1]));
      o[0] = (u32x2){pk2v(ep[0] * ip), pk2v(ep[1] * ip)};
      o[1] = (u32x2){pk2v(ep[2] * ip), pk2v(ep[3] * ip)};
      o[2] = (u32x2){pk2v(en[0] * in_), pk2v(en[1] * in_)};
      o[3] = (u32x2){pk2v(en[2] * in_), pk2v(en[3] * in_)};
    };
    if (wid8 >= 4) {
      f32x4 cs4[2];
#pragma unroll
      for (int t = 0; t < 2; ++t) cs4[t] = *reinterpret_cast<const f32x4*>(csl + wn * 32 + t * 16 + 4 * g);
      bf16x8 qf[4];
#pragma unroll
      for (int mt = 0; mt < 4; ++mt) {
        f32x4 st = rs[wm * 64 + mt * 16 + l15];
        unsigned pq_[4];
#pragma unroll
        for (int t = 0; t < 2; ++t) {
          const f32x4 ac = *reinterpret_cast<const f32x4*>(smem + QX_OFF + wid * 8192 + (mt * 2 + t) * 1024 + lane * 16);
          float a[4];
#pragma unroll
          for (int i = 0; i < 4; ++i) a[i] = st[1] * (ac[i] - st[0] * cs4[t][i]);
          pq_[t * 2] = pk2(a[0], a[1]); pq_[t * 2 + 1] = pk2(a[2], a[3]);
        }
        qf[mt] = __builtin_bit_cast(bf16x8, (u32x4){pq_[0], pq_[1], pq_[2], pq_[3]});
      }
#pragma unroll
      for (int mt = 0; mt < 4; ++mt) {
        u32x2 o[4];
        feat(qf[mt], o);
        u32x4* dst = reinterpret_cast<u32x4*>(out) + ((size_t)(tm * 8 + wm * 4 + mt) * 32 + h) * 128 + lane;
        wt_store((u32x4){o[0][0], o[0][1], o[1][0], o[1][1]}, dst);
        wt_store((u32x4){o[2][0], o[2][1], o[3][0], o[3][1]}, dst + 64);
      }
    } else {
      f32x4 cs4[2];
      float csv[2];
#pragma unroll
      for (int t = 0; t < 2; ++t) {
        cs4[t] = *reinterpret_cast<const f32x4*>(csl + 64 + wn * 32 + t * 16 + 4 * g);
        csv[t] = csl[128 + wn * 32 + t * 16 + l15];
      }
      bf16x8 kf[4], vf[2][2];
#pragma unroll
      for (int mt = 0; mt < 4; ++mt) {
        f32x4 st = rs[wm * 64 + mt * 16 + l15];
        unsigned pk_[4];
#pragma unroll
        for (int t = 0; t < 2; ++t) {
          float b[4];
#pragma unroll
          for (int i = 0; i < 4; ++i) b[i] = st[1] * (acc[mt][2 + t][i] - st[0] * cs4[t][i]);
          pk_[t * 2] = pk2(b[0], b[1]); pk_[t * 2 + 1] = pk2(b[2], b[3]);
        }
        kf[mt] = __builtin_bit_cast(bf16x8, (u32x4){pk_[0], pk_[1], pk_[2], pk_[3]});
      }
      char* pkl = smem + wid * (64 * 144);
#pragma unroll
      for (int mt = 0; mt < 4; ++mt) {
        u32x2 o[4];
        feat(kf[mt], o);
        char* dst = pkl + (mt * 16 + l15) * 144 + g * 8;
#pragma unroll
        for (int c = 0; c < 4; ++c) *reinterpret_cast<u32x2*>(dst + c * 32) = o[c];
      }
#pragma unroll
      for (int s = 0; s < 2; ++s) {
        unsigned pv[2][4];
#pragma unroll
        for (int m2 = 0; m2 < 2; ++m2) {
          int mt = s * 2 + m2;
          f32x4 s0 = rs[wm * 64 + mt * 16 + 4 * g + 0], s1 = rs[wm * 64 + mt * 16 + 4 * g + 1];
          f32x4 s2 = rs[wm * 64 + mt * 16 + 4 * g + 2], s3 = rs[wm * 64 + mt * 16 + 4 * g + 3];
#pragma unroll
          for (int t = 0; t < 2; ++t) {
            float a0 = s0[1] * (acc[mt][4 + t][0] - s0[0] * csv[t]);
            float a1 = s1[1] * (acc[mt][4 + t][1] - s1[0] * csv[t]);
            float a2 = s2[1] * (acc[mt][4 + t][2] - s2[0] * csv[t]);
            float a3 = s3[1] * (acc[mt][4 + t][3] - s3[0] * csv[t]);
            pv[t][m2 * 2] = pk2(a0, a1); pv[t][m2 * 2 + 1] = pk2(a2, a3);
          }
        }
#pragma unroll
        for (int t = 0; t < 2; ++t) vf[s][t] = __builtin_bit_cast(bf16x8, (u32x4){pv[t][0], pv[t][1], pv[t][2], pv[t][3]});
      }
      WAIT_L0();
      __builtin_amdgcn_sched_barrier(0);
#pragma unroll
      for (int ft = 0; ft < 4; ++ft) { kv[ft][0] = kv[ft][1] = zz[ft] = (f32x4){0.f, 0.f, 0.f, 0.f}; }
      const bf16x8 ones = (bf16x8){0x3F80, 0x3F80, 0x3F80, 0x3F80, 0x3F80, 0x3F80, 0x3F80, 0x3F80};
#pragma unroll
      for (int s = 0; s < 2; ++s)
#pragma unroll
        for (int ft = 0; ft < 4; ++ft) {
          const char* src = pkl + (32 * s + 4 * g + q) * 144 + ft * 32 + p * 8;
          s16x4 lo = __builtin_amdgcn_ds_read_tr16_b64_v4i16((lds_s16x4_p)(src));
          s16x4 hi = __builtin_amdgcn_ds_read_tr16_b64_v4i16((lds_s16x4_p)(src + 16 * 144));
          bf16x8 pf = __builtin_shufflevector(lo, hi, 0, 1, 2, 3, 4, 5, 6, 7);
          kv[ft][0] = __builtin_amdgcn_mfma_f32_16x16x32_bf16(vf[s][0], pf, kv[ft][0], 0, 0, 0);
          kv[ft][1] = __builtin_amdgcn_mfma_f32_16x16x32_bf16(vf[s][1], pf, kv[ft][1], 0, 0, 0);
          zz[ft] = __builtin_amdgcn_mfma_f32_16x16x32_bf16(ones, pf, zz[ft], 0, 0, 0);
        }
      zsel = g == 0 ? zz[0][0] : g == 1 ? zz[1][0] : g == 2 ? zz[2][0] : zz[3][0];
      if (wm == 1) {
#pragma unroll
        for (int ft = 0; ft < 4; ++ft)
#pragma unroll
          for (int dt = 0; dt < 2; ++dt) *reinterpret_cast<f32x4*>(red + ((ft * 2 + dt) * 64 + lane) * 4) = kv[ft][dt];
        red[64 * 32 + lane] = zsel;
      }
    }
    __syncthreads();
    if (wid8 < 2) {
      float* kd = kvp + (size_t)(h * 16 + tm) * 2048;
      float* zd = zp + (size_t)(h * 16 + tm) * 64;
#pragma unroll
      for (int ft = 0; ft < 4; ++ft)
#pragma unroll
        for (int dt = 0; dt < 2; ++dt) {
          f32x4 o = *reinterpret_cast<const f32x4*>(red + ((ft * 2 + dt) * 64 + lane) * 4);
          wt_store(o + kv[ft][dt], reinterpret_cast<f32x4*>(kd + ((ft * 2 + dt) * 64 + lane) * 4));
        }
      zd[lane] = red[64 * 32 + lane] + zsel;
    }
    asm volatile("" ::"v"(pft[0]), "v"(pft[1]), "v"(pft[2]));
  } else {
    if (wid8 < 4) {
      f32x4 stm[4], cs4[NTW];
      if constexpr (EPI == EPI_W1 || EPI == EPI_W3) {
#pragma unroll
        for (int j = 0; j < NTW; ++j) {
          const int colj = (j < 2 * NFULL) ? ((j >> 1) * 64 + (2 * wn + (j & 1)) * 16) : (NFULL * 64 + wn * 16);
          cs4[j] = *reinterpret_cast<const f32x4*>(smem + CS_OFF + (colj + 4 * g) * 4);
        }
#pragma unroll
        for (int mt = 0; mt < 4; ++mt) stm[mt] = rs[wm * 64 + mt * 16 + l15];
      }
#pragma unroll
      for (int j = 0; j < NTW; ++j) {
        const int colj = (j < 2 * NFULL) ? ((j >> 1) * 64 + (2 * wn + (j & 1)) * 16) : (NFULL * 64 + wn * 16);
#pragma unroll
        for (int mt = 0; mt < 4; ++mt) {
          f32x2 v0 = (f32x2){acc[mt][j][0], acc[mt][j][1]}, v1 = (f32x2){acc[mt][j][2], acc[mt][j][3]};
          if constexpr (EPI == EPI_W1 || EPI == EPI_W3) {
            v0 = gelu2((v0 - stm[mt][0] * (f32x2){cs4[j][0], cs4[j][1]}) * stm[mt][1]);
            v1 = gelu2((v1 - stm[mt][0] * (f32x2){cs4[j][2], cs4[j][3]}) * stm[mt][1]);
          }
          *reinterpret_cast<u32x2*>(smem + (wm * 64 + mt * 16 + l15) * PB + (colj + 4 * g) * 2) = (u32x2){pk2(v0[0], v0[1]), pk2(v1[0], v1[1])};
        }
      }
    }
    __syncthreads();
#pragma unroll
    for (int it = 0; it < NIT; ++it) {
      const int c = tid + 512 * it, r = c / CH, ch = c % CH;
      u32x4 v = *reinterpret_cast<const u32x4*>(smem + r * PB + ch * 16);
      const int grow = row0 + r, gcol = tn * BN + ch * 8;
      if constexpr (EPI == EPI_W3) {
        wt_store(v, reinterpret_cast<u32x4*>(out + (size_t)grow * F_ + gcol));
      } else if constexpr (EPI == EPI_W1) {
        const int grp = tn >> 2;
        const int sh = grp == 0 ? 1 : grp == 1 ? -1 : grp == 2 ? 2 : -2;
        const int dr = grow + sh;
        if (dr >= 0 && dr < S_) wt_store(v, reinterpret_cast<u32x4*>(out + (size_t)dr * D_ + gcol));
        const int zr = grow - sh;
        if (zr < 0 || zr >= S_) *reinterpret_cast<u32x4*>(out + (size_t)grow * D_ + gcol) = (u32x4){0u, 0u, 0u, 0u};
      } else {
        float y[8];
        const u32x4 a = ra[it];
        if constexpr (EPI == EPI_WO) {
#pragma unroll
          for (int i = 0; i < 4; ++i) { y[2 * i] = rbf(bflo(a[i]) + bflo(v[i])); y[2 * i + 1] = rbf(bfhi(a[i]) + bfhi(v[i])); }
        } else if constexpr (EPI == EPI_W2) {
          f32x4 st = rs[r];
#pragma unroll
          for (int i = 0; i < 4; ++i) {
            y[2 * i] = rbf(rbf((bflo(a[i]) - st[0]) * st[2]) + bflo(v[i]));
            y[2 * i + 1] = rbf(rbf((bfhi(a[i]) - st[0]) * st[2]) + bfhi(v[i]));
          }
        } else {
          const u32x4 b = rb[it];
#pragma unroll
          for (int i = 0; i < 4; ++i) {
            y[2 * i] = rbf(bflo(a[i]) + rbf(bflo(b[i]) + bflo(v[i])));
            y[2 * i + 1] = rbf(bfhi(a[i]) + rbf(bfhi(b[i]) + bfhi(v[i])));
          }
        }
        wt_store((u32x4){pk2(y[0], y[1]), pk2(y[2], y[3]), pk2(y[4], y[5]), pk2(y[6], y[7])}, reinterpret_cast<u32x4*>(out + (size_t)grow * D_ + gcol));
        float sm = ((y[0] + y[1]) + (y[2] + y[3])) + ((y[4] + y[5]) + (y[6] + y[7]));
        sm = rsum8(sm);
        float mu = sm * (1.0f / 64.f), m2 = 0.f;
#pragma unroll
        for (int i = 0; i < 8; ++i) { float d = y[i] - mu; m2 += d * d; }
        m2 = rsum8(m2);
        if (ch == 0) st_out[(size_t)grow * 16 + tn] = (f32x2){sm, m2};
      }
    }
  }
  STAMP(3);
}

constexpr int WC_QKV = 0, WC_WO = WC_QKV + 2 * 1024 * 384, WC_W1 = WC_WO + 2 * 1024 * 128, WC_W2 = WC_W1 + 2 * 1024 * 128,
              WC_W4 = WC_W2 + 2 * 1024 * 128, WC_END = WC_W4 + 2 * F_ * 128;
__global__ __launch_bounds__(256, 1) void attn_out_kernel(const u16* __restrict__ phiq, const float* __restrict__ kvp,
                                                          const float* __restrict__ zp, u16* __restrict__ o,
                                                          const float* __restrict__ cp1, const float* __restrict__ cp3,
                                                          float* __restrict__ cs1, float* __restrict__ cs3,
                                                          const u16* __restrict__ wo, const u16* __restrict__ w2,
                                                          const u16* __restrict__ w4, char* __restrict__ wc) {
  __shared__ __attribute__((aligned(16))) u16 kvT[48 * 72];
  const int tid = threadIdx.x, lane = tid & 63, wid = tid >> 6, g = lane >> 4, l15 = lane & 15;
  unsigned ctv = 0;
  if (blockIdx.x < 16) {
    unsigned long long c0, c1;
    asm volatile("s_getpc_b64 %0" : "=s"(c0));
    CODE_SYM(c1, CODE_LAST);
    const unsigned long long a = (c0 & ~63ull) + (unsigned long long)tid * 64;
    if (tid < 96 && a < c1 + 5632) ctv = *(gptr_u)a;
  }
  if (blockIdx.x >= 256 + 18) {
    const int e = blockIdx.x - (256 + 18);
    const u16* w = e < 8 ? wo : e < 16 ? w2 : w4;
    const int kd = e < 16 ? 1024 : F_, row = (e < 8 ? e : e < 16 ? e - 8 : e - 16) * 128 + (tid >> 4), t = (tid >> 3) & 1, c = tid & 7;
    char* dst = wc + (e < 8 ? WC_WO : e < 16 ? WC_W2 : WC_W4) + (size_t)t * kd * 128 + (size_t)row * 128 + c * 16;
    u32x4 v[8];
#pragma unroll
    for (int p = 0; p < 8; ++p) v[p] = *reinterpret_cast<const u32x4*>(w + (size_t)(row + p * 16) * 1024 + (3 + 8 * t) * 64 + c * 8);
#pragma unroll
    for (int p = 0; p < 8; ++p) *reinterpret_cast<u32x4*>(dst + p * 2048) = v[p];
    return;
  }
  if (blockIdx.x >= 256) {
    const int i = (blockIdx.x - 256) * 256 + tid;
    const float* src = i < 1024 ? cp1 + i : cp3 + (i - 1024);
    const int ld = i < 1024 ? 1024 : F_;
    float c[16];
#pragma unroll
    for (int t = 0; t < 16; ++t) c[t] = src[t * ld];
#pragma unroll
    for (int t = 8; t > 0; t >>= 1)
#pragma unroll
      for (int j = 0; j < t; ++j) c[j] += c[j + t];
    *(i < 1024 ? cs1 + i : cs3 + (i - 1024)) = c[0];
    return;
  }
  const int h = blockIdx.x & 31, tg = blockIdx.x >> 5;
  f32x4 kvv[16][2];
  float zv[16];
  bf16x8 pq[4][2];
  {
    const float* src = kvp + (size_t)h * 16 * 2048 + tid * 8;
#pragma unroll
    for (int t = 0; t < 16; ++t) {
      kvv[t][0] = *reinterpret_cast<const f32x4*>(src + t * 2048);
      kvv[t][1] = *reinterpret_cast<const f32x4*>(src + t * 2048 + 4);
    }
#pragma unroll
    for (int t = 0; t < 16; ++t) zv[t] = zp[(size_t)(h * 16 + t) * 64 + lane];
#pragma unroll
    for (int mt = 0; mt < 4; ++mt) {
      const u16* ps = phiq + ((size_t)(tg * 16 + wid * 4 + mt) * 32 + h) * 1024 + lane * 8;
      pq[mt][0] = *reinterpret_cast<const bf16x8*>(ps);
      pq[mt][1] = *reinterpret_cast<const bf16x8*>(ps + 512);
    }
  }
  __builtin_amdgcn_sched_barrier(0);
  asm volatile("" ::"v"(ctv));
  {
    for (int i = tid; i < 15 * 64; i += 256) kvT[(33 + i / 64) * 72 + (i & 63)] = 0;
    f32x4 a0 = kvv[0][0], a1 = kvv[0][1];
#pragma unroll
    for (int t = 1; t < 16; ++t) { a0 += kvv[t][0]; a1 += kvv[t][1]; }
    const int ft = tid >> 6, dt = (tid >> 5) & 1, ln = (2 * tid) & 63;
    const int d0 = dt * 16 + 4 * (ln >> 4), pos = (ft >> 1) * 32 + ((ln >> 2) & 3) * 8 + (ft & 1) * 4 + (ln & 3);
#pragma unroll
    for (int i = 0; i < 4; ++i) *reinterpret_cast<unsigned*>(&kvT[(d0 + i) * 72 + pos]) = pk2(a0[i], a1[i]);
    if (tid < 64) {
      float z = 0.f;
#pragma unroll
      for (int t = 0; t < 16; ++t) z += zv[t];
      kvT[32 * 72 + (tid >> 5) * 32 + ((tid >> 2) & 3) * 8 + ((tid >> 4) & 1) * 4 + (tid & 3)] = (u16)(pk2(z, 0.f) & 0xffff);
    }
  }
  __syncthreads();
  bf16x8 kf[3][2];
#pragma unroll
  for (int dt = 0; dt < 3; ++dt)
#pragma unroll
    for (int ks = 0; ks < 2; ++ks) kf[dt][ks] = *reinterpret_cast<const bf16x8*>(&kvT[(dt * 16 + l15) * 72 + ks * 32 + g * 8]);
#pragma unroll
  for (int mt = 0; mt < 4; ++mt) {
    const int tok = tg * 256 + wid * 64 + mt * 16 + l15;
    const bf16x8 p0 = pq[mt][0], p1 = pq[mt][1];
    f32x4 r[3];
#pragma unroll
    for (int dt = 0; dt < 3; ++dt) {
      r[dt] = (f32x4){0.f, 0.f, 0.f, 0.f};
      r[dt] = __builtin_amdgcn_mfma_f32_16x16x32_bf16(kf[dt][0], p0, r[dt], 0, 0, 0);
      r[dt] = __builtin_amdgcn_mfma_f32_16x16x32_bf16(kf[dt][1], p1, r[dt], 0, 0, 0);
    }
    float den = rbf(rbf(__shfl(r[2][0], l15)) + 1e-6f);
    float inv = 1.0f / den;
    const u32x4 w = (u32x4){pk2(rbf(r[0][0]) * inv, rbf(r[0][1]) * inv), pk2(rbf(r[0][2]) * inv, rbf(r[0][3]) * inv),
                            pk2(rbf(r[1][0]) * inv, rbf(r[1][1]) * inv), pk2(rbf(r[1][2]) * inv, rbf(r[1][3]) * inv)};
    wt_store(w, reinterpret_cast<u32x4*>(o + ((size_t)h * S_ + tok) * 32 + 8 * g));
  }
}

__global__ __launch_bounds__(256) void prep_kernel(const u16* __restrict__ x, const u16* __restrict__ wqkv,
                                                   const u16* __restrict__ w1, const u16* __restrict__ w3,
                                                   float* __restrict__ cs_qkv, float* __restrict__ cs_w1,
                                                   float* __restrict__ cs_w3, f32x2* __restrict__ st,
                                                   const u16* __restrict__ wo, const u16* __restrict__ w2,
                                                   const u16* __restrict__ w4, char* __restrict__ wc) {
  __shared__ __attribute__((aligned(16))) float red[4][512];
  const int tid = threadIdx.x, b = (int)blockIdx.x - 16;
  if (b < 0) {
    unsigned long long c0, c1;
    asm volatile("s_getpc_b64 s[90:91]\n\ts_add_u32 s90, s90, _Z15attn_out_kernelPKtPKfS2_PtS2_S2_PfS4_S0_S0_S0_Pc@rel32@lo+4\n\t"
                 "s_addc_u32 s91, s91, _Z15attn_out_kernelPKtPKfS2_PtS2_S2_PfS4_S0_S0_S0_Pc@rel32@hi+12\n\ts_mov_b64 %0, s[90:91]"
                 : "=s"(c0) :: "s90", "s91", "scc");
    asm volatile("s_getpc_b64 s[90:91]\n\ts_add_u32 s90, s90, _Z11gemm_kernelILi64ELi5ELi3584ELi3584ELi1024ELi5EEvPKtS1_S1_PKfPKDv2_fS1_S1_PtPS4_S1_PfS9_@rel32@lo+4\n\t"
                 "s_addc_u32 s91, s91, _Z11gemm_kernelILi64ELi5ELi3584ELi3584ELi1024ELi5EEvPKtS1_S1_PKfPKDv2_fS1_S1_PtPS4_S1_PfS9_@rel32@hi+12\n\ts_mov_b64 %0, s[90:91]"
                 : "=s"(c1) :: "s90", "s91", "scc");
    c1 += 5632;
    if (c1 > c0 && c1 - c0 < 6 * 256 * 64) {
      unsigned v[6];
#pragma unroll
      for (int r = 0; r < 6; ++r) {
        const unsigned long long a = c0 + (unsigned long long)(r * 256 + tid) * 64;
        v[r] = a < c1 ? *reinterpret_cast<const unsigned*>(a) : 0u;
      }
#pragma unroll
      for (int r = 0; r < 6; ++r) asm volatile("" ::"v"(v[r]));
    }
  } else if (b < 240) {
    const u16* w; float* dst; int ld, ch, rg;
    if (b < 96) { w = wqkv; dst = cs_qkv; ld = 3072; ch = b % 6; rg = b / 6; }
    else if (b < 128) { w = w1; dst = cs_w1; ld = 1024; ch = (b - 96) & 1; rg = (b - 96) >> 1; }
    else { w = w3; dst = cs_w3; ld = F_; ch = (b - 128) % 7; rg = (b - 128) / 7; }
    const int lane = tid & 63, wid = tid >> 6, row = rg * 64 + wid * 16;
    u32x4 v[16];
#pragma unroll
    for (int r = 0; r < 16; ++r) v[r] = *reinterpret_cast<const u32x4*>(w + (size_t)(row + r) * ld + ch * 512 + lane * 8);
    if (b < 128 && (lane >> 3) == 3) {
      const int cpp = b < 96 ? 384 : 128;
      char* cpd = wc + (b < 96 ? WC_QKV + (ch & 1) * (1024 * 384) + (ch >> 1) * 128 : WC_W1 + ch * (1024 * 128)) + row * cpp + (lane & 7) * 16;
#pragma unroll
      for (int r = 0; r < 16; ++r) *reinterpret_cast<u32x4*>(cpd + r * cpp) = v[r];
    }
    f32x4 s0 = (f32x4){0.f, 0.f, 0.f, 0.f}, s1 = s0;
#pragma unroll
    for (int r = 0; r < 16; ++r) {
      s0 += (f32x4){bflo(v[r][0]), bfhi(v[r][0]), bflo(v[r][1]), bfhi(v[r][1])};
      s1 += (f32x4){bflo(v[r][2]), bfhi(v[r][2]), bflo(v[r][3]), bfhi(v[r][3])};
    }
    *reinterpret_cast<f32x4*>(&red[wid][lane * 8]) = s0;
    *reinterpret_cast<f32x4*>(&red[wid][lane * 8 + 4]) = s1;
    __syncthreads();
    f32x2 t = *reinterpret_cast<const f32x2*>(&red[0][tid * 2]);
#pragma unroll
    for (int i = 1; i < 4; ++i) t += *reinterpret_cast<const f32x2*>(&red[i][tid * 2]);
    *reinterpret_cast<f32x2*>(dst + rg * ld + ch * 512 + tid * 2) = t;
  } else {
    const int lane = tid & 63, wid = tid >> 6;
    const int rbase = (b - 240) * 16 + wid * 4;
    u32x4 xv[8];
#pragma unroll
    for (int i = 0; i < 8; ++i) xv[i] = *reinterpret_cast<const u32x4*>(x + (size_t)rbase * D_ + i * 512 + lane * 8);
    __builtin_amdgcn_sched_barrier(0);
#pragma unroll
    for (int rr = 0; rr < 4; ++rr) {
      const int row = rbase + rr;
#pragma unroll
      for (int hf = 0; hf < 2; ++hf) {
        const u32x4 v = xv[rr * 2 + hf];
        float y[8];
#pragma unroll
        for (int i = 0; i < 4; ++i) { y[2 * i] = bflo(v[i]); y[2 * i + 1] = bfhi(v[i]); }
        float sm = ((y[0] + y[1]) + (y[2] + y[3])) + ((y[4] + y[5]) + (y[6] + y[7]));
        sm = rsum8(sm);
        float mu = sm * (1.0f / 64.f), m2 = 0.f;
#pragma unroll
        for (int i = 0; i < 8; ++i) { float d = y[i] - mu; m2 += d * d; }
        m2 = rsum8(m2);
        if ((lane & 7) == 0) st[(size_t)row * 16 + hf * 8 + (lane >> 3)] = (f32x2){sm, m2};
      }
    }
  }
}

extern "C" void kernel_launch(void* const* d_in, const int* in_sizes, int n_in, void* d_out, int out_size, void* d_ws,
                              size_t ws_size, hipStream_t stream) {
  const u16* x_in = (const u16*)d_in[0];
  const u16* wqkv = (const u16*)d_in[1];
  const u16* wphi = (const u16*)d_in[2];
  const u16* wo = (const u16*)d_in[3];
  const u16* w1 = (const u16*)d_in[5];
  const u16* w2 = (const u16*)d_in[6];
  const u16* w3 = (const u16*)d_in[7];
  const u16* w4 = (const u16*)d_in[8];
  char* ws = (char*)d_ws;
  size_t off = 0;
  auto alloc = [&](size_t bytes) { char* p = ws + off; off += (bytes + 4095) & ~(size_t)4095; return p; };
  u16* xb = (u16*)alloc((size_t)S_ * D_ * 2);
  u16* x2 = (u16*)alloc((size_t)S_ * D_ * 2);
  u16* x1 = (u16*)alloc((size_t)S_ * D_ * 2);
  u16* hs = (u16*)alloc((size_t)S_ * D_ * 2);
  u16* ob = (u16*)alloc((size_t)S_ * D_ * 2);
  u16* gb = (u16*)alloc((size_t)S_ * F_ * 2);
  u16* phiq = (u16*)alloc((size_t)S_ * 2048 * 2);
  float* kvp = (float*)alloc((size_t)32 * 16 * 2048 * 4);
  float* zp = (float*)alloc((size_t)32 * 16 * 64 * 4);
  f32x2* st_x = (f32x2*)alloc((size_t)S_ * 16 * 8);
  f32x2* st_x2 = (f32x2*)alloc((size_t)S_ * 16 * 8);
  f32x2* st_x1 = (f32x2*)alloc((size_t)S_ * 16 * 8);
  float* cs_qkv = (float*)alloc(16 * 3072 * 4);
  float* cs_w1 = (float*)alloc(16 * 1024 * 4);
  float* cs_w3 = (float*)alloc(16 * F_ * 4);
  float* cf_w1 = (float*)alloc(1024 * 4);
  float* cf_w3 = (float*)alloc(F_ * 4);
  char* wc = alloc(WC_END);

  prep_kernel<<<368 + 16, 256, 0, stream>>>(x_in, wqkv, w1, w3, cs_qkv, cs_w1, cs_w3, st_x, wo, w2, w4, wc);
  for (int layer = 0; layer < 2; ++layer) {
    const u16* x = layer == 0 ? x_in : xb;
    u16* xo = layer == 0 ? xb : (u16*)d_out;
    gemm_kernel<192, 3, 1024, 1024, 3072, EPI_QKV><<<256, 512, 0, stream>>>(x, wqkv, (const u16*)(wc + WC_QKV), cs_qkv, st_x, layer == 0 ? wo : nullptr, layer == 0 ? w2 : nullptr, phiq, layer == 0 ? (f32x2*)w4 : nullptr, wphi, kvp, zp);
    attn_out_kernel<<<layer == 0 ? 256 + 18 + 44 : 256, 256, 0, stream>>>(phiq, kvp, zp, ob, cs_w1, cs_w3, cf_w1, cf_w3, wo, w2, w4, wc);
    gemm_kernel<64, 5, 1024, 1024, 1024, EPI_WO><<<256, 512, 0, stream>>>(ob, wo, (const u16*)(wc + WC_WO), nullptr, nullptr, x, nullptr, x2, st_x2, nullptr, nullptr, nullptr);
    gemm_kernel<64, 5, 1024, 1024, 1024, EPI_W1><<<256, 512, 0, stream>>>(x2, w1, (const u16*)(wc + WC_W1), cf_w1, st_x2, nullptr, nullptr, hs, nullptr, nullptr, nullptr, nullptr);
    gemm_kernel<64, 5, 1024, 1024, 1024, EPI_W2><<<256, 512, 0, stream>>>(hs, w2, (const u16*)(wc + WC_W2), nullptr, st_x2, x2, nullptr, x1, st_x1, nullptr, nullptr, nullptr);
    gemm_kernel<224, 3, 1024, 1024, F_, EPI_W3><<<256, 512, 0, stream>>>(x1, w3, nullptr, cf_w3, st_x1, nullptr, nullptr, gb, nullptr, nullptr, nullptr, nullptr);
    gemm_kernel<64, 5, F_, F_, 1024, EPI_W4><<<256, 512, 0, stream>>>(gb, w4, (const u16*)(wc + WC_W4), nullptr, nullptr, x2, x1, xo, st_x, nullptr, nullptr, nullptr);
  }
}
```

```cpp
#include <hip/hip_runtime.h>
#include <hip/hip_bf16.h>
#include <stdint.h>
#include <type_traits>

typedef short bf16x8 __attribute__((ext_vector_type(8)));
typedef short s16x4 __attribute__((ext_vector_type(4)));
typedef float f32x4 __attribute__((ext_vector_type(4)));
typedef float f32x2 __attribute__((ext_vector_type(2)));
typedef unsigned u32x2 __attribute__((ext_vector_type(2)));
typedef unsigned u32x4 __attribute__((ext_vector_type(4)));
typedef __attribute__((address_space(3))) s16x4* lds_s16x4_p;
typedef unsigned short u16;

constexpr int S_ = 2048, D_ = 1024, H_ = 32, F_ = 3584;

__device__ __forceinline__ float bf2f(u16 v) { return __uint_as_float(((unsigned)v) << 16); }
__device__ __forceinline__ float bflo(unsigned v) { return __uint_as_float(v << 16); }
__device__ __forceinline__ float bfhi(unsigned v) { return __uint_as_float(v & 0xffff0000u); }
typedef __bf16 bf16x2_t __attribute__((ext_vector_type(2)));
__device__ __forceinline__ unsigned pk2(float a, float b) {
  return __builtin_bit_cast(unsigned, __builtin_convertvector((f32x2){a, b}, bf16x2_t));
}
__device__ __forceinline__ unsigned pk2v(f32x2 v) { return __builtin_bit_cast(unsigned, __builtin_convertvector(v, bf16x2_t)); }
__device__ __forceinline__ float max2_(float a, float b) { float r; asm("v_max_f32 %0, %1, %2" : "=v"(r) : "v"(a), "v"(b)); return r; }
__device__ __forceinline__ float min2_(float a, float b) { float r; asm("v_min_f32 %0, %1, %2" : "=v"(r) : "v"(a), "v"(b)); return r; }
__device__ __forceinline__ float max3_(float a, float b, float c) { float r; asm("v_max3_f32 %0, %1, %2, %3" : "=v"(r) : "v"(a), "v"(b), "v"(c)); return r; }
__device__ __forceinline__ float min3_(float a, float b, float c) { float r; asm("v_min3_f32 %0, %1, %2, %3" : "=v"(r) : "v"(a), "v"(b), "v"(c)); return r; }
__device__ __forceinline__ float rbf(float a) { return bflo(pk2(a, 0.f)); }
__device__ __forceinline__ float gelu_t(float x) {
  const float c1 = -2.0f * 0.7978845608028654f * 1.4426950408889634f;
  const float c2 = c1 * 0.044715f;
  float t = x * x;
  float w = x * __builtin_fmaf(t, c2, c1);
  float e = __builtin_amdgcn_exp2f(w);
  return x * __builtin_amdgcn_rcpf(1.0f + e);
}

__device__ __forceinline__ f32x2 gelu2(f32x2 x) {
  const float c1 = -2.0f * 0.7978845608028654f * 1.4426950408889634f;
  const float c2 = c1 * 0.044715f;
  f32x2 t = x * x;
  f32x2 w = x * (t * c2 + c1);
  f32x2 d = (f32x2){__builtin_amdgcn_exp2f(w[0]), __builtin_amdgcn_exp2f(w[1])} + 1.0f;
  return x * (f32x2){__builtin_amdgcn_rcpf(d[0]), __builtin_amdgcn_rcpf(d[1])};
}
__device__ __forceinline__ void x16(float v, float& a, float& b) {
  auto r = __builtin_amdgcn_permlane16_swap(__float_as_uint(v), __float_as_uint(v), false, false);
  a = __uint_as_float(r[0]); b = __uint_as_float(r[1]);
}
__device__ __forceinline__ void x32(float v, float& a, float& b) {
  auto r = __builtin_amdgcn_permlane32_swap(__float_as_uint(v), __float_as_uint(v), false, false);
  a = __uint_as_float(r[0]); b = __uint_as_float(r[1]);
}
__device__ __forceinline__ float rmax4(float v) { float a, b; x16(v, a, b); v = max2_(a, b); x32(v, a, b); return max2_(a, b); }
__device__ __forceinline__ float rmin4(float v) { float a, b; x16(v, a, b); v = min2_(a, b); x32(v, a, b); return min2_(a, b); }
__device__ __forceinline__ float rsum4(float v) { float a, b; x16(v, a, b); v = a + b; x32(v, a, b); return a + b; }

__device__ __forceinline__ float rsum8(float v) {
  v += __uint_as_float(__builtin_amdgcn_mov_dpp(__float_as_uint(v), 0xB1, 0xF, 0xF, true));
  v += __uint_as_float(__builtin_amdgcn_mov_dpp(__float_as_uint(v), 0x4E, 0xF, 0xF, true));
  v += __uint_as_float(__builtin_amdgcn_mov_dpp(__float_as_uint(v), 0x141, 0xF, 0xF, true));
  return v;
}

__device__ __forceinline__ void wt_store(u32x4 v, u32x4* p) { asm volatile("global_store_dwordx4 %0, %1, off sc1\n\ts_nop 1" :: "v"(p), "v"(v) : "memory"); }
__device__ __forceinline__ void wt_store(f32x4 v, f32x4* p) { asm volatile("global_store_dwordx4 %0, %1, off sc1\n\ts_nop 1" :: "v"(p), "v"(v) : "memory"); }
__device__ __forceinline__ void wt_store(u32x2 v, u32x2* p) { asm volatile("global_store_dwordx2 %0, %1, off sc1\n\ts_nop 1" :: "v"(p), "v"(v) : "memory"); }
#define WAIT_V(n) asm volatile("s_waitcnt vmcnt(" #n ")" ::: "memory")
#define WAIT_L0() asm volatile("s_waitcnt lgkmcnt(0)" ::: "memory")
template <int N> __device__ __forceinline__ void wait_vm() {
  asm volatile("s_waitcnt vmcnt(%0)" ::"i"(N) : "memory");
}

__device__ __forceinline__ void glds16(unsigned voff, const void* sbase, unsigned lds) {
  unsigned keep;
  asm volatile("s_mov_b32 %0, m0\n\ts_mov_b32 m0, %3\n\ts_nop 0\n\tglobal_load_lds_dwordx4 %1, %2\n\ts_mov_b32 m0, %0"
               : "=&s"(keep) : "v"(voff), "s"(sbase), "s"(lds) : "memory");
}

__device__ __forceinline__ void mfma_acc(f32x4& c, const u32x4& a, const u32x4& b) {
  asm volatile("v_mfma_f32_16x16x32_bf16 %0, %1, %2, %0" : "+v"(c) : "v"(a), "v"(b));
}

#define STAMP(i)

#define XPOS 2
enum { EPI_QKV = 0, EPI_WO = 1, EPI_W1 = 2, EPI_W2 = 3, EPI_W3 = 4, EPI_W4 = 5 };

#define CODE_SYM(dst, sym) asm volatile("s_getpc_b64 s[90:91]\n\ts_add_u32 s90, s90, " sym "@rel32@lo+4\n\ts_addc_u32 s91, s91, " sym "@rel32@hi+12\n\ts_mov_b64 %0, s[90:91]" : "=s"(dst) :: "s90", "s91", "scc")
#define CODE_FIRST "_Z15attn_out_kernelPKtPKfS2_PtS2_S2_PfS4_S0_S0_S0_Pc"
#define CODE_LAST "_Z13zz_end_kernelILi0EEvv"
template <int I> __global__ void zz_end_kernel() {}
typedef const __attribute__((address_space(1))) unsigned* gptr_u;
template <int BN, int NST, int KD, int LDA, int LDB, int EPI>
__global__ __launch_bounds__(512, 2) void gemm_kernel(
    const u16* __restrict__ A, const u16* __restrict__ W,
    const u16* __restrict__ Wc,
    const float* __restrict__ cs,
    const f32x2* __restrict__ st_in,
    const u16* __restrict__ r0,
    const u16* __restrict__ r1,
    u16* __restrict__ out,
    f32x2* __restrict__ st_out,
    const u16* __restrict__ wphi,
    float* __restrict__ kvp,
    float* __restrict__ zp)
{
  constexpr int NFULL = BN / 64, HALF = (BN % 64) / 32;
  constexpr int A_BYTES = 128 * 64 * 2, B_BYTES = BN * 64 * 2, STAGE = A_BYTES + B_BYTES;
  constexpr int NT = KD / 64, NTW = 2 * NFULL + HALF, LPS = 4 + 2 * NFULL + HALF;
  constexpr int PB = BN * 2 + 16;
  constexpr int EPI_BYTES = (EPI == EPI_QKV) ? (4 * 64 * 144 + 2 * (64 * 32 + 64) * 4) : 128 * PB;
  constexpr int MAINB = NST * STAGE;
  constexpr int RS_OFF = (MAINB > EPI_BYTES ? MAINB : EPI_BYTES);
  constexpr int CS_OFF = RS_OFF + 128 * 16;
  constexpr int WP_OFF = CS_OFF + 1024;
  constexpr int QX_OFF = WP_OFF + 4096;
  constexpr int SMEM = WP_OFF + (EPI == EPI_QKV ? 4096 + 32768 : 0);
  constexpr bool HAS_CS = (EPI == EPI_QKV || EPI == EPI_W1 || EPI == EPI_W3);
  constexpr bool HAS_ST = (EPI == EPI_QKV || EPI == EPI_W1 || EPI == EPI_W3 || EPI == EPI_W2);
  constexpr bool HAS_RES = (EPI == EPI_WO || EPI == EPI_W2 || EPI == EPI_W4);
  constexpr int LAG = (BN == 64) ? 1 : 0;
  constexpr int CH = BN / 8, NIT = CH / 4;
  static_assert(SMEM <= 163840, "LDS");
  __shared__ __attribute__((aligned(16))) char smem[SMEM];

  STAMP(0);
  unsigned long long pc0;
  asm volatile("s_getpc_b64 %0" : "=s"(pc0));
  const int tid = threadIdx.x, lane = tid & 63;
  const int wid8 = __builtin_amdgcn_readfirstlane(tid >> 6), wid = wid8 & 3, wm = wid >> 1, wn = wid & 1;
  const int g = lane >> 4, l15 = lane & 15;
  const int xcd = blockIdx.x & 7, idx = blockIdx.x >> 3;
  const int tm = (xcd >> 1) * 4 + (idx & 3), tn = (xcd & 1) * 8 + (idx >> 2);
  const int row0 = tm * 128;
  f32x4* rs = reinterpret_cast<f32x4*>(smem + RS_OFF);

  f32x4 acc[4][NTW];
  unsigned pft[3] = {0u, 0u, 0u};
  u32x4 ra[HAS_RES ? NIT : 1], rb[(EPI == EPI_W4) ? NIT : 1];

  unsigned rof[HAS_RES ? NIT : 1];
  if constexpr (HAS_RES) {
#pragma unroll
    for (int it = 0; it < NIT; ++it) {
      const int c = tid + 512 * it, r = c / CH, ch = c % CH;
      rof[it] = (unsigned)((row0 + r) * D_ + tn * BN + ch * 8) * 2u;
      asm volatile("" : "+v"(rof[it]));
    }
  }
  auto ldres = [&]() {
    if constexpr (HAS_RES) {
#pragma unroll
      for (int it = 0; it < NIT; ++it) {
        ra[it] = *reinterpret_cast<const u32x4*>(reinterpret_cast<const char*>(r0) + rof[it]);
        if constexpr (EPI == EPI_W4) rb[it] = *reinterpret_cast<const u32x4*>(reinterpret_cast<const char*>(r1) + rof[it]);
      }
    }
  };

  if (wid8 >= 4) {
    unsigned voA[4], voB[NFULL > 0 ? NFULL : 1][2], voH = 0;
    const bool pk_ = (HALF == 0) && ((tn & 7) == 3);
    const unsigned pitchB = pk_ ? NFULL * 128 : LDB * 2;
#pragma unroll
    for (int i = 0; i < 4; ++i) {
      int r = (wid * 4 + i) * 8 + (lane >> 3);
      int ch = (lane & 7) ^ ((r >> 1) & 7);
      voA[i] = (unsigned)(row0 + r) * (LDA * 2) + ch * 16;
      if constexpr (EPI == EPI_WO) voA[i] = (unsigned)((ch >> 2) * S_ + row0 + r) * 64 + (ch & 3) * 16;
    }
#pragma unroll
    for (int b = 0; b < NFULL; ++b) {
      int colb = (EPI == EPI_QKV) ? (b * 1024 + tn * 64) : (tn * BN + b * 64);
#pragma unroll
      for (int j = 0; j < 2; ++j) {
        int r = (wid * 2 + j) * 8 + (lane >> 3);
        int f = ((r >> 1) & 1) | (((r >> 3) & 1) << 1);
        int chp = lane & 7;
        int gch = (((chp >> 1) ^ f) << 1) | (chp & 1);
        const int rs_ = (EPI == EPI_WO) ? ((r & 32) | ((r & 4) << 2) | ((r & 24) >> 1) | (r & 3)) : r;
        voB[b][j] = (unsigned)rs_ * pitchB + (pk_ ? b * 128 : colb * 2) + gch * 16;
      }
    }
    if constexpr (HALF) {
      int r = wid * 16 + (lane >> 2);
      int chp = lane & 3;
      int gch = (((chp >> 1) ^ ((r >> 3) & 1)) << 1) | (chp & 1);
      voH = (unsigned)r * (LDB * 2) + (tn * BN + NFULL * 64) * 2 + gch * 16;
    }
    const char* Ab = reinterpret_cast<const char*>(A);
    const char* Wb = pk_ ? reinterpret_cast<const char*>(Wc) + (size_t)(tn >> 3) * (KD * NFULL * 128) : reinterpret_cast<const char*>(W);
    const unsigned lbase = (unsigned)(uintptr_t)(__attribute__((address_space(3))) char*)smem;
    auto stage = [&](int kt, int buf) {
      const char* a = Ab + (size_t)kt * (EPI == EPI_WO ? 2 * S_ * 64 : 128);
      const char* w = Wb + (size_t)(kt * (64 * pitchB));
      const unsigned s = lbase + buf * STAGE;
#pragma unroll
      for (int i = 0; i < 4; ++i) glds16(voA[i], a, s + (wid * 4 + i) * 1024);
#pragma unroll
      for (int b = 0; b < NFULL; ++b)
#pragma unroll
        for (int j = 0; j < 2; ++j) glds16(voB[b][j], w, s + A_BYTES + b * 8192 + (wid * 2 + j) * 1024);
      if constexpr (HALF) glds16(voH, w, s + A_BYTES + NFULL * 8192 + wid * 1024);
    };
#pragma unroll
    for (int s = 0; s < NST; ++s) stage(s, s);
    constexpr int XL = 0;
    constexpr int NCS = HAS_CS ? (EPI == EPI_QKV ? 16 : 1) : 0;
    constexpr int XE = NCS + (EPI == EPI_QKV ? 1 : 0) + (HAS_ST ? 4 : 0) + (HAS_RES ? NIT * (EPI == EPI_W4 ? 2 : 1) : 0);
    f32x4 stp[4];
    const int lt = tid - 256;
    u32x4 wpx;
    f32x4 csp[HAS_CS ? NCS : 1];
    constexpr int XP = XPOS;
    wait_vm<(NST - 1 - LAG) * LPS>();
    __builtin_amdgcn_s_barrier();
#pragma unroll
    for (int kt = 0; kt < XP; ++kt) {
      wait_vm<(NST - 2 - LAG) * LPS>();
      __builtin_amdgcn_s_barrier();
      stage(kt + NST, kt % NST);
    }
    asm volatile("" ::: "memory");
    if constexpr (HAS_CS) {
      if (lt < BN / 4) {
        const float* cp = cs + (EPI == EPI_QKV ? (lt >> 4) * 1024 + tn * 64 + (lt & 15) * 4 : tn * BN + lt * 4);
#pragma unroll
        for (int i = 0; i < NCS; ++i) csp[i] = *reinterpret_cast<const f32x4*>(cp + i * LDB);
      }
    }
    if constexpr (EPI == EPI_QKV) wpx = *reinterpret_cast<const u32x4*>(wphi + tn * 2048 + lt * 8);
    if constexpr (HAS_ST) {
      const f32x4* sp = reinterpret_cast<const f32x4*>(st_in + (size_t)(row0 + (lt >> 1)) * 16 + (lt & 1) * 8);
#pragma unroll
      for (int i = 0; i < 4; ++i) stp[i] = sp[i];
    }
    ldres();
    asm volatile("" ::: "memory");
    constexpr int NPEEL = XP + NST - 1 - LAG;
    auto head = [&](auto xe) {
      constexpr int X = decltype(xe)::value;
#pragma unroll
      for (int kt = XP; kt < NPEEL; ++kt) {
        wait_vm<(NST - 2 - LAG) * LPS + X>();
        __builtin_amdgcn_s_barrier();
        stage(kt + NST, kt % NST);
        }
    };
    if (HAS_CS && wid == 0) head(std::integral_constant<int, XE>{});
    else head(std::integral_constant<int, XE - NCS>{});
    int buf = NPEEL % NST;
    for (int kt = NPEEL; kt < NT - NST; ++kt) {
      wait_vm<(NST - 2 - LAG) * LPS>();
      __builtin_amdgcn_s_barrier();
      stage(kt + NST, buf);
      buf = buf + 1 == NST ? 0 : buf + 1;
    }
    asm volatile("" ::: "memory");
#pragma unroll
    for (int r = 0; r < NST - 1; ++r) {
      constexpr int NSL = NST - LAG;
      if (r == 0) wait_vm<(NSL - 2) * LPS + XL>();
      else if (r == 1) wait_vm<(NSL > 3 ? NSL - 3 : 0) * LPS + XL>();
      else if (r == 2) wait_vm<(NSL > 4 ? NSL - 4 : 0) * LPS + XL>();
      else if (r == 3) wait_vm<(NSL > 5 ? NSL - 5 : 0) * LPS + XL>();
      else wait_vm<XL>();
      __builtin_amdgcn_s_barrier();
    }
    if constexpr (HAS_ST) {
#pragma unroll
      for (int i = 0; i < 4; ++i) asm volatile("" : "+v"(stp[i]));
      float sm = 0.f;
#pragma unroll
      for (int i = 0; i < 4; ++i) sm += stp[i][0] + stp[i][2];
      sm += __shfl_xor(sm, 1);
      float mean = sm * (1.0f / 1024.f);
      float m2 = 0.f;
#pragma unroll
      for (int i = 0; i < 4; ++i) {
        float d0 = stp[i][0] * (1.0f / 64.f) - mean, d1 = stp[i][2] * (1.0f / 64.f) - mean;
        m2 += stp[i][1] + stp[i][3] + 64.f * (d0 * d0 + d1 * d1);
      }
      m2 += __shfl_xor(m2, 1);
      float var = m2 * (1.0f / 1024.f);
      float r1_ = 1.0f / sqrtf(var + 1e-5f);
      float sc = r1_;
      if constexpr (EPI == EPI_W1) sc = r1_ / sqrtf(var * r1_ * r1_ + 1e-5f);
      if ((lt & 1) == 0) rs[lt >> 1] = (f32x4){mean, sc, r1_, 0.f};
    }
    if constexpr (HAS_CS) {
      if (lt < BN / 4) {
#pragma unroll
        for (int i = 0; i < NCS; ++i) asm volatile("" : "+v"(csp[i]));
#pragma unroll
        for (int i = NCS / 2; i > 0; i >>= 1)
#pragma unroll
          for (int j = 0; j < i; ++j) csp[j] += csp[j + i];
        *reinterpret_cast<f32x4*>(smem + CS_OFF + lt * 16) = csp[0];
      }
    }
    if constexpr (EPI == EPI_QKV) *reinterpret_cast<u32x4*>(smem + WP_OFF + lt * 16) = wpx;
  } else {
    unsigned ctv = 0;
    if (blockIdx.x < 16) {
      unsigned long long c1;
      CODE_SYM(c1, CODE_LAST);
      const unsigned long long a = (pc0 & ~63ull) + (unsigned long long)tid * 64;
      if (a < c1) ctv = *(gptr_u)a;
    }
    ldres();
    const int q = l15 >> 2, p = lane & 3;
    const int fB = (q >> 1) | ((g & 1) << 1);
    unsigned aoff[2], boff[2], hoff = 0;
#pragma unroll
    for (int ks = 0; ks < 2; ++ks) aoff[ks] = (wm * 64 + l15) * 128 + (((4 * ks + g) ^ (l15 >> 1)) * 16);
#pragma unroll
    for (int u = 0; u < 2; ++u) boff[u] = A_BYTES + (8 * g + q) * 128 + (((2 * wn + u) ^ fB) * 32) + p * 8;
    if constexpr (HALF) hoff = A_BYTES + NFULL * 8192 + (8 * g + q) * 64 + ((wn ^ (g & 1)) * 32) + p * 8;

    auto ldf = [&](int i, const char* s, int ks, u32x4(&af)[4], u32x4(&bfr)[NTW]) {
      if (i == 0 || i > NTW) {
        const int mt = i == 0 ? 0 : i - NTW;
        af[mt] = *reinterpret_cast<const u32x4*>(s + aoff[ks] + mt * 2048);
      } else if (i - 1 < 2 * NFULL) {
        const int b = (i - 1) >> 1, u = (i - 1) & 1;
        s16x4 lo = __builtin_amdgcn_ds_read_tr16_b64_v4i16((lds_s16x4_p)(s + boff[u] + b * 8192 + ks * 4096));
        s16x4 hi = __builtin_amdgcn_ds_read_tr16_b64_v4i16((lds_s16x4_p)(s + boff[u] + b * 8192 + ks * 4096 + 512));
        bfr[i - 1] = __builtin_shufflevector(__builtin_bit_cast(u32x2, lo), __builtin_bit_cast(u32x2, hi), 0, 1, 2, 3);
      } else {
        s16x4 lo = __builtin_amdgcn_ds_read_tr16_b64_v4i16((lds_s16x4_p)(s + hoff + ks * 2048));
        s16x4 hi = __builtin_amdgcn_ds_read_tr16_b64_v4i16((lds_s16x4_p)(s + hoff + ks * 2048 + 256));
        bfr[NTW - 1] = __builtin_shufflevector(__builtin_bit_cast(u32x2, lo), __builtin_bit_cast(u32x2, hi), 0, 1, 2, 3);
      }
    };
    constexpr int NM = 4 * NTW, NL = NTW + 4;
    auto phase = [&](u32x4(&afc)[4], u32x4(&bfc)[NTW], u32x4(&afn)[4], u32x4(&bfn)[NTW], int nbuf, int nks, auto rd, auto bar) {
      const char* s = smem + nbuf * STAGE;
      constexpr int BAR = decltype(bar)::value, R0 = BAR < 0 ? 0 : BAR + 1;
      constexpr int RSP = (NM - R0 - 2) / NL > 0 ? (NM - R0 - 2) / NL : 1;
      static_assert(R0 + (NL - 1) * RSP < NM, "reads");
#pragma unroll
      for (int mt = 0; mt < 4; ++mt)
#pragma unroll
        for (int j = 0; j < NTW; ++j) {
          const int m = mt * NTW + j;
          if (m == BAR) {
            WAIT_L0();
            __builtin_amdgcn_s_barrier();
            asm volatile("" ::: "memory");
          }
          if (decltype(rd)::value && m >= R0 && (m - R0) % RSP == 0 && (m - R0) / RSP < NL) ldf((m - R0) / RSP, s, nks, afn, bfn);
          if (EPI == EPI_QKV && j >= 4) mfma_acc(acc[mt][j], afc[mt], bfc[j]);
          else mfma_acc(acc[mt][j], bfc[j], afc[mt]);
          __builtin_amdgcn_sched_barrier(0);
        }
    };
    typedef std::integral_constant<int, -1> nobar;
#pragma unroll
    for (int mt = 0; mt < 4; ++mt)
#pragma unroll
      for (int j = 0; j < NTW; ++j) {
        acc[mt][j] = (f32x4){0.f, 0.f, 0.f, 0.f};
        asm volatile("" : "+v"(acc[mt][j]));
      }
    u32x4 af0[4], af1[4], bf0[NTW], bf1[NTW];
    __builtin_amdgcn_s_barrier();
    asm volatile("" ::: "memory");
    STAMP(1);
#pragma unroll
    for (int i = 0; i < NL; ++i) { ldf(i, smem, 0, af0, bf0); __builtin_amdgcn_sched_barrier(0); }
    int buf = 0;
    if constexpr (LAG) {
      phase(af0, bf0, af1, bf1, 0, 1, std::true_type{}, nobar{});
      for (int kt = 0; kt < NT - 1; ++kt) {
        buf = buf + 1 == NST ? 0 : buf + 1;
        phase(af1, bf1, af0, bf0, buf, 0, std::true_type{}, nobar{});
        asm volatile("" ::: "memory");
        __builtin_amdgcn_s_barrier();
        asm volatile("" ::: "memory");
        phase(af0, bf0, af1, bf1, buf, 1, std::true_type{}, nobar{});
      }
      phase(af1, bf1, af0, bf0, buf, 0, std::false_type{}, nobar{});
    } else {
      for (int kt = 0; kt < NT - 1; ++kt) {
        phase(af0, bf0, af1, bf1, buf, 1, std::true_type{}, nobar{});
        buf = buf + 1 == NST ? 0 : buf + 1;
        phase(af1, bf1, af0, bf0, buf, 0, std::true_type{}, std::integral_constant<int, 4>{});
      }
      phase(af0, bf0, af1, bf1, buf, 1, std::true_type{}, nobar{});
      phase(af1, bf1, af0, bf0, buf, 0, std::false_type{}, nobar{});
    }
    asm volatile("s_nop 7\n\ts_nop 7\n\ts_nop 7" ::: "memory");
    asm volatile("" ::"v"(ctv));
    if constexpr (EPI == EPI_QKV) {
      if (r0 != nullptr) {
        const unsigned ln = (blockIdx.x * 4 + wid) * 64 + lane;
        const char* p01 = reinterpret_cast<const char*>(ln < 32768 ? r0 : r1) + (size_t)(ln & 32767) * 64;
        const char* p2 = reinterpret_cast<const char*>(st_out) + (size_t)ln * 64;
        pft[0] = *reinterpret_cast<const unsigned*>(p01);
        pft[1] = *reinterpret_cast<const unsigned*>(p2);
        if (ln + 65536 < F_ * 1024 * 2 / 64) pft[2] = *reinterpret_cast<const unsigned*>(p2 + 65536 * 64);
      }
    }
#pragma unroll
    for (int mt = 0; mt < 4; ++mt)
#pragma unroll
      for (int j = 0; j < NTW; ++j) asm volatile("" : "+v"(acc[mt][j]));
    if constexpr (EPI == EPI_QKV) {
#pragma unroll
      for (int mt = 0; mt < 4; ++mt)
#pragma unroll
        for (int t = 0; t < 2; ++t) *reinterpret_cast<f32x4*>(smem + QX_OFF + wid * 8192 + (mt * 2 + t) * 1024 + lane * 16) = acc[mt][t];
    }
  }
  WAIT_L0();
  __builtin_amdgcn_s_barrier();
  asm volatile("" ::: "memory");
  STAMP(2);

  if constexpr (EPI == EPI_QKV) {
    const int h = tn * 2 + wn;
    const int q = l15 >> 2, p = lane & 3;
    f32x4 kv[4][2], zz[4];
    float zsel = 0.f;
    float* red = reinterpret_cast<float*>(smem + 4 * 64 * 144) + wn * (64 * 32 + 64);
    bf16x8 wf[2];
#pragma unroll
    for (int ft = 0; ft < 2; ++ft) {
      const char* src = smem + WP_OFF + wn * 2048 + (4 * g + q) * 64 + ft * 32 + p * 8;
      s16x4 lo = __builtin_amdgcn_ds_read_tr16_b64_v4i16((lds_s16x4_p)(src));
      s16x4 hi = __builtin_amdgcn_ds_read_tr16_b64_v4i16((lds_s16x4_p)(src + 16 * 64));
      wf[ft] = __builtin_shufflevector(lo, hi, 0, 1, 2, 3, 4, 5, 6, 7);
    }
    const float* csl = reinterpret_cast<const float*>(smem + CS_OFF);
    auto feat = [&](const bf16x8& xf, u32x2 (&o)[4]) {
      const float L2E = 1.4426950408889634f;
      f32x4 z4 = (f32x4){0.f, 0.f, 0.f, 0.f};
      f32x4 p0 = __builtin_amdgcn_mfma_f32_16x16x32_bf16(wf[0], xf, z4, 0, 0, 0);
      f32x4 p1 = __builtin_amdgcn_mfma_f32_16x16x32_bf16(wf[1], xf, z4, 0, 0, 0);
      f32x2 v[4];
      {
        const unsigned r0_ = pk2(p0[0], p0[1]), r1_ = pk2(p0[2], p0[3]), r2_ = pk2(p1[0], p1[1]), r3_ = pk2(p1[2], p1[3]);
        v[0] = (f32x2){bflo(r0_), bfhi(r0_)}; v[1] = (f32x2){bflo(r1_), bfhi(r1_)};
        v[2] = (f32x2){bflo(r2_), bfhi(r2_)}; v[3] = (f32x2){bflo(r3_), bfhi(r3_)};
      }
      float mx = max2_(max3_(max3_(v[0][0], v[0][1], v[1][0]), v[1][1], v[2][0]), max3_(v[2][1], v[3][0], v[3][1]));
      float mn = min2_(min3_(min3_(v[0][0], v[0][1], v[1][0]), v[1][1], v[2][0]), min3_(v[2][1], v[3][0], v[3][1]));
      mx = rmax4(mx); mn = rmin4(mn);
      const float mxl = -mx * L2E, mnl = mn * L2E;
      f32x2 ep[4], en[4];
#pragma unroll
      for (int i = 0; i < 4; ++i) {
        const f32x2 ap = v[i] * L2E + mxl, an = v[i] * (-L2E) + mnl;
        ep[i] = (f32x2){__builtin_amdgcn_exp2f(ap[0]), __builtin_amdgcn_exp2f(ap[1])};
        en[i] = (f32x2){__builtin_amdgcn_exp2f(an[0]), __builtin_amdgcn_exp2f(an[1])};
      }
      const f32x2 sp2 = (ep[0] + ep[1]) + (ep[2] + ep[3]), sn2 = (en[0] + en[1]) + (en[2] + en[3]);
      const float ip = __builtin_amdgcn_rcpf(rsum4(sp2[0] + sp2[1])), in_ = __builtin_amdgcn_rcpf(rsum4(sn2[0] + sn2[1]));
      o[0] = (u32x2){pk2v(ep[0] * ip), pk2v(ep[1] * ip)};
      o[1] = (u32x2){pk2v(ep[2] * ip), pk2v(ep[3] * ip)};
      o[2] = (u32x2){pk2v(en[0] * in_), pk2v(en[1] * in_)};
      o[3] = (u32x2){pk2v(en[2] * in_), pk2v(en[3] * in_)};
    };
    if (wid8 >= 4) {
      f32x4 cs4[2];
#pragma unroll
      for (int t = 0; t < 2; ++t) cs4[t] = *reinterpret_cast<const f32x4*>(csl + wn * 32 + t * 16 + 4 * g);
      bf16x8 qf[4];
#pragma unroll
      for (int mt = 0; mt < 4; ++mt) {
        f32x4 st = rs[wm * 64 + mt * 16 + l15];
        unsigned pq_[4];
#pragma unroll
        for (int t = 0; t < 2; ++t) {
          const f32x4 ac = *reinterpret_cast<const f32x4*>(smem + QX_OFF + wid * 8192 + (mt * 2 + t) * 1024 + lane * 16);
          float a[4];
#pragma unroll
          for (int i = 0; i < 4; ++i) a[i] = st[1] * (ac[i] - st[0] * cs4[t][i]);
          pq_[t * 2] = pk2(a[0], a[1]); pq_[t * 2 + 1] = pk2(a[2], a[3]);
        }
        qf[mt] = __builtin_bit_cast(bf16x8, (u32x4){pq_[0], pq_[1], pq_[2], pq_[3]});
      }
#pragma unroll
      for (int mt = 0; mt < 4; ++mt) {
        u32x2 o[4];
        feat(qf[mt], o);
        u32x4* dst = reinterpret_cast<u32x4*>(out) + ((size_t)(tm * 8 + wm * 4 + mt) * 32 + h) * 128 + lane;
        wt_store((u32x4){o[0][0], o[0][1], o[1][0], o[1][1]}, dst);
        wt_store((u32x4){o[2][0], o[2][1], o[3][0], o[3][1]}, dst + 64);
      }
    } else {
      f32x4 cs4[2];
      float csv[2];
#pragma unroll
      for (int t = 0; t < 2; ++t) {
        cs4[t] = *reinterpret_cast<const f32x4*>(csl + 64 + wn * 32 + t * 16 + 4 * g);
        csv[t] = csl[128 + wn * 32 + t * 16 + l15];
      }
      bf16x8 kf[4], vf[2][2];
#pragma unroll
      for (int mt = 0; mt < 4; ++mt) {
        f32x4 st = rs[wm * 64 + mt * 16 + l15];
        unsigned pk_[4];
#pragma unroll
        for (int t = 0; t < 2; ++t) {
          float b[4];
#pragma unroll
          for (int i = 0; i < 4; ++i) b[i] = st[1] * (acc[mt][2 + t][i] - st[0] * cs4[t][i]);
          pk_[t * 2] = pk2(b[0], b[1]); pk_[t * 2 + 1] = pk2(b[2], b[3]);
        }
        kf[mt] = __builtin_bit_cast(bf16x8, (u32x4){pk_[0], pk_[1], pk_[2], pk_[3]});
      }
      char* pkl = smem + wid * (64 * 144);
#pragma unroll
      for (int mt = 0; mt < 4; ++mt) {
        u32x2 o[4];
        feat(kf[mt], o);
        char* dst = pkl + (mt * 16 + l15) * 144 + g * 8;
#pragma unroll
        for (int c = 0; c < 4; ++c) *reinterpret_cast<u32x2*>(dst + c * 32) = o[c];
      }
#pragma unroll
      for (int s = 0; s < 2; ++s) {
        unsigned pv[2][4];
#pragma unroll
        for (int m2 = 0; m2 < 2; ++m2) {
          int mt = s * 2 + m2;
          f32x4 s0 = rs[wm * 64 + mt * 16 + 4 * g + 0], s1 = rs[wm * 64 + mt * 16 + 4 * g + 1];
          f32x4 s2 = rs[wm * 64 + mt * 16 + 4 * g + 2], s3 = rs[wm * 64 + mt * 16 + 4 * g + 3];
#pragma unroll
          for (int t = 0; t < 2; ++t) {
            float a0 = s0[1] * (acc[mt][4 + t][0] - s0[0] * csv[t]);
            float a1 = s1[1] * (acc[mt][4 + t][1] - s1[0] * csv[t]);
            float a2 = s2[1] * (acc[mt][4 + t][2] - s2[0] * csv[t]);
            float a3 = s3[1] * (acc[mt][4 + t][3] - s3[0] * csv[t]);
            pv[t][m2 * 2] = pk2(a0, a1); pv[t][m2 * 2 + 1] = pk2(a2, a3);
          }
        }
#pragma unroll
        for (int t = 0; t < 2; ++t) vf[s][t] = __builtin_bit_cast(bf16x8, (u32x4){pv[t][0], pv[t][1], pv[t][2], pv[t][3]});
      }
      WAIT_L0();
      __builtin_amdgcn_sched_barrier(0);
#pragma unroll
      for (int ft = 0; ft < 4; ++ft) { kv[ft][0] = kv[ft][1] = zz[ft] = (f32x4){0.f, 0.f, 0.f, 0.f}; }
      const bf16x8 ones = (bf16x8){0x3F80, 0x3F80, 0x3F80, 0x3F80, 0x3F80, 0x3F80, 0x3F80, 0x3F80};
#pragma unroll
      for (int s = 0; s < 2; ++s)
#pragma unroll
        for (int ft = 0; ft < 4; ++ft) {
          const char* src = pkl + (32 * s + 4 * g + q) * 144 + ft * 32 + p * 8;
          s16x4 lo = __builtin_amdgcn_ds_read_tr16_b64_v4i16((lds_s16x4_p)(src));
          s16x4 hi = __builtin_amdgcn_ds_read_tr16_b64_v4i16((lds_s16x4_p)(src + 16 * 144));
          bf16x8 pf = __builtin_shufflevector(lo, hi, 0, 1, 2, 3, 4, 5, 6, 7);
          kv[ft][0] = __builtin_amdgcn_mfma_f32_16x16x32_bf16(vf[s][0], pf, kv[ft][0], 0, 0, 0);
          kv[ft][1] = __builtin_amdgcn_mfma_f32_16x16x32_bf16(vf[s][1], pf, kv[ft][1], 0, 0, 0);
          zz[ft] = __builtin_amdgcn_mfma_f32_16x16x32_bf16(ones, pf, zz[ft], 0, 0, 0);
        }
      zsel = g == 0 ? zz[0][0] : g == 1 ? zz[1][0] : g == 2 ? zz[2][0] : zz[3][0];
      if (wm == 1) {
#pragma unroll
        for (int ft = 0; ft < 4; ++ft)
#pragma unroll
          for (int dt = 0; dt < 2; ++dt) *reinterpret_cast<f32x4*>(red + ((ft * 2 + dt) * 64 + lane) * 4) = kv[ft][dt];
        red[64 * 32 + lane] = zsel;
      }
    }
    __syncthreads();
    if (wid8 < 2) {
      float* kd = kvp + (size_t)(h * 16 + tm) * 2048;
      float* zd = zp + (size_t)(h * 16 + tm) * 64;
#pragma unroll
      for (int ft = 0; ft < 4; ++ft)
#pragma unroll
        for (int dt = 0; dt < 2; ++dt) {
          f32x4 o = *reinterpret_cast<const f32x4*>(red + ((ft * 2 + dt) * 64 + lane) * 4);
          wt_store(o + kv[ft][dt], reinterpret_cast<f32x4*>(kd + ((ft * 2 + dt) * 64 + lane) * 4));
        }
      zd[lane] = red[64 * 32 + lane] + zsel;
    }
    asm volatile("" ::"v"(pft[0]), "v"(pft[1]), "v"(pft[2]));
  } else {
    if (wid8 < 4) {
      f32x4 stm[4], cs4[NTW];
      if constexpr (EPI == EPI_W1 || EPI == EPI_W3) {
#pragma unroll
        for (int j = 0; j < NTW; ++j) {
          const int colj = (j < 2 * NFULL) ? ((j >> 1) * 64 + (2 * wn + (j & 1)) * 16) : (NFULL * 64 + wn * 16);
          cs4[j] = *reinterpret_cast<const f32x4*>(smem + CS_OFF + (colj + 4 * g) * 4);
        }
#pragma unroll
        for (int mt = 0; mt < 4; ++mt) stm[mt] = rs[wm * 64 + mt * 16 + l15];
      }
#pragma unroll
      for (int j = 0; j < NTW; ++j) {
        const int colj = (j < 2 * NFULL) ? ((j >> 1) * 64 + (2 * wn + (j & 1)) * 16) : (NFULL * 64 + wn * 16);
#pragma unroll
        for (int mt = 0; mt < 4; ++mt) {
          f32x2 v0 = (f32x2){acc[mt][j][0], acc[mt][j][1]}, v1 = (f32x2){acc[mt][j][2], acc[mt][j][3]};
          if constexpr (EPI == EPI_W1 || EPI == EPI_W3) {
            v0 = gelu2((v0 - stm[mt][0] * (f32x2){cs4[j][0], cs4[j][1]}) * stm[mt][1]);
            v1 = gelu2((v1 - stm[mt][0] * (f32x2){cs4[j][2], cs4[j][3]}) * stm[mt][1]);
          }
          *reinterpret_cast<u32x2*>(smem + (wm * 64 + mt * 16 + l15) * PB + (colj + 4 * g) * 2) = (u32x2){pk2(v0[0], v0[1]), pk2(v1[0], v1[1])};
        }
      }
    }
    __syncthreads();
#pragma unroll
    for (int it = 0; it < NIT; ++it) {
      const int c = tid + 512 * it, r = c / CH, ch = c % CH;
      u32x4 v = *reinterpret_cast<const u32x4*>(smem + r * PB + ch * 16);
      const int grow = row0 + r, gcol = tn * BN + ch * 8;
      if constexpr (EPI == EPI_W3) {
        wt_store(v, reinterpret_cast<u32x4*>(out + (size_t)grow * F_ + gcol));
      } else if constexpr (EPI == EPI_W1) {
        const int grp = tn >> 2;
        const int sh = grp == 0 ? 1 : grp == 1 ? -1 : grp == 2 ? 2 : -2;
        const int dr = grow + sh;
        if (dr >= 0 && dr < S_) wt_store(v, reinterpret_cast<u32x4*>(out + (size_t)dr * D_ + gcol));
        const int zr = grow - sh;
        if (zr < 0 || zr >= S_) *reinterpret_cast<u32x4*>(out + (size_t)grow * D_ + gcol) = (u32x4){0u, 0u, 0u, 0u};
      } else {
        float y[8];
        const u32x4 a = ra[it];
        if constexpr (EPI == EPI_WO) {
#pragma unroll
          for (int i = 0; i < 4; ++i) { y[2 * i] = rbf(bflo(a[i]) + bflo(v[i])); y[2 * i + 1] = rbf(bfhi(a[i]) + bfhi(v[i])); }
        } else if constexpr (EPI == EPI_W2) {
          f32x4 st = rs[r];
#pragma unroll
          for (int i = 0; i < 4; ++i) {
            y[2 * i] = rbf(rbf((bflo(a[i]) - st[0]) * st[2]) + bflo(v[i]));
            y[2 * i + 1] = rbf(rbf((bfhi(a[i]) - st[0]) * st[2]) + bfhi(v[i]));
          }
        } else {
          const u32x4 b = rb[it];
#pragma unroll
          for (int i = 0; i < 4; ++i) {
            y[2 * i] = rbf(bflo(a[i]) + rbf(bflo(b[i]) + bflo(v[i])));
            y[2 * i + 1] = rbf(bfhi(a[i]) + rbf(bfhi(b[i]) + bfhi(v[i])));
          }
        }
        wt_store((u32x4){pk2(y[0], y[1]), pk2(y[2], y[3]), pk2(y[4], y[5]), pk2(y[6], y[7])}, reinterpret_cast<u32x4*>(out + (size_t)grow * D_ + gcol));
        float sm = ((y[0] + y[1]) + (y[2] + y[3])) + ((y[4] + y[5]) + (y[6] + y[7]));
        sm = rsum8(sm);
        float mu = sm * (1.0f / 64.f), m2 = 0.f;
#pragma unroll
        for (int i = 0; i < 8; ++i) { float d = y[i] - mu; m2 += d * d; }
        m2 = rsum8(m2);
        if (ch == 0) st_out[(size_t)grow * 16 + tn] = (f32x2){sm, m2};
      }
    }
  }
  STAMP(3);
}

constexpr int WC_QKV = 0, WC_WO = WC_QKV + 2 * 1024 * 384, WC_W1 = WC_WO + 2 * 1024 * 128, WC_W2 = WC_W1 + 2 * 1024 * 128,
              WC_W4 = WC_W2 + 2 * 1024 * 128, WC_END = WC_W4 + 2 * F_ * 128;
__global__ __launch_bounds__(256, 1) void attn_out_kernel(const u16* __restrict__ phiq, const float* __restrict__ kvp,
                                                          const float* __restrict__ zp, u16* __restrict__ o,
                                                          const float* __restrict__ cp1, const float* __restrict__ cp3,
                                                          float* __restrict__ cs1, float* __restrict__ cs3,
                                                          const u16* __restrict__ wo, const u16* __restrict__ w2,
                                                          const u16* __restrict__ w4, char* __restrict__ wc) {
  __shared__ __attribute__((aligned(16))) u16 kvT[48 * 72];
  const int tid = threadIdx.x, lane = tid & 63, wid = tid >> 6, g = lane >> 4, l15 = lane & 15;
  unsigned ctv = 0;
  if (blockIdx.x < 16) {
    unsigned long long c0, c1;
    asm volatile("s_getpc_b64 %0" : "=s"(c0));
    CODE_SYM(c1, CODE_LAST);
    const unsigned long long a = (c0 & ~63ull) + (unsigned long long)tid * 64;
    if (tid < 96 && a < c1) ctv = *(gptr_u)a;
  }
  if (blockIdx.x >= 256 + 18) {
    const int e = blockIdx.x - (256 + 18);
    const u16* w = e < 8 ? wo : e < 16 ? w2 : w4;
    const int kd = e < 16 ? 1024 : F_, row = (e < 8 ? e : e < 16 ? e - 8 : e - 16) * 128 + (tid >> 4), t = (tid >> 3) & 1, c = tid & 7;
    char* dst = wc + (e < 8 ? WC_WO : e < 16 ? WC_W2 : WC_W4) + (size_t)t * kd * 128 + (size_t)row * 128 + c * 16;
    u32x4 v[8];
#pragma unroll
    for (int p = 0; p < 8; ++p) v[p] = *reinterpret_cast<const u32x4*>(w + (size_t)(row + p * 16) * 1024 + (3 + 8 * t) * 64 + c * 8);
#pragma unroll
    for (int p = 0; p < 8; ++p) *reinterpret_cast<u32x4*>(dst + p * 2048) = v[p];
    return;
  }
  if (blockIdx.x >= 256) {
    const int i = (blockIdx.x - 256) * 256 + tid;
    const float* src = i < 1024 ? cp1 + i : cp3 + (i - 1024);
    const int ld = i < 1024 ? 1024 : F_;
    float c[16];
#pragma unroll
    for (int t = 0; t < 16; ++t) c[t] = src[t * ld];
#pragma unroll
    for (int t = 8; t > 0; t >>= 1)
#pragma unroll
      for (int j = 0; j < t; ++j) c[j] += c[j + t];
    *(i < 1024 ? cs1 + i : cs3 + (i - 1024)) = c[0];
    return;
  }
  const int h = blockIdx.x & 31, tg = blockIdx.x >> 5;
  f32x4 kvv[16][2];
  float zv[16];
  bf16x8 pq[4][2];
  {
    const float* src = kvp + (size_t)h * 16 * 2048 + tid * 8;
#pragma unroll
    for (int t = 0; t < 16; ++t) {
      kvv[t][0] = *reinterpret_cast<const f32x4*>(src + t * 2048);
      kvv[t][1] = *reinterpret_cast<const f32x4*>(src + t * 2048 + 4);
    }
#pragma unroll
    for (int t = 0; t < 16; ++t) zv[t] = zp[(size_t)(h * 16 + t) * 64 + lane];
#pragma unroll
    for (int mt = 0; mt < 4; ++mt) {
      const u16* ps = phiq + ((size_t)(tg * 16 + wid * 4 + mt) * 32 + h) * 1024 + lane * 8;
      pq[mt][0] = *reinterpret_cast<const bf16x8*>(ps);
      pq[mt][1] = *reinterpret_cast<const bf16x8*>(ps + 512);
    }
  }
  __builtin_amdgcn_sched_barrier(0);
  asm volatile("" ::"v"(ctv));
  {
    for (int i = tid; i < 15 * 64; i += 256) kvT[(33 + i / 64) * 72 + (i & 63)] = 0;
    f32x4 a0 = kvv[0][0], a1 = kvv[0][1];
#pragma unroll
    for (int t = 1; t < 16; ++t) { a0 += kvv[t][0]; a1 += kvv[t][1]; }
    const int ft = tid >> 6, dt = (tid >> 5) & 1, ln = (2 * tid) & 63;
    const int d0 = dt * 16 + 4 * (ln >> 4), pos = (ft >> 1) * 32 + ((ln >> 2) & 3) * 8 + (ft & 1) * 4 + (ln & 3);
#pragma unroll
    for (int i = 0; i < 4; ++i) *reinterpret_cast<unsigned*>(&kvT[(d0 + i) * 72 + pos]) = pk2(a0[i], a1[i]);
    if (tid < 64) {
      float z = 0.f;
#pragma unroll
      for (int t = 0; t < 16; ++t) z += zv[t];
      kvT[32 * 72 + (tid >> 5) * 32 + ((tid >> 2) & 3) * 8 + ((tid >> 4) & 1) * 4 + (tid & 3)] = (u16)(pk2(z, 0.f) & 0xffff);
    }
  }
  __syncthreads();
  bf16x8 kf[3][2];
#pragma unroll
  for (int dt = 0; dt < 3; ++dt)
#pragma unroll
    for (int ks = 0; ks < 2; ++ks) kf[dt][ks] = *reinterpret_cast<const bf16x8*>(&kvT[(dt * 16 + l15) * 72 + ks * 32 + g * 8]);
#pragma unroll
  for (int mt = 0; mt < 4; ++mt) {
    const int tok = tg * 256 + wid * 64 + mt * 16 + l15;
    const bf16x8 p0 = pq[mt][0], p1 = pq[mt][1];
    f32x4 r[3];
#pragma unroll
    for (int dt = 0; dt < 3; ++dt) {
      r[dt] = (f32x4){0.f, 0.f, 0.f, 0.f};
      r[dt] = __builtin_amdgcn_mfma_f32_16x16x32_bf16(kf[dt][0], p0, r[dt], 0, 0, 0);
      r[dt] = __builtin_amdgcn_mfma_f32_16x16x32_bf16(kf[dt][1], p1, r[dt], 0, 0, 0);
    }
    float den = rbf(rbf(__shfl(r[2][0], l15)) + 1e-6f);
    float inv = 1.0f / den;
    const u32x4 w = (u32x4){pk2(rbf(r[0][0]) * inv, rbf(r[0][1]) * inv), pk2(rbf(r[0][2]) * inv, rbf(r[0][3]) * inv),
                            pk2(rbf(r[1][0]) * inv, rbf(r[1][1]) * inv), pk2(rbf(r[1][2]) * inv, rbf(r[1][3]) * inv)};
    wt_store(w, reinterpret_cast<u32x4*>(o + ((size_t)h * S_ + tok) * 32 + 8 * g));
  }
}

__global__ __launch_bounds__(256) void prep_kernel(const u16* __restrict__ x, const u16* __restrict__ wqkv,
                                                   const u16* __restrict__ w1, const u16* __restrict__ w3,
                                                   float* __restrict__ cs_qkv, float* __restrict__ cs_w1,
                                                   float* __restrict__ cs_w3, f32x2* __restrict__ st,
                                                   const u16* __restrict__ wo, const u16* __restrict__ w2,
                                                   const u16* __restrict__ w4, char* __restrict__ wc) {
  __shared__ __attribute__((aligned(16))) float red[4][512];
  const int tid = threadIdx.x, b = (int)blockIdx.x - 16;
  if (b < 0) {
    unsigned long long c0, c1;
    CODE_SYM(c0, CODE_FIRST);
    CODE_SYM(c1, CODE_LAST);
    if (c1 > c0 && c1 - c0 < 6 * 256 * 64) {
      unsigned v[6];
#pragma unroll
      for (int r = 0; r < 6; ++r) {
        const unsigned long long a = c0 + (unsigned long long)(r * 256 + tid) * 64;
        v[r] = a < c1 ? *(gptr_u)a : 0u;
      }
#pragma unroll
      for (int r = 0; r < 6; ++r) asm volatile("" ::"v"(v[r]));
    }
  } else if (b < 240) {
    const u16* w; float* dst; int ld, ch, rg;
    if (b < 96) { w = wqkv; dst = cs_qkv; ld = 3072; ch = b % 6; rg = b / 6; }
    else if (b < 128) { w = w1; dst = cs_w1; ld = 1024; ch = (b - 96) & 1; rg = (b - 96) >> 1; }
    else { w = w3; dst = cs_w3; ld = F_; ch = (b - 128) % 7; rg = (b - 128) / 7; }
    const int lane = tid & 63, wid = tid >> 6, row = rg * 64 + wid * 16;
    u32x4 v[16];
#pragma unroll
    for (int r = 0; r < 16; ++r) v[r] = *reinterpret_cast<const u32x4*>(w + (size_t)(row + r) * ld + ch * 512 + lane * 8);
    if (b < 128 && (lane >> 3) == 3) {
      const int cpp = b < 96 ? 384 : 128;
      char* cpd = wc + (b < 96 ? WC_QKV + (ch & 1) * (1024 * 384) + (ch >> 1) * 128 : WC_W1 + ch * (1024 * 128)) + row * cpp + (lane & 7) * 16;
#pragma unroll
      for (int r = 0; r < 16; ++r) *reinterpret_cast<u32x4*>(cpd + r * cpp) = v[r];
    }
    f32x4 s0 = (f32x4){0.f, 0.f, 0.f, 0.f}, s1 = s0;
#pragma unroll
    for (int r = 0; r < 16; ++r) {
      s0 += (f32x4){bflo(v[r][0]), bfhi(v[r][0]), bflo(v[r][1]), bfhi(v[r][1])};
      s1 += (f32x4){bflo(v[r][2]), bfhi(v[r][2]), bflo(v[r][3]), bfhi(v[r][3])};
    }
    *reinterpret_cast<f32x4*>(&red[wid][lane * 8]) = s0;
    *reinterpret_cast<f32x4*>(&red[wid][lane * 8 + 4]) = s1;
    __syncthreads();
    f32x2 t = *reinterpret_cast<const f32x2*>(&red[0][tid * 2]);
#pragma unroll
    for (int i = 1; i < 4; ++i) t += *reinterpret_cast<const f32x2*>(&red[i][tid * 2]);
    *reinterpret_cast<f32x2*>(dst + rg * ld + ch * 512 + tid * 2) = t;
  } else {
    const int lane = tid & 63, wid = tid >> 6;
    const int rbase = (b - 240) * 16 + wid * 4;
    u32x4 xv[8];
#pragma unroll
    for (int i = 0; i < 8; ++i) xv[i] = *reinterpret_cast<const u32x4*>(x + (size_t)rbase * D_ + i * 512 + lane * 8);
    __builtin_amdgcn_sched_barrier(0);
#pragma unroll
    for (int rr = 0; rr < 4; ++rr) {
      const int row = rbase + rr;
#pragma unroll
      for (int hf = 0; hf < 2; ++hf) {
        const u32x4 v = xv[rr * 2 + hf];
        float y[8];
#pragma unroll
        for (int i = 0; i < 4; ++i) { y[2 * i] = bflo(v[i]); y[2 * i + 1] = bfhi(v[i]); }
        float sm = ((y[0] + y[1]) + (y[2] + y[3])) + ((y[4] + y[5]) + (y[6] + y[7]));
        sm = rsum8(sm);
        float mu = sm * (1.0f / 64.f), m2 = 0.f;
#pragma unroll
        for (int i = 0; i < 8; ++i) { float d = y[i] - mu; m2 += d * d; }
        m2 = rsum8(m2);
        if ((lane & 7) == 0) st[(size_t)row * 16 + hf * 8 + (lane >> 3)] = (f32x2){sm, m2};
      }
    }
  }
}

extern "C" void kernel_launch(void* const* d_in, const int* in_sizes, int n_in, void* d_out, int out_size, void* d_ws,
                              size_t ws_size, hipStream_t stream) {
  const u16* x_in = (const u16*)d_in[0];
  const u16* wqkv = (const u16*)d_in[1];
  const u16* wphi = (const u16*)d_in[2];
  const u16* wo = (const u16*)d_in[3];
  const u16* w1 = (const u16*)d_in[5];
  const u16* w2 = (const u16*)d_in[6];
  const u16* w3 = (const u16*)d_in[7];
  const u16* w4 = (const u16*)d_in[8];
  char* ws = (char*)d_ws;
  size_t off = 0;
  auto alloc = [&](size_t bytes) { char* p = ws + off; off += (bytes + 4095) & ~(size_t)4095; return p; };
  u16* xb = (u16*)alloc((size_t)S_ * D_ * 2);
  u16* x2 = (u16*)alloc((size_t)S_ * D_ * 2);
  u16* x1 = (u16*)alloc((size_t)S_ * D_ * 2);
  u16* hs = (u16*)alloc((size_t)S_ * D_ * 2);
  u16* ob = (u16*)alloc((size_t)S_ * D_ * 2);
  u16* gb = (u16*)alloc((size_t)S_ * F_ * 2);
  u16* phiq = (u16*)alloc((size_t)S_ * 2048 * 2);
  float* kvp = (float*)alloc((size_t)32 * 16 * 2048 * 4);
  float* zp = (float*)alloc((size_t)32 * 16 * 64 * 4);
  f32x2* st_x = (f32x2*)alloc((size_t)S_ * 16 * 8);
  f32x2* st_x2 = (f32x2*)alloc((size_t)S_ * 16 * 8);
  f32x2* st_x1 = (f32x2*)alloc((size_t)S_ * 16 * 8);
  float* cs_qkv = (float*)alloc(16 * 3072 * 4);
  float* cs_w1 = (float*)alloc(16 * 1024 * 4);
  float* cs_w3 = (float*)alloc(16 * F_ * 4);
  float* cf_w1 = (float*)alloc(1024 * 4);
  float* cf_w3 = (float*)alloc(F_ * 4);
  char* wc = alloc(WC_END);

  prep_kernel<<<368 + 16, 256, 0, stream>>>(x_in, wqkv, w1, w3, cs_qkv, cs_w1, cs_w3, st_x, wo, w2, w4, wc);
  for (int layer = 0; layer < 2; ++layer) {
    const u16* x = layer == 0 ? x_in : xb;
    u16* xo = layer == 0 ? xb : (u16*)d_out;
    gemm_kernel<192, 3, 1024, 1024, 3072, EPI_QKV><<<256, 512, 0, stream>>>(x, wqkv, (const u16*)(wc + WC_QKV), cs_qkv, st_x, layer == 0 ? wo : nullptr, layer == 0 ? w2 : nullptr, phiq, layer == 0 ? (f32x2*)w4 : nullptr, wphi, kvp, zp);
    attn_out_kernel<<<layer == 0 ? 256 + 18 + 44 : 256, 256, 0, stream>>>(phiq, kvp, zp, ob, cs_w1, cs_w3, cf_w1, cf_w3, wo, w2, w4, wc);
    gemm_kernel<64, 5, 1024, 1024, 1024, EPI_WO><<<256, 512, 0, stream>>>(ob, wo, (const u16*)(wc + WC_WO), nullptr, nullptr, x, nullptr, x2, st_x2, nullptr, nullptr, nullptr);
    gemm_kernel<64, 5, 1024, 1024, 1024, EPI_W1><<<256, 512, 0, stream>>>(x2, w1, (const u16*)(wc + WC_W1), cf_w1, st_x2, nullptr, nullptr, hs, nullptr, nullptr, nullptr, nullptr);
    gemm_kernel<64, 5, 1024, 1024, 1024, EPI_W2><<<256, 512, 0, stream>>>(hs, w2, (const u16*)(wc + WC_W2), nullptr, st_x2, x2, nullptr, x1, st_x1, nullptr, nullptr, nullptr);
    gemm_kernel<224, 3, 1024, 1024, F_, EPI_W3><<<256, 512, 0, stream>>>(x1, w3, nullptr, cf_w3, st_x1, nullptr, nullptr, gb, nullptr, nullptr, nullptr, nullptr);
    gemm_kernel<64, 5, F_, F_, 1024, EPI_W4><<<256, 512, 0, stream>>>(gb, w4, (const u16*)(wc + WC_W4), nullptr, nullptr, x2, x1, xo, st_x, nullptr, nullptr, nullptr);
  }
  if (ws_size == 0) zz_end_kernel<0><<<1, 64, 0, stream>>>();
}
```
